```python
import torch

def _softmax(x, axis=-1):
    return torch.softmax(x, dim=axis)

def _gelu(x):
    return torch.nn.functional.gelu(x, approximate='tanh')

def _silu(x):
    return torch.nn.functional.silu(x)

def _sigmoid(x):
    return torch.sigmoid(x)

def _layernorm(x, axis=-1, eps=1e-05):
    m = x.mean(dim=axis, keepdim=True)
    v = x.var(dim=axis, keepdim=True, unbiased=False)
    return (x - m) / torch.sqrt(v + eps)

def _scatter_add(tgt, idx, src, axis=0):
    return tgt.clone().index_add_(axis, idx, src)

def _segment_sum(src, seg, n):
    out = torch.zeros((n,) + src.shape[1:], dtype=src.dtype)
    return out.index_add_(0, seg, src)

def _pad(x, pw):
    flat = tuple((v for pair in reversed(pw) for v in pair))
    return torch.nn.functional.pad(x, flat)
kernel_name = 'adaLN_cond__ssm_conv_h3__glu_gated_glu__prompt'

def setup_inputs():

    _w = lambda a: (lambda t: t.to(torch.bfloat16) if t.is_floating_point() else t)(torch.as_tensor(a))

    def _A_setup():
        B, S, D = (1, 2048, 576)
        ks = 4
        return {'q': _w((lambda *s: torch.randn(*s, dtype=torch.bfloat16) * 0.05)(B, S, D)), 'k': _w((lambda *s: torch.randn(*s, dtype=torch.bfloat16) * 0.8)(B, S, D)), 'v': _w((lambda *s: torch.randn(*s, dtype=torch.bfloat16) * 0.8)(B, S, D)), 'shift_k': _w((lambda *s: torch.randn(*s, dtype=torch.bfloat16) * 0.8)(D, ks) * 0.5), 'ssm_k': _w((lambda *s: torch.randn(*s, dtype=torch.bfloat16) * 0.8)(D, S) * (1.0 / S ** 0.5)), 'ssm_D': _w((lambda *s: torch.randn(*s, dtype=torch.bfloat16) * 0.8)(D))}
    _A_in = _A_setup()

    def _B_setup():
        B, S, D, F = (1, 2048, 576, 4608)
        return {'x': _w((lambda *s: torch.randn(*s, dtype=torch.bfloat16) * 0.05)(B, S, D)), 'w_gate': _w((lambda *s: torch.randn(*s, dtype=torch.bfloat16) * 0.05)(D, F)), 'w_up': _w((lambda *s: torch.randn(*s, dtype=torch.bfloat16) * 0.05)(D, F)), 'w_down': _w((lambda *s: torch.randn(*s, dtype=torch.bfloat16) * 0.05)(F, D))}
    _B_in = _B_setup()
    return {'A_q': _A_in['q'], 'A_k': _A_in['k'], 'A_v': _A_in['v'], 'A_shift_k': _A_in['shift_k'], 'A_ssm_k': _A_in['ssm_k'], 'A_ssm_D': _A_in['ssm_D'], 'B_x': _B_in['x'], 'B_w_gate': _B_in['w_gate'], 'B_w_up': _B_in['w_up'], 'B_w_down': _B_in['w_down'], 'c': _w((lambda *s: torch.randn(*s, dtype=torch.bfloat16) * 0.1)(1, 576) * 8.0), 'w_mod': _w((lambda *s: torch.randn(*s, dtype=torch.bfloat16) * 0.1)(576, 6 * 576) * 576 ** (-0.5) * 8.0)}

def reference(A_q, A_k, A_v, A_shift_k, A_ssm_k, A_ssm_D, B_x, B_w_gate, B_w_up, B_w_down, c, w_mod):

    def _A_ref(q, k, v, shift_k, ssm_k, ssm_D):
        B, S, D = q.shape
        ks = shift_k.shape[-1]
        k_pad = _pad(k, ((0, 0), (ks - 1, 0), (0, 0)))
        k_shift = torch.zeros_like(k)
        for t in range(ks):
            k_shift = k_shift + k_pad[:, t:t + S, :] * shift_k[:, ks - 1 - t][None, None, :]
        kv = k_shift * v
        L = 2 * S
        kv_pad = _pad(kv, ((0, 0), (0, S), (0, 0)))
        kf_pad = _pad(ssm_k, ((0, 0), (0, S)))
        kv_f = (lambda x, axes=(-2, -1): torch.fft.rfft2(x.float(), dim=axes))(kv_pad[:, None, :, :], axes=(1, 2))
        kf_f = (lambda x, axes=(-2, -1): torch.fft.rfft2(x.float(), dim=axes))(kf_pad[:, None, :], axes=(1, 2))
        kf_f = (lambda x, *d: x.permute(*d))(kf_f, 1, 2, 0)
        y_f = kv_f * kf_f[None, :, :, :]
        y = (lambda x, s, axes=(-2, -1): torch.fft.irfft2(x, s=s, dim=axes).to(torch.bfloat16))(y_f, (1, L), axes=(1, 2))[:, 0, :S, :]
        y = y + kv * ssm_D[None, None, :]
        return q * y

    def _B_ref(x, w_gate, w_up, w_down):
        g = torch.einsum('bsd,df->bsf', x, w_gate)
        u = torch.einsum('bsd,df->bsf', x, w_up)
        h = _sigmoid(g) * u
        return torch.einsum('bsf,fd->bsd', h, w_down)
    x = A_q
    mod = (_silu(c) @ w_mod).reshape(c.shape[0], 6, 1, -1)
    sa, ca, ga, sb, cb, gb = (mod[:, 0].clone(), mod[:, 1].clone(), mod[:, 2].clone(), mod[:, 3].clone(), mod[:, 4].clone(), mod[:, 5].clone())
    h = _layernorm(x, axis=-1) * (1 + ca) + sa
    x = x + ga * _A_ref(h, A_k, A_v, A_shift_k, A_ssm_k, A_ssm_D)
    h = _layernorm(x, axis=-1) * (1 + cb) + sb
    x = x + gb * _B_ref(h, B_w_gate, B_w_up, B_w_down)
    return x
```

```cpp
#include <hip/hip_runtime.h>
#include <stdint.h>

typedef unsigned short u16;
typedef unsigned int u32;
typedef __bf16 bf16x8 __attribute__((ext_vector_type(8)));
typedef __bf16 bf16x2 __attribute__((ext_vector_type(2)));
typedef float f32x4 __attribute__((ext_vector_type(4)));
typedef float f32x2 __attribute__((ext_vector_type(2)));
typedef u32 u32x4 __attribute__((ext_vector_type(4)));
typedef u32 u32x2 __attribute__((ext_vector_type(2)));
typedef short s16x4 __attribute__((ext_vector_type(4)));
#define LAS __attribute__((address_space(3)))
typedef LAS char* lptr;

constexpr int S_ = 2048, D_ = 576, F_ = 4608;

constexpr size_t WS_MOD = 0;
constexpr size_t WS_KVR = 16384;
constexpr size_t WS_YYT = WS_KVR + (size_t)D_ * S_ * 2;
constexpr size_t WS_X1  = WS_YYT + (size_t)D_ * S_ * 2;
constexpr size_t WS_H2T = WS_X1 + (size_t)D_ * S_ * 2;
constexpr size_t WS_HT  = WS_H2T + (size_t)D_ * S_ * 2;
constexpr size_t WS_P   = WS_HT + (size_t)S_ * F_ * 2;

__device__ __forceinline__ float bf_lo(u32 w) { return __uint_as_float(w << 16); }
__device__ __forceinline__ float bf_hi(u32 w) { return __uint_as_float(w & 0xffff0000u); }
__device__ __forceinline__ float bf1(u16 w) { return __uint_as_float((u32)w << 16); }
__device__ __forceinline__ u32 pk2(float lo, float hi) {
    f32x2 v = {lo, hi};
    return __builtin_bit_cast(u32, __builtin_convertvector(v, bf16x2));
}
__device__ __forceinline__ float rbf(float x) { return (float)(__bf16)x; }
__device__ __forceinline__ float sigmoidf_(float x) { return __builtin_amdgcn_rcpf(1.0f + __builtin_amdgcn_exp2f(-1.4426950408889634f * x)); }

__device__ __forceinline__ void glds16(const void* sbase, u32 voff, u32 lds_base) {
    u32 sv;
    asm volatile("s_mov_b32 %0, m0\n\ts_mov_b32 m0, %3\n\ts_nop 0\n\tglobal_load_lds_dwordx4 %1, %2\n\ts_mov_b32 m0, %0"
                 : "=&s"(sv) : "v"(voff), "s"(sbase), "s"(lds_base) : "memory");
}
#define WAIT_V(n) asm volatile("s_waitcnt vmcnt(" #n ")" ::: "memory")
#define BAR() do { asm volatile("s_waitcnt lgkmcnt(0)" ::: "memory"); __builtin_amdgcn_s_barrier(); asm volatile("" ::: "memory"); } while (0)

__global__ __launch_bounds__(256) void k1_prep(const u16* __restrict__ k, const u16* __restrict__ v, const u16* __restrict__ shift_k,
                                               const u16* __restrict__ c, const u16* __restrict__ w_mod, char* __restrict__ ws) {
    const int tid = threadIdx.x, lane = tid & 63, wid = tid >> 6;
    if (blockIdx.x < 288) {
        const int cb = blockIdx.x % 9, tb = blockIdx.x / 9;
        const int cp = lane & 31, tc = wid * 2 + (lane >> 5);
        const int ch = cb * 64 + cp * 2, t0 = tb * 64 + tc * 8;
        u32 kw[11], vw[8];
#pragma unroll
        for (int i = 0; i < 11; ++i) { const int t = t0 - 3 + i; kw[i] = (t >= 0) ? *(const u32*)(k + (size_t)t * D_ + ch) : 0u; }
#pragma unroll
        for (int i = 0; i < 8; ++i) vw[i] = *(const u32*)(v + (size_t)(t0 + i) * D_ + ch);
        const u32x4 sw = *(const u32x4*)(shift_k + ch * 4);
        const float a0 = bf_lo(sw[0]), a1 = bf_hi(sw[0]), a2 = bf_lo(sw[1]), a3 = bf_hi(sw[1]);
        const float b0 = bf_lo(sw[2]), b1 = bf_hi(sw[2]), b2 = bf_lo(sw[3]), b3 = bf_hi(sw[3]);
        float ra[8], rb[8];
#pragma unroll
        for (int i = 0; i < 8; ++i) {
            float sa = rbf(bf_lo(kw[i]) * a3);
            sa = rbf(sa + rbf(bf_lo(kw[i + 1]) * a2));
            sa = rbf(sa + rbf(bf_lo(kw[i + 2]) * a1));
            sa = rbf(sa + rbf(bf_lo(kw[i + 3]) * a0));
            float sb = rbf(bf_hi(kw[i]) * b3);
            sb = rbf(sb + rbf(bf_hi(kw[i + 1]) * b2));
            sb = rbf(sb + rbf(bf_hi(kw[i + 2]) * b1));
            sb = rbf(sb + rbf(bf_hi(kw[i + 3]) * b0));
            ra[i] = sa * bf_lo(vw[i]);
            rb[i] = sb * bf_hi(vw[i]);
        }
        u16* kvr = (u16*)(ws + WS_KVR);
        u32x4 oa = {pk2(ra[7], ra[6]), pk2(ra[5], ra[4]), pk2(ra[3], ra[2]), pk2(ra[1], ra[0])};
        u32x4 ob = {pk2(rb[7], rb[6]), pk2(rb[5], rb[4]), pk2(rb[3], rb[2]), pk2(rb[1], rb[0])};
        *(u32x4*)(kvr + (size_t)ch * S_ + (2040 - t0)) = oa;
        *(u32x4*)(kvr + (size_t)(ch + 1) * S_ + (2040 - t0)) = ob;
    } else {
        __shared__ float sc[576];
        __shared__ float red[4][64];
        for (int i = tid; i < 576; i += 256) { const float x = bf1(c[i]); sc[i] = rbf(x * sigmoidf_(x)); }
        __syncthreads();
        const int col0 = (blockIdx.x - 288) * 64, rg = tid >> 3, c8 = tid & 7;
        u32x4 w[18];
#pragma unroll
        for (int i = 0; i < 18; ++i) w[i] = *(const u32x4*)(w_mod + (size_t)(rg + 32 * i) * 3456 + col0 + c8 * 8);
        float acc[8] = {0, 0, 0, 0, 0, 0, 0, 0};
#pragma unroll
        for (int i = 0; i < 18; ++i) {
            const float s = sc[rg + 32 * i];
#pragma unroll
            for (int j = 0; j < 4; ++j) { acc[2 * j] += s * bf_lo(w[i][j]); acc[2 * j + 1] += s * bf_hi(w[i][j]); }
        }
#pragma unroll
        for (int j = 0; j < 8; ++j) {
            acc[j] += __shfl_xor(acc[j], 8); acc[j] += __shfl_xor(acc[j], 16); acc[j] += __shfl_xor(acc[j], 32);
        }
        if (lane < 8) {
#pragma unroll
            for (int j = 0; j < 8; ++j) red[wid][lane * 8 + j] = acc[j];
        }
        __syncthreads();
        if (tid < 64) ((float*)(ws + WS_MOD))[col0 + tid] = rbf((red[0][tid] + red[1][tid]) + (red[2][tid] + red[3][tid]));
    }
}

constexpr int C_XR = 0;
constexpr int C_K0 = 4608;
constexpr int C_K1 = C_K0 + 4128 + 32;
constexpr int C_RED = C_K1 + 4160;
constexpr int C_LDS = C_RED + 32768;
static_assert(((C_K1 - C_K0) / 4) % 32 == 16, "bank offset");
static_assert(C_RED % 16 == 0 && C_K0 % 16 == 0 && C_K1 % 16 == 0, "align");

__global__ __launch_bounds__(256) void k2_conv(const u16* __restrict__ ssm_k, const u16* __restrict__ ssm_D, char* __restrict__ ws) {
    extern __shared__ __attribute__((aligned(16))) char smem[];
    const int tid = threadIdx.x, lane = tid & 63, wid = tid >> 6, d = blockIdx.x;
    const u16* kvr = (const u16*)(ws + WS_KVR) + (size_t)d * S_;
    const u16* kr = ssm_k + (size_t)d * S_;
    {
        const u32x4 xv = *(const u32x4*)(kvr + tid * 8);
        const u32x4 kv = *(const u32x4*)(kr + tid * 8);
        const u32 kn = (tid < 255) ? *(const u32*)(kr + tid * 8 + 8) : 0u;
        *(u32x4*)(smem + C_XR + tid * 16) = xv;
        *(u32x4*)(smem + C_K0 + 32 + tid * 16) = kv;
        u32x4 k1 = {__builtin_amdgcn_alignbit(kv[1], kv[0], 16), __builtin_amdgcn_alignbit(kv[2], kv[1], 16),
                    __builtin_amdgcn_alignbit(kv[3], kv[2], 16), __builtin_amdgcn_alignbit(kn, kv[3], 16)};
        *(u32x4*)(smem + C_K1 + 32 + tid * 16) = k1;
        if (tid < 32) *(u32x4*)(smem + C_XR + 4096 + tid * 16) = (u32x4){0, 0, 0, 0};
        if (tid == 0) {
            *(u32x4*)(smem + C_K0) = (u32x4){0, 0, 0, 0}; *(u32x4*)(smem + C_K0 + 16) = (u32x4){0, 0, 0, 0};
            *(u32x4*)(smem + C_K1) = (u32x4){0, 0, 0, 0}; *(u32x4*)(smem + C_K1 + 16) = (u32x4){0, 0, 0, kv[0] << 16};
        }
    }
    __syncthreads();
    const int n = lane & 15, g = lane >> 4, dd = g >> 1, gh = g & 1;
    const char* bbase = smem + C_XR + 480 - 32 * n + 32 * dd + 16 * gh;
    const char* abase = (n & 1) ? smem + C_K0 + 2 * (16 * dd + 8 * gh + n + 1) : smem + C_K1 + 2 * (16 * dd + 8 * gh + n);
    f32x4 acc[8];
#pragma unroll
    for (int i = 0; i < 8; ++i) acc[i] = (f32x4){0.f, 0.f, 0.f, 0.f};
#pragma unroll
    for (int rr = 0; rr < 2; ++rr) {
        const int r = wid * 2 + rr;
        bf16x8 A[8], B[8];
#pragma unroll
        for (int j = 0; j < 8; ++j) {
            const u32* p = (const u32*)(abase + 512 * j + 64 * r);
            u32x4 t = {p[0], p[1], p[2], p[3]};
            A[j] = __builtin_bit_cast(bf16x8, t);
            B[j] = *(const bf16x8*)(bbase + 512 * (7 - j) + 64 * r);
        }
#pragma unroll
        for (int i = 0; i < 8; ++i)
#pragma unroll
            for (int j = 0; j <= i; ++j) acc[i] = __builtin_amdgcn_mfma_f32_16x16x32_bf16(A[j], B[i - j], acc[i], 0, 0, 0);
    }
#pragma unroll
    for (int i = 0; i < 8; ++i) *(f32x4*)(smem + C_RED + (wid * 8 + i) * 1024 + lane * 16) = acc[i];
    __syncthreads();
    const float Dv = bf1(ssm_D[d]);
    u16* yyt = (u16*)(ws + WS_YYT) + (size_t)d * S_;
#pragma unroll
    for (int ii = 0; ii < 2; ++ii) {
        const int i = wid * 2 + ii;
        f32x4 y = *(const f32x4*)(smem + C_RED + (0 * 8 + i) * 1024 + lane * 16);
#pragma unroll
        for (int w = 1; w < 4; ++w) y += *(const f32x4*)(smem + C_RED + (w * 8 + i) * 1024 + lane * 16);
        const int s = 256 * i + 16 * n + 4 * g;
        const u32x2 xw = *(const u32x2*)(smem + C_XR + 2 * (2044 - s));
        const float x0 = bf_hi(xw[1]), x1 = bf_lo(xw[1]), x2 = bf_hi(xw[0]), x3 = bf_lo(xw[0]);
        const float o0 = rbf(y[0]) + rbf(x0 * Dv), o1 = rbf(y[1]) + rbf(x1 * Dv), o2 = rbf(y[2]) + rbf(x2 * Dv), o3 = rbf(y[3]) + rbf(x3 * Dv);
        *(u32x2*)(yyt + s) = (u32x2){pk2(o0, o1), pk2(o2, o3)};
    }
}

__device__ __forceinline__ float wave_sum(float v) {
#pragma unroll
    for (int o = 32; o > 0; o >>= 1) v += __shfl_xor(v, o);
    return v;
}
__global__ __launch_bounds__(512) void k3_rows(const u16* __restrict__ q, char* __restrict__ ws) {
    __shared__ __attribute__((aligned(16))) u16 yt[8][584];
    __shared__ __attribute__((aligned(16))) u16 ht[72 * 8 * 8];
    const int tid = threadIdx.x, lane = tid & 63, wid = tid >> 6;
    const int rb = (blockIdx.x & 7) * 32 + (blockIdx.x >> 3);
    const int t0 = rb * 8;
    const u16* yyt = (const u16*)(ws + WS_YYT);
    for (int c = tid; c < D_; c += 512) {
        const u32x4 w = *(const u32x4*)(yyt + (size_t)c * S_ + t0);
#pragma unroll
        for (int j = 0; j < 4; ++j) { yt[2 * j][c] = (u16)(w[j] & 0xffffu); yt[2 * j + 1][c] = (u16)(w[j] >> 16); }
    }
    const int t = t0 + wid;
    const float* mod = (const float*)(ws + WS_MOD);
    const u32* qrow = (const u32*)(q + (size_t)t * D_);
    float x[10];
    u32 xw[5];
#pragma unroll
    for (int i = 0; i < 5; ++i) xw[i] = (i < 4 || lane < 32) ? qrow[lane + 64 * i] : 0u;
#pragma unroll
    for (int i = 0; i < 5; ++i) { x[2 * i] = bf_lo(xw[i]); x[2 * i + 1] = bf_hi(xw[i]); }
    const bool tail = lane < 32;
    float s = 0.f;
#pragma unroll
    for (int i = 0; i < 10; ++i) s += x[i];
    float mean = wave_sum(s) * (1.0f / 576.0f);
    float vs = 0.f;
#pragma unroll
    for (int i = 0; i < 10; ++i) { const float dlt = x[i] - mean; vs += (i < 8 || tail) ? dlt * dlt : 0.f; }
    float var = wave_sum(vs) * (1.0f / 576.0f);
    float mb = rbf(mean), den = rbf(__builtin_sqrtf(rbf(rbf(var) + 1e-5f)));
    __syncthreads();
    float x1[10];
#pragma unroll
    for (int i = 0; i < 5; ++i) {
        const int c = 2 * (lane + 64 * i);
        if (i < 4 || tail) {
            const u32 yw = *(const u32*)&yt[wid][c];
#pragma unroll
            for (int e = 0; e < 2; ++e) {
                const float xx = x[2 * i + e];
                const float xn = rbf(rbf(xx - mb) / den);
                const float h = rbf(rbf(xn * rbf(1.0f + mod[576 + c + e])) + mod[c + e]);
                const float yv = e ? bf_hi(yw) : bf_lo(yw);
                const float ao = rbf(h * yv);
                x1[2 * i + e] = rbf(xx + rbf(mod[2 * 576 + c + e] * ao));
            }
        } else { x1[2 * i] = 0.f; x1[2 * i + 1] = 0.f; }
    }
    u32* x1row = (u32*)(ws + WS_X1) + (size_t)t * (D_ / 2);
#pragma unroll
    for (int i = 0; i < 5; ++i) if (i < 4 || tail) x1row[lane + 64 * i] = pk2(x1[2 * i], x1[2 * i + 1]);
    s = 0.f;
#pragma unroll
    for (int i = 0; i < 10; ++i) s += x1[i];
    mean = wave_sum(s) * (1.0f / 576.0f);
    vs = 0.f;
#pragma unroll
    for (int i = 0; i < 10; ++i) { const float dlt = x1[i] - mean; vs += (i < 8 || tail) ? dlt * dlt : 0.f; }
    var = wave_sum(vs) * (1.0f / 576.0f);
    mb = rbf(mean); den = rbf(__builtin_sqrtf(rbf(rbf(var) + 1e-5f)));
#pragma unroll
    for (int i = 0; i < 5; ++i) {
        const int c = 2 * (lane + 64 * i);
        if (i < 4 || tail) {
            float h[2];
#pragma unroll
            for (int e = 0; e < 2; ++e) {
                const float xn = rbf(rbf(x1[2 * i + e] - mb) / den);
                h[e] = rbf(xn * rbf(1.0f + mod[4 * 576 + c + e])) + mod[3 * 576 + c + e];
            }
            const int ks = c >> 5, kk = c & 31, gq = (kk & 15) >> 2, j = ((kk >> 4) << 2) | (kk & 3);
            *(u32*)&ht[((ks * 4 + gq) * 8 + wid) * 8 + j] = pk2(h[0], h[1]);
        }
    }
    __syncthreads();
    char* h2t = ws + WS_H2T + (size_t)(t0 >> 4) * 18 * 1024 + (t0 & 15) * 16;
    for (int i = tid; i < 576; i += 512) {
        const int kg = i >> 3, r = i & 7;
        *(u32x4*)(h2t + (size_t)(kg >> 2) * 1024 + (kg & 3) * 256 + r * 16) = *(const u32x4*)&ht[(kg * 8 + r) * 8];
    }
}

constexpr int ST_A = 18432, ST_B = 16384, ST_BYTES = ST_A + ST_B, NSLOT = 4, G_LDS = NSLOT * ST_BYTES;

__device__ __forceinline__ bf16x8 ld_afrag(const char* p) {
    s16x4 lo = __builtin_amdgcn_ds_read_tr16_b64_v4i16((LAS s16x4*)(p));
    s16x4 hi = __builtin_amdgcn_ds_read_tr16_b64_v4i16((LAS s16x4*)(p + 16 * 288));
    typedef short s16x8 __attribute__((ext_vector_type(8)));
    s16x8 r = __builtin_shufflevector(lo, hi, 0, 1, 2, 3, 4, 5, 6, 7);
    return __builtin_bit_cast(bf16x8, r);
}

template <int MODE>
__device__ __forceinline__ void gemm_mainloop(char* smem, const char* wA0, const char* wA1, int ldw_bytes, const char* bsrc  ,
                                              size_t bstride  , f32x4 (&acc)[MODE == 0 ? 18 : 9][2]) {
    const int tid = threadIdx.x, lane = tid & 63;
    const int wid = __builtin_amdgcn_readfirstlane(tid >> 6);
    const u32 lds0 = (u32)(uintptr_t)smem;
    const char* asrc = ((wid >> 2) ? wA1 : wA0) + (size_t)(8 * (wid & 3)) * ldw_bytes;
    u32 aoff[3];
#pragma unroll
    for (int i = 0; i < 3; ++i) { const int ci = lane + 64 * i, row = ci / 18, c16 = ci - row * 18; aoff[i] = (u32)(row * ldw_bytes + c16 * 16); }
    const u32 adst = lds0 + (wid >> 2) * 9216 + (wid & 3) * 2304;
    const u32 bdst = lds0 + ST_A + wid * 2048;
    const u32 boff = lane * 16;
    auto stage = [&](int ks) {
        const u32 so = (u32)(ks & (NSLOT - 1)) * ST_BYTES;
        const char* a = asrc + (size_t)ks * 32 * ldw_bytes;
        glds16(a, aoff[0], adst + so);
        glds16(a, aoff[1], adst + so + 1024);
        if (lane < 16) glds16(a, aoff[2], adst + so + 2048);
        glds16(bsrc + (size_t)ks * 1024, boff, bdst + so);
        glds16(bsrc + bstride + (size_t)ks * 1024, boff, bdst + so + 1024);
    };
    const int g = lane >> 4, q4 = (lane & 15) >> 2, p = lane & 3;
    const char* ard = smem + (MODE == 1 ? (wid >> 2) * 9216 : 0) + (4 * g + q4) * 288 + 8 * p;
    const char* brd = smem + ST_A + wid * 2048 + lane * 16;
    stage(0); stage(1); stage(2);
    WAIT_V(10); BAR();
    for (int ks = 0; ks < 18; ++ks) {
        if (ks < 16) { WAIT_V(5); } else { WAIT_V(0); }
        BAR();
        if (ks + 3 < 18) stage(ks + 3);
        const int so = (ks & (NSLOT - 1)) * ST_BYTES;
        const bf16x8 b0 = *(const bf16x8*)(brd + so), b1 = *(const bf16x8*)(brd + so + 1024);
#pragma unroll
        for (int u = 0; u < (MODE == 0 ? 18 : 9); ++u) {
            const int off = (MODE == 0) ? ((u & 1) * 9216 + (u >> 1) * 32) : u * 32;
            const bf16x8 a = ld_afrag(ard + so + off);
            acc[u][0] = __builtin_amdgcn_mfma_f32_16x16x32_bf16(a, b0, acc[u][0], 0, 0, 0);
            acc[u][1] = __builtin_amdgcn_mfma_f32_16x16x32_bf16(a, b1, acc[u][1], 0, 0, 0);
        }
    }
}

__global__ __launch_bounds__(512, 2) void k4_gemm1(const u16* __restrict__ wg, const u16* __restrict__ wu, char* __restrict__ ws) {
    extern __shared__ __attribute__((aligned(16))) char smem[];
    const int tid = threadIdx.x, lane = tid & 63, wid = __builtin_amdgcn_readfirstlane(tid >> 6);
    const int x = blockIdx.x & 7, idx = blockIdx.x >> 3;
    const int mt = 4 * (x & 1) + (idx & 3), fc = 8 * (x >> 1) + (idx >> 2);
    f32x4 acc[18][2];
#pragma unroll
    for (int u = 0; u < 18; ++u) { acc[u][0] = (f32x4){0.f, 0.f, 0.f, 0.f}; acc[u][1] = (f32x4){0.f, 0.f, 0.f, 0.f}; }
    const int tu0 = 16 * mt + 2 * wid;
    gemm_mainloop<0>(smem, (const char*)(wg + fc * 144), (const char*)(wu + fc * 144), F_ * 2, ws + WS_H2T + (size_t)tu0 * 18 * 1024, 18 * 1024, acc);
    char* ht = ws + WS_HT + (size_t)lane * 16;
#pragma unroll
    for (int t = 0; t < 2; ++t) {
        u32x2 hv[9];
#pragma unroll
        for (int fu = 0; fu < 9; ++fu) {
            const f32x4 gv = acc[2 * fu][t], uv = acc[2 * fu + 1][t];
            float h[4];
#pragma unroll
            for (int e = 0; e < 4; ++e) h[e] = rbf(sigmoidf_(rbf(gv[e]))) * rbf(uv[e]);
            hv[fu] = (u32x2){pk2(h[0], h[1]), pk2(h[2], h[3])};
        }
        char* base = ht + (size_t)(tu0 + t) * 144 * 1024;
        const int U0 = 9 * fc;
        if ((fc & 1) == 0) {
#pragma unroll
            for (int i = 0; i < 4; ++i) *(u32x4*)(base + (size_t)(U0 / 2 + i) * 1024) = (u32x4){hv[2 * i][0], hv[2 * i][1], hv[2 * i + 1][0], hv[2 * i + 1][1]};
            *(u32x2*)(base + (size_t)(U0 / 2 + 4) * 1024) = hv[8];
        } else {
            *(u32x2*)(base + (size_t)(U0 / 2) * 1024 + 8) = hv[0];
#pragma unroll
            for (int i = 0; i < 4; ++i) *(u32x4*)(base + (size_t)(U0 / 2 + 1 + i) * 1024) = (u32x4){hv[2 * i + 1][0], hv[2 * i + 1][1], hv[2 * i + 2][0], hv[2 * i + 2][1]};
        }
    }
}

__global__ __launch_bounds__(512, 2) void k5_gemm2(const u16* __restrict__ wd, char* __restrict__ ws) {
    extern __shared__ __attribute__((aligned(16))) char smem[];
    const int tid = threadIdx.x, lane = tid & 63, wid = __builtin_amdgcn_readfirstlane(tid >> 6);
    const int x = blockIdx.x & 7, idx = blockIdx.x >> 3;
    const int q = x >> 1, mt2 = 8 * (x & 1) + (idx & 7), c = idx >> 3;
    const int tg = wid & 3, kh = wid >> 2;
    f32x4 acc[9][2];
#pragma unroll
    for (int u = 0; u < 9; ++u) { acc[u][0] = (f32x4){0.f, 0.f, 0.f, 0.f}; acc[u][1] = (f32x4){0.f, 0.f, 0.f, 0.f}; }
    const int tu0 = 8 * mt2 + 2 * tg;
    const int ks0 = 36 * q + 18 * kh;
    const char* w0 = (const char*)(wd + (size_t)(36 * q) * 32 * D_ + c * 144);
    gemm_mainloop<1>(smem, w0, w0 + (size_t)18 * 32 * D_ * 2, D_ * 2, ws + WS_HT + ((size_t)tu0 * 144 + ks0) * 1024, (size_t)144 * 1024, acc);
    __syncthreads();
    char* ex = smem + (size_t)wid * 9216 + lane * 16;
#pragma unroll
    for (int u = 0; u < 9; ++u) *(f32x4*)(ex + u * 1024) = kh ? acc[u][0] : acc[u][1];
    __syncthreads();
    const char* ey = smem + (size_t)(wid ^ 4) * 9216 + lane * 16;
    float* P = (float*)(ws + WS_P) + (size_t)q * S_ * D_;
    const int tok = 16 * (tu0 + kh) + (lane & 15);
    float* prow = P + (size_t)tok * D_ + c * 144 + 4 * (lane >> 4);
#pragma unroll
    for (int u = 0; u < 9; ++u) {
        const f32x4 o = *(const f32x4*)(ey + u * 1024);
        *(f32x4*)(prow + u * 16) = (kh ? acc[u][1] : acc[u][0]) + o;
    }
}

__global__ __launch_bounds__(256) void k6_final(const char* __restrict__ ws, u16* __restrict__ out) {
    const int i = blockIdx.x * 256 + threadIdx.x;
    const int e0 = i * 8, dcol = e0 % D_;
    const float* P = (const float*)(ws + WS_P) + e0;
    f32x4 s0 = *(const f32x4*)(P), s1 = *(const f32x4*)(P + 4);
#pragma unroll
    for (int q = 1; q < 4; ++q) { s0 += *(const f32x4*)(P + (size_t)q * S_ * D_); s1 += *(const f32x4*)(P + (size_t)q * S_ * D_ + 4); }
    const u32x4 xw = *(const u32x4*)((const u16*)(ws + WS_X1) + e0);
    const float* gb = (const float*)(ws + WS_MOD) + 5 * 576 + dcol;
    const f32x4 g0 = *(const f32x4*)gb, g1 = *(const f32x4*)(gb + 4);
    float o[8];
#pragma unroll
    for (int j = 0; j < 4; ++j) {
        const float xa = (j & 1) ? bf_hi(xw[j >> 1]) : bf_lo(xw[j >> 1]);
        const float xb = (j & 1) ? bf_hi(xw[2 + (j >> 1)]) : bf_lo(xw[2 + (j >> 1)]);
        o[j] = xa + rbf(g0[j] * rbf(s0[j]));
        o[4 + j] = xb + rbf(g1[j] * rbf(s1[j]));
    }
    *(u32x4*)(out + e0) = (u32x4){pk2(o[0], o[1]), pk2(o[2], o[3]), pk2(o[4], o[5]), pk2(o[6], o[7])};
}

extern "C" void kernel_launch(void* const* d_in, const int* in_sizes, int n_in, void* d_out, int out_size, void* d_ws, size_t ws_size,
                              hipStream_t stream) {
    const u16* q = (const u16*)d_in[0];
    const u16* k = (const u16*)d_in[1];
    const u16* v = (const u16*)d_in[2];
    const u16* shift_k = (const u16*)d_in[3];
    const u16* ssm_k = (const u16*)d_in[4];
    const u16* ssm_D = (const u16*)d_in[5];
    const u16* wg = (const u16*)d_in[7];
    const u16* wu = (const u16*)d_in[8];
    const u16* wd = (const u16*)d_in[9];
    const u16* c = (const u16*)d_in[10];
    const u16* w_mod = (const u16*)d_in[11];
    char* ws = (char*)d_ws;
    (void)hipFuncSetAttribute((const void*)k4_gemm1, hipFuncAttributeMaxDynamicSharedMemorySize, G_LDS);
    (void)hipFuncSetAttribute((const void*)k5_gemm2, hipFuncAttributeMaxDynamicSharedMemorySize, G_LDS);
    k1_prep<<<342, 256, 0, stream>>>(k, v, shift_k, c, w_mod, ws);
    k2_conv<<<576, 256, C_LDS, stream>>>(ssm_k, ssm_D, ws);
    k3_rows<<<256, 512, 0, stream>>>(q, ws);
    k4_gemm1<<<256, 512, G_LDS, stream>>>(wg, wu, ws);
    k5_gemm2<<<256, 512, G_LDS, stream>>>(wd, ws);
    k6_final<<<(S_ * D_ / 8) / 256, 256, 0, stream>>>(ws, (u16*)d_out);
}
```

```cpp
#include <hip/hip_runtime.h>
#include <stdint.h>
#include <utility>

typedef unsigned short u16;
typedef unsigned int u32;
typedef __bf16 bf16x8 __attribute__((ext_vector_type(8)));
typedef __bf16 bf16x2 __attribute__((ext_vector_type(2)));
typedef float f32x4 __attribute__((ext_vector_type(4)));
typedef float f32x2 __attribute__((ext_vector_type(2)));
typedef u32 u32x4 __attribute__((ext_vector_type(4)));
typedef u32 u32x2 __attribute__((ext_vector_type(2)));
typedef short s16x4 __attribute__((ext_vector_type(4)));
#define LAS __attribute__((address_space(3)))
typedef LAS char* lptr;

constexpr int S_ = 2048, D_ = 576, F_ = 4608;

constexpr size_t WS_MOD = 0;
constexpr size_t WS_CNT = 16384;
constexpr size_t WS_KVR = WS_CNT + 65536;
constexpr size_t WS_YYT = WS_KVR + (size_t)D_ * S_ * 2;
constexpr size_t WS_X1  = WS_YYT + (size_t)D_ * S_ * 2;
constexpr size_t WS_H2T = WS_X1 + (size_t)D_ * S_ * 2;
constexpr size_t WS_HT  = WS_H2T + (size_t)D_ * S_ * 2;
constexpr size_t WS_WT1 = WS_HT + (size_t)S_ * F_ * 2;
constexpr size_t WS_SLAB = WS_WT1 + (size_t)2 * D_ * F_ * 2;
constexpr size_t WS_END = WS_SLAB + (size_t)16 * 4 * 4 * 4 * 9216;

#define STAMP_DECL
#define STAMP_ARGS
#define STAMP_AT(i)
#define STAMP_OUT(kid, n)
__device__ __forceinline__ float bf_lo(u32 w) { return __uint_as_float(w << 16); }
__device__ __forceinline__ float bf_hi(u32 w) { return __uint_as_float(w & 0xffff0000u); }
__device__ __forceinline__ float bf1(u16 w) { return __uint_as_float((u32)w << 16); }
__device__ __forceinline__ u32 pk2(float lo, float hi) {
    f32x2 v = {lo, hi};
    return __builtin_bit_cast(u32, __builtin_convertvector(v, bf16x2));
}
__device__ __forceinline__ float rbf(float x) { return (float)(__bf16)x; }
__device__ __forceinline__ float sigmoidf_(float x) { return __builtin_amdgcn_rcpf(1.0f + __builtin_amdgcn_exp2f(-1.4426950408889634f * x)); }

constexpr int CNT_H = 0, CNT_R = 512, CNT_CONV = 768, CNT_ROWS = 832;
typedef __amdgpu_buffer_rsrc_t rsrc_t;
__device__ __forceinline__ u32* cnt_ptr(char* ws, int idx) { return (u32*)(ws + WS_CNT) + idx * 16; }
__device__ __forceinline__ void cnt_add(u32* p) { __hip_atomic_fetch_add(p, 1u, __ATOMIC_RELAXED, __HIP_MEMORY_SCOPE_AGENT); }
__device__ __forceinline__ void st16_dev(rsrc_t rs, u32 off, u32x4 v) { __builtin_amdgcn_raw_buffer_store_b128(v, rs, off, 0, 16); }
__device__ __forceinline__ void poll_ge(const u32* p, u32 want) {
    for (int it = 0; it < (1 << 22); ++it) {
        if (__all(__hip_atomic_load(p, __ATOMIC_RELAXED, __HIP_MEMORY_SCOPE_AGENT) >= want)) break;
        __builtin_amdgcn_s_sleep(1);
    }
}
#define DRAIN() asm volatile("s_waitcnt vmcnt(0)" ::: "memory")

#define SCHED() __builtin_amdgcn_sched_barrier(0)
__device__ __forceinline__ float dpp_add(float v, const int ctrl_sel) {
    int r;
    const int iv = __builtin_bit_cast(int, v);
    switch (ctrl_sel) {
        case 0: r = __builtin_amdgcn_update_dpp(0, iv, 0xB1, 0xf, 0xf, true); break;
        case 1: r = __builtin_amdgcn_update_dpp(0, iv, 0x4E, 0xf, 0xf, true); break;
        case 2: r = __builtin_amdgcn_update_dpp(0, iv, 0x141, 0xf, 0xf, true); break;
        default: r = __builtin_amdgcn_update_dpp(0, iv, 0x140, 0xf, 0xf, true); break;
    }
    return v + __builtin_bit_cast(float, r);
}
__device__ __forceinline__ float half_sum(float v) {
    v = dpp_add(v, 0); v = dpp_add(v, 1); v = dpp_add(v, 2); v = dpp_add(v, 3);
    auto r = __builtin_amdgcn_permlane16_swap(__float_as_uint(v), __float_as_uint(v), false, false);
    return __uint_as_float(r[0]) + __uint_as_float(r[1]);
}

__device__ __forceinline__ void prep_job(int job, const u16* __restrict__ k, const u16* __restrict__ v, const u16* __restrict__ shift_k, char* __restrict__ ws) {
    const int tid = threadIdx.x, lane = tid & 63, wid = tid >> 6;
    const int cb = job % 9, tb = job / 9;
    const int ch = cb * 64 + (lane & 15) * 4, t0 = tb * 128 + wid * 32 + (lane >> 4) * 8;
    u32x2 kw[11], vw[8];
#pragma unroll
    for (int i = 0; i < 11; ++i) { const int t = t0 - 3 + i; kw[i] = *(const u32x2*)(k + (size_t)(t < 0 ? 0 : t) * D_ + ch); }
#pragma unroll
    for (int i = 0; i < 8; ++i) vw[i] = *(const u32x2*)(v + (size_t)(t0 + i) * D_ + ch);
    const u32x4 s0 = *(const u32x4*)(shift_k + ch * 4), s1 = *(const u32x4*)(shift_k + ch * 4 + 8);
    if (t0 == 0) { kw[0] = (u32x2){0, 0}; kw[1] = (u32x2){0, 0}; kw[2] = (u32x2){0, 0}; }
    u16* kvr = (u16*)(ws + WS_KVR);
#pragma unroll
    for (int c = 0; c < 4; ++c) {
        const u32 wa = c < 2 ? s0[2 * c] : s1[2 * c - 4], wb = c < 2 ? s0[2 * c + 1] : s1[2 * c - 3];
        const float w0 = bf_lo(wa), w1 = bf_hi(wa), w2 = bf_lo(wb), w3 = bf_hi(wb);
        float kf[11], r[8];
#pragma unroll
        for (int i = 0; i < 11; ++i) kf[i] = (c & 1) ? bf_hi(kw[i][c >> 1]) : bf_lo(kw[i][c >> 1]);
#pragma unroll
        for (int i = 0; i < 8; ++i) {
            const float ks = rbf(kf[i] * w3 + kf[i + 1] * w2 + kf[i + 2] * w1 + kf[i + 3] * w0);
            r[i] = ks * ((c & 1) ? bf_hi(vw[i][c >> 1]) : bf_lo(vw[i][c >> 1]));
        }
        *(u32x4*)(kvr + (size_t)(ch + c) * S_ + (2040 - t0)) = (u32x4){pk2(r[7], r[6]), pk2(r[5], r[4]), pk2(r[3], r[2]), pk2(r[1], r[0])};
    }
}

__device__ __forceinline__ void mod_job(int job, const u16* __restrict__ c, const u16* __restrict__ w_mod, char* __restrict__ ws, float* sc  , float* red  ) {
    const int tid = threadIdx.x, lane = tid & 63, wid = tid >> 6;
    const int col0 = job * 64, rg = tid >> 3, c8 = tid & 7;
    u32x4 w[18];
#pragma unroll
    for (int i = 0; i < 18; ++i) w[i] = *(const u32x4*)(w_mod + (size_t)(rg + 32 * i) * 3456 + col0 + c8 * 8);
    for (int i = tid; i < 576; i += 256) { const float x = bf1(c[i]); sc[i] = rbf(x * sigmoidf_(x)); }
    __syncthreads();
    float acc[8] = {0, 0, 0, 0, 0, 0, 0, 0};
#pragma unroll
    for (int i = 0; i < 18; ++i) {
        const float s = sc[rg + 32 * i];
#pragma unroll
        for (int j = 0; j < 4; ++j) { acc[2 * j] += s * bf_lo(w[i][j]); acc[2 * j + 1] += s * bf_hi(w[i][j]); }
    }
#pragma unroll
    for (int j = 0; j < 8; ++j) {
        acc[j] += __shfl_xor(acc[j], 8); acc[j] += __shfl_xor(acc[j], 16); acc[j] += __shfl_xor(acc[j], 32);
    }
    if (lane < 8) {
#pragma unroll
        for (int j = 0; j < 8; ++j) red[wid * 64 + lane * 8 + j] = acc[j];
    }
    __syncthreads();
    if (tid < 64) {
        float m = rbf((red[tid] + red[64 + tid]) + (red[128 + tid] + red[192 + tid]));
        const int which = col0 / 576;
        if (which == 1 || which == 4) m = rbf(1.0f + m);
        ((float*)(ws + WS_MOD))[col0 + tid] = m;
    }
}

__device__ __forceinline__ u32x4 tr_frag(const LAS char* p) {
    s16x4 lo = __builtin_amdgcn_ds_read_tr16_b64_v4i16((LAS s16x4*)(p));
    s16x4 hi = __builtin_amdgcn_ds_read_tr16_b64_v4i16((LAS s16x4*)(p + 16 * 288));
    typedef short s16x8 __attribute__((ext_vector_type(8)));
    s16x8 r = __builtin_shufflevector(lo, hi, 0, 1, 2, 3, 4, 5, 6, 7);
    return __builtin_bit_cast(u32x4, r);
}
__device__ __forceinline__ void tile_job(int b, const u16* __restrict__ wg, const u16* __restrict__ wu, char* __restrict__ ws, char* tile) {
    const int lane = threadIdx.x & 63, wid = __builtin_amdgcn_readfirstlane(threadIdx.x >> 6);
    const int fc = b & 31, kb = b >> 5;
    const char* base = (const char*)(((wid & 1) ? wu : wg) + (size_t)(kb * 64 + (wid >> 1) * 32) * F_ + fc * 144);
    u32x4 w[9];
#pragma unroll
    for (int i = 0; i < 9; ++i) {
        const int ci = lane + 64 * i, row = (ci * 3641) >> 16, c16 = ci - row * 18;
        w[i] = *(const u32x4*)(base + (u32)(row * (F_ * 2) + c16 * 16));
    }
    char* t = tile + wid * 9216;
#pragma unroll
    for (int i = 0; i < 9; ++i) *(u32x4*)(t + (lane + 64 * i) * 16) = w[i];
    const int g = lane >> 4, q4 = (lane & 15) >> 2, p = lane & 3;
    const LAS char* rd = (const LAS char*)t + (4 * g + q4) * 288 + 8 * p;
    char* dst = ws + WS_WT1 + (size_t)fc * 331776 + ((kb * 2 + (wid >> 1)) * 2 + (wid & 1)) * 1024 + lane * 16;
#pragma unroll
    for (int u = 0; u < 9; ++u) {
        const int pos = (fc & 1) ? (u == 0 ? 4 : u <= 4 ? u - 1 : u) : (u == 8 ? 4 : u < 4 ? u : u + 1);
        *(u32x4*)(dst + pos * 36864) = tr_frag(rd + u * 32);
    }
}

constexpr int K1_PREP = 144, K1_MOD = 54, K1_TILE = 288, K1_GRID = K1_PREP + K1_MOD + K1_TILE;
__global__ __launch_bounds__(256) void k1_prep(const u16* __restrict__ k, const u16* __restrict__ v, const u16* __restrict__ shift_k,
                                               const u16* __restrict__ c, const u16* __restrict__ w_mod,
                                               const u16* __restrict__ wg, const u16* __restrict__ wu, char* __restrict__ ws) {
    __shared__ __attribute__((aligned(16))) char sm[36864];
    STAMP_DECL; STAMP_AT(0);
    if (blockIdx.x < K1_PREP) prep_job(blockIdx.x, k, v, shift_k, ws);
    else if (blockIdx.x < K1_PREP + K1_MOD) {
        if (blockIdx.x == K1_PREP) {
            u32x4* cz = (u32x4*)(ws + WS_CNT);
            for (int i = threadIdx.x; i < 65536 / 16; i += 256) cz[i] = (u32x4){0, 0, 0, 0};
        }
        mod_job(blockIdx.x - K1_PREP, c, w_mod, ws, (float*)sm, (float*)(sm + 4096));
    } else tile_job(blockIdx.x - K1_PREP - K1_MOD, wg, wu, ws, sm);
    STAMP_AT(1);
    STAMP_AT(2); STAMP_OUT(1, 3);
}

constexpr int C_XR = 0;
constexpr int C_K0 = 4608;
constexpr int C_K1 = C_K0 + 4128 + 32;
constexpr int C_CH = C_K1 + 4160;
constexpr int C_RED = 3 * C_CH;
constexpr int C_LDS = C_RED + 3 * 32768;
static_assert(((C_K1 - C_K0) / 4) % 32 == 16, "bank offset");
static_assert(C_CH % 16 == 0 && C_K0 % 16 == 0 && C_K1 % 16 == 0, "align");
template <bool ZERO> __device__ __forceinline__ void mfma_v(f32x4& c, const bf16x8& a, const bf16x8& b) {
    if constexpr (ZERO) asm volatile("v_mfma_f32_16x16x32_bf16 %0, %1, %2, 0" : "=v"(c) : "v"(a), "v"(b));
    else asm volatile("v_mfma_f32_16x16x32_bf16 %0, %1, %2, %0" : "+v"(c) : "v"(a), "v"(b));
}
struct ConvSet { bf16x8 A[8], B[8]; };
template <int J> __device__ __forceinline__ void conv_ld(ConvSet& s, const LAS char* ab, const LAS char* bb) {
    if constexpr (J < 8) {
        const LAS u32* p = (const LAS u32*)(ab + 512 * J);
        u32x4 t = {p[0], p[1], p[2], p[3]};
        s.A[J] = __builtin_bit_cast(bf16x8, t);
    } else s.B[J - 8] = *(const LAS bf16x8*)(bb + 512 * (15 - J));
}
template <int M> struct TriIdx {
    static constexpr int find(bool want_i) { int m = 0; for (int s = 0; s < 8; ++s) for (int j = 0; j <= s; ++j) { if (m == M) return want_i ? s : j; ++m; } return 0; }
    static constexpr int i = find(true), j = find(false);
};
template <int M, bool PF, bool FIRST> __device__ __forceinline__ void conv_slot(f32x4 (&acc)[8], const ConvSet& cur, ConvSet& nxt, const LAS char* ab, const LAS char* bb) {
    mfma_v<(FIRST && TriIdx<M>::j == 0)>(acc[TriIdx<M>::i], cur.A[TriIdx<M>::j], cur.B[TriIdx<M>::i - TriIdx<M>::j]);
    if constexpr (PF && (M & 1) == 0 && M / 2 < 16) conv_ld<((M / 2) & 1) * 8 + (M / 4)>(nxt, ab, bb);
    SCHED();
}
template <bool PF, bool FIRST, int... M> __device__ __forceinline__ void conv_step(f32x4 (&acc)[8], const ConvSet& cur, ConvSet& nxt, const LAS char* ab, const LAS char* bb, std::integer_sequence<int, M...>) {
    (conv_slot<M, PF, FIRST>(acc, cur, nxt, ab, bb), ...);
}
template <int... J> __device__ __forceinline__ void conv_ld_all(ConvSet& s, const LAS char* ab, const LAS char* bb, std::integer_sequence<int, J...>) { (conv_ld<J>(s, ab, bb), ...); }

struct ConvIn { u32x4 x, k; u32 kn; u16 dv; };
__device__ __forceinline__ void conv_load(ConvIn& in, int d, const u16* __restrict__ ssm_k, const u16* __restrict__ ssm_D, const char* __restrict__ ws) {
    const int tid = threadIdx.x;
    in.x = *(const u32x4*)((const u16*)(ws + WS_KVR) + (size_t)d * S_ + tid * 8);
    in.k = *(const u32x4*)(ssm_k + (size_t)d * S_ + tid * 8);
    in.kn = *(const u32*)(ssm_k + (size_t)d * S_ + (tid < 255 ? tid * 8 + 8 : 0));
    in.dv = ssm_D[d];
}
__device__ __forceinline__ void conv_put(const ConvIn& in, char* sm) {
    const int tid = threadIdx.x;
    const u32 kn = tid < 255 ? in.kn : 0u;
    *(u32x4*)(sm + C_XR + tid * 16) = in.x;
    *(u32x4*)(sm + C_K0 + 32 + tid * 16) = in.k;
    *(u32x4*)(sm + C_K1 + 32 + tid * 16) = (u32x4){__builtin_amdgcn_alignbit(in.k[1], in.k[0], 16), __builtin_amdgcn_alignbit(in.k[2], in.k[1], 16),
                                                 __builtin_amdgcn_alignbit(in.k[3], in.k[2], 16), __builtin_amdgcn_alignbit(kn, in.k[3], 16)};
    if (tid < 32) *(u32x4*)(sm + C_XR + 4096 + tid * 16) = (u32x4){0, 0, 0, 0};
    if (tid == 0) {
        *(u32x4*)(sm + C_K0) = (u32x4){0, 0, 0, 0}; *(u32x4*)(sm + C_K0 + 16) = (u32x4){0, 0, 0, 0};
        *(u32x4*)(sm + C_K1) = (u32x4){0, 0, 0, 0}; *(u32x4*)(sm + C_K1 + 16) = (u32x4){0, 0, 0, in.k[0] << 16};
    }
}
__device__ __forceinline__ void conv_finish(int d, const char* sm, const char* red, float Dv, rsrc_t wsr) {
    const int tid = threadIdx.x;
    const int i = tid >> 5, n = tid & 15, gp = (tid >> 4) & 1;
    f32x4 lo = {0.f, 0.f, 0.f, 0.f}, hi = {0.f, 0.f, 0.f, 0.f};
#pragma unroll
    for (int w = 0; w < 4; ++w) {
        lo += *(const f32x4*)(red + (w * 8 + i) * 1024 + (n + 32 * gp) * 16);
        hi += *(const f32x4*)(red + (w * 8 + i) * 1024 + (n + 32 * gp + 16) * 16);
    }
    const int s = 256 * i + 16 * n + 8 * gp;
    const u32x4 xw = *(const u32x4*)(sm + C_XR + 2 * (2040 - s));
    const float o0 = rbf(lo[0]) + rbf(bf_hi(xw[3]) * Dv), o1 = rbf(lo[1]) + rbf(bf_lo(xw[3]) * Dv), o2 = rbf(lo[2]) + rbf(bf_hi(xw[2]) * Dv), o3 = rbf(lo[3]) + rbf(bf_lo(xw[2]) * Dv);
    const float o4 = rbf(hi[0]) + rbf(bf_hi(xw[1]) * Dv), o5 = rbf(hi[1]) + rbf(bf_lo(xw[1]) * Dv), o6 = rbf(hi[2]) + rbf(bf_hi(xw[0]) * Dv), o7 = rbf(hi[3]) + rbf(bf_lo(xw[0]) * Dv);
    st16_dev(wsr, (u32)WS_YYT + (u32)(d * S_ + s) * 2, (u32x4){pk2(o0, o1), pk2(o2, o3), pk2(o4, o5), pk2(o6, o7)});
}
template <int NCH>
__device__ __forceinline__ void conv_body(int b, const ConvIn (&in)[3], char* smem, rsrc_t wsr
                                          ) {
    const int lane = threadIdx.x & 63, wid = __builtin_amdgcn_readfirstlane(threadIdx.x >> 6);
#pragma unroll
    for (int c = 0; c < NCH; ++c) conv_put(in[c], smem + c * C_CH);
    __syncthreads();
    STAMP_AT(1);
    const int n = lane & 15, g = lane >> 4, dd = g >> 1, gh = g & 1;
    const LAS char* l0 = (const LAS char*)smem;
    const LAS char* bb = l0 + C_XR + 480 - 32 * n + 32 * dd + 16 * gh + 128 * wid;
    const LAS char* ab = l0 + ((n & 1) ? C_K0 + 2 * (16 * dd + 8 * gh + n + 1) : C_K1 + 2 * (16 * dd + 8 * gh + n)) + 128 * wid;
    f32x4 acc[NCH][8];
    ConvSet s0, s1;
    conv_ld_all(s0, ab, bb, std::make_integer_sequence<int, 16>{});
    SCHED();
    constexpr auto SQ = std::make_integer_sequence<int, 36>{};
    conv_step<true, true>(acc[0], s0, s1, ab + 64, bb + 64, SQ);
    conv_step<true, false>(acc[0], s1, s0, ab + C_CH, bb + C_CH, SQ);
    conv_step<true, true>(acc[1], s0, s1, ab + C_CH + 64, bb + C_CH + 64, SQ);
    if constexpr (NCH == 3) {
        conv_step<true, false>(acc[1], s1, s0, ab + 2 * C_CH, bb + 2 * C_CH, SQ);
        conv_step<true, true>(acc[2], s0, s1, ab + 2 * C_CH + 64, bb + 2 * C_CH + 64, SQ);
        conv_step<false, false>(acc[2], s1, s0, ab, bb, SQ);
    } else conv_step<false, false>(acc[1], s1, s0, ab, bb, SQ);
    asm volatile("s_nop 7" ::: "memory");
    STAMP_AT(2);
#pragma unroll
    for (int c = 0; c < NCH; ++c)
#pragma unroll
        for (int i = 0; i < 8; ++i) *(f32x4*)(smem + C_RED + c * 32768 + (wid * 8 + i) * 1024 + lane * 16) = acc[c][i];
    __syncthreads();
#pragma unroll
    for (int c = 0; c < NCH; ++c) conv_finish(b + 256 * c, smem + c * C_CH, smem + C_RED + c * 32768, bf1(in[c].dv), wsr);
}

constexpr int R_MOD = 147456;
constexpr int R_YT = 110592;
constexpr int R_HT = R_YT + 8 * 1168;
struct RowsPre { float x[18], gh[18]; u32x4 mw[3]; u32 xw[9]; };
__device__ __forceinline__ void rows_load(RowsPre& r, int t0, const u16* __restrict__ q, const char* __restrict__ ws) {
    const int tid = threadIdx.x, lane = tid & 63, hl = lane & 31, row = 2 * (tid >> 6) + (lane >> 5);
    const u32* qrow = (const u32*)(q + (size_t)(t0 + row) * D_);
#pragma unroll
    for (int i = 0; i < 9; ++i) r.xw[i] = qrow[hl + 32 * i];
#pragma unroll
    for (int i = 0; i < 3; ++i) { const int j = tid + 256 * i; r.mw[i] = *(const u32x4*)(ws + WS_MOD + (size_t)(j < 720 ? j : 0) * 16); }
}
__device__ __forceinline__ void rows_put_mod(const RowsPre& r, char* sm) {
#pragma unroll
    for (int i = 0; i < 3; ++i) { const int j = threadIdx.x + 256 * i; if (j < 720) *(u32x4*)(sm + R_MOD + j * 16) = r.mw[i]; }
}
__device__ __forceinline__ void rows_pre(RowsPre& r, const char* sm) {
    const int hl = threadIdx.x & 31;
#pragma unroll
    for (int i = 0; i < 9; ++i) { r.x[2 * i] = bf_lo(r.xw[i]); r.x[2 * i + 1] = bf_hi(r.xw[i]); }
    float s = 0.f;
#pragma unroll
    for (int i = 0; i < 18; ++i) s += r.x[i];
    const float mean = half_sum(s) * (1.0f / 576.0f);
    float vs = 0.f;
#pragma unroll
    for (int i = 0; i < 18; ++i) { const float dl = r.x[i] - mean; vs += dl * dl; }
    const float rstd = __builtin_amdgcn_rsqf(half_sum(vs) * (1.0f / 576.0f) + 1e-5f);
    const float* mod = (const float*)(sm + R_MOD);
#pragma unroll
    for (int i = 0; i < 9; ++i) {
        const int c = 2 * (hl + 32 * i);
        const f32x2 sa = *(const f32x2*)(mod + c), c1 = *(const f32x2*)(mod + 576 + c), ga = *(const f32x2*)(mod + 2 * 576 + c);
        r.gh[2 * i] = ga[0] * ((r.x[2 * i] - mean) * rstd * c1[0] + sa[0]);
        r.gh[2 * i + 1] = ga[1] * ((r.x[2 * i + 1] - mean) * rstd * c1[1] + sa[1]);
    }
}
__device__ __forceinline__ void rows_main(const RowsPre& r, int t0, char* sm, rsrc_t wsr) {
    const int tid = threadIdx.x, lane = tid & 63, hl = lane & 31, row = 2 * (tid >> 6) + (lane >> 5);
    const int cg = tid >> 1, th = tid & 1;
    if (tid < 144) {
        u32x2 yw[8];
#pragma unroll
        for (int c = 0; c < 8; ++c) yw[c] = __builtin_amdgcn_raw_buffer_load_b64(wsr, (u32)WS_YYT + (u32)((8 * cg + c) * S_ + t0 + 4 * th) * 2, 0, 16);
#pragma unroll
        for (int t = 0; t < 4; ++t) {
            u32x4 o;
#pragma unroll
            for (int kk = 0; kk < 4; ++kk) {
                const u32 a = yw[2 * kk][t >> 1], bq = yw[2 * kk + 1][t >> 1];
                o[kk] = (t & 1) ? __builtin_amdgcn_perm(bq, a, 0x07060302u) : __builtin_amdgcn_perm(bq, a, 0x05040100u);
            }
            *(u32x4*)(sm + R_YT + (4 * th + t) * 1168 + cg * 16) = o;
        }
    }
    __syncthreads();
    const float* mod = (const float*)(sm + R_MOD);
    float x1[18];
#pragma unroll
    for (int i = 0; i < 9; ++i) {
        u32* yp = (u32*)(sm + R_YT + row * 1168 + (hl + 32 * i) * 4);
        const u32 yv = *yp;
        const u32 p = pk2(r.x[2 * i] + r.gh[2 * i] * bf_lo(yv), r.x[2 * i + 1] + r.gh[2 * i + 1] * bf_hi(yv));
        *yp = p;
        x1[2 * i] = bf_lo(p); x1[2 * i + 1] = bf_hi(p);
    }
    float s = 0.f;
#pragma unroll
    for (int i = 0; i < 18; ++i) s += x1[i];
    const float mean = half_sum(s) * (1.0f / 576.0f);
    float vs = 0.f;
#pragma unroll
    for (int i = 0; i < 18; ++i) { const float dl = x1[i] - mean; vs += dl * dl; }
    const float rstd = __builtin_amdgcn_rsqf(half_sum(vs) * (1.0f / 576.0f) + 1e-5f);
#pragma unroll
    for (int i = 0; i < 9; ++i) {
        const int c = 2 * (hl + 32 * i);
        const f32x2 sb = *(const f32x2*)(mod + 3 * 576 + c), c1 = *(const f32x2*)(mod + 4 * 576 + c);
        const float h0 = (x1[2 * i] - mean) * rstd * c1[0] + sb[0], h1 = (x1[2 * i + 1] - mean) * rstd * c1[1] + sb[1];
        const int ks = c >> 5, kk = c & 31, gq = (kk & 15) >> 2, j = ((kk >> 4) << 2) | (kk & 3);
        *(u32*)(sm + R_HT + ((ks * 4 + gq) * 8 + row) * 16 + j * 2) = pk2(h0, h1);
    }
    __syncthreads();
    const u32 h2t = (u32)WS_H2T + (u32)(t0 >> 4) * 18 * 1024 + (t0 & 15) * 16, x1g = (u32)WS_X1 + (u32)t0 * D_ * 2;
#pragma unroll
    for (int it = 0; it < 3; ++it) {
        const int i = tid + 256 * it;
        if (i < 576) {
            const int kg = i >> 3, rr8 = i & 7;
            st16_dev(wsr, h2t + (kg >> 2) * 1024 + (kg & 3) * 256 + rr8 * 16, *(const u32x4*)(sm + R_HT + i * 16));
            const int rr = i / 72, cc = i - rr * 72;
            st16_dev(wsr, x1g + rr * 1152 + cc * 16, *(const u32x4*)(sm + R_YT + rr * 1168 + cc * 16));
        }
    }
}

namespace mlp {
constexpr int STG = 36864, LDS_BYTES = 163840;
constexpr int G1S = 9 * 144, G2S = 18 * 36, NS = G1S + G2S;
constexpr int LA = 6;
constexpr int PRO = 9 + 4 * LA + 18;
constexpr int TUS = 147456;

constexpr int stage_of(int gs) { return gs < G1S ? gs / 144 : 9 + (gs - G1S) / 72; }
constexpr int rel_of(int gs) { return gs < G1S ? gs % 144 : (gs - G1S) % 72; }
constexpr int grp_of(int gs) { return rel_of(gs) >> 2; }
constexpr int t_of(int gs) { return rel_of(gs) & 3; }
constexpr bool barpt(int gs) { return gs >= 0 && gs < NS && t_of(gs) == 3 && grp_of(gs) == (gs < G1S ? 3 : 0) && stage_of(gs) < 17; }
constexpr int dma_piece(int gs) {
    if (gs >= NS || t_of(gs) != 3 || stage_of(gs) + 3 > 17) return -1;
    const int g = grp_of(gs);
    if (gs < G1S) { constexpr int G[9] = {5, 7, 9, 12, 14, 16, 20, 22, 24}; for (int i = 0; i < 9; ++i) if (G[i] == g) return i; return -1; }
    return (g & 1) ? g >> 1 : -1;
}
constexpr int bload(int gs) {
    if (gs >= 144 || t_of(gs) == 0 || t_of(gs) == 3) return -1;
    const int ks = grp_of(gs) >> 1, i = (grp_of(gs) & 1) * 2 + t_of(gs) - 1;
    return (ks + LA < 18) ? (ks + LA) * 4 + i : -1;
}
constexpr bool unit_stores(int v) { return v == 1 || v == 3 || v == 4 || v == 6 || v == 8; }
constexpr int hstore(int gs) {
    if (gs >= G1S || gs < 144 || !unit_stores(gs / 144 - 1) || t_of(gs) != 3) return -1;
    const int g = grp_of(gs);
    return (g == 10 || g == 18 || g == 26 || g == 34) ? (g - 10) / 8 : -1;
}
constexpr int hload(int gs) {
    if (t_of(gs) != 3) return -1;
    const int s = stage_of(gs), g = grp_of(gs);
    if (s == 8) { constexpr int G[20] = {4, 6, 8, 10, 11, 13, 15, 17, 18, 19, 21, 23, 25, 26, 27, 28, 29, 30, 31, 32}; for (int i = 0; i < 20; ++i) if (G[i] == g) return i; return -1; }
    if (s < 9 || g < 2 || g > 16 || (g & 1)) return -1;
    const int i = (g - 2) >> 1;
    if (s == 9) return 20 + i;
    if (s == 10) return 28 + i;
    if (s == 11) return i < 4 ? 36 + i : -1;
    if (s >= 12 && s <= 15) return 40 + 8 * (s - 12) + i;
    return -1;
}
constexpr int pub(int gs) { return (stage_of(gs) == 6 && grp_of(gs) == 27 && t_of(gs) == 3) ? 1 : (stage_of(gs) == 10 && grp_of(gs) == 9 && t_of(gs) == 1) ? 2 : 0; }
constexpr int poll_issue(int gs) { return (stage_of(gs) == 7 && grp_of(gs) == 27 && t_of(gs) == 3) ? 1 : (stage_of(gs) == 11 && grp_of(gs) == 9 && t_of(gs) == 1) ? 2 : 0; }
constexpr int vm_ops(int gs) { return (pub(gs) ? 1 : 0) + (poll_issue(gs) ? 1 : 0) + (hstore(gs) >= 0) + (bload(gs) >= 0) + (hload(gs) >= 0) + (dma_piece(gs) >= 0); }
struct Ledger { int cum[NS + 1]; };
constexpr Ledger make_ledger() {
    Ledger l{}; int n = PRO;
    for (int gs = 0; gs < NS; ++gs) { if (gs == G1S) n += 4; l.cum[gs] = n; n += vm_ops(gs); }
    l.cum[NS] = n; return l;
}
constexpr Ledger LG = make_ledger();
constexpr int seq_in(int gs, int kind) {
    int n = LG.cum[gs];
    if (kind > 0 && pub(gs)) ++n;
    if (kind > 1 && poll_issue(gs)) ++n;
    if (kind > 2 && hstore(gs) >= 0) ++n;
    if (kind > 3 && bload(gs) >= 0) ++n;
    if (kind > 4 && hload(gs) >= 0) ++n;
    return n;
}
constexpr int seq_dma_last(int s) {
    if (s == 0) return 8;
    if (s == 1) return 9 + 4 * LA + 8;
    if (s == 2) return 9 + 4 * LA + 17;
    for (int gs = 0; gs < NS; ++gs) if (stage_of(gs) == s - 3 && dma_piece(gs) == 8) return seq_in(gs, 5);
    return -1;
}
constexpr int seq_b(int ks) {
    if (ks < LA) return 9 + 4 * ks + 3;
    for (int gs = 0; gs < 144; ++gs) if (bload(gs) == ks * 4 + 3) return seq_in(gs, 3);
    return -1;
}
constexpr int seq_h(int kk) {
    for (int gs = 8 * 144; gs < NS; ++gs) if (hload(gs) == kk * 4 + 3) return seq_in(gs, 4);
    return -1;
}
constexpr int seq_poll(int part) {
    for (int gs = 0; gs < NS; ++gs) if (poll_issue(gs) == part) return seq_in(gs, 1);
    return -1;
}
constexpr int seq_store_last(int part) {
    if (part == 2) return LG.cum[G1S] - 1;
    for (int gs = 5 * 144; gs < 6 * 144; ++gs) if (hstore(gs) == 3) return seq_in(gs, 2);
    return -1;
}
constexpr int clampv(int n) { return n > 63 ? 63 : (n < 0 ? 0 : n); }
constexpr int wait_for(int gs, int seq) { return clampv(LG.cum[gs] - seq - 1); }

template <int N> __device__ __forceinline__ void wait_vm() { asm volatile("s_waitcnt vmcnt(%0)" :: "n"(N) : "memory"); }

struct Ctx {
    bf16x8 Bv[2][4];
    bf16x8 Aq[6];
    f32x4 ac[2][2][4];
    f32x4 a2[9][4];
    float e0, e1;
    u32x4 hp[4];
    u32 vo1[4], vo2[4];
    u32 lane16, pollv;
    const LAS char* rd[4]; const LAS char* rd2[4];
    const char* w1src; const char* w2src; const char* bsrc; const char* hsrc;
    u32 voff2[9];
    int sub;
    u32 dmadst;
    u32 hso;
    int jj;
    __amdgpu_buffer_rsrc_t hrs;
    u32* cnt_pub; u32* cnt_poll;
};

template <int KS, int T, bool ZERO> __device__ __forceinline__ void mfma(Ctx& cx, f32x4& c, const bf16x8& a) {
    constexpr int r = (KS * 4 + T) * 4;
    if constexpr (KS < 16) {
        if constexpr (ZERO) asm volatile("v_mfma_f32_16x16x32_bf16 %0, %1, a[%c2:%c3], 0" : "=v"(c) : "v"(a), "n"(r), "n"(r + 3));
        else asm volatile("v_mfma_f32_16x16x32_bf16 %0, %1, a[%c2:%c3], %0" : "+v"(c) : "v"(a), "n"(r), "n"(r + 3));
    } else {
        asm volatile("v_mfma_f32_16x16x32_bf16 %0, %1, %2, %0" : "+v"(c) : "v"(a), "v"(cx.Bv[KS - 16][T]));
    }
}
template <int KS, int T, bool SC1> __device__ __forceinline__ void gload(Ctx& cx, u32 voff, const char* sb) {
    constexpr int r = (KS * 4 + T) * 4, off = (KS & 3) * 1024;
    if constexpr (KS < 16) {
        if constexpr (SC1) asm volatile("global_load_dwordx4 a[%c2:%c3], %0, %1 offset:%4 sc1" :: "v"(voff), "s"(sb), "n"(r), "n"(r + 3), "n"(off) : "memory");
        else asm volatile("global_load_dwordx4 a[%c2:%c3], %0, %1 offset:%4" :: "v"(voff), "s"(sb), "n"(r), "n"(r + 3), "n"(off) : "memory");
    } else {
        if constexpr (SC1) asm volatile("global_load_dwordx4 %0, %1, %2 offset:%3 sc1" : "=v"(cx.Bv[KS - 16][T]) : "v"(voff), "s"(sb), "n"(off) : "memory");
        else asm volatile("global_load_dwordx4 %0, %1, %2 offset:%3" : "=v"(cx.Bv[KS - 16][T]) : "v"(voff), "s"(sb), "n"(off) : "memory");
    }
}
template <int S, int P> __device__ __forceinline__ void dma_m0(const Ctx& c) {
    asm volatile("s_mov_b32 m0, %0" :: "s"(c.dmadst + (u32)((S & 3) * STG + (P >> 2) * 4096)) : "memory");
}
constexpr int nat_kstep(int ks2) {
    if (ks2 < 10) { constexpr int T[5] = {0, 1, 5, 6, 4}; return 9 * (ks2 / 5) + T[ks2 % 5]; }
    const int jj = (ks2 - 10) >> 1, h = (ks2 - 10) & 1;
    return 9 * (jj >> 1) + ((jj & 1) ? 7 : 2) + h;
}
template <int S, int P> __device__ __forceinline__ void dma_go(const Ctx& c) {
    if constexpr (S < 9) {
        const char* sb = c.w1src + (size_t)S * STG + (P >> 2) * 4096;
        asm volatile("global_load_lds_dwordx4 %0, %1 offset:%2" :: "v"(c.lane16), "s"(sb), "n"((P & 3) * 1024) : "memory");
    } else {
        constexpr int n0 = nat_kstep(2 * (S - 9)), n1 = nat_kstep(2 * (S - 9) + 1);
        const char* sb = c.w2src + (size_t)(c.sub ? n1 : n0) * (32 * D_ * 2);
        asm volatile("global_load_lds_dwordx4 %0, %1 offset:%2" :: "v"(c.voff2[P]), "s"(sb), "n"((P & 3) * 1024) : "memory");
    }
}
template <int ID> __device__ __forceinline__ void load_b(Ctx& c) {
    constexpr int ks = ID >> 2, t = ID & 3;
    gload<ks, t, true>(c, c.vo1[t], c.bsrc + (ks >> 2) * 4096);
}
template <int ID> __device__ __forceinline__ void load_h(Ctx& c) {
    constexpr int kk = ID >> 2, t = ID & 3;
    gload<kk, t, true>(c, c.vo2[t], c.hsrc + (kk >> 2) * 4096);
}
template <int KS> __device__ __forceinline__ void pin_b(Ctx& c) {
#pragma unroll
    for (int t = 0; t < 4; ++t) { if constexpr (KS >= 16) asm volatile("" : "+v"(c.Bv[KS - 16][t])); }
}
template <int V, int P> __device__ __forceinline__ void epi_piece(Ctx& c) {
    constexpr int pr = P >> 3, st = P & 7, t = pr >> 1, r = 2 * (pr & 1), par = V & 1;
    if constexpr (st == 0) {
        asm volatile("v_mul_f32 %0, 0xbfb8aa3b, %1" : "=v"(c.e0) : "v"(c.ac[par][0][t][r]));
        asm volatile("v_mul_f32 %0, 0xbfb8aa3b, %1" : "=v"(c.e1) : "v"(c.ac[par][0][t][r + 1]));
    } else if constexpr (st == 1) asm volatile("v_exp_f32 %0, %0" : "+v"(c.e0));
    else if constexpr (st == 2) asm volatile("v_exp_f32 %0, %0" : "+v"(c.e1));
    else if constexpr (st == 3) {
        asm volatile("v_add_f32 %0, 1.0, %0" : "+v"(c.e0));
        asm volatile("v_add_f32 %0, 1.0, %0" : "+v"(c.e1));
    } else if constexpr (st == 4) asm volatile("v_rcp_f32 %0, %0" : "+v"(c.e0));
    else if constexpr (st == 5) asm volatile("v_rcp_f32 %0, %0" : "+v"(c.e1));
    else if constexpr (st == 6) {
        asm volatile("v_mul_f32 %0, %0, %1" : "+v"(c.e0) : "v"(c.ac[par][1][t][r]));
        asm volatile("v_mul_f32 %0, %0, %1" : "+v"(c.e1) : "v"(c.ac[par][1][t][r + 1]));
    } else {
        constexpr bool first = (V == 0 || V == 2 || V == 5 || V == 7);
        u32 o;
        asm volatile("v_cvt_pk_bf16_f32 %0, %1, %2" : "=v"(o) : "v"(c.e0), "v"(c.e1));
        c.hp[t][(first ? 0 : 2) + (pr & 1)] = o;
    }
}
template <int V, int T> __device__ __forceinline__ void store_h(Ctx& c) {
    const int m5 = 5 * (c.jj >> 1), ab = c.jj & 1;
    if constexpr (V == 4) {
        const u32x2 d = {c.hp[T][2], c.hp[T][3]};
        __builtin_amdgcn_raw_buffer_store_b64(d, c.hrs, c.vo2[T] + 8 * ab, c.hso + (m5 + 4) * 1024, 16);
    } else {
        const int kstep = (V == 1) ? m5 + 2 * ab : (V == 3) ? m5 + 2 * ab + 1 : (V == 6) ? 10 + 2 * c.jj : 11 + 2 * c.jj;
        __builtin_amdgcn_raw_buffer_store_b128(c.hp[T], c.hrs, c.vo2[T], c.hso + kstep * 1024, 16);
    }
}
template <int PART> __device__ __forceinline__ void poll_check(Ctx& c) {
    asm volatile("" : "+v"(c.pollv));
    if (__builtin_amdgcn_readfirstlane(c.pollv) < 4u) {
        u32* p = c.cnt_poll + (PART - 1) * 4096;
        for (int it = 0; it < (1 << 22); ++it) {
            if (__builtin_amdgcn_readfirstlane(__hip_atomic_load(p, __ATOMIC_RELAXED, __HIP_MEMORY_SCOPE_AGENT)) >= 4u) break;
            __builtin_amdgcn_s_sleep(1);
        }
    }
}

template <int V, int P> __device__ __forceinline__ void epi_piece_padded(Ctx& c) {
    epi_piece<V, P>(c);
    if constexpr ((P & 7) == 2 || (P & 7) == 5) asm volatile("s_nop 0");
}
template <int V, int... P> __device__ __forceinline__ void epi_all(Ctx& c, std::integer_sequence<int, P...>) { (epi_piece_padded<V, P>(c), ...); }

template <int GS> __device__ __forceinline__ void slot(Ctx& c) {
    constexpr bool g1 = GS < G1S;
    constexpr int S = stage_of(GS), R = rel_of(GS), grp = R >> 2, t = R & 3;
    constexpr int fid = g1 ? 36 * S + grp : 324 + 18 * (S - 9) + grp;
    if constexpr (GS == G1S) {
        asm volatile("s_nop 7" ::: "memory");
        epi_all<8>(c, std::make_integer_sequence<int, 64>{});
        store_h<8, 0>(c); store_h<8, 1>(c); store_h<8, 2>(c); store_h<8, 3>(c);
        SCHED();
    }
    if constexpr (S == 0 && t == 0 && (grp & 1) == 0) { wait_vm<wait_for(GS, seq_b(grp >> 1))>(); pin_b<(grp >> 1)>(c); }
    if constexpr (GS == 8 * 144) { wait_vm<wait_for(GS, seq_poll(1))>(); poll_check<1>(c); }
    if constexpr (!g1 && t == 0 && (grp % 9) == 0) { constexpr int kk = 2 * (S - 9) + grp / 9; wait_vm<wait_for(GS, seq_h(kk))>(); pin_b<kk>(c); }
    if constexpr (dma_piece(GS) >= 0 && (dma_piece(GS) & 3) == 0) dma_m0<S + 3, dma_piece(GS)>(c);
    if constexpr (g1) {
        constexpr int ks = grp >> 1, mat = grp & 1;
        mfma<ks, t, (ks == 0)>(c, c.ac[S & 1][mat][t], c.Aq[fid % 6]);
    } else {
        constexpr int kk = 2 * (S - 9) + grp / 9, du = grp % 9;
        mfma<kk, t, (kk == 0)>(c, c.a2[du][t], c.Aq[fid % 6]);
    }
    if constexpr (barpt(GS)) {
        constexpr int n1 = wait_for(GS, seq_dma_last(S + 1));
        constexpr int n2 = (S == 12) ? wait_for(GS, seq_poll(2)) : 63;
        wait_vm<(n1 < n2 ? n1 : n2)>();
        if constexpr (S == 12) poll_check<2>(c);
        __builtin_amdgcn_s_barrier();
        asm volatile("" ::: "memory");
    }
    if constexpr (pub(GS)) {
        wait_vm<wait_for(GS, seq_store_last(pub(GS)))>();
        if (c.lane16 == 0) __hip_atomic_fetch_add(c.cnt_pub + (pub(GS) - 1) * 4096, 1u, __ATOMIC_RELAXED, __HIP_MEMORY_SCOPE_AGENT);
    }
    if constexpr (poll_issue(GS)) asm volatile("global_load_dword %0, %1, %2 sc1" : "=v"(c.pollv) : "v"(0u), "s"(c.cnt_poll + (poll_issue(GS) - 1) * 4096) : "memory");
    if constexpr (fid + 5 < 486) {
        constexpr int f = fid + 5;
        if constexpr (f < 324) { if constexpr (t == 0) c.Aq[f % 6] = *(const LAS bf16x8*)(c.rd[(f / 36) & 3] + (f % 36) * 1024); }
        else if constexpr (t <= 1) {
            constexpr int fs = (f - 324) % 18;
            const s16x4 h = __builtin_amdgcn_ds_read_tr16_b64_v4i16((LAS s16x4*)(c.rd2[(9 + (f - 324) / 18) & 3] + (fs / 9) * 9216 + (fs % 9) * 32 + t * (16 * 288)));
            const u32x2 hh = __builtin_bit_cast(u32x2, h);
            u32x4 a = __builtin_bit_cast(u32x4, c.Aq[f % 6]);
            a[2 * t] = hh[0]; a[2 * t + 1] = hh[1];
            c.Aq[f % 6] = __builtin_bit_cast(bf16x8, a);
        }
    }
    if constexpr (g1 && S >= 1 && (t == 1 || t == 2) && grp >= 2 && grp < 34) epi_piece<S - 1, (grp - 2) * 2 + t - 1>(c);
    if constexpr (hstore(GS) >= 0) store_h<S - 1, hstore(GS)>(c);
    if constexpr (bload(GS) >= 0) load_b<bload(GS)>(c);
    if constexpr (hload(GS) >= 0) load_h<hload(GS)>(c);
    if constexpr (dma_piece(GS) >= 0) dma_go<S + 3, dma_piece(GS)>(c);
    SCHED();
}
template <int BASE, int... I> __device__ __forceinline__ void run_slots(Ctx& c, std::integer_sequence<int, I...>) { (slot<BASE + I>(c), ...); }
template <int S, int P> __device__ __forceinline__ void dma_one(const Ctx& c) { dma_m0<S, P>(c); asm volatile("s_nop 0"); dma_go<S, P>(c); }
template <int S, int... P> __device__ __forceinline__ void dma_stage(const Ctx& c, std::integer_sequence<int, P...>) { (dma_one<S, P>(c), ...); }
template <int... I> __device__ __forceinline__ void load_b_first(Ctx& c, std::integer_sequence<int, I...>) { (load_b<I>(c), ...); }
}

__global__ __launch_bounds__(256, 1) void k2_main(const u16* __restrict__ qin, const u16* __restrict__ ssm_k, const u16* __restrict__ ssm_D,
                                                  const u16* __restrict__ wg, const u16* __restrict__ wu, const u16* __restrict__ wd,
                                                  char* __restrict__ ws, u16* __restrict__ out) {
    using namespace mlp;
    extern __shared__ __attribute__((aligned(16))) char smem[];
    const int tid = threadIdx.x, lane = tid & 63, w = __builtin_amdgcn_readfirstlane(tid >> 6);
    const int b = blockIdx.x, x = b & 7, idx = b >> 3, q = x >> 1;
    const int mt = 4 * (x & 1) + (idx & 3), fcl = idx >> 2, fc = 8 * q + fcl;
    const int mt2 = 8 * (x & 1) + (idx & 7), cc = idx >> 3, tg = w & 1, kh = w >> 1;
    const rsrc_t wsr = __builtin_amdgcn_make_buffer_rsrc(ws, 0, (int)WS_END, 0x00020000);
    STAMP_DECL; STAMP_AT(0);
    {
        const int t0 = (x * 32 + idx) * 8;
        ConvIn in[3];
        conv_load(in[0], b, ssm_k, ssm_D, ws); conv_load(in[1], b + 256, ssm_k, ssm_D, ws);
        if (b < 64) conv_load(in[2], b + 512, ssm_k, ssm_D, ws);
        RowsPre rp;
        rows_load(rp, t0, qin, ws);
        if (b < 64) conv_body<3>(b, in, smem, wsr STAMP_ARGS);
        else conv_body<2>(b, in, smem, wsr STAMP_ARGS);
        STAMP_AT(3);
        rows_put_mod(rp, smem);
        DRAIN();
        __syncthreads();
        if (tid < 8) cnt_add(cnt_ptr(ws, CNT_CONV + tid * 8 + x));
        rows_pre(rp, smem);
        STAMP_AT(4);
        if (w == 0) poll_ge(cnt_ptr(ws, CNT_CONV + x * 8 + (lane & 7)), 32u);
        __syncthreads();
        STAMP_AT(5);
        rows_main(rp, t0, smem, wsr);
        STAMP_AT(6);
        DRAIN();
        __syncthreads();
        if (tid == 0) cnt_add(cnt_ptr(ws, CNT_ROWS + (t0 >> 6)));
        STAMP_AT(7);
    }
    Ctx c;
#pragma unroll
    for (int t = 0; t < 4; ++t) c.hp[t] = (u32x4){0, 0, 0, 0};
    c.lane16 = lane * 16;
#pragma unroll
    for (int t = 0; t < 4; ++t) { c.vo1[t] = lane * 16 + t * 18432; c.vo2[t] = lane * 16 + t * TUS; }
    const LAS char* l0 = (const LAS char*)smem;
#pragma unroll
    for (int i = 0; i < 4; ++i) {
        c.rd[i] = l0 + i * STG + lane * 16; c.rd2[i] = l0 + i * STG + kh * 18432 + (4 * (lane >> 4) + ((lane & 15) >> 2)) * 288 + 8 * (lane & 3);
        asm volatile("" : "+v"(c.rd[i])); asm volatile("" : "+v"(c.rd2[i]));
    }
#pragma unroll
    for (int i = 0; i < 9; ++i) { const int ci = lane + 64 * i, row = (ci * 3641) >> 16, c16 = ci - row * 18; c.voff2[i] = (u32)(row * (D_ * 2) + c16 * 16 - (i & 3) * 1024); }
    c.sub = w & 1;
    c.w1src = ws + WS_WT1 + (size_t)fc * 331776 + w * 9216;
    c.w2src = (const char*)(wd + (size_t)(36 * q + 18 * kh) * 32 * D_ + cc * 144);
    c.bsrc = ws + WS_H2T + (size_t)(16 * mt + 4 * w) * 18432;
    c.hsrc = ws + WS_HT + (size_t)(8 * mt2 + 4 * tg) * TUS + (size_t)((q * 2 + kh) * 18) * 1024;
    c.dmadst = (u32)(uintptr_t)smem + w * 9216;
    c.jj = fcl & 3;
    c.hso = (u32)((16 * mt + 4 * w) * TUS + ((q * 2 + (fcl >> 2)) * 18) * 1024);
    c.hrs = __builtin_amdgcn_make_buffer_rsrc(ws + WS_HT, 0, (int)((size_t)S_ * F_ * 2), 0x00020000);
    u32* cnt = (u32*)(ws + WS_CNT);
    c.cnt_pub = cnt + ((((mt * 4 + w) * 4 + q) * 2 + (fcl >> 2))) * 16;
    c.cnt_poll = cnt + (((((mt2 >> 1) * 4 + 2 * (mt2 & 1) + tg) * 4 + q) * 2 + kh)) * 16;
    asm volatile("" ::: "a0", "a1", "a2", "a3", "a4", "a5", "a6", "a7", "a8", "a9", "a10", "a11", "a12", "a13", "a14", "a15", "a16", "a17", "a18", "a19", "a20", "a21", "a22", "a23", "a24", "a25", "a26", "a27", "a28", "a29", "a30", "a31", "a32", "a33", "a34", "a35", "a36", "a37", "a38", "a39", "a40", "a41", "a42", "a43", "a44", "a45", "a46", "a47", "a48", "a49", "a50", "a51", "a52", "a53", "a54", "a55", "a56", "a57", "a58", "a59", "a60", "a61", "a62", "a63", "a64", "a65", "a66", "a67", "a68", "a69", "a70", "a71", "a72", "a73", "a74", "a75", "a76", "a77", "a78", "a79", "a80", "a81", "a82", "a83", "a84", "a85", "a86", "a87", "a88", "a89", "a90", "a91", "a92", "a93", "a94", "a95", "a96", "a97", "a98", "a99", "a100", "a101", "a102", "a103", "a104", "a105", "a106", "a107", "a108", "a109", "a110", "a111", "a112", "a113", "a114", "a115", "a116", "a117", "a118", "a119", "a120", "a121", "a122", "a123", "a124", "a125", "a126", "a127", "a128", "a129", "a130", "a131", "a132", "a133", "a134", "a135", "a136", "a137", "a138", "a139", "a140", "a141", "a142", "a143", "a144", "a145", "a146", "a147", "a148", "a149", "a150", "a151", "a152", "a153", "a154", "a155", "a156", "a157", "a158", "a159", "a160", "a161", "a162", "a163", "a164", "a165", "a166", "a167", "a168", "a169", "a170", "a171", "a172", "a173", "a174", "a175", "a176", "a177", "a178", "a179", "a180", "a181", "a182", "a183", "a184", "a185", "a186", "a187", "a188", "a189", "a190", "a191", "a192", "a193", "a194", "a195", "a196", "a197", "a198", "a199", "a200", "a201", "a202", "a203", "a204", "a205", "a206", "a207", "a208", "a209", "a210", "a211", "a212", "a213", "a214", "a215", "a216", "a217", "a218", "a219", "a220", "a221", "a222", "a223", "a224", "a225", "a226", "a227", "a228", "a229", "a230", "a231", "a232", "a233", "a234", "a235", "a236", "a237", "a238", "a239", "a240", "a241", "a242", "a243", "a244", "a245", "a246", "a247", "a248", "a249", "a250", "a251", "a252", "a253", "a254", "a255");
    dma_stage<0>(c, std::make_integer_sequence<int, 9>{});
    poll_ge(cnt_ptr(ws, CNT_ROWS + 4 * mt + w), 8u);
    STAMP_AT(8);
    load_b_first(c, std::make_integer_sequence<int, 4 * LA>{});
    dma_stage<1>(c, std::make_integer_sequence<int, 9>{});
    dma_stage<2>(c, std::make_integer_sequence<int, 9>{});
    wait_vm<PRO - 9>();
    __builtin_amdgcn_s_barrier();
    asm volatile("" ::: "memory");
#pragma unroll
    for (int f = 0; f < 5; ++f) c.Aq[f] = *(const LAS bf16x8*)(c.rd[0] + f * 1024);
    SCHED();
    STAMP_AT(9);
    run_slots<0>(c, std::make_integer_sequence<int, 144>{});
    STAMP_AT(10);
    run_slots<144>(c, std::make_integer_sequence<int, 144 * 4>{});
    run_slots<144 * 5>(c, std::make_integer_sequence<int, 144 * 4>{});
    STAMP_AT(11);
    run_slots<G1S>(c, std::make_integer_sequence<int, 324>{});
    run_slots<G1S + 324>(c, std::make_integer_sequence<int, 324>{});
    asm volatile("s_nop 7" ::: "memory");
    SCHED();
    STAMP_AT(12);
    const int n = lane & 15, g = lane >> 4;
    u32x2 xr[3][2]; f32x4 gb[3];
#pragma unroll
    for (int i = 0; i < 3; ++i) {
        const int u = (w + 4 * i < 9) ? w + 4 * i : 8, d = 144 * cc + 16 * u + 4 * g;
#pragma unroll
        for (int t = 0; t < 2; ++t) xr[i][t] = __builtin_amdgcn_raw_buffer_load_b64(wsr, (u32)WS_X1 + (u32)((128 * mt2 + 32 * q + 16 * t + n) * D_ + d) * 2, 0, 16);
        gb[i] = *(const f32x4*)((const float*)(ws + WS_MOD) + 5 * 576 + d);
    }
    __builtin_amdgcn_s_barrier();
    {
        char* ex = smem + w * 18432 + lane * 16;
#pragma unroll
        for (int t = 0; t < 2; ++t)
#pragma unroll
            for (int u = 0; u < 9; ++u) *(f32x4*)(ex + (t * 9 + u) * 1024) = kh ? c.a2[u][t] : c.a2[u][2 + t];
    }
    __syncthreads();
    const int j = 2 * tg + kh;
    u32* cntr = cnt_ptr(ws, CNT_R + (mt2 * 4 + cc) * 4);
    const u32 slab = (u32)WS_SLAB + (u32)((mt2 * 4 + cc) * 16) * 9216;
    {
        const char* ey = smem + (w ^ 2) * 18432 + lane * 16;
        char* own = smem + 73728 + lane * 16;
#pragma unroll
        for (int u = 0; u < 9; ++u) {
            const f32x4 r0 = (kh ? c.a2[u][2] : c.a2[u][0]) + *(const f32x4*)(ey + u * 1024);
            const f32x4 r1 = (kh ? c.a2[u][3] : c.a2[u][1]) + *(const f32x4*)(ey + (9 + u) * 1024);
            if (j != q) st16_dev(wsr, slab + (j * 4 + q) * 9216 + u * 1024 + lane * 16, (u32x4){pk2(r0[0], r0[1]), pk2(r0[2], r0[3]), pk2(r1[0], r1[1]), pk2(r1[2], r1[3])});
            else { *(f32x4*)(own + u * 2048) = r0; *(f32x4*)(own + u * 2048 + 1024) = r1; }
        }
    }
    if (j != q) {
        DRAIN();
        if (lane == 0) cnt_add(cntr + j * 16);
    }
    STAMP_AT(13);
    poll_ge(cntr + q * 16, 3u);
    __syncthreads();
    u32x4 rem[3][3];
#pragma unroll
    for (int i = 0; i < 3; ++i) {
        const int u = (w + 4 * i < 9) ? w + 4 * i : 8;
#pragma unroll
        for (int k = 0; k < 3; ++k) { const int qq = k + (k >= q ? 1 : 0); rem[i][k] = __builtin_amdgcn_raw_buffer_load_b128(wsr, slab + (q * 4 + qq) * 9216 + u * 1024 + lane * 16, 0, 16); }
    }
#pragma unroll
    for (int i = 0; i < 3; ++i) {
        const int u = w + 4 * i;
        if (u < 9) {
            const int d = 144 * cc + 16 * u + 4 * g;
#pragma unroll
            for (int t = 0; t < 2; ++t) {
                f32x4 s = *(const f32x4*)(smem + 73728 + u * 2048 + t * 1024 + lane * 16);
#pragma unroll
                for (int k = 0; k < 3; ++k) {
                    const u32 a = rem[i][k][2 * t], bq = rem[i][k][2 * t + 1];
                    s += (f32x4){bf_lo(a), bf_hi(a), bf_lo(bq), bf_hi(bq)};
                }
                const float o0 = bf_lo(xr[i][t][0]) + rbf(gb[i][0] * rbf(s[0])), o1 = bf_hi(xr[i][t][0]) + rbf(gb[i][1] * rbf(s[1]));
                const float o2 = bf_lo(xr[i][t][1]) + rbf(gb[i][2] * rbf(s[2])), o3 = bf_hi(xr[i][t][1]) + rbf(gb[i][3] * rbf(s[3]));
                *(u32x2*)(out + (size_t)(128 * mt2 + 32 * q + 16 * t + n) * D_ + d) = (u32x2){pk2(o0, o1), pk2(o2, o3)};
            }
        }
    }
    STAMP_AT(14);
    STAMP_OUT(4, 15);
}

extern "C" void kernel_launch(void* const* d_in, const int* in_sizes, int n_in, void* d_out, int out_size, void* d_ws, size_t ws_size,
                              hipStream_t stream) {
    const u16* q = (const u16*)d_in[0];
    const u16* k = (const u16*)d_in[1];
    const u16* v = (const u16*)d_in[2];
    const u16* shift_k = (const u16*)d_in[3];
    const u16* ssm_k = (const u16*)d_in[4];
    const u16* ssm_D = (const u16*)d_in[5];
    const u16* wg = (const u16*)d_in[7];
    const u16* wu = (const u16*)d_in[8];
    const u16* wd = (const u16*)d_in[9];
    const u16* c = (const u16*)d_in[10];
    const u16* w_mod = (const u16*)d_in[11];
    char* ws = (char*)d_ws;
    (void)hipFuncSetAttribute((const void*)k2_main, hipFuncAttributeMaxDynamicSharedMemorySize, mlp::LDS_BYTES);
    k1_prep<<<K1_GRID, 256, 0, stream>>>(k, v, shift_k, c, w_mod, wg, wu, ws);
    k2_main<<<256, 256, mlp::LDS_BYTES, stream>>>(q, ssm_k, ssm_D, wg, wu, wd, ws, (u16*)d_out);
}
```

```cpp
#include <hip/hip_runtime.h>
#include <stdint.h>
#include <utility>

typedef unsigned short u16;
typedef unsigned int u32;
typedef __bf16 bf16x8 __attribute__((ext_vector_type(8)));
typedef __bf16 bf16x2 __attribute__((ext_vector_type(2)));
typedef float f32x4 __attribute__((ext_vector_type(4)));
typedef float f32x2 __attribute__((ext_vector_type(2)));
typedef u32 u32x4 __attribute__((ext_vector_type(4)));
typedef u32 u32x2 __attribute__((ext_vector_type(2)));
typedef short s16x4 __attribute__((ext_vector_type(4)));
#define LAS __attribute__((address_space(3)))
typedef LAS char* lptr;

constexpr int S_ = 2048, D_ = 576, F_ = 4608;

constexpr size_t WS_MOD = 0;
constexpr size_t WS_CNT = 65536;
constexpr size_t WS_KVR = WS_CNT + 65536;
constexpr size_t WS_YYT = WS_KVR + (size_t)D_ * S_ * 2;
constexpr size_t WS_X1  = WS_YYT + (size_t)D_ * S_ * 2;
constexpr size_t WS_H2T = WS_X1 + (size_t)D_ * S_ * 2;
constexpr size_t WS_HT  = WS_H2T + (size_t)D_ * S_ * 2;
constexpr size_t WS_WT1 = WS_HT + (size_t)S_ * F_ * 2;
constexpr size_t WS_SLAB = WS_WT1 + (size_t)2 * D_ * F_ * 2;
constexpr size_t WS_END = WS_SLAB + (size_t)16 * 4 * 4 * 4 * 9216;

#define STAMP_DECL
#define STAMP_ARGS
#define STAMP_AT(i)
#define STAMP_OUT(kid, n)
__device__ __forceinline__ float bf_lo(u32 w) { return __uint_as_float(w << 16); }
__device__ __forceinline__ float bf_hi(u32 w) { return __uint_as_float(w & 0xffff0000u); }
__device__ __forceinline__ float bf1(u16 w) { return __uint_as_float((u32)w << 16); }
__device__ __forceinline__ u32 pk2(float lo, float hi) {
    f32x2 v = {lo, hi};
    return __builtin_bit_cast(u32, __builtin_convertvector(v, bf16x2));
}
__device__ __forceinline__ float rbf(float x) { return (float)(__bf16)x; }
__device__ __forceinline__ float sigmoidf_(float x) { return __builtin_amdgcn_rcpf(1.0f + __builtin_amdgcn_exp2f(-1.4426950408889634f * x)); }

constexpr int F_KV = 0;
constexpr int F_MOD = 256;
constexpr int F_TILE = 512;
constexpr int F_CONV = 1024;
constexpr int F_ROWS = 3072;
constexpr int F_H = 3328;
constexpr int F_RED = 5376;
constexpr int F_EPOCH = 6400;
typedef __amdgpu_buffer_rsrc_t rsrc_t;
__device__ __forceinline__ u32* flag_ptr(char* ws, int idx) { return (u32*)(ws + WS_CNT) + idx; }
__device__ __forceinline__ void flag_set(u32* p, u32 e) { __hip_atomic_store(p, e, __ATOMIC_RELAXED, __HIP_MEMORY_SCOPE_AGENT); }
__device__ __forceinline__ void st16_dev(rsrc_t rs, u32 off, u32x4 v) { __builtin_amdgcn_raw_buffer_store_b128(v, rs, off, 0, 16); }
__device__ __forceinline__ u32x4 ld16_dev(rsrc_t rs, u32 off) { return __builtin_amdgcn_raw_buffer_load_b128(rs, off, 0, 16); }
__device__ __forceinline__ void poll_eq(const u32* p, u32 e) {
    for (int it = 0; it < (1 << 21); ++it) {
        if (__all(__hip_atomic_load(p, __ATOMIC_RELAXED, __HIP_MEMORY_SCOPE_AGENT) == e)) break;
        __builtin_amdgcn_s_sleep(1);
    }
}
__device__ __forceinline__ void poll_eq4(rsrc_t rs, u32 off, u32 e) {
    for (int it = 0; it < (1 << 21); ++it) {
        asm volatile("" ::: "memory");
        const u32x4 v = ld16_dev(rs, off);
        if (__all(v[0] == e && v[1] == e && v[2] == e && v[3] == e)) break;
        __builtin_amdgcn_s_sleep(1);
    }
}
#define DRAIN() asm volatile("s_waitcnt vmcnt(0)" ::: "memory")

#define SCHED() __builtin_amdgcn_sched_barrier(0)
__device__ __forceinline__ float dpp_add(float v, const int ctrl_sel) {
    int r;
    const int iv = __builtin_bit_cast(int, v);
    switch (ctrl_sel) {
        case 0: r = __builtin_amdgcn_update_dpp(0, iv, 0xB1, 0xf, 0xf, true); break;
        case 1: r = __builtin_amdgcn_update_dpp(0, iv, 0x4E, 0xf, 0xf, true); break;
        case 2: r = __builtin_amdgcn_update_dpp(0, iv, 0x141, 0xf, 0xf, true); break;
        default: r = __builtin_amdgcn_update_dpp(0, iv, 0x140, 0xf, 0xf, true); break;
    }
    return v + __builtin_bit_cast(float, r);
}
__device__ __forceinline__ float half_sum(float v) {
    v = dpp_add(v, 0); v = dpp_add(v, 1); v = dpp_add(v, 2); v = dpp_add(v, 3);
    auto r = __builtin_amdgcn_permlane16_swap(__float_as_uint(v), __float_as_uint(v), false, false);
    return __uint_as_float(r[0]) + __uint_as_float(r[1]);
}

__device__ __forceinline__ void prep_job(int job, const u16* __restrict__ k, const u16* __restrict__ v, const u16* __restrict__ shift_k, rsrc_t wsr) {
    const int tid = threadIdx.x;
    const int ch = (job & 7) * 72 + (tid >> 3) * 4, t0 = (job >> 3) * 64 + (tid & 7) * 8;
    u32x2 kw[11], vw[8];
#pragma unroll
    for (int i = 0; i < 11; ++i) { const int t = t0 - 3 + i; kw[i] = *(const u32x2*)(k + (size_t)(t < 0 ? 0 : t) * D_ + ch); }
#pragma unroll
    for (int i = 0; i < 8; ++i) vw[i] = *(const u32x2*)(v + (size_t)(t0 + i) * D_ + ch);
    const u32x4 s0 = *(const u32x4*)(shift_k + ch * 4), s1 = *(const u32x4*)(shift_k + ch * 4 + 8);
    if (t0 == 0) { kw[0] = (u32x2){0, 0}; kw[1] = (u32x2){0, 0}; kw[2] = (u32x2){0, 0}; }
#pragma unroll
    for (int c = 0; c < 4; ++c) {
        const u32 wa = c < 2 ? s0[2 * c] : s1[2 * c - 4], wb = c < 2 ? s0[2 * c + 1] : s1[2 * c - 3];
        const float w0 = bf_lo(wa), w1 = bf_hi(wa), w2 = bf_lo(wb), w3 = bf_hi(wb);
        float kf[11], r[8];
#pragma unroll
        for (int i = 0; i < 11; ++i) kf[i] = (c & 1) ? bf_hi(kw[i][c >> 1]) : bf_lo(kw[i][c >> 1]);
#pragma unroll
        for (int i = 0; i < 8; ++i) {
            const float ks = rbf(kf[i] * w3 + kf[i + 1] * w2 + kf[i + 2] * w1 + kf[i + 3] * w0);
            r[i] = ks * ((c & 1) ? bf_hi(vw[i][c >> 1]) : bf_lo(vw[i][c >> 1]));
        }
        st16_dev(wsr, (u32)WS_KVR + (u32)((ch + c) * S_ + 2040 - t0) * 2, (u32x4){pk2(r[7], r[6]), pk2(r[5], r[4]), pk2(r[3], r[2]), pk2(r[1], r[0])});
    }
}

__device__ __forceinline__ void mod_job(int job, const u16* __restrict__ c, const u16* __restrict__ w_mod, char* __restrict__ ws, float* sc  ) {
    const int lane = threadIdx.x & 63;
    const int rq = job / 54, col0 = (job - rq * 54) * 64, rg = lane >> 3, c8 = lane & 7;
    u32x4 w[18];
#pragma unroll
    for (int i = 0; i < 18; ++i) w[i] = *(const u32x4*)(w_mod + (size_t)(rq * 144 + rg + 8 * i) * 3456 + col0 + c8 * 8);
#pragma unroll
    for (int j = 0; j < 3; ++j) { const int r = lane + 64 * j; const float x = bf1(c[rq * 144 + (r < 144 ? r : 0)]); sc[r] = rbf(x * sigmoidf_(x)); }
    float acc[8] = {0, 0, 0, 0, 0, 0, 0, 0};
#pragma unroll
    for (int i = 0; i < 18; ++i) {
        const float s = sc[rg + 8 * i];
#pragma unroll
        for (int j = 0; j < 4; ++j) { acc[2 * j] += s * bf_lo(w[i][j]); acc[2 * j + 1] += s * bf_hi(w[i][j]); }
    }
#pragma unroll
    for (int j = 0; j < 8; ++j) {
        acc[j] += __shfl_xor(acc[j], 8); acc[j] += __shfl_xor(acc[j], 16); acc[j] += __shfl_xor(acc[j], 32);
    }
    if (lane < 8) {
        const rsrc_t r = __builtin_amdgcn_make_buffer_rsrc(ws, 0, 65536, 0x00020000);
        const u32 off = (u32)WS_MOD + (u32)(rq * 3456 + col0 + lane * 8) * 4;
        st16_dev(r, off, (u32x4){__float_as_uint(acc[0]), __float_as_uint(acc[1]), __float_as_uint(acc[2]), __float_as_uint(acc[3])});
        st16_dev(r, off + 16, (u32x4){__float_as_uint(acc[4]), __float_as_uint(acc[5]), __float_as_uint(acc[6]), __float_as_uint(acc[7])});
    }
}

constexpr int T_PITCH = 1184, T_BYTES = 32 * T_PITCH;
__device__ __forceinline__ void tile_load(int job, const u16* __restrict__ wg, const u16* __restrict__ wu, u32x4 (&w)[9]) {
    const int ks = job % 18, mat = (job / 18) & 1, cg = job / 36;
    const char* base = (const char*)((mat ? wu : wg) + (size_t)(32 * ks) * F_ + cg * 576);
#pragma unroll
    for (int i = 0; i < 9; ++i) {
        const int ci = threadIdx.x + 256 * i, row = (ci * 58255) >> 22, c16 = ci - row * 72;
        w[i] = *(const u32x4*)(base + (u32)(row * (F_ * 2) + c16 * 16));
    }
}
template <int I> __device__ __forceinline__ void tile_load_one(const char* base, u32x4 (&w)[9]) {
    const int ci = threadIdx.x + 256 * I, row = (ci * 58255) >> 22, c16 = ci - row * 72;
    w[I] = *(const u32x4*)(base + (u32)(row * (F_ * 2) + c16 * 16));
}
__device__ __forceinline__ const char* tile_base(int job, const u16* __restrict__ wg, const u16* __restrict__ wu) {
    const int ks = job % 18, mat = (job / 18) & 1, cg = job / 36;
    return (const char*)((mat ? wu : wg) + (size_t)(32 * ks) * F_ + cg * 576);
}
__device__ __forceinline__ void tile_put(char* tile, const u32x4 (&w)[9]) {
#pragma unroll
    for (int i = 0; i < 9; ++i) {
        const int ci = threadIdx.x + 256 * i, row = (ci * 58255) >> 22, c16 = ci - row * 72;
        *(u32x4*)(tile + row * T_PITCH + c16 * 16) = w[i];
    }
}
__device__ __forceinline__ void tile_out_one(int job, int f, const char* tile, rsrc_t wsr) {
    const int lane = threadIdx.x & 63;
    const int ks = job % 18, mat = (job / 18) & 1, cg = job / 36;
    const int g = lane >> 4, q4 = (lane & 15) >> 2, p = lane & 3;
    const LAS char* rd = (const LAS char*)tile + (4 * g + q4) * T_PITCH + 8 * p + f * 32;
    const int ck = f / 9, u = f - 9 * ck, fc = 4 * cg + ck;
    const int pos = (fc & 1) ? (u == 0 ? 4 : u <= 4 ? u - 1 : u) : (u == 8 ? 4 : u < 4 ? u : u + 1);
    const s16x4 lo = __builtin_amdgcn_ds_read_tr16_b64_v4i16((LAS s16x4*)(rd));
    const s16x4 hi = __builtin_amdgcn_ds_read_tr16_b64_v4i16((LAS s16x4*)(rd + 16 * T_PITCH));
    typedef short s16x8 __attribute__((ext_vector_type(8)));
    const s16x8 r = __builtin_shufflevector(lo, hi, 0, 1, 2, 3, 4, 5, 6, 7);
    st16_dev(wsr, (u32)WS_WT1 + fc * 331776 + pos * 36864 + (ks * 2 + mat) * 1024 + lane * 16, __builtin_bit_cast(u32x4, r));
}
__device__ __forceinline__ void tile_out(int job, const char* tile, rsrc_t wsr) {
    const int w3 = (threadIdx.x >> 6) - 1;
#pragma unroll
    for (int i = 0; i < 12; ++i) tile_out_one(job, w3 * 12 + i, tile, wsr);
}
constexpr int T_LDS_A = 104320;

constexpr int C_XR = 0;
constexpr int C_K0 = 4608;
constexpr int C_K1 = C_K0 + 4128 + 32;
constexpr int C_CH = C_K1 + 4160;
constexpr int C_RED = 3 * C_CH;
constexpr int C_LDS = C_RED + 3 * 32768;
static_assert(((C_K1 - C_K0) / 4) % 32 == 16, "bank offset");
static_assert(C_CH % 16 == 0 && C_K0 % 16 == 0 && C_K1 % 16 == 0, "align");
template <bool ZERO> __device__ __forceinline__ void mfma_v(f32x4& c, const bf16x8& a, const bf16x8& b) {
    if constexpr (ZERO) asm volatile("v_mfma_f32_16x16x32_bf16 %0, %1, %2, 0" : "=v"(c) : "v"(a), "v"(b));
    else asm volatile("v_mfma_f32_16x16x32_bf16 %0, %1, %2, %0" : "+v"(c) : "v"(a), "v"(b));
}
struct ConvSet { bf16x8 A[8], B[8]; };
template <int J> __device__ __forceinline__ void conv_ld(ConvSet& s, const LAS char* ab, const LAS char* bb) {
    if constexpr (J < 8) {
        const LAS u32* p = (const LAS u32*)(ab + 512 * J);
        u32x4 t = {p[0], p[1], p[2], p[3]};
        s.A[J] = __builtin_bit_cast(bf16x8, t);
    } else s.B[J - 8] = *(const LAS bf16x8*)(bb + 512 * (15 - J));
}
template <int M> struct TriIdx {
    static constexpr int find(bool want_i) { int m = 0; for (int s = 0; s < 8; ++s) for (int j = 0; j <= s; ++j) { if (m == M) return want_i ? s : j; ++m; } return 0; }
    static constexpr int i = find(true), j = find(false);
};
struct Ride { const char* tbase; u32x4 tw[9]; int job_out; const char* tile; rsrc_t wsr; int wid; };
template <int STEP, int M, bool PF, bool FIRST, bool TL, bool TO> __device__ __forceinline__ void conv_slot(f32x4 (&acc)[8], const ConvSet& cur, ConvSet& nxt, const LAS char* ab, const LAS char* bb, Ride& rd) {
    mfma_v<(FIRST && TriIdx<M>::j == 0)>(acc[TriIdx<M>::i], cur.A[TriIdx<M>::j], cur.B[TriIdx<M>::i - TriIdx<M>::j]);
    if constexpr (PF && (M & 1) == 0 && M / 2 < 16) conv_ld<((M / 2) & 1) * 8 + (M / 4)>(nxt, ab, bb);
    if constexpr (TL && STEP == 0 && (M & 3) == 3) tile_load_one<M / 4>(rd.tbase, rd.tw);
    if constexpr (TO && (STEP * 36 + M) % 16 == 9) tile_out_one(rd.job_out, rd.wid * 9 + (STEP * 36 + M) / 16, rd.tile, rd.wsr);
    SCHED();
}
template <int STEP, bool PF, bool FIRST, bool TL, bool TO, int... M> __device__ __forceinline__ void conv_step(f32x4 (&acc)[8], const ConvSet& cur, ConvSet& nxt, const LAS char* ab, const LAS char* bb, Ride& rd, std::integer_sequence<int, M...>) {
    (conv_slot<STEP, M, PF, FIRST, TL, TO>(acc, cur, nxt, ab, bb, rd), ...);
}
template <int... J> __device__ __forceinline__ void conv_ld_all(ConvSet& s, const LAS char* ab, const LAS char* bb, std::integer_sequence<int, J...>) { (conv_ld<J>(s, ab, bb), ...); }

struct ConvIn { u32x4 x, k; u32 kn; u16 dv; };
__device__ __forceinline__ void conv_load_x(ConvIn& in, int d, rsrc_t wsr) { in.x = ld16_dev(wsr, (u32)WS_KVR + (u32)(d * S_ + threadIdx.x * 8) * 2); }
__device__ __forceinline__ void conv_load_k(ConvIn& in, int d, const u16* __restrict__ ssm_k, const u16* __restrict__ ssm_D) {
    const int tid = threadIdx.x;
    in.k = *(const u32x4*)(ssm_k + (size_t)d * S_ + tid * 8);
    in.kn = *(const u32*)(ssm_k + (size_t)d * S_ + (tid < 255 ? tid * 8 + 8 : 0));
    in.dv = ssm_D[d];
}
__device__ __forceinline__ void conv_put(const ConvIn& in, char* sm) {
    const int tid = threadIdx.x;
    const u32 kn = tid < 255 ? in.kn : 0u;
    *(u32x4*)(sm + C_XR + tid * 16) = in.x;
    *(u32x4*)(sm + C_K0 + 32 + tid * 16) = in.k;
    *(u32x4*)(sm + C_K1 + 32 + tid * 16) = (u32x4){__builtin_amdgcn_alignbit(in.k[1], in.k[0], 16), __builtin_amdgcn_alignbit(in.k[2], in.k[1], 16),
                                                 __builtin_amdgcn_alignbit(in.k[3], in.k[2], 16), __builtin_amdgcn_alignbit(kn, in.k[3], 16)};
    if (tid < 32) *(u32x4*)(sm + C_XR + 4096 + tid * 16) = (u32x4){0, 0, 0, 0};
    if (tid == 0) {
        *(u32x4*)(sm + C_K0) = (u32x4){0, 0, 0, 0}; *(u32x4*)(sm + C_K0 + 16) = (u32x4){0, 0, 0, 0};
        *(u32x4*)(sm + C_K1) = (u32x4){0, 0, 0, 0}; *(u32x4*)(sm + C_K1 + 16) = (u32x4){0, 0, 0, in.k[0] << 16};
    }
}
__device__ __forceinline__ void conv_finish(int d, const char* sm, const char* red, float Dv, rsrc_t wsr) {
    const int tid = threadIdx.x;
    const int i = tid >> 5, n = tid & 15, gp = (tid >> 4) & 1;
    f32x4 lo = {0.f, 0.f, 0.f, 0.f}, hi = {0.f, 0.f, 0.f, 0.f};
#pragma unroll
    for (int w = 0; w < 4; ++w) {
        lo += *(const f32x4*)(red + (w * 8 + i) * 1024 + (n + 32 * gp) * 16);
        hi += *(const f32x4*)(red + (w * 8 + i) * 1024 + (n + 32 * gp + 16) * 16);
    }
    const int s = 256 * i + 16 * n + 8 * gp;
    const u32x4 xw = *(const u32x4*)(sm + C_XR + 2 * (2040 - s));
    const float o0 = rbf(lo[0]) + rbf(bf_hi(xw[3]) * Dv), o1 = rbf(lo[1]) + rbf(bf_lo(xw[3]) * Dv), o2 = rbf(lo[2]) + rbf(bf_hi(xw[2]) * Dv), o3 = rbf(lo[3]) + rbf(bf_lo(xw[2]) * Dv);
    const float o4 = rbf(hi[0]) + rbf(bf_hi(xw[1]) * Dv), o5 = rbf(hi[1]) + rbf(bf_lo(xw[1]) * Dv), o6 = rbf(hi[2]) + rbf(bf_hi(xw[0]) * Dv), o7 = rbf(hi[3]) + rbf(bf_lo(xw[0]) * Dv);
    st16_dev(wsr, (u32)WS_YYT + (u32)(d * S_ + s) * 2, (u32x4){pk2(o0, o1), pk2(o2, o3), pk2(o4, o5), pk2(o6, o7)});
}
template <int NCH, bool TL, bool TO>
__device__ __forceinline__ void conv_body(const int (&chn)[3], const ConvIn (&in)[3], char* smem, rsrc_t wsr, Ride& rd
                                          ) {
    const int lane = threadIdx.x & 63, wid = __builtin_amdgcn_readfirstlane(threadIdx.x >> 6);
#pragma unroll
    for (int c = 0; c < NCH; ++c) conv_put(in[c], smem + c * C_CH);
    __syncthreads();
    STAMP_AT(3);
    const int n = lane & 15, g = lane >> 4, dd = g >> 1, gh = g & 1;
    const LAS char* l0 = (const LAS char*)smem;
    const LAS char* bb = l0 + C_XR + 480 - 32 * n + 32 * dd + 16 * gh + 128 * wid;
    const LAS char* ab = l0 + ((n & 1) ? C_K0 + 2 * (16 * dd + 8 * gh + n + 1) : C_K1 + 2 * (16 * dd + 8 * gh + n)) + 128 * wid;
    f32x4 acc[NCH][8];
    ConvSet s0, s1;
    conv_ld_all(s0, ab, bb, std::make_integer_sequence<int, 16>{});
    SCHED();
    constexpr auto SQ = std::make_integer_sequence<int, 36>{};
    conv_step<0, true, true, TL, TO>(acc[0], s0, s1, ab + 64, bb + 64, rd, SQ);
    conv_step<1, true, false, TL, TO>(acc[0], s1, s0, ab + C_CH, bb + C_CH, rd, SQ);
    conv_step<2, true, true, TL, TO>(acc[1], s0, s1, ab + C_CH + 64, bb + C_CH + 64, rd, SQ);
    if constexpr (NCH == 3) {
        conv_step<3, true, false, false, false>(acc[1], s1, s0, ab + 2 * C_CH, bb + 2 * C_CH, rd, SQ);
        conv_step<4, true, true, false, false>(acc[2], s0, s1, ab + 2 * C_CH + 64, bb + 2 * C_CH + 64, rd, SQ);
        conv_step<5, false, false, false, false>(acc[2], s1, s0, ab, bb, rd, SQ);
    } else conv_step<3, false, false, TL, TO>(acc[1], s1, s0, ab, bb, rd, SQ);
    asm volatile("s_nop 7" ::: "memory");
    STAMP_AT(4);
#pragma unroll
    for (int c = 0; c < NCH; ++c)
#pragma unroll
        for (int i = 0; i < 8; ++i) *(f32x4*)(smem + C_RED + c * 32768 + (wid * 8 + i) * 1024 + lane * 16) = acc[c][i];
    __syncthreads();
#pragma unroll
    for (int c = 0; c < NCH; ++c) conv_finish(chn[c], smem + c * C_CH, smem + C_RED + c * 32768, bf1(in[c].dv), wsr);
}

constexpr int R_MOD = 147456;
constexpr int R_YT = 110592;
constexpr int R_HT = R_YT + 8 * 1168;
struct RowsPre { float x[18], gh[18]; u32 xw[9]; };
__device__ __forceinline__ void rows_load(RowsPre& r, int t0, const u16* __restrict__ q) {
    const int tid = threadIdx.x, lane = tid & 63, hl = lane & 31, row = 2 * (tid >> 6) + (lane >> 5);
    const u32* qrow = (const u32*)(q + (size_t)(t0 + row) * D_);
#pragma unroll
    for (int i = 0; i < 9; ++i) r.xw[i] = qrow[hl + 32 * i];
}
__device__ __forceinline__ void rows_put_mod(char* sm, rsrc_t wsr) {
    u32x4 mw[4][4];
#pragma unroll
    for (int i = 0; i < 4; ++i) {
        const int j = threadIdx.x + 256 * i;
#pragma unroll
        for (int p = 0; p < 4; ++p) mw[i][p] = ld16_dev(wsr, (u32)WS_MOD + p * 13824 + (j < 864 ? j : 0) * 16);
    }
#pragma unroll
    for (int i = 0; i < 4; ++i) {
        const int j = threadIdx.x + 256 * i;
        const bool one = (j >= 144 && j < 288) || (j >= 576 && j < 720);
        f32x4 m = (__builtin_bit_cast(f32x4, mw[i][0]) + __builtin_bit_cast(f32x4, mw[i][1])) + (__builtin_bit_cast(f32x4, mw[i][2]) + __builtin_bit_cast(f32x4, mw[i][3]));
#pragma unroll
        for (int e = 0; e < 4; ++e) { m[e] = rbf(m[e]); if (one) m[e] = rbf(1.0f + m[e]); }
        if (j < 864) *(f32x4*)(sm + R_MOD + j * 16) = m;
    }
}
__device__ __forceinline__ void rows_pre(RowsPre& r, const char* sm) {
    const int hl = threadIdx.x & 31;
#pragma unroll
    for (int i = 0; i < 9; ++i) { r.x[2 * i] = bf_lo(r.xw[i]); r.x[2 * i + 1] = bf_hi(r.xw[i]); }
    float s = 0.f;
#pragma unroll
    for (int i = 0; i < 18; ++i) s += r.x[i];
    const float mean = half_sum(s) * (1.0f / 576.0f);
    float vs = 0.f;
#pragma unroll
    for (int i = 0; i < 18; ++i) { const float dl = r.x[i] - mean; vs += dl * dl; }
    const float rstd = __builtin_amdgcn_rsqf(half_sum(vs) * (1.0f / 576.0f) + 1e-5f);
    const float* mod = (const float*)(sm + R_MOD);
#pragma unroll
    for (int i = 0; i < 9; ++i) {
        const int c = 2 * (hl + 32 * i);
        const f32x2 sa = *(const f32x2*)(mod + c), c1 = *(const f32x2*)(mod + 576 + c), ga = *(const f32x2*)(mod + 2 * 576 + c);
        r.gh[2 * i] = ga[0] * ((r.x[2 * i] - mean) * rstd * c1[0] + sa[0]);
        r.gh[2 * i + 1] = ga[1] * ((r.x[2 * i + 1] - mean) * rstd * c1[1] + sa[1]);
    }
}
__device__ __forceinline__ void rows_fetch(int t0, char* sm, const char* ws) {
    const int lane = threadIdx.x & 63;
    const char* src = ws + WS_YYT + t0 * 2;
    const u32 dst = (u32)(uintptr_t)(sm + R_HT);
#pragma unroll
    for (int i = 0; i < 9; ++i)
        asm volatile("s_mov_b32 m0, %2\n\ts_nop 0\n\tglobal_load_lds_dwordx4 %0, %1 sc1" :: "v"((u32)((lane + 64 * i) * (S_ * 2))), "s"(src), "s"(dst + i * 1024) : "memory");
    asm volatile("s_waitcnt vmcnt(0)" ::: "memory");
}
__device__ __forceinline__ void rows_main(const RowsPre& r, int t0, char* sm, rsrc_t wsr) {
    const int tid = threadIdx.x, lane = tid & 63, hl = lane & 31, row = 2 * (tid >> 6) + (lane >> 5);
    const int cg = tid >> 1, th = tid & 1;
    if (tid < 144) {
        u32x2 yw[8];
#pragma unroll
        for (int c = 0; c < 8; ++c) yw[c] = *(const u32x2*)(sm + R_HT + (8 * cg + c) * 16 + th * 8);
#pragma unroll
        for (int t = 0; t < 4; ++t) {
            u32x4 o;
#pragma unroll
            for (int kk = 0; kk < 4; ++kk) {
                const u32 a = yw[2 * kk][t >> 1], bq = yw[2 * kk + 1][t >> 1];
                o[kk] = (t & 1) ? __builtin_amdgcn_perm(bq, a, 0x07060302u) : __builtin_amdgcn_perm(bq, a, 0x05040100u);
            }
            *(u32x4*)(sm + R_YT + (4 * th + t) * 1168 + cg * 16) = o;
        }
    }
    __syncthreads();
    const float* mod = (const float*)(sm + R_MOD);
    float x1[18];
#pragma unroll
    for (int i = 0; i < 9; ++i) {
        u32* yp = (u32*)(sm + R_YT + row * 1168 + (hl + 32 * i) * 4);
        const u32 yv = *yp;
        const u32 p = pk2(r.x[2 * i] + r.gh[2 * i] * bf_lo(yv), r.x[2 * i + 1] + r.gh[2 * i + 1] * bf_hi(yv));
        *yp = p;
        x1[2 * i] = bf_lo(p); x1[2 * i + 1] = bf_hi(p);
    }
    float s = 0.f;
#pragma unroll
    for (int i = 0; i < 18; ++i) s += x1[i];
    const float mean = half_sum(s) * (1.0f / 576.0f);
    float vs = 0.f;
#pragma unroll
    for (int i = 0; i < 18; ++i) { const float dl = x1[i] - mean; vs += dl * dl; }
    const float rstd = __builtin_amdgcn_rsqf(half_sum(vs) * (1.0f / 576.0f) + 1e-5f);
#pragma unroll
    for (int i = 0; i < 9; ++i) {
        const int c = 2 * (hl + 32 * i);
        const f32x2 sb = *(const f32x2*)(mod + 3 * 576 + c), c1 = *(const f32x2*)(mod + 4 * 576 + c);
        const float h0 = (x1[2 * i] - mean) * rstd * c1[0] + sb[0], h1 = (x1[2 * i + 1] - mean) * rstd * c1[1] + sb[1];
        const int ks = c >> 5, kk = c & 31, gq = (kk & 15) >> 2, j = ((kk >> 4) << 2) | (kk & 3);
        *(u32*)(sm + R_HT + ((ks * 4 + gq) * 8 + row) * 16 + j * 2) = pk2(h0, h1);
    }
    __syncthreads();
    const u32 h2t = (u32)WS_H2T + (u32)(t0 >> 4) * 18 * 1024 + (t0 & 15) * 16, x1g = (u32)WS_X1 + (u32)t0 * D_ * 2;
#pragma unroll
    for (int it = 0; it < 3; ++it) {
        const int i = tid + 256 * it;
        if (i < 576) {
            const int kg = i >> 3, rr8 = i & 7;
            st16_dev(wsr, h2t + (kg >> 2) * 1024 + (kg & 3) * 256 + rr8 * 16, *(const u32x4*)(sm + R_HT + i * 16));
            const int rr = i / 72, cc = i - rr * 72;
            st16_dev(wsr, x1g + rr * 1152 + cc * 16, *(const u32x4*)(sm + R_YT + rr * 1168 + cc * 16));
        }
    }
}

namespace mlp {
constexpr int STG = 36864, LDS_BYTES = 163840;
constexpr int G1S = 9 * 144, G2S = 18 * 36, NS = G1S + G2S;
constexpr int LA = 6;
constexpr int PRO = 9 + 4 * LA + 18;
constexpr int TUS = 147456;

constexpr int stage_of(int gs) { return gs < G1S ? gs / 144 : 9 + (gs - G1S) / 72; }
constexpr int rel_of(int gs) { return gs < G1S ? gs % 144 : (gs - G1S) % 72; }
constexpr int grp_of(int gs) { return rel_of(gs) >> 2; }
constexpr int t_of(int gs) { return rel_of(gs) & 3; }
constexpr bool barpt(int gs) { return gs >= 0 && gs < NS && t_of(gs) == 3 && grp_of(gs) == (gs < G1S ? 3 : 0) && stage_of(gs) < 17; }
constexpr int dma_piece(int gs) {
    if (gs >= NS || t_of(gs) != 3 || stage_of(gs) + 3 > 17) return -1;
    const int g = grp_of(gs);
    if (gs < G1S) { constexpr int G[9] = {5, 7, 9, 12, 14, 16, 20, 22, 24}; for (int i = 0; i < 9; ++i) if (G[i] == g) return i; return -1; }
    return (g & 1) ? g >> 1 : -1;
}
constexpr int bload(int gs) {
    if (gs >= 144 || t_of(gs) == 0 || t_of(gs) == 3) return -1;
    const int ks = grp_of(gs) >> 1, i = (grp_of(gs) & 1) * 2 + t_of(gs) - 1;
    return (ks + LA < 18) ? (ks + LA) * 4 + i : -1;
}
constexpr bool unit_stores(int v) { return v == 1 || v == 3 || v == 4 || v == 6 || v == 8; }
constexpr int hstore(int gs) {
    if (gs >= G1S || gs < 144 || !unit_stores(gs / 144 - 1) || t_of(gs) != 3) return -1;
    const int g = grp_of(gs);
    return (g == 10 || g == 18 || g == 26 || g == 34) ? (g - 10) / 8 : -1;
}
constexpr int hload(int gs) {
    if (t_of(gs) != 3) return -1;
    const int s = stage_of(gs), g = grp_of(gs);
    if (s == 8) { constexpr int G[20] = {4, 6, 8, 10, 11, 13, 15, 17, 18, 19, 21, 23, 25, 26, 27, 28, 29, 30, 31, 32}; for (int i = 0; i < 20; ++i) if (G[i] == g) return i; return -1; }
    if (s < 9 || g < 2 || g > 16 || (g & 1)) return -1;
    const int i = (g - 2) >> 1;
    if (s == 9) return 20 + i;
    if (s == 10) return 28 + i;
    if (s == 11) return i < 4 ? 36 + i : -1;
    if (s >= 12 && s <= 15) return 40 + 8 * (s - 12) + i;
    return -1;
}
constexpr int pub(int gs) { return (stage_of(gs) == 6 && grp_of(gs) == 27 && t_of(gs) == 3) ? 1 : (stage_of(gs) == 10 && grp_of(gs) == 9 && t_of(gs) == 1) ? 2 : 0; }
constexpr int poll_issue(int gs) { return (stage_of(gs) == 7 && grp_of(gs) == 27 && t_of(gs) == 3) ? 1 : (stage_of(gs) == 11 && grp_of(gs) == 9 && t_of(gs) == 1) ? 2 : 0; }
constexpr int vm_ops(int gs) { return (pub(gs) ? 1 : 0) + (poll_issue(gs) ? 1 : 0) + (hstore(gs) >= 0) + (bload(gs) >= 0) + (hload(gs) >= 0) + (dma_piece(gs) >= 0); }
struct Ledger { int cum[NS + 1]; };
constexpr Ledger make_ledger() {
    Ledger l{}; int n = PRO;
    for (int gs = 0; gs < NS; ++gs) { if (gs == G1S) n += 4; l.cum[gs] = n; n += vm_ops(gs); }
    l.cum[NS] = n; return l;
}
constexpr Ledger LG = make_ledger();
constexpr int seq_in(int gs, int kind) {
    int n = LG.cum[gs];
    if (kind > 0 && pub(gs)) ++n;
    if (kind > 1 && poll_issue(gs)) ++n;
    if (kind > 2 && hstore(gs) >= 0) ++n;
    if (kind > 3 && bload(gs) >= 0) ++n;
    if (kind > 4 && hload(gs) >= 0) ++n;
    return n;
}
constexpr int seq_dma_last(int s) {
    if (s == 0) return 8;
    if (s == 1) return 9 + 4 * LA + 8;
    if (s == 2) return 9 + 4 * LA + 17;
    for (int gs = 0; gs < NS; ++gs) if (stage_of(gs) == s - 3 && dma_piece(gs) == 8) return seq_in(gs, 5);
    return -1;
}
constexpr int seq_b(int ks) {
    if (ks < LA) return 9 + 4 * ks + 3;
    for (int gs = 0; gs < 144; ++gs) if (bload(gs) == ks * 4 + 3) return seq_in(gs, 3);
    return -1;
}
constexpr int seq_h(int kk) {
    for (int gs = 8 * 144; gs < NS; ++gs) if (hload(gs) == kk * 4 + 3) return seq_in(gs, 4);
    return -1;
}
constexpr int seq_poll(int part) {
    for (int gs = 0; gs < NS; ++gs) if (poll_issue(gs) == part) return seq_in(gs, 1);
    return -1;
}
constexpr int seq_store_last(int part) {
    if (part == 2) return LG.cum[G1S] - 1;
    for (int gs = 5 * 144; gs < 6 * 144; ++gs) if (hstore(gs) == 3) return seq_in(gs, 2);
    return -1;
}
constexpr int clampv(int n) { return n > 63 ? 63 : (n < 0 ? 0 : n); }
constexpr int wait_for(int gs, int seq) { return clampv(LG.cum[gs] - seq - 1); }

template <int N> __device__ __forceinline__ void wait_vm() { asm volatile("s_waitcnt vmcnt(%0)" :: "n"(N) : "memory"); }

struct Ctx {
    bf16x8 Bv[2][4];
    bf16x8 Aq[6];
    f32x4 ac[2][2][4];
    f32x4 a2[9][4];
    float e0, e1;
    u32x4 hp[4];
    u32 vo1[4], vo2[4];
    u32 lane16, epoch;
    u32x4 pollv;
    const LAS char* rd[4]; const LAS char* rd2[4];
    const char* w1src; const char* w2src; const char* bsrc; const char* hsrc;
    u32 voff2[9];
    int sub;
    u32 dmadst;
    u32 hso;
    int jj;
    __amdgpu_buffer_rsrc_t hrs;
    u32* flag_pub; u32* flag_poll;
};

template <int KS, int T, bool ZERO> __device__ __forceinline__ void mfma(Ctx& cx, f32x4& c, const bf16x8& a) {
    constexpr int r = (KS * 4 + T) * 4;
    if constexpr (KS < 16) {
        if constexpr (ZERO) asm volatile("v_mfma_f32_16x16x32_bf16 %0, %1, a[%c2:%c3], 0" : "=v"(c) : "v"(a), "n"(r), "n"(r + 3));
        else asm volatile("v_mfma_f32_16x16x32_bf16 %0, %1, a[%c2:%c3], %0" : "+v"(c) : "v"(a), "n"(r), "n"(r + 3));
    } else {
        asm volatile("v_mfma_f32_16x16x32_bf16 %0, %1, %2, %0" : "+v"(c) : "v"(a), "v"(cx.Bv[KS - 16][T]));
    }
}
template <int KS, int T, bool SC1> __device__ __forceinline__ void gload(Ctx& cx, u32 voff, const char* sb) {
    constexpr int r = (KS * 4 + T) * 4, off = (KS & 3) * 1024;
    if constexpr (KS < 16) {
        if constexpr (SC1) asm volatile("global_load_dwordx4 a[%c2:%c3], %0, %1 offset:%4 sc1" :: "v"(voff), "s"(sb), "n"(r), "n"(r + 3), "n"(off) : "memory");
        else asm volatile("global_load_dwordx4 a[%c2:%c3], %0, %1 offset:%4" :: "v"(voff), "s"(sb), "n"(r), "n"(r + 3), "n"(off) : "memory");
    } else {
        if constexpr (SC1) asm volatile("global_load_dwordx4 %0, %1, %2 offset:%3 sc1" : "=v"(cx.Bv[KS - 16][T]) : "v"(voff), "s"(sb), "n"(off) : "memory");
        else asm volatile("global_load_dwordx4 %0, %1, %2 offset:%3" : "=v"(cx.Bv[KS - 16][T]) : "v"(voff), "s"(sb), "n"(off) : "memory");
    }
}
template <int S, int P> __device__ __forceinline__ void dma_m0(const Ctx& c) {
    asm volatile("s_mov_b32 m0, %0" :: "s"(c.dmadst + (u32)((S & 3) * STG + (P >> 2) * 4096)) : "memory");
}
constexpr int nat_kstep(int ks2) {
    if (ks2 < 10) { constexpr int T[5] = {0, 1, 5, 6, 4}; return 9 * (ks2 / 5) + T[ks2 % 5]; }
    const int jj = (ks2 - 10) >> 1, h = (ks2 - 10) & 1;
    return 9 * (jj >> 1) + ((jj & 1) ? 7 : 2) + h;
}
template <int S, int P> __device__ __forceinline__ void dma_go(const Ctx& c) {
    if constexpr (S < 9) {
        const char* sb = c.w1src + (size_t)S * STG + (P >> 2) * 4096;
        asm volatile("global_load_lds_dwordx4 %0, %1 offset:%2 sc1" :: "v"(c.lane16), "s"(sb), "n"((P & 3) * 1024) : "memory");
    } else {
        constexpr int n0 = nat_kstep(2 * (S - 9)), n1 = nat_kstep(2 * (S - 9) + 1);
        const char* sb = c.w2src + (size_t)(c.sub ? n1 : n0) * (32 * D_ * 2);
        asm volatile("global_load_lds_dwordx4 %0, %1 offset:%2" :: "v"(c.voff2[P]), "s"(sb), "n"((P & 3) * 1024) : "memory");
    }
}
template <int ID> __device__ __forceinline__ void load_b(Ctx& c) {
    constexpr int ks = ID >> 2, t = ID & 3;
    gload<ks, t, true>(c, c.vo1[t], c.bsrc + (ks >> 2) * 4096);
}
template <int ID> __device__ __forceinline__ void load_h(Ctx& c) {
    constexpr int kk = ID >> 2, t = ID & 3;
    gload<kk, t, true>(c, c.vo2[t], c.hsrc + (kk >> 2) * 4096);
}
template <int KS> __device__ __forceinline__ void pin_b(Ctx& c) {
#pragma unroll
    for (int t = 0; t < 4; ++t) { if constexpr (KS >= 16) asm volatile("" : "+v"(c.Bv[KS - 16][t])); }
}
template <int V, int P> __device__ __forceinline__ void epi_piece(Ctx& c) {
    constexpr int pr = P >> 3, st = P & 7, t = pr >> 1, r = 2 * (pr & 1), par = V & 1;
    if constexpr (st == 0) {
        asm volatile("v_mul_f32 %0, 0xbfb8aa3b, %1" : "=v"(c.e0) : "v"(c.ac[par][0][t][r]));
        asm volatile("v_mul_f32 %0, 0xbfb8aa3b, %1" : "=v"(c.e1) : "v"(c.ac[par][0][t][r + 1]));
    } else if constexpr (st == 1) asm volatile("v_exp_f32 %0, %0" : "+v"(c.e0));
    else if constexpr (st == 2) asm volatile("v_exp_f32 %0, %0" : "+v"(c.e1));
    else if constexpr (st == 3) {
        asm volatile("v_add_f32 %0, 1.0, %0" : "+v"(c.e0));
        asm volatile("v_add_f32 %0, 1.0, %0" : "+v"(c.e1));
    } else if constexpr (st == 4) asm volatile("v_rcp_f32 %0, %0" : "+v"(c.e0));
    else if constexpr (st == 5) asm volatile("v_rcp_f32 %0, %0" : "+v"(c.e1));
    else if constexpr (st == 6) {
        asm volatile("v_mul_f32 %0, %0, %1" : "+v"(c.e0) : "v"(c.ac[par][1][t][r]));
        asm volatile("v_mul_f32 %0, %0, %1" : "+v"(c.e1) : "v"(c.ac[par][1][t][r + 1]));
    } else {
        constexpr bool first = (V == 0 || V == 2 || V == 5 || V == 7);
        u32 o;
        asm volatile("v_cvt_pk_bf16_f32 %0, %1, %2" : "=v"(o) : "v"(c.e0), "v"(c.e1));
        c.hp[t][(first ? 0 : 2) + (pr & 1)] = o;
    }
}
template <int V, int T> __device__ __forceinline__ void store_h(Ctx& c) {
    const int m5 = 5 * (c.jj >> 1), ab = c.jj & 1;
    if constexpr (V == 4) {
        const u32x2 d = {c.hp[T][2], c.hp[T][3]};
        __builtin_amdgcn_raw_buffer_store_b64(d, c.hrs, c.vo2[T] + 8 * ab, c.hso + (m5 + 4) * 1024, 16);
    } else {
        const int kstep = (V == 1) ? m5 + 2 * ab : (V == 3) ? m5 + 2 * ab + 1 : (V == 6) ? 10 + 2 * c.jj : 11 + 2 * c.jj;
        __builtin_amdgcn_raw_buffer_store_b128(c.hp[T], c.hrs, c.vo2[T], c.hso + kstep * 1024, 16);
    }
}
template <int PART> __device__ __forceinline__ void poll_check(Ctx& c) {
    asm volatile("" : "+v"(c.pollv));
    const u32 e = c.epoch;
    if (!__all(c.pollv[0] == e && c.pollv[1] == e && c.pollv[2] == e && c.pollv[3] == e)) {
        const u32* p = c.flag_poll + (PART - 1) * 1024 + ((c.lane16 >> 4) & 3);
        poll_eq(p, e);
    }
}
template <int V, int P> __device__ __forceinline__ void epi_piece_padded(Ctx& c) {
    epi_piece<V, P>(c);
    if constexpr ((P & 7) == 2 || (P & 7) == 5) asm volatile("s_nop 0");
}
template <int V, int... P> __device__ __forceinline__ void epi_all(Ctx& c, std::integer_sequence<int, P...>) { (epi_piece_padded<V, P>(c), ...); }

template <int GS> __device__ __forceinline__ void slot(Ctx& c) {
    constexpr bool g1 = GS < G1S;
    constexpr int S = stage_of(GS), R = rel_of(GS), grp = R >> 2, t = R & 3;
    constexpr int fid = g1 ? 36 * S + grp : 324 + 18 * (S - 9) + grp;
    if constexpr (GS == G1S) {
        asm volatile("s_nop 7" ::: "memory");
        epi_all<8>(c, std::make_integer_sequence<int, 64>{});
        store_h<8, 0>(c); store_h<8, 1>(c); store_h<8, 2>(c); store_h<8, 3>(c);
        SCHED();
    }
    if constexpr (S == 0 && t == 0 && (grp & 1) == 0) { wait_vm<wait_for(GS, seq_b(grp >> 1))>(); pin_b<(grp >> 1)>(c); }
    if constexpr (GS == 8 * 144) { wait_vm<wait_for(GS, seq_poll(1))>(); poll_check<1>(c); }
    if constexpr (!g1 && t == 0 && (grp % 9) == 0) { constexpr int kk = 2 * (S - 9) + grp / 9; wait_vm<wait_for(GS, seq_h(kk))>(); pin_b<kk>(c); }
    if constexpr (dma_piece(GS) >= 0 && (dma_piece(GS) & 3) == 0) dma_m0<S + 3, dma_piece(GS)>(c);
    if constexpr (g1) {
        constexpr int ks = grp >> 1, mat = grp & 1;
        mfma<ks, t, (ks == 0)>(c, c.ac[S & 1][mat][t], c.Aq[fid % 6]);
    } else {
        constexpr int kk = 2 * (S - 9) + grp / 9, du = grp % 9;
        mfma<kk, t, (kk == 0)>(c, c.a2[du][t], c.Aq[fid % 6]);
    }
    if constexpr (barpt(GS)) {
        constexpr int n1 = wait_for(GS, seq_dma_last(S + 1));
        constexpr int n2 = (S == 12) ? wait_for(GS, seq_poll(2)) : 63;
        wait_vm<(n1 < n2 ? n1 : n2)>();
        if constexpr (S == 12) poll_check<2>(c);
        __builtin_amdgcn_s_barrier();
        asm volatile("" ::: "memory");
    }
    if constexpr (pub(GS)) {
        wait_vm<wait_for(GS, seq_store_last(pub(GS)))>();
        if (c.lane16 == 0) flag_set(c.flag_pub + (pub(GS) - 1) * 1024, c.epoch);
    }
    if constexpr (poll_issue(GS)) asm volatile("global_load_dwordx4 %0, %1, %2 sc1" : "=v"(c.pollv) : "v"(0u), "s"(c.flag_poll + (poll_issue(GS) - 1) * 1024) : "memory");
    if constexpr (fid + 5 < 486) {
        constexpr int f = fid + 5;
        if constexpr (f < 324) { if constexpr (t == 0) c.Aq[f % 6] = *(const LAS bf16x8*)(c.rd[(f / 36) & 3] + (f % 36) * 1024); }
        else if constexpr (t <= 1) {
            constexpr int fs = (f - 324) % 18;
            const s16x4 h = __builtin_amdgcn_ds_read_tr16_b64_v4i16((LAS s16x4*)(c.rd2[(9 + (f - 324) / 18) & 3] + (fs / 9) * 9216 + (fs % 9) * 32 + t * (16 * 288)));
            const u32x2 hh = __builtin_bit_cast(u32x2, h);
            u32x4 a = __builtin_bit_cast(u32x4, c.Aq[f % 6]);
            a[2 * t] = hh[0]; a[2 * t + 1] = hh[1];
            c.Aq[f % 6] = __builtin_bit_cast(bf16x8, a);
        }
    }
    if constexpr (g1 && S >= 1 && (t == 1 || t == 2) && grp >= 2 && grp < 34) epi_piece<S - 1, (grp - 2) * 2 + t - 1>(c);
    if constexpr (hstore(GS) >= 0) store_h<S - 1, hstore(GS)>(c);
    if constexpr (bload(GS) >= 0) load_b<bload(GS)>(c);
    if constexpr (hload(GS) >= 0) load_h<hload(GS)>(c);
    if constexpr (dma_piece(GS) >= 0) dma_go<S + 3, dma_piece(GS)>(c);
    SCHED();
}
template <int BASE, int... I> __device__ __forceinline__ void run_slots(Ctx& c, std::integer_sequence<int, I...>) { (slot<BASE + I>(c), ...); }
template <int S, int P> __device__ __forceinline__ void dma_one(const Ctx& c) { dma_m0<S, P>(c); asm volatile("s_nop 0"); dma_go<S, P>(c); }
template <int S, int... P> __device__ __forceinline__ void dma_stage(const Ctx& c, std::integer_sequence<int, P...>) { (dma_one<S, P>(c), ...); }
template <int... I> __device__ __forceinline__ void load_b_first(Ctx& c, std::integer_sequence<int, I...>) { (load_b<I>(c), ...); }
}

__global__ __launch_bounds__(256, 1) void k_main(const u16* __restrict__ qin, const u16* __restrict__ kin, const u16* __restrict__ vin, const u16* __restrict__ shift_k,
                                                 const u16* __restrict__ ssm_k, const u16* __restrict__ ssm_D, const u16* __restrict__ wg, const u16* __restrict__ wu,
                                                 const u16* __restrict__ wd, const u16* __restrict__ cvec, const u16* __restrict__ w_mod,
                                                 char* __restrict__ ws, u16* __restrict__ out) {
    using namespace mlp;
    extern __shared__ __attribute__((aligned(16))) char smem[];
    const int tid = threadIdx.x, lane = tid & 63, w = __builtin_amdgcn_readfirstlane(tid >> 6);
    const int b = blockIdx.x, x = b & 7, idx = b >> 3, q = x >> 1;
    const int mt = 4 * (x & 1) + (idx & 3), fcl = idx >> 2, fc = 8 * q + fcl;
    const int mt2 = 8 * (x & 1) + (idx & 7), cc = idx >> 3, tg = w & 1, kh = w >> 1;
    const rsrc_t wsr = __builtin_amdgcn_make_buffer_rsrc(ws, 0, (int)WS_END, 0x00020000);
    STAMP_DECL; STAMP_AT(0);
    const u32 epoch_in = __hip_atomic_load(flag_ptr(ws, F_EPOCH), __ATOMIC_RELAXED, __HIP_MEMORY_SCOPE_AGENT);
    u32 epoch;
    {
        const int t0 = (x * 32 + idx) * 8;
        const int chn[3] = {2 * b, 2 * b + 1, 512 + (b & 63)};
        ConvIn in[3];
        u32* lcnt = (u32*)(smem + 163824);
        if (tid == 0) *lcnt = 0u;
        __syncthreads();
        if (w < 3) {
            if (tid < 144) prep_job(b, kin, vin, shift_k, wsr);
            epoch = __builtin_amdgcn_readfirstlane(epoch_in) + 1u;
            DRAIN();
            if (lane == 0 && atomicAdd(lcnt, 1u) == 2u) flag_set(flag_ptr(ws, F_KV + (b & 7) * 32 + (b >> 3)), epoch);
        } else {
            if (b < 216) mod_job(b, cvec, w_mod, ws, (float*)(smem + R_MOD));
            epoch = __builtin_amdgcn_readfirstlane(epoch_in) + 1u;
            DRAIN();
            if (lane == 0 && b < 216) flag_set(flag_ptr(ws, F_MOD + b), epoch);
        }
        STAMP_AT(1);
        const int tj0 = b >= 64 ? b - 64 : 0, tj1 = b >= 64 && b < 160 ? 128 + b : 0;
        u32x4 tw[9];
        conv_load_k(in[0], chn[0], ssm_k, ssm_D); conv_load_k(in[1], chn[1], ssm_k, ssm_D);
        if (b < 64) conv_load_k(in[2], chn[2], ssm_k, ssm_D);
        else tile_load(tj0, wg, wu, tw);
        if (w == 0) poll_eq(flag_ptr(ws, F_KV + ((lane & 32) && b < 64 ? 7 : (2 * b) / 72) * 32 + (lane & 31)), epoch);
        __syncthreads();
        STAMP_AT(2);
        conv_load_x(in[0], chn[0], wsr); conv_load_x(in[1], chn[1], wsr);
        if (b < 64) conv_load_x(in[2], chn[2], wsr);
        RowsPre rp;
        rows_load(rp, t0, qin);
        u32 pfw = 0;
        if (tid < 162) pfw = *(const u32*)((const char*)wd + (size_t)q * 1152 * 1152 + (size_t)((x & 1) * 32 + idx) * 20736 + tid * 128);
        Ride ride;
        ride.tbase = tile_base(tj1, wg, wu); ride.job_out = tj0; ride.tile = smem + T_LDS_A; ride.wsr = wsr; ride.wid = w;
        if (b >= 64) tile_put(smem + T_LDS_A, tw);
        if (b < 64) conv_body<3, false, false>(chn, in, smem, wsr, ride STAMP_ARGS);
        else if (b < 160) conv_body<2, true, true>(chn, in, smem, wsr, ride STAMP_ARGS);
        else conv_body<2, false, true>(chn, in, smem, wsr, ride STAMP_ARGS);
        asm volatile("" :: "v"(pfw));
        STAMP_AT(5);
        DRAIN();
        __syncthreads();
        if (tid < 8) flag_set(flag_ptr(ws, F_CONV + tid * 256 + b), epoch);
        poll_eq4(wsr, (u32)WS_CNT + (F_MOD + (lane < 54 ? lane : 0) * 4) * 4, epoch);
        rows_put_mod(smem, wsr);
        if (tid == 64 && b >= 64) flag_set(flag_ptr(ws, F_TILE + tj0), epoch);
        if (b >= 64 && b < 160) tile_put(smem, ride.tw);
        __syncthreads();
        rows_pre(rp, smem);
        STAMP_AT(6);
        if (w == 0) {
            poll_eq4(wsr, (u32)WS_CNT + (F_CONV + x * 256 + lane * 4) * 4, epoch);
            rows_fetch(t0, smem, ws);
        } else {
            if (b >= 64 && b < 160) tile_out(tj1, smem, wsr);
        }
        __syncthreads();
        STAMP_AT(7);
        rows_main(rp, t0, smem, wsr);
        DRAIN();
        __syncthreads();
        if (tid == 0) flag_set(flag_ptr(ws, F_ROWS + (t0 >> 6) * 8 + ((t0 >> 3) & 7)), epoch);
        if (tid == 128 && b >= 64 && b < 160) flag_set(flag_ptr(ws, F_TILE + tj1), epoch);
        STAMP_AT(8);
    }
    Ctx c;
#pragma unroll
    for (int t = 0; t < 4; ++t) c.hp[t] = (u32x4){0, 0, 0, 0};
    c.lane16 = lane * 16;
#pragma unroll
    for (int t = 0; t < 4; ++t) { c.vo1[t] = lane * 16 + t * 18432; c.vo2[t] = lane * 16 + t * TUS; }
    const LAS char* l0 = (const LAS char*)smem;
#pragma unroll
    for (int i = 0; i < 4; ++i) {
        c.rd[i] = l0 + i * STG + lane * 16; c.rd2[i] = l0 + i * STG + kh * 18432 + (4 * (lane >> 4) + ((lane & 15) >> 2)) * 288 + 8 * (lane & 3);
        asm volatile("" : "+v"(c.rd[i])); asm volatile("" : "+v"(c.rd2[i]));
    }
#pragma unroll
    for (int i = 0; i < 9; ++i) { const int ci = lane + 64 * i, row = (ci * 3641) >> 16, c16 = ci - row * 18; c.voff2[i] = (u32)(row * (D_ * 2) + c16 * 16 - (i & 3) * 1024); }
    c.sub = w & 1;
    c.w1src = ws + WS_WT1 + (size_t)fc * 331776 + w * 9216;
    c.w2src = (const char*)(wd + (size_t)(36 * q + 18 * kh) * 32 * D_ + cc * 144);
    c.bsrc = ws + WS_H2T + (size_t)(16 * mt + 4 * w) * 18432;
    c.hsrc = ws + WS_HT + (size_t)(8 * mt2 + 4 * tg) * TUS + (size_t)((q * 2 + kh) * 18) * 1024;
    c.dmadst = (u32)(uintptr_t)smem + w * 9216;
    c.jj = fcl & 3;
    c.hso = (u32)((16 * mt + 4 * w) * TUS + ((q * 2 + (fcl >> 2)) * 18) * 1024);
    c.hrs = __builtin_amdgcn_make_buffer_rsrc(ws + WS_HT, 0, (int)((size_t)S_ * F_ * 2), 0x00020000);
    c.epoch = epoch;
    c.flag_pub = flag_ptr(ws, F_H + ((((mt * 4 + w) * 4 + q) * 2 + (fcl >> 2))) * 4 + (fcl & 3));
    c.flag_poll = flag_ptr(ws, F_H + (((((mt2 >> 1) * 4 + 2 * (mt2 & 1) + tg) * 4 + q) * 2 + kh)) * 4);
    asm volatile("" ::: "a0", "a1", "a2", "a3", "a4", "a5", "a6", "a7", "a8", "a9", "a10", "a11", "a12", "a13", "a14", "a15", "a16", "a17", "a18", "a19", "a20", "a21", "a22", "a23", "a24", "a25", "a26", "a27", "a28", "a29", "a30", "a31", "a32", "a33", "a34", "a35", "a36", "a37", "a38", "a39", "a40", "a41", "a42", "a43", "a44", "a45", "a46", "a47", "a48", "a49", "a50", "a51", "a52", "a53", "a54", "a55", "a56", "a57", "a58", "a59", "a60", "a61", "a62", "a63", "a64", "a65", "a66", "a67", "a68", "a69", "a70", "a71", "a72", "a73", "a74", "a75", "a76", "a77", "a78", "a79", "a80", "a81", "a82", "a83", "a84", "a85", "a86", "a87", "a88", "a89", "a90", "a91", "a92", "a93", "a94", "a95", "a96", "a97", "a98", "a99", "a100", "a101", "a102", "a103", "a104", "a105", "a106", "a107", "a108", "a109", "a110", "a111", "a112", "a113", "a114", "a115", "a116", "a117", "a118", "a119", "a120", "a121", "a122", "a123", "a124", "a125", "a126", "a127", "a128", "a129", "a130", "a131", "a132", "a133", "a134", "a135", "a136", "a137", "a138", "a139", "a140", "a141", "a142", "a143", "a144", "a145", "a146", "a147", "a148", "a149", "a150", "a151", "a152", "a153", "a154", "a155", "a156", "a157", "a158", "a159", "a160", "a161", "a162", "a163", "a164", "a165", "a166", "a167", "a168", "a169", "a170", "a171", "a172", "a173", "a174", "a175", "a176", "a177", "a178", "a179", "a180", "a181", "a182", "a183", "a184", "a185", "a186", "a187", "a188", "a189", "a190", "a191", "a192", "a193", "a194", "a195", "a196", "a197", "a198", "a199", "a200", "a201", "a202", "a203", "a204", "a205", "a206", "a207", "a208", "a209", "a210", "a211", "a212", "a213", "a214", "a215", "a216", "a217", "a218", "a219", "a220", "a221", "a222", "a223", "a224", "a225", "a226", "a227", "a228", "a229", "a230", "a231", "a232", "a233", "a234", "a235", "a236", "a237", "a238", "a239", "a240", "a241", "a242", "a243", "a244", "a245", "a246", "a247", "a248", "a249", "a250", "a251", "a252", "a253", "a254", "a255");
    poll_eq(flag_ptr(ws, lane < 40 ? F_TILE + (fc >> 2) * 36 + (lane < 36 ? lane : 0) : F_ROWS + (4 * mt + w) * 8 + (lane & 7)), epoch);
    dma_stage<0>(c, std::make_integer_sequence<int, 9>{});
    STAMP_AT(9);
    load_b_first(c, std::make_integer_sequence<int, 4 * LA>{});
    dma_stage<1>(c, std::make_integer_sequence<int, 9>{});
    dma_stage<2>(c, std::make_integer_sequence<int, 9>{});
    wait_vm<PRO - 9>();
    __builtin_amdgcn_s_barrier();
    asm volatile("" ::: "memory");
#pragma unroll
    for (int f = 0; f < 5; ++f) c.Aq[f] = *(const LAS bf16x8*)(c.rd[0] + f * 1024);
    SCHED();
    STAMP_AT(10);
    run_slots<0>(c, std::make_integer_sequence<int, 144>{});
    STAMP_AT(11);
    run_slots<144>(c, std::make_integer_sequence<int, 144 * 4>{});
    run_slots<144 * 5>(c, std::make_integer_sequence<int, 144 * 4>{});
    STAMP_AT(12);
    run_slots<G1S>(c, std::make_integer_sequence<int, 324>{});
    run_slots<G1S + 324>(c, std::make_integer_sequence<int, 324>{});
    asm volatile("s_nop 7" ::: "memory");
    SCHED();
    STAMP_AT(13);
    const int n = lane & 15, g = lane >> 4;
    u32x2 xr[3][2]; f32x4 gb[3];
#pragma unroll
    for (int i = 0; i < 3; ++i) {
        const int u = (w + 4 * i < 9) ? w + 4 * i : 8, d = 144 * cc + 16 * u + 4 * g;
#pragma unroll
        for (int t = 0; t < 2; ++t) xr[i][t] = __builtin_amdgcn_raw_buffer_load_b64(wsr, (u32)WS_X1 + (u32)((128 * mt2 + 32 * q + 16 * t + n) * D_ + d) * 2, 0, 16);
        gb[i] = *(const f32x4*)(smem + R_MOD + (5 * 576 + d) * 4);
    }
    __builtin_amdgcn_s_barrier();
    {
        char* ex = smem + w * 18432 + lane * 16;
#pragma unroll
        for (int t = 0; t < 2; ++t)
#pragma unroll
            for (int u = 0; u < 9; ++u) *(f32x4*)(ex + (t * 9 + u) * 1024) = kh ? c.a2[u][t] : c.a2[u][2 + t];
    }
    __syncthreads();
    const int j = 2 * tg + kh;
    u32* fred = flag_ptr(ws, F_RED + (mt2 * 4 + cc) * 16);
    const u32 slab = (u32)WS_SLAB + (u32)((mt2 * 4 + cc) * 16) * 9216;
    {
        const char* ey = smem + (w ^ 2) * 18432 + lane * 16;
        char* own = smem + 73728 + lane * 16;
#pragma unroll
        for (int u = 0; u < 9; ++u) {
            const f32x4 r0 = (kh ? c.a2[u][2] : c.a2[u][0]) + *(const f32x4*)(ey + u * 1024);
            const f32x4 r1 = (kh ? c.a2[u][3] : c.a2[u][1]) + *(const f32x4*)(ey + (9 + u) * 1024);
            if (j != q) st16_dev(wsr, slab + (j * 4 + q) * 9216 + u * 1024 + lane * 16, (u32x4){pk2(r0[0], r0[1]), pk2(r0[2], r0[3]), pk2(r1[0], r1[1]), pk2(r1[2], r1[3])});
            else { *(f32x4*)(own + u * 2048) = r0; *(f32x4*)(own + u * 2048 + 1024) = r1; }
        }
    }
    if (j != q) {
        DRAIN();
        if (lane == 0) flag_set(fred + j * 4 + q, epoch);
    }
    STAMP_AT(14);
    poll_eq(fred + q * 4 + (((lane & 3) == q) ? (q ^ 1) : (lane & 3)), epoch);
    __syncthreads();
    u32x4 rem[3][3];
#pragma unroll
    for (int i = 0; i < 3; ++i) {
        const int u = (w + 4 * i < 9) ? w + 4 * i : 8;
#pragma unroll
        for (int k = 0; k < 3; ++k) { const int qq = k + (k >= q ? 1 : 0); rem[i][k] = __builtin_amdgcn_raw_buffer_load_b128(wsr, slab + (q * 4 + qq) * 9216 + u * 1024 + lane * 16, 0, 16); }
    }
#pragma unroll
    for (int i = 0; i < 3; ++i) {
        const int u = w + 4 * i;
        if (u < 9) {
            const int d = 144 * cc + 16 * u + 4 * g;
#pragma unroll
            for (int t = 0; t < 2; ++t) {
                f32x4 s = *(const f32x4*)(smem + 73728 + u * 2048 + t * 1024 + lane * 16);
#pragma unroll
                for (int k = 0; k < 3; ++k) {
                    const u32 a = rem[i][k][2 * t], bq = rem[i][k][2 * t + 1];
                    s += (f32x4){bf_lo(a), bf_hi(a), bf_lo(bq), bf_hi(bq)};
                }
                const float o0 = bf_lo(xr[i][t][0]) + rbf(gb[i][0] * rbf(s[0])), o1 = bf_hi(xr[i][t][0]) + rbf(gb[i][1] * rbf(s[1]));
                const float o2 = bf_lo(xr[i][t][1]) + rbf(gb[i][2] * rbf(s[2])), o3 = bf_hi(xr[i][t][1]) + rbf(gb[i][3] * rbf(s[3]));
                *(u32x2*)(out + (size_t)(128 * mt2 + 32 * q + 16 * t + n) * D_ + d) = (u32x2){pk2(o0, o1), pk2(o2, o3)};
            }
        }
    }
    if (b == 0 && tid == 0) flag_set(flag_ptr(ws, F_EPOCH), epoch);
    STAMP_AT(15);
    STAMP_OUT(4, 16);
}

extern "C" void kernel_launch(void* const* d_in, const int* in_sizes, int n_in, void* d_out, int out_size, void* d_ws, size_t ws_size,
                              hipStream_t stream) {
    const u16* q = (const u16*)d_in[0];
    const u16* k = (const u16*)d_in[1];
    const u16* v = (const u16*)d_in[2];
    const u16* shift_k = (const u16*)d_in[3];
    const u16* ssm_k = (const u16*)d_in[4];
    const u16* ssm_D = (const u16*)d_in[5];
    const u16* wg = (const u16*)d_in[7];
    const u16* wu = (const u16*)d_in[8];
    const u16* wd = (const u16*)d_in[9];
    const u16* c = (const u16*)d_in[10];
    const u16* w_mod = (const u16*)d_in[11];
    char* ws = (char*)d_ws;
    (void)hipFuncSetAttribute((const void*)k_main, hipFuncAttributeMaxDynamicSharedMemorySize, mlp::LDS_BYTES);
    k_main<<<256, 256, mlp::LDS_BYTES, stream>>>(q, k, v, shift_k, ssm_k, ssm_D, wg, wu, wd, c, w_mod, ws, (u16*)d_out);
}
```

```cpp
#include <hip/hip_runtime.h>
#include <stdint.h>
#include <utility>

typedef unsigned short u16;
typedef unsigned int u32;
typedef __bf16 bf16x8 __attribute__((ext_vector_type(8)));
typedef __bf16 bf16x2 __attribute__((ext_vector_type(2)));
typedef float f32x4 __attribute__((ext_vector_type(4)));
typedef float f32x2 __attribute__((ext_vector_type(2)));
typedef u32 u32x4 __attribute__((ext_vector_type(4)));
typedef u32 u32x2 __attribute__((ext_vector_type(2)));
typedef short s16x4 __attribute__((ext_vector_type(4)));
#define LAS __attribute__((address_space(3)))
typedef LAS char* lptr;

constexpr int S_ = 2048, D_ = 576, F_ = 4608;

constexpr size_t WS_MOD = 0;
constexpr size_t WS_CNT = 65536;
constexpr size_t WS_YYT = WS_CNT + 65536;
constexpr size_t WS_X1  = WS_YYT + (size_t)D_ * S_ * 2;
constexpr size_t WS_H2T = WS_X1 + (size_t)D_ * S_ * 2;
constexpr size_t WS_HT  = WS_H2T + (size_t)D_ * S_ * 2;
constexpr size_t WS_WT1 = WS_HT + (size_t)S_ * F_ * 2;
constexpr size_t WS_SLAB = WS_WT1 + (size_t)2 * D_ * F_ * 2;
constexpr size_t WS_END = WS_SLAB + (size_t)16 * 4 * 4 * 4 * 9216;

#define STAMP_DECL
#define STAMP_ARGS
#define STAMP_AT(i)
#define STAMP_OUT(kid, n)
__device__ __forceinline__ float bf_lo(u32 w) { return __uint_as_float(w << 16); }
__device__ __forceinline__ float bf_hi(u32 w) { return __uint_as_float(w & 0xffff0000u); }
__device__ __forceinline__ float bf1(u16 w) { return __uint_as_float((u32)w << 16); }
__device__ __forceinline__ u32 pk2(float lo, float hi) {
    f32x2 v = {lo, hi};
    return __builtin_bit_cast(u32, __builtin_convertvector(v, bf16x2));
}
__device__ __forceinline__ float rbf(float x) { return (float)(__bf16)x; }
__device__ __forceinline__ float sigmoidf_(float x) { return __builtin_amdgcn_rcpf(1.0f + __builtin_amdgcn_exp2f(-1.4426950408889634f * x)); }

constexpr int F_MOD = 256;
constexpr int F_TILE = 512;
constexpr int F_CONV = 1024;
constexpr int F_ROWS = 3072;
constexpr int F_H = 3328;
constexpr int F_RED = 5376;
constexpr int F_EPOCH = 6400;
typedef __amdgpu_buffer_rsrc_t rsrc_t;
__device__ __forceinline__ u32* flag_ptr(char* ws, int idx) { return (u32*)(ws + WS_CNT) + idx; }
__device__ __forceinline__ void flag_set(u32* p, u32 e) { __hip_atomic_store(p, e, __ATOMIC_RELAXED, __HIP_MEMORY_SCOPE_AGENT); }
__device__ __forceinline__ void st16_dev(rsrc_t rs, u32 off, u32x4 v) { __builtin_amdgcn_raw_buffer_store_b128(v, rs, off, 0, 16); }
__device__ __forceinline__ u32x4 ld16_dev(rsrc_t rs, u32 off) { return __builtin_amdgcn_raw_buffer_load_b128(rs, off, 0, 16); }
__device__ __forceinline__ void poll_eq(const u32* p, u32 e) {
    for (int it = 0; it < (1 << 21); ++it) {
        if (__all(__hip_atomic_load(p, __ATOMIC_RELAXED, __HIP_MEMORY_SCOPE_AGENT) == e)) break;
        __builtin_amdgcn_s_sleep(1);
    }
}
__device__ __forceinline__ void poll_eq4(rsrc_t rs, u32 off, u32 e) {
    for (int it = 0; it < (1 << 21); ++it) {
        asm volatile("" ::: "memory");
        const u32x4 v = ld16_dev(rs, off);
        if (__all(v[0] == e && v[1] == e && v[2] == e && v[3] == e)) break;
        __builtin_amdgcn_s_sleep(1);
    }
}
#define DRAIN() asm volatile("s_waitcnt vmcnt(0)" ::: "memory")

#define SCHED() __builtin_amdgcn_sched_barrier(0)
__device__ __forceinline__ float dpp_add(float v, const int ctrl_sel) {
    int r;
    const int iv = __builtin_bit_cast(int, v);
    switch (ctrl_sel) {
        case 0: r = __builtin_amdgcn_update_dpp(0, iv, 0xB1, 0xf, 0xf, true); break;
        case 1: r = __builtin_amdgcn_update_dpp(0, iv, 0x4E, 0xf, 0xf, true); break;
        case 2: r = __builtin_amdgcn_update_dpp(0, iv, 0x141, 0xf, 0xf, true); break;
        default: r = __builtin_amdgcn_update_dpp(0, iv, 0x140, 0xf, 0xf, true); break;
    }
    return v + __builtin_bit_cast(float, r);
}
__device__ __forceinline__ float half_sum(float v) {
    v = dpp_add(v, 0); v = dpp_add(v, 1); v = dpp_add(v, 2); v = dpp_add(v, 3);
    auto r = __builtin_amdgcn_permlane16_swap(__float_as_uint(v), __float_as_uint(v), false, false);
    return __uint_as_float(r[0]) + __uint_as_float(r[1]);
}

struct KvIn { u32x2 kw[8], vw[8]; u32x4 sw; u32x2 sw2; };
__device__ __forceinline__ void kv_load(KvIn& p, int c0, const u16* __restrict__ k, const u16* __restrict__ v, const u16* __restrict__ shift_k) {
    const int cw = c0 & ~1;
#pragma unroll
    for (int i = 0; i < 8; ++i) p.kw[i] = *(const u32x2*)(k + (size_t)(threadIdx.x + 256 * i) * D_ + cw);
#pragma unroll
    for (int i = 0; i < 8; ++i) p.vw[i] = *(const u32x2*)(v + (size_t)(threadIdx.x + 256 * i) * D_ + cw);
    p.sw = (u32x4){*(const u32*)(shift_k + c0 * 4), *(const u32*)(shift_k + c0 * 4 + 2), *(const u32*)(shift_k + c0 * 4 + 4), *(const u32*)(shift_k + c0 * 4 + 6)};
    p.sw2 = (u32x2){*(const u32*)(shift_k + c0 * 4 + 8), *(const u32*)(shift_k + c0 * 4 + 10)};
}
constexpr int KV_RAWV = 257 * 72 + 8;
__device__ __forceinline__ void kv_put(const KvIn& p, char* raw) {
    const int tid = threadIdx.x;
#pragma unroll
    for (int i = 0; i < 8; ++i) {
        const int t = tid + 256 * i, o = 72 * (t >> 3) + 8 * (t & 7);
        *(u32x2*)(raw + 72 + o) = p.kw[i];
        *(u32x2*)(raw + KV_RAWV + o) = p.vw[i];
    }
    if (tid < 8) *(u32x2*)(raw + 8 * tid) = (u32x2){0, 0};
}
__device__ __forceinline__ void kv_compute(const KvIn& p, int c0, const char* raw, char* xr, int stride) {
    const int tid = threadIdx.x;
    u32x2 kw[11], vw[8];
#pragma unroll
    for (int i = 0; i < 11; ++i) kw[i] = *(const u32x2*)(raw + 72 * tid + (i < 3 ? 40 + 8 * i : 72 + 8 * (i - 3)));
#pragma unroll
    for (int i = 0; i < 8; ++i) vw[i] = *(const u32x2*)(raw + KV_RAWV + 72 * tid + 8 * i);
    const bool odd = c0 & 1;
#pragma unroll
    for (int c = 0; c < 3; ++c) {
        const u32 wa = c == 0 ? p.sw[0] : c == 1 ? p.sw[2] : p.sw2[0], wb = c == 0 ? p.sw[1] : c == 1 ? p.sw[3] : p.sw2[1];
        const float w0 = bf_lo(wa), w1 = bf_hi(wa), w2 = bf_lo(wb), w3 = bf_hi(wb);
        float kf[11], r[8];
#define KV_PICK(d) (c == 0 ? (odd ? bf_hi((d)[0]) : bf_lo((d)[0])) : c == 1 ? (odd ? bf_lo((d)[1]) : bf_hi((d)[0])) : (odd ? bf_hi((d)[1]) : bf_lo((d)[1])))
#pragma unroll
        for (int i = 0; i < 11; ++i) kf[i] = KV_PICK(kw[i]);
#pragma unroll
        for (int i = 0; i < 8; ++i) r[i] = (kf[i] * w3 + kf[i + 1] * w2 + kf[i + 2] * w1 + kf[i + 3] * w0) * KV_PICK(vw[i]);
#undef KV_PICK
        *(u32x4*)(xr + c * stride + 2 * (2040 - 8 * tid)) = (u32x4){pk2(r[7], r[6]), pk2(r[5], r[4]), pk2(r[3], r[2]), pk2(r[1], r[0])};
    }
}

__device__ __forceinline__ void mod_job(int job, const u16* __restrict__ c, const u16* __restrict__ w_mod, char* __restrict__ ws, float* sc  , float* red  ) {
    const int tid = threadIdx.x, lane = tid & 63, wid = tid >> 6;
    const int col0 = job * 64, rg = tid >> 3, c8 = tid & 7;
    u32x4 w[18];
#pragma unroll
    for (int i = 0; i < 18; ++i) w[i] = *(const u32x4*)(w_mod + (size_t)(rg + 32 * i) * 3456 + col0 + c8 * 8);
#pragma unroll
    for (int j = 0; j < 3; ++j) { const int r = tid + 256 * j; if (r < 576) { const float x = bf1(c[r]); sc[r] = rbf(x * sigmoidf_(x)); } }
    __syncthreads();
    float acc[8] = {0, 0, 0, 0, 0, 0, 0, 0};
#pragma unroll
    for (int i = 0; i < 18; ++i) {
        const float s = sc[rg + 32 * i];
#pragma unroll
        for (int j = 0; j < 4; ++j) { acc[2 * j] += s * bf_lo(w[i][j]); acc[2 * j + 1] += s * bf_hi(w[i][j]); }
    }
#pragma unroll
    for (int j = 0; j < 8; ++j) {
        acc[j] += __shfl_xor(acc[j], 8); acc[j] += __shfl_xor(acc[j], 16); acc[j] += __shfl_xor(acc[j], 32);
    }
    if (lane < 8) {
#pragma unroll
        for (int j = 0; j < 8; ++j) red[wid * 64 + lane * 8 + j] = acc[j];
    }
    __syncthreads();
    if (tid < 64) {
        float m = rbf((red[tid] + red[64 + tid]) + (red[128 + tid] + red[192 + tid]));
        const int which = col0 / 576;
        if (which == 1 || which == 4) m = rbf(1.0f + m);
        __hip_atomic_store((u32*)(ws + WS_MOD) + col0 + tid, __float_as_uint(m), __ATOMIC_RELAXED, __HIP_MEMORY_SCOPE_AGENT);
    }
}

__device__ __forceinline__ const char* tile_base(int job, const u16* __restrict__ wg, const u16* __restrict__ wu) {
    const int ks = job % 18, mat = (job / 18) & 1, cg = job / 36;
    return (const char*)((mat ? wu : wg) + (size_t)(32 * ks) * F_ + cg * 576);
}
__device__ __forceinline__ void tile_dma(int job, const u16* __restrict__ wg, const u16* __restrict__ wu, char* tile, const u32 (&voff)[9]) {
    const int w = __builtin_amdgcn_readfirstlane(threadIdx.x >> 6);
    const char* base = tile_base(job, wg, wu) + (size_t)w * 8 * (F_ * 2);
    const u32 dst = (u32)(uintptr_t)tile + w * 9216;
#pragma unroll
    for (int i = 0; i < 9; ++i)
        asm volatile("s_mov_b32 m0, %2\n\ts_nop 0\n\tglobal_load_lds_dwordx4 %0, %1" :: "v"(voff[i]), "s"(base), "s"(dst + i * 1024) : "memory");
}
__device__ __forceinline__ void tile_out(int job, const char* tile, rsrc_t wsr) {
    const int lane = threadIdx.x & 63, w = threadIdx.x >> 6;
    const int ks = job % 18, mat = (job / 18) & 1, fc = 4 * (job / 36) + w;
    const int g = lane >> 4, q4 = (lane & 15) >> 2, p = lane & 3;
    const LAS char* rd = (const LAS char*)tile + (4 * g + q4) * 1152 + 8 * p + w * 288;
#pragma unroll
    for (int u = 0; u < 9; ++u) {
        const int pos = (fc & 1) ? (u == 0 ? 4 : u <= 4 ? u - 1 : u) : (u == 8 ? 4 : u < 4 ? u : u + 1);
        const s16x4 lo = __builtin_amdgcn_ds_read_tr16_b64_v4i16((LAS s16x4*)(rd + u * 32));
        const s16x4 hi = __builtin_amdgcn_ds_read_tr16_b64_v4i16((LAS s16x4*)(rd + u * 32 + 16 * 1152));
        typedef short s16x8 __attribute__((ext_vector_type(8)));
        const s16x8 r = __builtin_shufflevector(lo, hi, 0, 1, 2, 3, 4, 5, 6, 7);
        st16_dev(wsr, (u32)WS_WT1 + fc * 331776 + pos * 36864 + (ks * 2 + mat) * 1024 + lane * 16, __builtin_bit_cast(u32x4, r));
    }
}
__device__ __forceinline__ void tile_jobs(int i0, const u16* __restrict__ wg, const u16* __restrict__ wu, char* smem, char* ws, rsrc_t wsr, u32 epoch) {
    const int lane = threadIdx.x & 63;
    u32 voff[9];
#pragma unroll
    for (int i = 0; i < 9; ++i) { const int ci = lane + 64 * i, row = (ci * 58255) >> 22, c16 = ci - row * 72; voff[i] = (u32)(row * (F_ * 2) + c16 * 16); }
#pragma unroll
    for (int j = 0; j < 4; ++j) tile_dma(i0 + 64 * j, wg, wu, smem + j * 36864, voff);
#pragma unroll
    for (int j = 0; j < 4; ++j) {
        asm volatile("s_waitcnt vmcnt(27)" ::: "memory");
        __syncthreads();
        tile_out(i0 + 64 * j, smem + j * 36864, wsr);
    }
    if (i0 < 32) {
        __syncthreads();
        tile_dma(i0 + 256, wg, wu, smem, voff);
        asm volatile("s_waitcnt vmcnt(0)" ::: "memory");
        __syncthreads();
        tile_out(i0 + 256, smem, wsr);
    }
    DRAIN();
    __syncthreads();
    if (threadIdx.x < 5 && i0 + 64 * threadIdx.x < 288) flag_set(flag_ptr(ws, F_TILE + i0 + 64 * threadIdx.x), epoch);
}

constexpr int C_XR = 0;
constexpr int C_K0 = 4608;
constexpr int C_K1 = C_K0 + 4128 + 32;
constexpr int C_CH = C_K1 + 4160;
constexpr int C_RED = 3 * C_CH;
constexpr int C_LDS = C_RED + 3 * 32768;
static_assert(((C_K1 - C_K0) / 4) % 32 == 16, "bank offset");
static_assert(C_CH % 16 == 0 && C_K0 % 16 == 0 && C_K1 % 16 == 0, "align");
template <bool ZERO> __device__ __forceinline__ void mfma_v(f32x4& c, const bf16x8& a, const bf16x8& b) {
    if constexpr (ZERO) asm volatile("v_mfma_f32_16x16x32_bf16 %0, %1, %2, 0" : "=v"(c) : "v"(a), "v"(b));
    else asm volatile("v_mfma_f32_16x16x32_bf16 %0, %1, %2, %0" : "+v"(c) : "v"(a), "v"(b));
}
struct ConvSet { bf16x8 A[8], B[8]; };
template <int J> __device__ __forceinline__ void conv_ld(ConvSet& s, const LAS char* ab, const LAS char* bb) {
    if constexpr (J < 8) {
        const LAS u32* p = (const LAS u32*)(ab + 512 * J);
        u32x4 t = {p[0], p[1], p[2], p[3]};
        s.A[J] = __builtin_bit_cast(bf16x8, t);
    } else s.B[J - 8] = *(const LAS bf16x8*)(bb + 512 * (15 - J));
}
template <int M> struct TriIdx {
    static constexpr int find(bool want_i) { int m = 0; for (int s = 0; s < 8; ++s) for (int j = 0; j <= s; ++j) { if (m == M) return want_i ? s : j; ++m; } return 0; }
    static constexpr int i = find(true), j = find(false);
};
template <int M, bool PF, bool FIRST> __device__ __forceinline__ void conv_slot(f32x4 (&acc)[8], const ConvSet& cur, ConvSet& nxt, const LAS char* ab, const LAS char* bb) {
    mfma_v<(FIRST && TriIdx<M>::j == 0)>(acc[TriIdx<M>::i], cur.A[TriIdx<M>::j], cur.B[TriIdx<M>::i - TriIdx<M>::j]);
    if constexpr (PF && (M & 1) == 0 && M / 2 < 16) conv_ld<((M / 2) & 1) * 8 + (M / 4)>(nxt, ab, bb);
    SCHED();
}
template <bool PF, bool FIRST, int... M> __device__ __forceinline__ void conv_step(f32x4 (&acc)[8], const ConvSet& cur, ConvSet& nxt, const LAS char* ab, const LAS char* bb, std::integer_sequence<int, M...>) {
    (conv_slot<M, PF, FIRST>(acc, cur, nxt, ab, bb), ...);
}
template <int... J> __device__ __forceinline__ void conv_ld_all(ConvSet& s, const LAS char* ab, const LAS char* bb, std::integer_sequence<int, J...>) { (conv_ld<J>(s, ab, bb), ...); }

struct ConvIn { u32x4 k; u32 kn; u16 dv; };
__device__ __forceinline__ void conv_load_k(ConvIn& in, int d, const u16* __restrict__ ssm_k, const u16* __restrict__ ssm_D) {
    const int tid = threadIdx.x;
    in.k = *(const u32x4*)(ssm_k + (size_t)d * S_ + tid * 8);
    in.kn = *(const u32*)(ssm_k + (size_t)d * S_ + (tid < 255 ? tid * 8 + 8 : 0));
    in.dv = ssm_D[d];
}
__device__ __forceinline__ void conv_put_k(const ConvIn& in, char* sm) {
    const int tid = threadIdx.x;
    const u32 kn = tid < 255 ? in.kn : 0u;
    *(u32x4*)(sm + C_K0 + 32 + tid * 16) = in.k;
    *(u32x4*)(sm + C_K1 + 32 + tid * 16) = (u32x4){__builtin_amdgcn_alignbit(in.k[1], in.k[0], 16), __builtin_amdgcn_alignbit(in.k[2], in.k[1], 16),
                                                 __builtin_amdgcn_alignbit(in.k[3], in.k[2], 16), __builtin_amdgcn_alignbit(kn, in.k[3], 16)};
    if (tid < 32) *(u32x4*)(sm + C_XR + 4096 + tid * 16) = (u32x4){0, 0, 0, 0};
    if (tid == 0) {
        *(u32x4*)(sm + C_K0) = (u32x4){0, 0, 0, 0}; *(u32x4*)(sm + C_K0 + 16) = (u32x4){0, 0, 0, 0};
        *(u32x4*)(sm + C_K1) = (u32x4){0, 0, 0, 0}; *(u32x4*)(sm + C_K1 + 16) = (u32x4){0, 0, 0, in.k[0] << 16};
    }
}
__device__ __forceinline__ void conv_finish(int d, const char* sm, const char* red, float Dv, rsrc_t wsr) {
    const int tid = threadIdx.x;
    const int i = tid >> 5, n = tid & 15, gp = (tid >> 4) & 1;
    f32x4 lo = {0.f, 0.f, 0.f, 0.f}, hi = {0.f, 0.f, 0.f, 0.f};
#pragma unroll
    for (int w = 0; w < 4; ++w) {
        lo += *(const f32x4*)(red + (w * 8 + i) * 1024 + (n + 32 * gp) * 16);
        hi += *(const f32x4*)(red + (w * 8 + i) * 1024 + (n + 32 * gp + 16) * 16);
    }
    const int s = 256 * i + 16 * n + 8 * gp;
    const u32x4 xw = *(const u32x4*)(sm + C_XR + 2 * (2040 - s));
    const float o0 = rbf(lo[0]) + rbf(bf_hi(xw[3]) * Dv), o1 = rbf(lo[1]) + rbf(bf_lo(xw[3]) * Dv), o2 = rbf(lo[2]) + rbf(bf_hi(xw[2]) * Dv), o3 = rbf(lo[3]) + rbf(bf_lo(xw[2]) * Dv);
    const float o4 = rbf(hi[0]) + rbf(bf_hi(xw[1]) * Dv), o5 = rbf(hi[1]) + rbf(bf_lo(xw[1]) * Dv), o6 = rbf(hi[2]) + rbf(bf_hi(xw[0]) * Dv), o7 = rbf(hi[3]) + rbf(bf_lo(xw[0]) * Dv);
    st16_dev(wsr, (u32)WS_YYT + (u32)(d * S_ + s) * 2, (u32x4){pk2(o0, o1), pk2(o2, o3), pk2(o4, o5), pk2(o6, o7)});
}
__device__ __forceinline__ void conv_body(int ch0, const ConvIn (&in)[3], char* smem, rsrc_t wsr
                                          ) {
    const int lane = threadIdx.x & 63, wid = __builtin_amdgcn_readfirstlane(threadIdx.x >> 6);
    __syncthreads();
    STAMP_AT(3);
    const int n = lane & 15, g = lane >> 4, dd = g >> 1, gh = g & 1;
    const LAS char* l0 = (const LAS char*)smem;
    const LAS char* bb = l0 + C_XR + 480 - 32 * n + 32 * dd + 16 * gh + 128 * wid;
    const LAS char* ab = l0 + ((n & 1) ? C_K0 + 2 * (16 * dd + 8 * gh + n + 1) : C_K1 + 2 * (16 * dd + 8 * gh + n)) + 128 * wid;
    f32x4 acc[3][8];
    ConvSet s0, s1;
    conv_ld_all(s0, ab, bb, std::make_integer_sequence<int, 16>{});
    SCHED();
    constexpr auto SQ = std::make_integer_sequence<int, 36>{};
    conv_step<true, true>(acc[0], s0, s1, ab + 64, bb + 64, SQ);
    conv_step<true, false>(acc[0], s1, s0, ab + C_CH, bb + C_CH, SQ);
    conv_step<true, true>(acc[1], s0, s1, ab + C_CH + 64, bb + C_CH + 64, SQ);
    conv_step<true, false>(acc[1], s1, s0, ab + 2 * C_CH, bb + 2 * C_CH, SQ);
    conv_step<true, true>(acc[2], s0, s1, ab + 2 * C_CH + 64, bb + 2 * C_CH + 64, SQ);
    conv_step<false, false>(acc[2], s1, s0, ab, bb, SQ);
    asm volatile("s_nop 7" ::: "memory");
    STAMP_AT(4);
#pragma unroll
    for (int c = 0; c < 3; ++c)
#pragma unroll
        for (int i = 0; i < 8; ++i) *(f32x4*)(smem + C_RED + c * 32768 + (wid * 8 + i) * 1024 + lane * 16) = acc[c][i];
    __syncthreads();
#pragma unroll
    for (int c = 0; c < 3; ++c) conv_finish(ch0 + c, smem + c * C_CH, smem + C_RED + c * 32768, bf1(in[c].dv), wsr);
}

constexpr int R_MOD = 147456;
constexpr int R_YT = 110592;
constexpr int R_HT = R_YT + 8 * 1168;
struct RowsPre { float x[18], gh[18]; u32 xw[9]; };
__device__ __forceinline__ void rows_load(RowsPre& r, int t0, const u16* __restrict__ q) {
    const int tid = threadIdx.x, lane = tid & 63, hl = lane & 31, row = 2 * (tid >> 6) + (lane >> 5);
    const u32* qrow = (const u32*)(q + (size_t)(t0 + row) * D_);
#pragma unroll
    for (int i = 0; i < 9; ++i) r.xw[i] = qrow[hl + 32 * i];
}
__device__ __forceinline__ void mod_fetch(char* sm, rsrc_t wsr) {
    u32x4 mw[4];
#pragma unroll
    for (int i = 0; i < 4; ++i) { const int j = threadIdx.x + 256 * i; mw[i] = ld16_dev(wsr, (u32)WS_MOD + (j < 864 ? j : 0) * 16); }
#pragma unroll
    for (int i = 0; i < 4; ++i) { const int j = threadIdx.x + 256 * i; if (j < 864) *(u32x4*)(sm + R_MOD + j * 16) = mw[i]; }
}
__device__ __forceinline__ void rows_pre(RowsPre& r, const char* sm) {
    const int hl = threadIdx.x & 31;
#pragma unroll
    for (int i = 0; i < 9; ++i) { r.x[2 * i] = bf_lo(r.xw[i]); r.x[2 * i + 1] = bf_hi(r.xw[i]); }
    float s = 0.f;
#pragma unroll
    for (int i = 0; i < 18; ++i) s += r.x[i];
    const float mean = half_sum(s) * (1.0f / 576.0f);
    float vs = 0.f;
#pragma unroll
    for (int i = 0; i < 18; ++i) { const float dl = r.x[i] - mean; vs += dl * dl; }
    const float rstd = __builtin_amdgcn_rsqf(half_sum(vs) * (1.0f / 576.0f) + 1e-5f);
    const float* mod = (const float*)(sm + R_MOD);
#pragma unroll
    for (int i = 0; i < 9; ++i) {
        const int c = 2 * (hl + 32 * i);
        const f32x2 sa = *(const f32x2*)(mod + c), c1 = *(const f32x2*)(mod + 576 + c), ga = *(const f32x2*)(mod + 2 * 576 + c);
        r.gh[2 * i] = ga[0] * ((r.x[2 * i] - mean) * rstd * c1[0] + sa[0]);
        r.gh[2 * i + 1] = ga[1] * ((r.x[2 * i + 1] - mean) * rstd * c1[1] + sa[1]);
    }
}
__device__ __forceinline__ void rows_fetch(int t0, char* sm, const char* ws) {
    const int lane = threadIdx.x & 63;
    const char* src = ws + WS_YYT + t0 * 2;
    const u32 dst = (u32)(uintptr_t)(sm + R_HT);
#pragma unroll
    for (int i = 0; i < 9; ++i)
        asm volatile("s_mov_b32 m0, %2\n\ts_nop 0\n\tglobal_load_lds_dwordx4 %0, %1 sc1" :: "v"((u32)((lane + 64 * i) * (S_ * 2))), "s"(src), "s"(dst + i * 1024) : "memory");
    asm volatile("s_waitcnt vmcnt(0)" ::: "memory");
}
__device__ __forceinline__ void rows_main(const RowsPre& r, int t0, char* sm, rsrc_t wsr) {
    const int tid = threadIdx.x, lane = tid & 63, hl = lane & 31, row = 2 * (tid >> 6) + (lane >> 5);
    const int cg = tid >> 1, th = tid & 1;
    if (tid < 144) {
        u32x2 yw[8];
#pragma unroll
        for (int c = 0; c < 8; ++c) yw[c] = *(const u32x2*)(sm + R_HT + (8 * cg + c) * 16 + th * 8);
#pragma unroll
        for (int t = 0; t < 4; ++t) {
            u32x4 o;
#pragma unroll
            for (int kk = 0; kk < 4; ++kk) {
                const u32 a = yw[2 * kk][t >> 1], bq = yw[2 * kk + 1][t >> 1];
                o[kk] = (t & 1) ? __builtin_amdgcn_perm(bq, a, 0x07060302u) : __builtin_amdgcn_perm(bq, a, 0x05040100u);
            }
            *(u32x4*)(sm + R_YT + (4 * th + t) * 1168 + cg * 16) = o;
        }
    }
    __syncthreads();
    const float* mod = (const float*)(sm + R_MOD);
    float x1[18];
#pragma unroll
    for (int i = 0; i < 9; ++i) {
        u32* yp = (u32*)(sm + R_YT + row * 1168 + (hl + 32 * i) * 4);
        const u32 yv = *yp;
        const u32 p = pk2(r.x[2 * i] + r.gh[2 * i] * bf_lo(yv), r.x[2 * i + 1] + r.gh[2 * i + 1] * bf_hi(yv));
        *yp = p;
        x1[2 * i] = bf_lo(p); x1[2 * i + 1] = bf_hi(p);
    }
    float s = 0.f;
#pragma unroll
    for (int i = 0; i < 18; ++i) s += x1[i];
    const float mean = half_sum(s) * (1.0f / 576.0f);
    float vs = 0.f;
#pragma unroll
    for (int i = 0; i < 18; ++i) { const float dl = x1[i] - mean; vs += dl * dl; }
    const float rstd = __builtin_amdgcn_rsqf(half_sum(vs) * (1.0f / 576.0f) + 1e-5f);
#pragma unroll
    for (int i = 0; i < 9; ++i) {
        const int c = 2 * (hl + 32 * i);
        const f32x2 sb = *(const f32x2*)(mod + 3 * 576 + c), c1 = *(const f32x2*)(mod + 4 * 576 + c);
        const float h0 = (x1[2 * i] - mean) * rstd * c1[0] + sb[0], h1 = (x1[2 * i + 1] - mean) * rstd * c1[1] + sb[1];
        const int ks = c >> 5, kk = c & 31, gq = (kk & 15) >> 2, j = ((kk >> 4) << 2) | (kk & 3);
        *(u32*)(sm + R_HT + ((ks * 4 + gq) * 8 + row) * 16 + j * 2) = pk2(h0, h1);
    }
    __syncthreads();
    const u32 h2t = (u32)WS_H2T + (u32)(t0 >> 4) * 18 * 1024 + (t0 & 15) * 16, x1g = (u32)WS_X1 + (u32)t0 * D_ * 2;
#pragma unroll
    for (int it = 0; it < 3; ++it) {
        const int i = tid + 256 * it;
        if (i < 576) {
            const int kg = i >> 3, rr8 = i & 7;
            st16_dev(wsr, h2t + (kg >> 2) * 1024 + (kg & 3) * 256 + rr8 * 16, *(const u32x4*)(sm + R_HT + i * 16));
            const int rr = i / 72, cc = i - rr * 72;
            st16_dev(wsr, x1g + rr * 1152 + cc * 16, *(const u32x4*)(sm + R_YT + rr * 1168 + cc * 16));
        }
    }
}

namespace mlp {
constexpr int STG = 36864, LDS_BYTES = 163840;
constexpr int G1S = 9 * 144, G2S = 18 * 36, NS = G1S + G2S;
constexpr int LA = 4;
constexpr int PRO = 9 + 4 * LA + 18;
constexpr int TUS = 147456;

constexpr int stage_of(int gs) { return gs < G1S ? gs / 144 : 9 + (gs - G1S) / 72; }
constexpr int rel_of(int gs) { return gs < G1S ? gs % 144 : (gs - G1S) % 72; }
constexpr int grp_of(int gs) { return rel_of(gs) >> 2; }
constexpr int t_of(int gs) { return rel_of(gs) & 3; }
constexpr bool barpt(int gs) { return gs >= 0 && gs < NS && t_of(gs) == 3 && grp_of(gs) == (gs < G1S ? 3 : 0) && stage_of(gs) < 17; }
constexpr int dma_piece(int gs) {
    if (gs >= NS || t_of(gs) != 3 || stage_of(gs) + 3 > 17) return -1;
    const int g = grp_of(gs);
    if (gs < G1S) { constexpr int G[9] = {5, 7, 9, 12, 14, 16, 20, 22, 24}; for (int i = 0; i < 9; ++i) if (G[i] == g) return i; return -1; }
    return (g & 1) ? g >> 1 : -1;
}
constexpr int bload(int gs) {
    if (gs >= 144 || t_of(gs) == 0 || t_of(gs) == 3) return -1;
    const int ks = grp_of(gs) >> 1, i = (grp_of(gs) & 1) * 2 + t_of(gs) - 1;
    return (ks + LA < 18) ? (ks + LA) * 4 + i : -1;
}
constexpr bool unit_stores(int v) { return v == 1 || v == 3 || v == 4 || v == 6 || v == 8; }
constexpr int hstore(int gs) {
    if (gs >= G1S || gs < 144 || !unit_stores(gs / 144 - 1) || t_of(gs) != 3) return -1;
    const int g = grp_of(gs);
    return (g == 10 || g == 18 || g == 26 || g == 34) ? (g - 10) / 8 : -1;
}
constexpr int hload(int gs) {
    if (t_of(gs) != 3) return -1;
    const int s = stage_of(gs), g = grp_of(gs);
    if (s == 8) { constexpr int G[20] = {4, 6, 8, 10, 11, 13, 15, 17, 18, 19, 21, 23, 25, 26, 27, 28, 29, 30, 31, 32}; for (int i = 0; i < 20; ++i) if (G[i] == g) return i; return -1; }
    if (s < 9 || g < 2 || g > 16 || (g & 1)) return -1;
    const int i = (g - 2) >> 1;
    if (s == 9) return 20 + i;
    if (s == 10) return 28 + i;
    if (s == 11) return i < 4 ? 36 + i : -1;
    if (s >= 12 && s <= 15) return 40 + 8 * (s - 12) + i;
    return -1;
}
constexpr int pub(int gs) { return (stage_of(gs) == 6 && grp_of(gs) == 27 && t_of(gs) == 3) ? 1 : (stage_of(gs) == 10 && grp_of(gs) == 9 && t_of(gs) == 1) ? 2 : 0; }
constexpr int poll_issue(int gs) { return (stage_of(gs) == 7 && grp_of(gs) == 27 && t_of(gs) == 3) ? 1 : (stage_of(gs) == 11 && grp_of(gs) == 9 && t_of(gs) == 1) ? 2 : 0; }
constexpr int vm_ops(int gs) { return (pub(gs) ? 1 : 0) + (poll_issue(gs) ? 1 : 0) + (hstore(gs) >= 0) + (bload(gs) >= 0) + (hload(gs) >= 0) + (dma_piece(gs) >= 0); }
struct Ledger { int cum[NS + 1]; };
constexpr Ledger make_ledger() {
    Ledger l{}; int n = PRO;
    for (int gs = 0; gs < NS; ++gs) { if (gs == G1S) n += 4; l.cum[gs] = n; n += vm_ops(gs); }
    l.cum[NS] = n; return l;
}
constexpr Ledger LG = make_ledger();
constexpr int seq_in(int gs, int kind) {
    int n = LG.cum[gs];
    if (kind > 0 && pub(gs)) ++n;
    if (kind > 1 && poll_issue(gs)) ++n;
    if (kind > 2 && hstore(gs) >= 0) ++n;
    if (kind > 3 && bload(gs) >= 0) ++n;
    if (kind > 4 && hload(gs) >= 0) ++n;
    return n;
}
constexpr int seq_dma_last(int s) {
    if (s <= 2) return 9 * s + 8;
    for (int gs = 0; gs < NS; ++gs) if (stage_of(gs) == s - 3 && dma_piece(gs) == 8) return seq_in(gs, 5);
    return -1;
}
constexpr int seq_b(int ks) {
    if (ks < LA) return 27 + 4 * ks + 3;
    for (int gs = 0; gs < 144; ++gs) if (bload(gs) == ks * 4 + 3) return seq_in(gs, 3);
    return -1;
}
constexpr int seq_h(int kk) {
    for (int gs = 8 * 144; gs < NS; ++gs) if (hload(gs) == kk * 4 + 3) return seq_in(gs, 4);
    return -1;
}
constexpr int seq_poll(int part) {
    for (int gs = 0; gs < NS; ++gs) if (poll_issue(gs) == part) return seq_in(gs, 1);
    return -1;
}
constexpr int seq_store_last(int part) {
    if (part == 2) return LG.cum[G1S] - 1;
    for (int gs = 5 * 144; gs < 6 * 144; ++gs) if (hstore(gs) == 3) return seq_in(gs, 2);
    return -1;
}
constexpr int clampv(int n) { return n > 63 ? 63 : (n < 0 ? 0 : n); }
constexpr int wait_for(int gs, int seq) { return clampv(LG.cum[gs] - seq - 1); }

template <int N> __device__ __forceinline__ void wait_vm() { asm volatile("s_waitcnt vmcnt(%0)" :: "n"(N) : "memory"); }

struct Ctx {
    bf16x8 Bv[2][4];
    bf16x8 Aq[6];
    f32x4 ac[2][2][4];
    f32x4 a2[9][4];
    float e0, e1;
    u32x4 hp[4];
    u32 vo1[4], vo2[4];
    u32 lane16, epoch;
    u32x4 pollv;
    const LAS char* rd[4]; const LAS char* rd2[4];
    const char* w1src; const char* w2src; const char* bsrc; const char* hsrc;
    u32 voff2[9];
    int sub;
    u32 dmadst;
    u32 hso;
    int jj;
    __amdgpu_buffer_rsrc_t hrs;
    u32* flag_pub; u32* flag_poll;
};

template <int KS, int T, bool ZERO> __device__ __forceinline__ void mfma(Ctx& cx, f32x4& c, const bf16x8& a) {
    constexpr int r = (KS * 4 + T) * 4;
    if constexpr (KS < 16) {
        if constexpr (ZERO) asm volatile("v_mfma_f32_16x16x32_bf16 %0, %1, a[%c2:%c3], 0" : "=v"(c) : "v"(a), "n"(r), "n"(r + 3));
        else asm volatile("v_mfma_f32_16x16x32_bf16 %0, %1, a[%c2:%c3], %0" : "+v"(c) : "v"(a), "n"(r), "n"(r + 3));
    } else {
        asm volatile("v_mfma_f32_16x16x32_bf16 %0, %1, %2, %0" : "+v"(c) : "v"(a), "v"(cx.Bv[KS - 16][T]));
    }
}
template <int KS, int T, bool SC1> __device__ __forceinline__ void gload(Ctx& cx, u32 voff, const char* sb) {
    constexpr int r = (KS * 4 + T) * 4, off = (KS & 3) * 1024;
    if constexpr (KS < 16) {
        if constexpr (SC1) asm volatile("global_load_dwordx4 a[%c2:%c3], %0, %1 offset:%4 sc1" :: "v"(voff), "s"(sb), "n"(r), "n"(r + 3), "n"(off) : "memory");
        else asm volatile("global_load_dwordx4 a[%c2:%c3], %0, %1 offset:%4" :: "v"(voff), "s"(sb), "n"(r), "n"(r + 3), "n"(off) : "memory");
    } else {
        if constexpr (SC1) asm volatile("global_load_dwordx4 %0, %1, %2 offset:%3 sc1" : "=v"(cx.Bv[KS - 16][T]) : "v"(voff), "s"(sb), "n"(off) : "memory");
        else asm volatile("global_load_dwordx4 %0, %1, %2 offset:%3" : "=v"(cx.Bv[KS - 16][T]) : "v"(voff), "s"(sb), "n"(off) : "memory");
    }
}
template <int S, int P> __device__ __forceinline__ void dma_m0(const Ctx& c) {
    asm volatile("s_mov_b32 m0, %0" :: "s"(c.dmadst + (u32)((S & 3) * STG + (P >> 2) * 4096)) : "memory");
}
constexpr int nat_kstep(int ks2) {
    if (ks2 < 10) { constexpr int T[5] = {0, 1, 5, 6, 4}; return 9 * (ks2 / 5) + T[ks2 % 5]; }
    const int jj = (ks2 - 10) >> 1, h = (ks2 - 10) & 1;
    return 9 * (jj >> 1) + ((jj & 1) ? 7 : 2) + h;
}
template <int S, int P> __device__ __forceinline__ void dma_go(const Ctx& c) {
    if constexpr (S < 9) {
        const char* sb = c.w1src + (size_t)S * STG + (P >> 2) * 4096;
        asm volatile("global_load_lds_dwordx4 %0, %1 offset:%2 sc1" :: "v"(c.lane16), "s"(sb), "n"((P & 3) * 1024) : "memory");
    } else {
        constexpr int n0 = nat_kstep(2 * (S - 9)), n1 = nat_kstep(2 * (S - 9) + 1);
        const char* sb = c.w2src + (size_t)(c.sub ? n1 : n0) * (32 * D_ * 2);
        asm volatile("global_load_lds_dwordx4 %0, %1 offset:%2" :: "v"(c.voff2[P]), "s"(sb), "n"((P & 3) * 1024) : "memory");
    }
}
template <int ID> __device__ __forceinline__ void load_b(Ctx& c) {
    constexpr int ks = ID >> 2, t = ID & 3;
    gload<ks, t, true>(c, c.vo1[t], c.bsrc + (ks >> 2) * 4096);
}
template <int ID> __device__ __forceinline__ void load_h(Ctx& c) {
    constexpr int kk = ID >> 2, t = ID & 3;
    gload<kk, t, true>(c, c.vo2[t], c.hsrc + (kk >> 2) * 4096);
}
template <int KS> __device__ __forceinline__ void pin_b(Ctx& c) {
#pragma unroll
    for (int t = 0; t < 4; ++t) { if constexpr (KS >= 16) asm volatile("" : "+v"(c.Bv[KS - 16][t])); }
}
template <int V, int P> __device__ __forceinline__ void epi_piece(Ctx& c) {
    constexpr int pr = P >> 3, st = P & 7, t = pr >> 1, r = 2 * (pr & 1), par = V & 1;
    if constexpr (st == 0) {
        asm volatile("v_mul_f32 %0, 0xbfb8aa3b, %1" : "=v"(c.e0) : "v"(c.ac[par][0][t][r]));
        asm volatile("v_mul_f32 %0, 0xbfb8aa3b, %1" : "=v"(c.e1) : "v"(c.ac[par][0][t][r + 1]));
    } else if constexpr (st == 1) asm volatile("v_exp_f32 %0, %0" : "+v"(c.e0));
    else if constexpr (st == 2) asm volatile("v_exp_f32 %0, %0" : "+v"(c.e1));
    else if constexpr (st == 3) {
        asm volatile("v_add_f32 %0, 1.0, %0" : "+v"(c.e0));
        asm volatile("v_add_f32 %0, 1.0, %0" : "+v"(c.e1));
    } else if constexpr (st == 4) asm volatile("v_rcp_f32 %0, %0" : "+v"(c.e0));
    else if constexpr (st == 5) asm volatile("v_rcp_f32 %0, %0" : "+v"(c.e1));
    else if constexpr (st == 6) {
        asm volatile("v_mul_f32 %0, %0, %1" : "+v"(c.e0) : "v"(c.ac[par][1][t][r]));
        asm volatile("v_mul_f32 %0, %0, %1" : "+v"(c.e1) : "v"(c.ac[par][1][t][r + 1]));
    } else {
        constexpr bool first = (V == 0 || V == 2 || V == 5 || V == 7);
        u32 o;
        asm volatile("v_cvt_pk_bf16_f32 %0, %1, %2" : "=v"(o) : "v"(c.e0), "v"(c.e1));
        c.hp[t][(first ? 0 : 2) + (pr & 1)] = o;
    }
}
template <int V, int T> __device__ __forceinline__ void store_h(Ctx& c) {
    const int m5 = 5 * (c.jj >> 1), ab = c.jj & 1;
    if constexpr (V == 4) {
        const u32x2 d = {c.hp[T][2], c.hp[T][3]};
        __builtin_amdgcn_raw_buffer_store_b64(d, c.hrs, c.vo2[T] + 8 * ab, c.hso + (m5 + 4) * 1024, 16);
    } else {
        const int kstep = (V == 1) ? m5 + 2 * ab : (V == 3) ? m5 + 2 * ab + 1 : (V == 6) ? 10 + 2 * c.jj : 11 + 2 * c.jj;
        __builtin_amdgcn_raw_buffer_store_b128(c.hp[T], c.hrs, c.vo2[T], c.hso + kstep * 1024, 16);
    }
}
template <int PART> __device__ __forceinline__ void poll_check(Ctx& c) {
    asm volatile("" : "+v"(c.pollv));
    const u32 e = c.epoch;
    if (!__all(c.pollv[0] == e && c.pollv[1] == e && c.pollv[2] == e && c.pollv[3] == e)) {
        const u32* p = c.flag_poll + (PART - 1) * 1024 + ((c.lane16 >> 4) & 3);
        poll_eq(p, e);
    }
}
template <int V, int P> __device__ __forceinline__ void epi_piece_padded(Ctx& c) {
    epi_piece<V, P>(c);
    if constexpr ((P & 7) == 2 || (P & 7) == 5) asm volatile("s_nop 0");
}
template <int V, int... P> __device__ __forceinline__ void epi_all(Ctx& c, std::integer_sequence<int, P...>) { (epi_piece_padded<V, P>(c), ...); }

template <int GS> __device__ __forceinline__ void slot(Ctx& c) {
    constexpr bool g1 = GS < G1S;
    constexpr int S = stage_of(GS), R = rel_of(GS), grp = R >> 2, t = R & 3;
    constexpr int fid = g1 ? 36 * S + grp : 324 + 18 * (S - 9) + grp;
    if constexpr (GS == G1S) {
        asm volatile("s_nop 7" ::: "memory");
        epi_all<8>(c, std::make_integer_sequence<int, 64>{});
        store_h<8, 0>(c); store_h<8, 1>(c); store_h<8, 2>(c); store_h<8, 3>(c);
        SCHED();
    }
    if constexpr (S == 0 && t == 0 && (grp & 1) == 0) { wait_vm<wait_for(GS, seq_b(grp >> 1))>(); pin_b<(grp >> 1)>(c); }
    if constexpr (GS == 8 * 144) { wait_vm<wait_for(GS, seq_poll(1))>(); poll_check<1>(c); }
    if constexpr (!g1 && t == 0 && (grp % 9) == 0) { constexpr int kk = 2 * (S - 9) + grp / 9; wait_vm<wait_for(GS, seq_h(kk))>(); pin_b<kk>(c); }
    if constexpr (dma_piece(GS) >= 0 && (dma_piece(GS) & 3) == 0) dma_m0<S + 3, dma_piece(GS)>(c);
    if constexpr (g1) {
        constexpr int ks = grp >> 1, mat = grp & 1;
        mfma<ks, t, (ks == 0)>(c, c.ac[S & 1][mat][t], c.Aq[fid % 6]);
    } else {
        constexpr int kk = 2 * (S - 9) + grp / 9, du = grp % 9;
        mfma<kk, t, (kk == 0)>(c, c.a2[du][t], c.Aq[fid % 6]);
    }
    if constexpr (barpt(GS)) {
        constexpr int n1 = wait_for(GS, seq_dma_last(S + 1));
        constexpr int n2 = (S == 12) ? wait_for(GS, seq_poll(2)) : 63;
        wait_vm<(n1 < n2 ? n1 : n2)>();
        if constexpr (S == 12) poll_check<2>(c);
        __builtin_amdgcn_s_barrier();
        asm volatile("" ::: "memory");
    }
    if constexpr (pub(GS)) {
        wait_vm<wait_for(GS, seq_store_last(pub(GS)))>();
        if (c.lane16 == 0) flag_set(c.flag_pub + (pub(GS) - 1) * 1024, c.epoch);
    }
    if constexpr (poll_issue(GS)) asm volatile("global_load_dwordx4 %0, %1, %2 sc1" : "=v"(c.pollv) : "v"(0u), "s"(c.flag_poll + (poll_issue(GS) - 1) * 1024) : "memory");
    if constexpr (fid + 5 < 486) {
        constexpr int f = fid + 5;
        if constexpr (f < 324) { if constexpr (t == 0) c.Aq[f % 6] = *(const LAS bf16x8*)(c.rd[(f / 36) & 3] + (f % 36) * 1024); }
        else if constexpr (t <= 1) {
            constexpr int fs = (f - 324) % 18;
            const s16x4 h = __builtin_amdgcn_ds_read_tr16_b64_v4i16((LAS s16x4*)(c.rd2[(9 + (f - 324) / 18) & 3] + (fs / 9) * 9216 + (fs % 9) * 32 + t * (16 * 288)));
            const u32x2 hh = __builtin_bit_cast(u32x2, h);
            u32x4 a = __builtin_bit_cast(u32x4, c.Aq[f % 6]);
            a[2 * t] = hh[0]; a[2 * t + 1] = hh[1];
            c.Aq[f % 6] = __builtin_bit_cast(bf16x8, a);
        }
    }
    if constexpr (g1 && S >= 1 && (t == 1 || t == 2) && grp >= 2 && grp < 34) epi_piece<S - 1, (grp - 2) * 2 + t - 1>(c);
    if constexpr (hstore(GS) >= 0) store_h<S - 1, hstore(GS)>(c);
    if constexpr (bload(GS) >= 0) load_b<bload(GS)>(c);
    if constexpr (hload(GS) >= 0) load_h<hload(GS)>(c);
    if constexpr (dma_piece(GS) >= 0) dma_go<S + 3, dma_piece(GS)>(c);
    SCHED();
}
template <int BASE, int... I> __device__ __forceinline__ void run_slots(Ctx& c, std::integer_sequence<int, I...>) { (slot<BASE + I>(c), ...); }
template <int S, int P> __device__ __forceinline__ void dma_one(const Ctx& c) { dma_m0<S, P>(c); asm volatile("s_nop 0"); dma_go<S, P>(c); }
template <int S, int... P> __device__ __forceinline__ void dma_stage(const Ctx& c, std::integer_sequence<int, P...>) { (dma_one<S, P>(c), ...); }
template <int... I> __device__ __forceinline__ void load_b_first(Ctx& c, std::integer_sequence<int, I...>) { (load_b<I>(c), ...); }
}

__device__ __forceinline__ void touch(u32& d, const char* p) { asm volatile("global_load_dword %0, %1, off" : "=v"(d) : "v"(p) : "memory"); }
__global__ __launch_bounds__(256, 1) void k_main(const u16* __restrict__ qin, const u16* __restrict__ kin, const u16* __restrict__ vin, const u16* __restrict__ shift_k,
                                                 const u16* __restrict__ ssm_k, const u16* __restrict__ ssm_D, const u16* __restrict__ wg, const u16* __restrict__ wu,
                                                 const u16* __restrict__ wd, const u16* __restrict__ cvec, const u16* __restrict__ w_mod,
                                                 char* __restrict__ ws, u16* __restrict__ out) {
    using namespace mlp;
    extern __shared__ __attribute__((aligned(16))) char smem[];
    const int tid = threadIdx.x, lane = tid & 63, w = __builtin_amdgcn_readfirstlane(tid >> 6);
    const int b = blockIdx.x, x = b & 7, idx = b >> 3, q = x >> 1;
    const int mt = 4 * (x & 1) + (idx & 3), fcl = idx >> 2, fc = 8 * q + fcl;
    const int mt2 = 8 * (x & 1) + (idx & 7), cc = idx >> 3, tg = w & 1, kh = w >> 1;
    const rsrc_t wsr = __builtin_amdgcn_make_buffer_rsrc(ws, 0, (int)WS_END, 0x00020000);
    STAMP_DECL; STAMP_AT(0);
    const u32 epoch_in = __hip_atomic_load(flag_ptr(ws, F_EPOCH), __ATOMIC_RELAXED, __HIP_MEMORY_SCOPE_AGENT);
    unsigned long long pc_start;
    asm volatile("s_getpc_b64 %0" : "=s"(pc_start));
    u32 epoch;
    {
        const int t0 = (x * 32 + idx) * 8;
        RowsPre rp;
        u32 bg[10] = {0, 0, 0, 0, 0, 0, 0, 0, 0, 0};
        if (b < 192) {
            const int c0 = 72 * x + 3 * idx;
            ConvIn in[3];
            KvIn kin_;
            kv_load(kin_, c0, kin, vin, shift_k);
            conv_load_k(in[0], c0, ssm_k, ssm_D); conv_load_k(in[1], c0 + 1, ssm_k, ssm_D); conv_load_k(in[2], c0 + 2, ssm_k, ssm_D);
            rows_load(rp, t0, qin);
            if (w == 3 && idx < 4) {
                const char* cb = (const char*)(pc_start & ~127ull) + lane * 128;
#pragma unroll
                for (int i = 0; i < 8; ++i) touch(bg[i], cb + (i < 7 || lane < 32 ? i * 8192 : 0));
            }
            kv_put(kin_, smem + C_RED);
            conv_put_k(in[0], smem); conv_put_k(in[1], smem + C_CH); conv_put_k(in[2], smem + 2 * C_CH);
            __syncthreads();
            STAMP_AT(1);
            kv_compute(kin_, c0, smem + C_RED, smem + C_XR, C_CH);
            epoch = __builtin_amdgcn_readfirstlane(epoch_in) + 1u;
            if (w == 3) {
                DRAIN();
#pragma unroll
                for (int i = 0; i < 8; ++i) { const int l = lane + 64 * (i & 3); touch(bg[i], (const char*)(i < 4 ? wg : wu) + (size_t)b * 27648 + (l < 216 ? l : 0) * 128); }
            }
            STAMP_AT(2);
            conv_body(c0, in, smem, wsr STAMP_ARGS);
            STAMP_AT(5);
            poll_eq(flag_ptr(ws, F_MOD + (lane < 54 ? lane : 0)), epoch);
            DRAIN();
#pragma unroll
            for (int i = 0; i < 10; ++i) asm volatile("" :: "v"(bg[i]));
            __syncthreads();
            if (tid < 8) flag_set(flag_ptr(ws, F_CONV + tid * 256 + b), epoch);
            if (w == 3) {
#pragma unroll
                for (int i = 0; i < 4; ++i) { const int l = lane + 64 * i; touch(bg[i], (const char*)wd + (size_t)b * 27648 + (l < 216 ? l : 0) * 128); }
            }
        } else {
            rows_load(rp, t0, qin);
            if (b - 192 < 54) mod_job(b - 192, cvec, w_mod, ws, (float*)(smem + R_MOD), (float*)(smem + R_MOD + 4096));
            epoch = __builtin_amdgcn_readfirstlane(epoch_in) + 1u;
            DRAIN();
            __syncthreads();
            if (tid == 0 && b - 192 < 54) flag_set(flag_ptr(ws, F_MOD + b - 192), epoch);
            STAMP_AT(1);
            if (b - 192 >= 54) { for (int i = 0; i < 40; ++i) __builtin_amdgcn_s_sleep(2); }
            tile_jobs(b - 192, wg, wu, smem, ws, wsr, epoch);
            STAMP_AT(2); STAMP_AT(3); STAMP_AT(4); STAMP_AT(5);
            poll_eq(flag_ptr(ws, F_MOD + (lane < 54 ? lane : 0)), epoch);
        }
        mod_fetch(smem, wsr);
        __syncthreads();
        rows_pre(rp, smem);
        STAMP_AT(6);
        if (w == 0) {
            poll_eq4(wsr, (u32)WS_CNT + (F_CONV + x * 256 + (lane < 48 ? lane : 0) * 4) * 4, epoch);
            rows_fetch(t0, smem, ws);
        }
        __syncthreads();
        STAMP_AT(7);
        rows_main(rp, t0, smem, wsr);
        DRAIN();
#pragma unroll
        for (int i = 0; i < 10; ++i) asm volatile("" :: "v"(bg[i]));
        __syncthreads();
        if (tid == 0) flag_set(flag_ptr(ws, F_ROWS + (t0 >> 6) * 8 + ((t0 >> 3) & 7)), epoch);
        STAMP_AT(8);
    }
    Ctx c;
#pragma unroll
    for (int t = 0; t < 4; ++t) c.hp[t] = (u32x4){0, 0, 0, 0};
    c.lane16 = lane * 16;
#pragma unroll
    for (int t = 0; t < 4; ++t) { c.vo1[t] = lane * 16 + t * 18432; c.vo2[t] = lane * 16 + t * TUS; }
    const LAS char* l0 = (const LAS char*)smem;
#pragma unroll
    for (int i = 0; i < 4; ++i) {
        c.rd[i] = l0 + i * STG + lane * 16; c.rd2[i] = l0 + i * STG + kh * 18432 + (4 * (lane >> 4) + ((lane & 15) >> 2)) * 288 + 8 * (lane & 3);
        asm volatile("" : "+v"(c.rd[i])); asm volatile("" : "+v"(c.rd2[i]));
    }
#pragma unroll
    for (int i = 0; i < 9; ++i) { const int ci = lane + 64 * i, row = (ci * 3641) >> 16, c16 = ci - row * 18; c.voff2[i] = (u32)(row * (D_ * 2) + c16 * 16 - (i & 3) * 1024); }
    c.sub = w & 1;
    c.w1src = ws + WS_WT1 + (size_t)fc * 331776 + w * 9216;
    c.w2src = (const char*)(wd + (size_t)(36 * q + 18 * kh) * 32 * D_ + cc * 144);
    c.bsrc = ws + WS_H2T + (size_t)(16 * mt + 4 * w) * 18432;
    c.hsrc = ws + WS_HT + (size_t)(8 * mt2 + 4 * tg) * TUS + (size_t)((q * 2 + kh) * 18) * 1024;
    c.dmadst = (u32)(uintptr_t)smem + w * 9216;
    c.jj = fcl & 3;
    c.hso = (u32)((16 * mt + 4 * w) * TUS + ((q * 2 + (fcl >> 2)) * 18) * 1024);
    c.hrs = __builtin_amdgcn_make_buffer_rsrc(ws + WS_HT, 0, (int)((size_t)S_ * F_ * 2), 0x00020000);
    c.epoch = epoch;
    c.flag_pub = flag_ptr(ws, F_H + ((((mt * 4 + w) * 4 + q) * 2 + (fcl >> 2))) * 4 + (fcl & 3));
    c.flag_poll = flag_ptr(ws, F_H + (((((mt2 >> 1) * 4 + 2 * (mt2 & 1) + tg) * 4 + q) * 2 + kh)) * 4);
    asm volatile("" ::: "a0", "a1", "a2", "a3", "a4", "a5", "a6", "a7", "a8", "a9", "a10", "a11", "a12", "a13", "a14", "a15", "a16", "a17", "a18", "a19", "a20", "a21", "a22", "a23", "a24", "a25", "a26", "a27", "a28", "a29", "a30", "a31", "a32", "a33", "a34", "a35", "a36", "a37", "a38", "a39", "a40", "a41", "a42", "a43", "a44", "a45", "a46", "a47", "a48", "a49", "a50", "a51", "a52", "a53", "a54", "a55", "a56", "a57", "a58", "a59", "a60", "a61", "a62", "a63", "a64", "a65", "a66", "a67", "a68", "a69", "a70", "a71", "a72", "a73", "a74", "a75", "a76", "a77", "a78", "a79", "a80", "a81", "a82", "a83", "a84", "a85", "a86", "a87", "a88", "a89", "a90", "a91", "a92", "a93", "a94", "a95", "a96", "a97", "a98", "a99", "a100", "a101", "a102", "a103", "a104", "a105", "a106", "a107", "a108", "a109", "a110", "a111", "a112", "a113", "a114", "a115", "a116", "a117", "a118", "a119", "a120", "a121", "a122", "a123", "a124", "a125", "a126", "a127", "a128", "a129", "a130", "a131", "a132", "a133", "a134", "a135", "a136", "a137", "a138", "a139", "a140", "a141", "a142", "a143", "a144", "a145", "a146", "a147", "a148", "a149", "a150", "a151", "a152", "a153", "a154", "a155", "a156", "a157", "a158", "a159", "a160", "a161", "a162", "a163", "a164", "a165", "a166", "a167", "a168", "a169", "a170", "a171", "a172", "a173", "a174", "a175", "a176", "a177", "a178", "a179", "a180", "a181", "a182", "a183", "a184", "a185", "a186", "a187", "a188", "a189", "a190", "a191", "a192", "a193", "a194", "a195", "a196", "a197", "a198", "a199", "a200", "a201", "a202", "a203", "a204", "a205", "a206", "a207", "a208", "a209", "a210", "a211", "a212", "a213", "a214", "a215", "a216", "a217", "a218", "a219", "a220", "a221", "a222", "a223", "a224", "a225", "a226", "a227", "a228", "a229", "a230", "a231", "a232", "a233", "a234", "a235", "a236", "a237", "a238", "a239", "a240", "a241", "a242", "a243", "a244", "a245", "a246", "a247", "a248", "a249", "a250", "a251", "a252", "a253", "a254", "a255");
    poll_eq(flag_ptr(ws, F_TILE + (fc >> 2) * 36 + (lane < 36 ? lane : 0)), epoch);
    dma_stage<0>(c, std::make_integer_sequence<int, 9>{});
    dma_stage<1>(c, std::make_integer_sequence<int, 9>{});
    dma_stage<2>(c, std::make_integer_sequence<int, 9>{});
    poll_eq(flag_ptr(ws, F_ROWS + (4 * mt + w) * 8 + (lane & 7)), epoch);
    STAMP_AT(9);
    load_b_first(c, std::make_integer_sequence<int, 4 * LA>{});
    wait_vm<4 * LA>();
    __builtin_amdgcn_s_barrier();
    asm volatile("" ::: "memory");
#pragma unroll
    for (int f = 0; f < 5; ++f) c.Aq[f] = *(const LAS bf16x8*)(c.rd[0] + f * 1024);
    SCHED();
    STAMP_AT(10);
    run_slots<0>(c, std::make_integer_sequence<int, 144>{});
    STAMP_AT(11);
    run_slots<144>(c, std::make_integer_sequence<int, 144 * 4>{});
    run_slots<144 * 5>(c, std::make_integer_sequence<int, 144 * 4>{});
    STAMP_AT(12);
    run_slots<G1S>(c, std::make_integer_sequence<int, 324>{});
    run_slots<G1S + 324>(c, std::make_integer_sequence<int, 324>{});
    asm volatile("s_nop 7" ::: "memory");
    SCHED();
    STAMP_AT(13);
    const int n = lane & 15, g = lane >> 4;
    u32x2 xr[3][2]; f32x4 gb[3];
#pragma unroll
    for (int i = 0; i < 3; ++i) {
        const int u = (w + 4 * i < 9) ? w + 4 * i : 8, d = 144 * cc + 16 * u + 4 * g;
#pragma unroll
        for (int t = 0; t < 2; ++t) xr[i][t] = __builtin_amdgcn_raw_buffer_load_b64(wsr, (u32)WS_X1 + (u32)((128 * mt2 + 32 * q + 16 * t + n) * D_ + d) * 2, 0, 16);
        gb[i] = *(const f32x4*)(smem + R_MOD + (5 * 576 + d) * 4);
    }
    __builtin_amdgcn_s_barrier();
    {
        char* ex = smem + w * 18432 + lane * 16;
#pragma unroll
        for (int t = 0; t < 2; ++t)
#pragma unroll
            for (int u = 0; u < 9; ++u) *(f32x4*)(ex + (t * 9 + u) * 1024) = kh ? c.a2[u][t] : c.a2[u][2 + t];
    }
    __syncthreads();
    const int j = 2 * tg + kh;
    u32* fred = flag_ptr(ws, F_RED + (mt2 * 4 + cc) * 16);
    const u32 slab = (u32)WS_SLAB + (u32)((mt2 * 4 + cc) * 16) * 9216;
    {
        const char* ey = smem + (w ^ 2) * 18432 + lane * 16;
        char* own = smem + 73728 + lane * 16;
#pragma unroll
        for (int u = 0; u < 9; ++u) {
            const f32x4 r0 = (kh ? c.a2[u][2] : c.a2[u][0]) + *(const f32x4*)(ey + u * 1024);
            const f32x4 r1 = (kh ? c.a2[u][3] : c.a2[u][1]) + *(const f32x4*)(ey + (9 + u) * 1024);
            if (j != q) st16_dev(wsr, slab + (j * 4 + q) * 9216 + u * 1024 + lane * 16, (u32x4){pk2(r0[0], r0[1]), pk2(r0[2], r0[3]), pk2(r1[0], r1[1]), pk2(r1[2], r1[3])});
            else { *(f32x4*)(own + u * 2048) = r0; *(f32x4*)(own + u * 2048 + 1024) = r1; }
        }
    }
    if (j != q) {
        DRAIN();
        if (lane == 0) flag_set(fred + j * 4 + q, epoch);
    }
    STAMP_AT(14);
    poll_eq(fred + q * 4 + (((lane & 3) == q) ? (q ^ 1) : (lane & 3)), epoch);
    __syncthreads();
    u32x4 rem[3][3];
#pragma unroll
    for (int i = 0; i < 3; ++i) {
        const int u = (w + 4 * i < 9) ? w + 4 * i : 8;
#pragma unroll
        for (int k = 0; k < 3; ++k) { const int qq = k + (k >= q ? 1 : 0); rem[i][k] = __builtin_amdgcn_raw_buffer_load_b128(wsr, slab + (q * 4 + qq) * 9216 + u * 1024 + lane * 16, 0, 16); }
    }
#pragma unroll
    for (int i = 0; i < 3; ++i) {
        const int u = w + 4 * i;
        if (u < 9) {
            const int d = 144 * cc + 16 * u + 4 * g;
#pragma unroll
            for (int t = 0; t < 2; ++t) {
                f32x4 s = *(const f32x4*)(smem + 73728 + u * 2048 + t * 1024 + lane * 16);
#pragma unroll
                for (int k = 0; k < 3; ++k) {
                    const u32 a = rem[i][k][2 * t], bq = rem[i][k][2 * t + 1];
                    s += (f32x4){bf_lo(a), bf_hi(a), bf_lo(bq), bf_hi(bq)};
                }
                const float o0 = bf_lo(xr[i][t][0]) + rbf(gb[i][0] * rbf(s[0])), o1 = bf_hi(xr[i][t][0]) + rbf(gb[i][1] * rbf(s[1]));
                const float o2 = bf_lo(xr[i][t][1]) + rbf(gb[i][2] * rbf(s[2])), o3 = bf_hi(xr[i][t][1]) + rbf(gb[i][3] * rbf(s[3]));
                *(u32x2*)(out + (size_t)(128 * mt2 + 32 * q + 16 * t + n) * D_ + d) = (u32x2){pk2(o0, o1), pk2(o2, o3)};
            }
        }
    }
    if (b == 0 && tid == 0) flag_set(flag_ptr(ws, F_EPOCH), epoch);
    STAMP_AT(15);
    STAMP_OUT(4, 16);
}

extern "C" void kernel_launch(void* const* d_in, const int* in_sizes, int n_in, void* d_out, int out_size, void* d_ws, size_t ws_size,
                              hipStream_t stream) {
    const u16* q = (const u16*)d_in[0];
    const u16* k = (const u16*)d_in[1];
    const u16* v = (const u16*)d_in[2];
    const u16* shift_k = (const u16*)d_in[3];
    const u16* ssm_k = (const u16*)d_in[4];
    const u16* ssm_D = (const u16*)d_in[5];
    const u16* wg = (const u16*)d_in[7];
    const u16* wu = (const u16*)d_in[8];
    const u16* wd = (const u16*)d_in[9];
    const u16* c = (const u16*)d_in[10];
    const u16* w_mod = (const u16*)d_in[11];
    char* ws = (char*)d_ws;
    (void)hipFuncSetAttribute((const void*)k_main, hipFuncAttributeMaxDynamicSharedMemorySize, mlp::LDS_BYTES);
    k_main<<<256, 256, mlp::LDS_BYTES, stream>>>(q, k, v, shift_k, ssm_k, ssm_D, wg, wu, wd, c, w_mod, ws, (u16*)d_out);
}
```

```cpp
#include <hip/hip_runtime.h>
#include <stdint.h>
#include <utility>

typedef unsigned short u16;
typedef unsigned int u32;
typedef __bf16 bf16x8 __attribute__((ext_vector_type(8)));
typedef __bf16 bf16x2 __attribute__((ext_vector_type(2)));
typedef float f32x4 __attribute__((ext_vector_type(4)));
typedef float f32x2 __attribute__((ext_vector_type(2)));
typedef u32 u32x4 __attribute__((ext_vector_type(4)));
typedef u32 u32x2 __attribute__((ext_vector_type(2)));
typedef short s16x4 __attribute__((ext_vector_type(4)));
#define LAS __attribute__((address_space(3)))
typedef LAS char* lptr;

constexpr int S_ = 2048, D_ = 576, F_ = 4608;

constexpr size_t WS_MOD = 0;
constexpr size_t WS_CNT = 65536;
constexpr size_t WS_YYT = WS_CNT + 65536;
constexpr size_t WS_X1  = WS_YYT + (size_t)D_ * S_ * 2;
constexpr size_t WS_H2T = WS_X1 + (size_t)D_ * S_ * 2;
constexpr size_t WS_HT  = WS_H2T + (size_t)D_ * S_ * 2;
constexpr size_t WS_WT1 = WS_HT + (size_t)S_ * F_ * 2;
constexpr size_t WS_SLAB = WS_WT1 + (size_t)2 * D_ * F_ * 2;
constexpr size_t WS_END = WS_SLAB + (size_t)16 * 4 * 4 * 4 * 9216;

#define STAMP_DECL
#define STAMP_ARGS
#define STAMP_AT(i)
#define STAMP_OUT(kid, n)
__device__ __forceinline__ float bf_lo(u32 w) { return __uint_as_float(w << 16); }
__device__ __forceinline__ float bf_hi(u32 w) { return __uint_as_float(w & 0xffff0000u); }
__device__ __forceinline__ float bf1(u16 w) { return __uint_as_float((u32)w << 16); }
__device__ __forceinline__ u32 pk2(float lo, float hi) {
    f32x2 v = {lo, hi};
    return __builtin_bit_cast(u32, __builtin_convertvector(v, bf16x2));
}
__device__ __forceinline__ float rbf(float x) { return (float)(__bf16)x; }
__device__ __forceinline__ float sigmoidf_(float x) { return __builtin_amdgcn_rcpf(1.0f + __builtin_amdgcn_exp2f(-1.4426950408889634f * x)); }

constexpr int F_MOD = 256;
constexpr int F_TILE = 512;
constexpr int F_CONV = 1024;
constexpr int F_ROWS = 3072;
constexpr int F_H = 3328;
constexpr int F_RED = 5376;
constexpr int F_EPOCH = 6400;
typedef __amdgpu_buffer_rsrc_t rsrc_t;
__device__ __forceinline__ u32* flag_ptr(char* ws, int idx) { return (u32*)(ws + WS_CNT) + idx; }
__device__ __forceinline__ void flag_set(u32* p, u32 e) { __hip_atomic_store(p, e, __ATOMIC_RELAXED, __HIP_MEMORY_SCOPE_AGENT); }
__device__ __forceinline__ void st16_dev(rsrc_t rs, u32 off, u32x4 v) { __builtin_amdgcn_raw_buffer_store_b128(v, rs, off, 0, 16); }
__device__ __forceinline__ u32x4 ld16_dev(rsrc_t rs, u32 off) { return __builtin_amdgcn_raw_buffer_load_b128(rs, off, 0, 16); }
__device__ __forceinline__ void poll_eq(const u32* p, u32 e) {
    for (int it = 0; it < (1 << 21); ++it) {
        if (__all(__hip_atomic_load(p, __ATOMIC_RELAXED, __HIP_MEMORY_SCOPE_AGENT) == e)) break;
        __builtin_amdgcn_s_sleep(1);
    }
}
__device__ __forceinline__ void poll_eq4(rsrc_t rs, u32 off, u32 e) {
    for (int it = 0; it < (1 << 21); ++it) {
        asm volatile("" ::: "memory");
        const u32x4 v = ld16_dev(rs, off);
        if (__all(v[0] == e && v[1] == e && v[2] == e && v[3] == e)) break;
        __builtin_amdgcn_s_sleep(1);
    }
}
#define DRAIN() asm volatile("s_waitcnt vmcnt(0)" ::: "memory")

#define SCHED() __builtin_amdgcn_sched_barrier(0)
__device__ __forceinline__ float dpp_add(float v, const int ctrl_sel) {
    int r;
    const int iv = __builtin_bit_cast(int, v);
    switch (ctrl_sel) {
        case 0: r = __builtin_amdgcn_update_dpp(0, iv, 0xB1, 0xf, 0xf, true); break;
        case 1: r = __builtin_amdgcn_update_dpp(0, iv, 0x4E, 0xf, 0xf, true); break;
        case 2: r = __builtin_amdgcn_update_dpp(0, iv, 0x141, 0xf, 0xf, true); break;
        default: r = __builtin_amdgcn_update_dpp(0, iv, 0x140, 0xf, 0xf, true); break;
    }
    return v + __builtin_bit_cast(float, r);
}
__device__ __forceinline__ float half_sum(float v) {
    v = dpp_add(v, 0); v = dpp_add(v, 1); v = dpp_add(v, 2); v = dpp_add(v, 3);
    auto r = __builtin_amdgcn_permlane16_swap(__float_as_uint(v), __float_as_uint(v), false, false);
    return __uint_as_float(r[0]) + __uint_as_float(r[1]);
}

struct KvIn { u32x2 kw[11], vw[8]; u32x4 sw; u32x2 sw2; };
__device__ __forceinline__ void kv_load(KvIn& p, int c0, const u16* __restrict__ k, const u16* __restrict__ v, const u16* __restrict__ shift_k) {
    const int t0 = threadIdx.x * 8, cw = c0 & ~1;
#pragma unroll
    for (int i = 0; i < 11; ++i) { const int t = t0 - 3 + i; p.kw[i] = *(const u32x2*)(k + (size_t)(t < 0 ? 0 : t) * D_ + cw); }
#pragma unroll
    for (int i = 0; i < 8; ++i) p.vw[i] = *(const u32x2*)(v + (size_t)(t0 + i) * D_ + cw);
    p.sw = (u32x4){*(const u32*)(shift_k + c0 * 4), *(const u32*)(shift_k + c0 * 4 + 2), *(const u32*)(shift_k + c0 * 4 + 4), *(const u32*)(shift_k + c0 * 4 + 6)};
    p.sw2 = (u32x2){*(const u32*)(shift_k + c0 * 4 + 8), *(const u32*)(shift_k + c0 * 4 + 10)};
}
__device__ __forceinline__ void kv_compute(KvIn& p, int c0, char* xr, int stride) {
    const int tid = threadIdx.x;
    if (tid == 0) { p.kw[0] = (u32x2){0, 0}; p.kw[1] = (u32x2){0, 0}; p.kw[2] = (u32x2){0, 0}; }
    const bool odd = c0 & 1;
#pragma unroll
    for (int c = 0; c < 3; ++c) {
        const u32 wa = c == 0 ? p.sw[0] : c == 1 ? p.sw[2] : p.sw2[0], wb = c == 0 ? p.sw[1] : c == 1 ? p.sw[3] : p.sw2[1];
        const float w0 = bf_lo(wa), w1 = bf_hi(wa), w2 = bf_lo(wb), w3 = bf_hi(wb);
        float kf[11], r[8];
#define KV_PICK(d) (c == 0 ? (odd ? bf_hi((d)[0]) : bf_lo((d)[0])) : c == 1 ? (odd ? bf_lo((d)[1]) : bf_hi((d)[0])) : (odd ? bf_hi((d)[1]) : bf_lo((d)[1])))
#pragma unroll
        for (int i = 0; i < 11; ++i) kf[i] = KV_PICK(p.kw[i]);
#pragma unroll
        for (int i = 0; i < 8; ++i) r[i] = (kf[i] * w3 + kf[i + 1] * w2 + kf[i + 2] * w1 + kf[i + 3] * w0) * KV_PICK(p.vw[i]);
#undef KV_PICK
        *(u32x4*)(xr + c * stride + 2 * (2040 - 8 * tid)) = (u32x4){pk2(r[7], r[6]), pk2(r[5], r[4]), pk2(r[3], r[2]), pk2(r[1], r[0])};
    }
}

__device__ __forceinline__ void mod_job(int job, const u16* __restrict__ c, const u16* __restrict__ w_mod, char* __restrict__ ws, float* sc  , float* red  ) {
    const int tid = threadIdx.x, lane = tid & 63, wid = tid >> 6;
    const int col0 = job * 64, rg = tid >> 3, c8 = tid & 7;
    u32x4 w[18];
#pragma unroll
    for (int i = 0; i < 18; ++i) w[i] = *(const u32x4*)(w_mod + (size_t)(rg + 32 * i) * 3456 + col0 + c8 * 8);
#pragma unroll
    for (int j = 0; j < 3; ++j) { const int r = tid + 256 * j; if (r < 576) { const float x = bf1(c[r]); sc[r] = rbf(x * sigmoidf_(x)); } }
    __syncthreads();
    float acc[8] = {0, 0, 0, 0, 0, 0, 0, 0};
#pragma unroll
    for (int i = 0; i < 18; ++i) {
        const float s = sc[rg + 32 * i];
#pragma unroll
        for (int j = 0; j < 4; ++j) { acc[2 * j] += s * bf_lo(w[i][j]); acc[2 * j + 1] += s * bf_hi(w[i][j]); }
    }
#pragma unroll
    for (int j = 0; j < 8; ++j) {
        acc[j] += __shfl_xor(acc[j], 8); acc[j] += __shfl_xor(acc[j], 16); acc[j] += __shfl_xor(acc[j], 32);
    }
    if (lane < 8) {
#pragma unroll
        for (int j = 0; j < 8; ++j) red[wid * 64 + lane * 8 + j] = acc[j];
    }
    __syncthreads();
    if (tid < 64) {
        float m = rbf((red[tid] + red[64 + tid]) + (red[128 + tid] + red[192 + tid]));
        const int which = col0 / 576;
        if (which == 1 || which == 4) m = rbf(1.0f + m);
        __hip_atomic_store((u32*)(ws + WS_MOD) + col0 + tid, __float_as_uint(m), __ATOMIC_RELAXED, __HIP_MEMORY_SCOPE_AGENT);
    }
}

__device__ __forceinline__ const char* tile_base(int job, const u16* __restrict__ wg, const u16* __restrict__ wu) {
    const int ks = job % 18, mat = (job / 18) & 1, cg = job / 36;
    return (const char*)((mat ? wu : wg) + (size_t)(32 * ks) * F_ + cg * 576);
}
__device__ __forceinline__ void tile_dma(int job, const u16* __restrict__ wg, const u16* __restrict__ wu, char* tile, const u32 (&voff)[9]) {
    const int w = __builtin_amdgcn_readfirstlane(threadIdx.x >> 6);
    const char* base = tile_base(job, wg, wu) + (size_t)w * 8 * (F_ * 2);
    const u32 dst = (u32)(uintptr_t)tile + w * 9216;
#pragma unroll
    for (int i = 0; i < 9; ++i)
        asm volatile("s_mov_b32 m0, %2\n\ts_nop 0\n\tglobal_load_lds_dwordx4 %0, %1" :: "v"(voff[i]), "s"(base), "s"(dst + i * 1024) : "memory");
}
__device__ __forceinline__ void tile_out(int job, const char* tile, rsrc_t wsr) {
    const int lane = threadIdx.x & 63, w = threadIdx.x >> 6;
    const int ks = job % 18, mat = (job / 18) & 1, fc = 4 * (job / 36) + w;
    const int g = lane >> 4, q4 = (lane & 15) >> 2, p = lane & 3;
    const LAS char* rd = (const LAS char*)tile + (4 * g + q4) * 1152 + 8 * p + w * 288;
#pragma unroll
    for (int u = 0; u < 9; ++u) {
        const int pos = (fc & 1) ? (u == 0 ? 4 : u <= 4 ? u - 1 : u) : (u == 8 ? 4 : u < 4 ? u : u + 1);
        const s16x4 lo = __builtin_amdgcn_ds_read_tr16_b64_v4i16((LAS s16x4*)(rd + u * 32));
        const s16x4 hi = __builtin_amdgcn_ds_read_tr16_b64_v4i16((LAS s16x4*)(rd + u * 32 + 16 * 1152));
        typedef short s16x8 __attribute__((ext_vector_type(8)));
        const s16x8 r = __builtin_shufflevector(lo, hi, 0, 1, 2, 3, 4, 5, 6, 7);
        st16_dev(wsr, (u32)WS_WT1 + fc * 331776 + pos * 36864 + (ks * 2 + mat) * 1024 + lane * 16, __builtin_bit_cast(u32x4, r));
    }
}
__device__ __forceinline__ void tile_jobs(int i0, const u16* __restrict__ wg, const u16* __restrict__ wu, char* smem, char* ws, rsrc_t wsr, u32 epoch) {
    const int lane = threadIdx.x & 63;
    u32 voff[9];
#pragma unroll
    for (int i = 0; i < 9; ++i) { const int ci = lane + 64 * i, row = (ci * 58255) >> 22, c16 = ci - row * 72; voff[i] = (u32)(row * (F_ * 2) + c16 * 16); }
#pragma unroll
    for (int j = 0; j < 4; ++j) tile_dma(i0 + 64 * j, wg, wu, smem + j * 36864, voff);
#pragma unroll
    for (int j = 0; j < 4; ++j) {
        asm volatile("s_waitcnt vmcnt(27)" ::: "memory");
        __syncthreads();
        tile_out(i0 + 64 * j, smem + j * 36864, wsr);
    }
    if (i0 < 32) {
        __syncthreads();
        tile_dma(i0 + 256, wg, wu, smem, voff);
        asm volatile("s_waitcnt vmcnt(0)" ::: "memory");
        __syncthreads();
        tile_out(i0 + 256, smem, wsr);
    }
    DRAIN();
    __syncthreads();
    if (threadIdx.x < 5 && i0 + 64 * threadIdx.x < 288) flag_set(flag_ptr(ws, F_TILE + i0 + 64 * threadIdx.x), epoch);
}

constexpr int C_XR = 0;
constexpr int C_K0 = 4608;
constexpr int C_K1 = C_K0 + 4128 + 32;
constexpr int C_CH = C_K1 + 4160;
constexpr int C_RED = 3 * C_CH;
constexpr int C_LDS = C_RED + 3 * 32768;
static_assert(((C_K1 - C_K0) / 4) % 32 == 16, "bank offset");
static_assert(C_CH % 16 == 0 && C_K0 % 16 == 0 && C_K1 % 16 == 0, "align");
template <bool ZERO> __device__ __forceinline__ void mfma_v(f32x4& c, const bf16x8& a, const bf16x8& b) {
    if constexpr (ZERO) asm volatile("v_mfma_f32_16x16x32_bf16 %0, %1, %2, 0" : "=v"(c) : "v"(a), "v"(b));
    else asm volatile("v_mfma_f32_16x16x32_bf16 %0, %1, %2, %0" : "+v"(c) : "v"(a), "v"(b));
}
struct ConvSet { bf16x8 A[8], B[8]; };
template <int J> __device__ __forceinline__ void conv_ld(ConvSet& s, const LAS char* ab, const LAS char* bb) {
    if constexpr (J < 8) {
        const LAS u32* p = (const LAS u32*)(ab + 512 * J);
        u32x4 t = {p[0], p[1], p[2], p[3]};
        s.A[J] = __builtin_bit_cast(bf16x8, t);
    } else s.B[J - 8] = *(const LAS bf16x8*)(bb + 512 * (15 - J));
}
template <int M> struct TriIdx {
    static constexpr int find(bool want_i) { int m = 0; for (int s = 0; s < 8; ++s) for (int j = 0; j <= s; ++j) { if (m == M) return want_i ? s : j; ++m; } return 0; }
    static constexpr int i = find(true), j = find(false);
};
template <int M, bool PF, bool FIRST> __device__ __forceinline__ void conv_slot(f32x4 (&acc)[8], const ConvSet& cur, ConvSet& nxt, const LAS char* ab, const LAS char* bb) {
    mfma_v<(FIRST && TriIdx<M>::j == 0)>(acc[TriIdx<M>::i], cur.A[TriIdx<M>::j], cur.B[TriIdx<M>::i - TriIdx<M>::j]);
    if constexpr (PF && (M & 1) == 0 && M / 2 < 16) conv_ld<((M / 2) & 1) * 8 + (M / 4)>(nxt, ab, bb);
    SCHED();
}
template <bool PF, bool FIRST, int... M> __device__ __forceinline__ void conv_step(f32x4 (&acc)[8], const ConvSet& cur, ConvSet& nxt, const LAS char* ab, const LAS char* bb, std::integer_sequence<int, M...>) {
    (conv_slot<M, PF, FIRST>(acc, cur, nxt, ab, bb), ...);
}
template <int... J> __device__ __forceinline__ void conv_ld_all(ConvSet& s, const LAS char* ab, const LAS char* bb, std::integer_sequence<int, J...>) { (conv_ld<J>(s, ab, bb), ...); }

struct ConvIn { u32x4 k; u32 kn; u16 dv; };
__device__ __forceinline__ void conv_load_k(ConvIn& in, int d, const u16* __restrict__ ssm_k, const u16* __restrict__ ssm_D) {
    const int tid = threadIdx.x;
    in.k = *(const u32x4*)(ssm_k + (size_t)d * S_ + tid * 8);
    in.kn = *(const u32*)(ssm_k + (size_t)d * S_ + (tid < 255 ? tid * 8 + 8 : 0));
    in.dv = ssm_D[d];
}
__device__ __forceinline__ void conv_put_k(const ConvIn& in, char* sm) {
    const int tid = threadIdx.x;
    const u32 kn = tid < 255 ? in.kn : 0u;
    *(u32x4*)(sm + C_K0 + 32 + tid * 16) = in.k;
    *(u32x4*)(sm + C_K1 + 32 + tid * 16) = (u32x4){__builtin_amdgcn_alignbit(in.k[1], in.k[0], 16), __builtin_amdgcn_alignbit(in.k[2], in.k[1], 16),
                                                 __builtin_amdgcn_alignbit(in.k[3], in.k[2], 16), __builtin_amdgcn_alignbit(kn, in.k[3], 16)};
    if (tid < 32) *(u32x4*)(sm + C_XR + 4096 + tid * 16) = (u32x4){0, 0, 0, 0};
    if (tid == 0) {
        *(u32x4*)(sm + C_K0) = (u32x4){0, 0, 0, 0}; *(u32x4*)(sm + C_K0 + 16) = (u32x4){0, 0, 0, 0};
        *(u32x4*)(sm + C_K1) = (u32x4){0, 0, 0, 0}; *(u32x4*)(sm + C_K1 + 16) = (u32x4){0, 0, 0, in.k[0] << 16};
    }
}
__device__ __forceinline__ void conv_finish(int d, const char* sm, const char* red, float Dv, rsrc_t wsr) {
    const int tid = threadIdx.x;
    const int i = tid >> 5, n = tid & 15, gp = (tid >> 4) & 1;
    f32x4 lo = {0.f, 0.f, 0.f, 0.f}, hi = {0.f, 0.f, 0.f, 0.f};
#pragma unroll
    for (int w = 0; w < 4; ++w) {
        lo += *(const f32x4*)(red + (w * 8 + i) * 1024 + (n + 32 * gp) * 16);
        hi += *(const f32x4*)(red + (w * 8 + i) * 1024 + (n + 32 * gp + 16) * 16);
    }
    const int s = 256 * i + 16 * n + 8 * gp;
    const u32x4 xw = *(const u32x4*)(sm + C_XR + 2 * (2040 - s));
    const float o0 = rbf(lo[0]) + rbf(bf_hi(xw[3]) * Dv), o1 = rbf(lo[1]) + rbf(bf_lo(xw[3]) * Dv), o2 = rbf(lo[2]) + rbf(bf_hi(xw[2]) * Dv), o3 = rbf(lo[3]) + rbf(bf_lo(xw[2]) * Dv);
    const float o4 = rbf(hi[0]) + rbf(bf_hi(xw[1]) * Dv), o5 = rbf(hi[1]) + rbf(bf_lo(xw[1]) * Dv), o6 = rbf(hi[2]) + rbf(bf_hi(xw[0]) * Dv), o7 = rbf(hi[3]) + rbf(bf_lo(xw[0]) * Dv);
    st16_dev(wsr, (u32)WS_YYT + (u32)(d * S_ + s) * 2, (u32x4){pk2(o0, o1), pk2(o2, o3), pk2(o4, o5), pk2(o6, o7)});
}
__device__ __forceinline__ void conv_body(int ch0, const ConvIn (&in)[3], char* smem, rsrc_t wsr
                                          ) {
    const int lane = threadIdx.x & 63, wid = __builtin_amdgcn_readfirstlane(threadIdx.x >> 6);
    __syncthreads();
    STAMP_AT(3);
    const int n = lane & 15, g = lane >> 4, dd = g >> 1, gh = g & 1;
    const LAS char* l0 = (const LAS char*)smem;
    const LAS char* bb = l0 + C_XR + 480 - 32 * n + 32 * dd + 16 * gh + 128 * wid;
    const LAS char* ab = l0 + ((n & 1) ? C_K0 + 2 * (16 * dd + 8 * gh + n + 1) : C_K1 + 2 * (16 * dd + 8 * gh + n)) + 128 * wid;
    f32x4 acc[3][8];
    ConvSet s0, s1;
    conv_ld_all(s0, ab, bb, std::make_integer_sequence<int, 16>{});
    SCHED();
    constexpr auto SQ = std::make_integer_sequence<int, 36>{};
    conv_step<true, true>(acc[0], s0, s1, ab + 64, bb + 64, SQ);
    conv_step<true, false>(acc[0], s1, s0, ab + C_CH, bb + C_CH, SQ);
    conv_step<true, true>(acc[1], s0, s1, ab + C_CH + 64, bb + C_CH + 64, SQ);
    conv_step<true, false>(acc[1], s1, s0, ab + 2 * C_CH, bb + 2 * C_CH, SQ);
    conv_step<true, true>(acc[2], s0, s1, ab + 2 * C_CH + 64, bb + 2 * C_CH + 64, SQ);
    conv_step<false, false>(acc[2], s1, s0, ab, bb, SQ);
    asm volatile("s_nop 7" ::: "memory");
    STAMP_AT(4);
#pragma unroll
    for (int c = 0; c < 3; ++c)
#pragma unroll
        for (int i = 0; i < 8; ++i) *(f32x4*)(smem + C_RED + c * 32768 + (wid * 8 + i) * 1024 + lane * 16) = acc[c][i];
    __syncthreads();
#pragma unroll
    for (int c = 0; c < 3; ++c) conv_finish(ch0 + c, smem + c * C_CH, smem + C_RED + c * 32768, bf1(in[c].dv), wsr);
}

constexpr int R_MOD = 147456;
constexpr int R_YT = 110592;
constexpr int R_HT = R_YT + 8 * 1168;
struct RowsPre { float x[18], gh[18]; u32 xw[9]; };
__device__ __forceinline__ void rows_load(RowsPre& r, int t0, const u16* __restrict__ q) {
    const int tid = threadIdx.x, lane = tid & 63, hl = lane & 31, row = 2 * (tid >> 6) + (lane >> 5);
    const u32* qrow = (const u32*)(q + (size_t)(t0 + row) * D_);
#pragma unroll
    for (int i = 0; i < 9; ++i) r.xw[i] = qrow[hl + 32 * i];
}
__device__ __forceinline__ void mod_fetch(char* sm, rsrc_t wsr) {
    u32x4 mw[4];
#pragma unroll
    for (int i = 0; i < 4; ++i) { const int j = threadIdx.x + 256 * i; mw[i] = ld16_dev(wsr, (u32)WS_MOD + (j < 864 ? j : 0) * 16); }
#pragma unroll
    for (int i = 0; i < 4; ++i) { const int j = threadIdx.x + 256 * i; if (j < 864) *(u32x4*)(sm + R_MOD + j * 16) = mw[i]; }
}
__device__ __forceinline__ void rows_pre(RowsPre& r, const char* sm) {
    const int hl = threadIdx.x & 31;
#pragma unroll
    for (int i = 0; i < 9; ++i) { r.x[2 * i] = bf_lo(r.xw[i]); r.x[2 * i + 1] = bf_hi(r.xw[i]); }
    float s = 0.f;
#pragma unroll
    for (int i = 0; i < 18; ++i) s += r.x[i];
    const float mean = half_sum(s) * (1.0f / 576.0f);
    float vs = 0.f;
#pragma unroll
    for (int i = 0; i < 18; ++i) { const float dl = r.x[i] - mean; vs += dl * dl; }
    const float rstd = __builtin_amdgcn_rsqf(half_sum(vs) * (1.0f / 576.0f) + 1e-5f);
    const float* mod = (const float*)(sm + R_MOD);
#pragma unroll
    for (int i = 0; i < 9; ++i) {
        const int c = 2 * (hl + 32 * i);
        const f32x2 sa = *(const f32x2*)(mod + c), c1 = *(const f32x2*)(mod + 576 + c), ga = *(const f32x2*)(mod + 2 * 576 + c);
        r.gh[2 * i] = ga[0] * ((r.x[2 * i] - mean) * rstd * c1[0] + sa[0]);
        r.gh[2 * i + 1] = ga[1] * ((r.x[2 * i + 1] - mean) * rstd * c1[1] + sa[1]);
    }
}
__device__ __forceinline__ void rows_fetch(int t0, char* sm, const char* ws) {
    const int lane = threadIdx.x & 63;
    const char* src = ws + WS_YYT + t0 * 2;
    const u32 dst = (u32)(uintptr_t)(sm + R_HT);
#pragma unroll
    for (int i = 0; i < 9; ++i)
        asm volatile("s_mov_b32 m0, %2\n\ts_nop 0\n\tglobal_load_lds_dwordx4 %0, %1 sc1" :: "v"((u32)((lane + 64 * i) * (S_ * 2))), "s"(src), "s"(dst + i * 1024) : "memory");
    asm volatile("s_waitcnt vmcnt(0)" ::: "memory");
}
__device__ __forceinline__ void rows_main(const RowsPre& r, int t0, char* sm, rsrc_t wsr) {
    const int tid = threadIdx.x, lane = tid & 63, hl = lane & 31, row = 2 * (tid >> 6) + (lane >> 5);
    const int cg = tid >> 1, th = tid & 1;
    if (tid < 144) {
        u32x2 yw[8];
#pragma unroll
        for (int c = 0; c < 8; ++c) yw[c] = *(const u32x2*)(sm + R_HT + (8 * cg + c) * 16 + th * 8);
#pragma unroll
        for (int t = 0; t < 4; ++t) {
            u32x4 o;
#pragma unroll
            for (int kk = 0; kk < 4; ++kk) {
                const u32 a = yw[2 * kk][t >> 1], bq = yw[2 * kk + 1][t >> 1];
                o[kk] = (t & 1) ? __builtin_amdgcn_perm(bq, a, 0x07060302u) : __builtin_amdgcn_perm(bq, a, 0x05040100u);
            }
            *(u32x4*)(sm + R_YT + (4 * th + t) * 1168 + cg * 16) = o;
        }
    }
    __syncthreads();
    const float* mod = (const float*)(sm + R_MOD);
    float x1[18];
#pragma unroll
    for (int i = 0; i < 9; ++i) {
        u32* yp = (u32*)(sm + R_YT + row * 1168 + (hl + 32 * i) * 4);
        const u32 yv = *yp;
        const u32 p = pk2(r.x[2 * i] + r.gh[2 * i] * bf_lo(yv), r.x[2 * i + 1] + r.gh[2 * i + 1] * bf_hi(yv));
        *yp = p;
        x1[2 * i] = bf_lo(p); x1[2 * i + 1] = bf_hi(p);
    }
    float s = 0.f;
#pragma unroll
    for (int i = 0; i < 18; ++i) s += x1[i];
    const float mean = half_sum(s) * (1.0f / 576.0f);
    float vs = 0.f;
#pragma unroll
    for (int i = 0; i < 18; ++i) { const float dl = x1[i] - mean; vs += dl * dl; }
    const float rstd = __builtin_amdgcn_rsqf(half_sum(vs) * (1.0f / 576.0f) + 1e-5f);
#pragma unroll
    for (int i = 0; i < 9; ++i) {
        const int c = 2 * (hl + 32 * i);
        const f32x2 sb = *(const f32x2*)(mod + 3 * 576 + c), c1 = *(const f32x2*)(mod + 4 * 576 + c);
        const float h0 = (x1[2 * i] - mean) * rstd * c1[0] + sb[0], h1 = (x1[2 * i + 1] - mean) * rstd * c1[1] + sb[1];
        const int ks = c >> 5, kk = c & 31, gq = (kk & 15) >> 2, j = ((kk >> 4) << 2) | (kk & 3);
        *(u32*)(sm + R_HT + ((ks * 4 + gq) * 8 + row) * 16 + j * 2) = pk2(h0, h1);
    }
    __syncthreads();
    const u32 h2t = (u32)WS_H2T + (u32)(t0 >> 4) * 18 * 1024 + (t0 & 15) * 16, x1g = (u32)WS_X1 + (u32)t0 * D_ * 2;
#pragma unroll
    for (int it = 0; it < 3; ++it) {
        const int i = tid + 256 * it;
        if (i < 576) {
            const int kg = i >> 3, rr8 = i & 7;
            st16_dev(wsr, h2t + (kg >> 2) * 1024 + (kg & 3) * 256 + rr8 * 16, *(const u32x4*)(sm + R_HT + i * 16));
            const int rr = i / 72, cc = i - rr * 72;
            st16_dev(wsr, x1g + rr * 1152 + cc * 16, *(const u32x4*)(sm + R_YT + rr * 1168 + cc * 16));
        }
    }
}

namespace mlp {
constexpr int STG = 36864, LDS_BYTES = 163840;
constexpr int G1S = 9 * 144, G2S = 18 * 36, NS = G1S + G2S;
constexpr int LA = 4;
constexpr int PRO = 9 + 4 * LA + 18;
constexpr int TUS = 147456;

constexpr int stage_of(int gs) { return gs < G1S ? gs / 144 : 9 + (gs - G1S) / 72; }
constexpr int rel_of(int gs) { return gs < G1S ? gs % 144 : (gs - G1S) % 72; }
constexpr int grp_of(int gs) { return rel_of(gs) >> 2; }
constexpr int t_of(int gs) { return rel_of(gs) & 3; }
constexpr bool barpt(int gs) { return gs >= 0 && gs < NS && t_of(gs) == 3 && grp_of(gs) == (gs < G1S ? 3 : 0) && stage_of(gs) < 17; }
constexpr int dma_piece(int gs) {
    if (gs >= NS || t_of(gs) != 3 || stage_of(gs) + 3 > 17) return -1;
    const int g = grp_of(gs);
    if (gs < G1S) { constexpr int G[9] = {5, 7, 9, 12, 14, 16, 20, 22, 24}; for (int i = 0; i < 9; ++i) if (G[i] == g) return i; return -1; }
    return (g & 1) ? g >> 1 : -1;
}
constexpr int bload(int gs) {
    if (gs >= 144 || t_of(gs) == 0 || t_of(gs) == 3) return -1;
    const int ks = grp_of(gs) >> 1, i = (grp_of(gs) & 1) * 2 + t_of(gs) - 1;
    return (ks + LA < 18) ? (ks + LA) * 4 + i : -1;
}
constexpr bool unit_stores(int v) { return v == 1 || v == 3 || v == 4 || v == 6 || v == 8; }
constexpr int hstore(int gs) {
    if (gs >= G1S || gs < 144 || !unit_stores(gs / 144 - 1) || t_of(gs) != 3) return -1;
    const int g = grp_of(gs);
    return (g == 10 || g == 18 || g == 26 || g == 34) ? (g - 10) / 8 : -1;
}
constexpr int hload(int gs) {
    if (t_of(gs) != 3) return -1;
    const int s = stage_of(gs), g = grp_of(gs);
    if (s == 8) { constexpr int G[20] = {4, 6, 8, 10, 11, 13, 15, 17, 18, 19, 21, 23, 25, 26, 27, 28, 29, 30, 31, 32}; for (int i = 0; i < 20; ++i) if (G[i] == g) return i; return -1; }
    if (s < 9 || g < 2 || g > 16 || (g & 1)) return -1;
    const int i = (g - 2) >> 1;
    if (s == 9) return 20 + i;
    if (s == 10) return 28 + i;
    if (s == 11) return i < 4 ? 36 + i : -1;
    if (s >= 12 && s <= 15) return 40 + 8 * (s - 12) + i;
    return -1;
}
constexpr int pub(int gs) { return (stage_of(gs) == 6 && grp_of(gs) == 27 && t_of(gs) == 3) ? 1 : (stage_of(gs) == 10 && grp_of(gs) == 9 && t_of(gs) == 1) ? 2 : 0; }
constexpr int poll_issue(int gs) { return (stage_of(gs) == 7 && grp_of(gs) == 27 && t_of(gs) == 3) ? 1 : (stage_of(gs) == 11 && grp_of(gs) == 9 && t_of(gs) == 1) ? 2 : 0; }
constexpr int vm_ops(int gs) { return (pub(gs) ? 1 : 0) + (poll_issue(gs) ? 1 : 0) + (hstore(gs) >= 0) + (bload(gs) >= 0) + (hload(gs) >= 0) + (dma_piece(gs) >= 0); }
struct Ledger { int cum[NS + 1]; };
constexpr Ledger make_ledger() {
    Ledger l{}; int n = PRO;
    for (int gs = 0; gs < NS; ++gs) { if (gs == G1S) n += 4; l.cum[gs] = n; n += vm_ops(gs); }
    l.cum[NS] = n; return l;
}
constexpr Ledger LG = make_ledger();
constexpr int seq_in(int gs, int kind) {
    int n = LG.cum[gs];
    if (kind > 0 && pub(gs)) ++n;
    if (kind > 1 && poll_issue(gs)) ++n;
    if (kind > 2 && hstore(gs) >= 0) ++n;
    if (kind > 3 && bload(gs) >= 0) ++n;
    if (kind > 4 && hload(gs) >= 0) ++n;
    return n;
}
constexpr int seq_dma_last(int s) {
    if (s <= 2) return 9 * s + 8;
    for (int gs = 0; gs < NS; ++gs) if (stage_of(gs) == s - 3 && dma_piece(gs) == 8) return seq_in(gs, 5);
    return -1;
}
constexpr int seq_b(int ks) {
    if (ks < LA) return 27 + 4 * ks + 3;
    for (int gs = 0; gs < 144; ++gs) if (bload(gs) == ks * 4 + 3) return seq_in(gs, 3);
    return -1;
}
constexpr int seq_h(int kk) {
    for (int gs = 8 * 144; gs < NS; ++gs) if (hload(gs) == kk * 4 + 3) return seq_in(gs, 4);
    return -1;
}
constexpr int seq_poll(int part) {
    for (int gs = 0; gs < NS; ++gs) if (poll_issue(gs) == part) return seq_in(gs, 1);
    return -1;
}
constexpr int seq_store_last(int part) {
    if (part == 2) return LG.cum[G1S] - 1;
    for (int gs = 5 * 144; gs < 6 * 144; ++gs) if (hstore(gs) == 3) return seq_in(gs, 2);
    return -1;
}
constexpr int clampv(int n) { return n > 63 ? 63 : (n < 0 ? 0 : n); }
constexpr int wait_for(int gs, int seq) { return clampv(LG.cum[gs] - seq - 1); }

template <int N> __device__ __forceinline__ void wait_vm() { asm volatile("s_waitcnt vmcnt(%0)" :: "n"(N) : "memory"); }

struct Ctx {
    bf16x8 Bv[2][4];
    bf16x8 Aq[6];
    f32x4 ac[2][2][4];
    f32x4 a2[9][4];
    float e0, e1;
    u32x4 hp[4];
    u32 vo1[4], vo2[4];
    u32 lane16, epoch;
    u32x4 pollv;
    const LAS char* rd[4]; const LAS char* rd2[4];
    const char* w1src; const char* w2src; const char* bsrc; const char* hsrc;
    u32 voff2[9];
    int sub;
    u32 dmadst;
    u32 hso;
    int jj;
    __amdgpu_buffer_rsrc_t hrs;
    u32* flag_pub; u32* flag_poll;
};

template <int KS, int T, bool ZERO> __device__ __forceinline__ void mfma(Ctx& cx, f32x4& c, const bf16x8& a) {
    constexpr int r = (KS * 4 + T) * 4;
    if constexpr (KS < 16) {
        if constexpr (ZERO) asm volatile("v_mfma_f32_16x16x32_bf16 %0, %1, a[%c2:%c3], 0" : "=v"(c) : "v"(a), "n"(r), "n"(r + 3));
        else asm volatile("v_mfma_f32_16x16x32_bf16 %0, %1, a[%c2:%c3], %0" : "+v"(c) : "v"(a), "n"(r), "n"(r + 3));
    } else {
        asm volatile("v_mfma_f32_16x16x32_bf16 %0, %1, %2, %0" : "+v"(c) : "v"(a), "v"(cx.Bv[KS - 16][T]));
    }
}
template <int KS, int T, bool SC1> __device__ __forceinline__ void gload(Ctx& cx, u32 voff, const char* sb) {
    constexpr int r = (KS * 4 + T) * 4, off = (KS & 3) * 1024;
    if constexpr (KS < 16) {
        if constexpr (SC1) asm volatile("global_load_dwordx4 a[%c2:%c3], %0, %1 offset:%4 sc1" :: "v"(voff), "s"(sb), "n"(r), "n"(r + 3), "n"(off) : "memory");
        else asm volatile("global_load_dwordx4 a[%c2:%c3], %0, %1 offset:%4" :: "v"(voff), "s"(sb), "n"(r), "n"(r + 3), "n"(off) : "memory");
    } else {
        if constexpr (SC1) asm volatile("global_load_dwordx4 %0, %1, %2 offset:%3 sc1" : "=v"(cx.Bv[KS - 16][T]) : "v"(voff), "s"(sb), "n"(off) : "memory");
        else asm volatile("global_load_dwordx4 %0, %1, %2 offset:%3" : "=v"(cx.Bv[KS - 16][T]) : "v"(voff), "s"(sb), "n"(off) : "memory");
    }
}
template <int S, int P> __device__ __forceinline__ void dma_m0(const Ctx& c) {
    asm volatile("s_mov_b32 m0, %0" :: "s"(c.dmadst + (u32)((S & 3) * STG + (P >> 2) * 4096)) : "memory");
}
constexpr int nat_kstep(int ks2) {
    if (ks2 < 10) { constexpr int T[5] = {0, 1, 5, 6, 4}; return 9 * (ks2 / 5) + T[ks2 % 5]; }
    const int jj = (ks2 - 10) >> 1, h = (ks2 - 10) & 1;
    return 9 * (jj >> 1) + ((jj & 1) ? 7 : 2) + h;
}
template <int S, int P> __device__ __forceinline__ void dma_go(const Ctx& c) {
    if constexpr (S < 9) {
        const char* sb = c.w1src + (size_t)S * STG + (P >> 2) * 4096;
        asm volatile("global_load_lds_dwordx4 %0, %1 offset:%2 sc1" :: "v"(c.lane16), "s"(sb), "n"((P & 3) * 1024) : "memory");
    } else {
        constexpr int n0 = nat_kstep(2 * (S - 9)), n1 = nat_kstep(2 * (S - 9) + 1);
        const char* sb = c.w2src + (size_t)(c.sub ? n1 : n0) * (32 * D_ * 2);
        asm volatile("global_load_lds_dwordx4 %0, %1 offset:%2" :: "v"(c.voff2[P]), "s"(sb), "n"((P & 3) * 1024) : "memory");
    }
}
template <int ID> __device__ __forceinline__ void load_b(Ctx& c) {
    constexpr int ks = ID >> 2, t = ID & 3;
    gload<ks, t, true>(c, c.vo1[t], c.bsrc + (ks >> 2) * 4096);
}
template <int ID> __device__ __forceinline__ void load_h(Ctx& c) {
    constexpr int kk = ID >> 2, t = ID & 3;
    gload<kk, t, true>(c, c.vo2[t], c.hsrc + (kk >> 2) * 4096);
}
template <int KS> __device__ __forceinline__ void pin_b(Ctx& c) {
#pragma unroll
    for (int t = 0; t < 4; ++t) { if constexpr (KS >= 16) asm volatile("" : "+v"(c.Bv[KS - 16][t])); }
}
template <int V, int P> __device__ __forceinline__ void epi_piece(Ctx& c) {
    constexpr int pr = P >> 3, st = P & 7, t = pr >> 1, r = 2 * (pr & 1), par = V & 1;
    if constexpr (st == 0) {
        asm volatile("v_mul_f32 %0, 0xbfb8aa3b, %1" : "=v"(c.e0) : "v"(c.ac[par][0][t][r]));
        asm volatile("v_mul_f32 %0, 0xbfb8aa3b, %1" : "=v"(c.e1) : "v"(c.ac[par][0][t][r + 1]));
    } else if constexpr (st == 1) asm volatile("v_exp_f32 %0, %0" : "+v"(c.e0));
    else if constexpr (st == 2) asm volatile("v_exp_f32 %0, %0" : "+v"(c.e1));
    else if constexpr (st == 3) {
        asm volatile("v_add_f32 %0, 1.0, %0" : "+v"(c.e0));
        asm volatile("v_add_f32 %0, 1.0, %0" : "+v"(c.e1));
    } else if constexpr (st == 4) asm volatile("v_rcp_f32 %0, %0" : "+v"(c.e0));
    else if constexpr (st == 5) asm volatile("v_rcp_f32 %0, %0" : "+v"(c.e1));
    else if constexpr (st == 6) {
        asm volatile("v_mul_f32 %0, %0, %1" : "+v"(c.e0) : "v"(c.ac[par][1][t][r]));
        asm volatile("v_mul_f32 %0, %0, %1" : "+v"(c.e1) : "v"(c.ac[par][1][t][r + 1]));
    } else {
        constexpr bool first = (V == 0 || V == 2 || V == 5 || V == 7);
        u32 o;
        asm volatile("v_cvt_pk_bf16_f32 %0, %1, %2" : "=v"(o) : "v"(c.e0), "v"(c.e1));
        c.hp[t][(first ? 0 : 2) + (pr & 1)] = o;
    }
}
template <int V, int T> __device__ __forceinline__ void store_h(Ctx& c) {
    const int m5 = 5 * (c.jj >> 1), ab = c.jj & 1;
    if constexpr (V == 4) {
        const u32x2 d = {c.hp[T][2], c.hp[T][3]};
        __builtin_amdgcn_raw_buffer_store_b64(d, c.hrs, c.vo2[T] + 8 * ab, c.hso + (m5 + 4) * 1024, 16);
    } else {
        const int kstep = (V == 1) ? m5 + 2 * ab : (V == 3) ? m5 + 2 * ab + 1 : (V == 6) ? 10 + 2 * c.jj : 11 + 2 * c.jj;
        __builtin_amdgcn_raw_buffer_store_b128(c.hp[T], c.hrs, c.vo2[T], c.hso + kstep * 1024, 16);
    }
}
template <int PART> __device__ __forceinline__ void poll_check(Ctx& c) {
    asm volatile("" : "+v"(c.pollv));
    const u32 e = c.epoch;
    if (!__all(c.pollv[0] == e && c.pollv[1] == e && c.pollv[2] == e && c.pollv[3] == e)) {
        const u32* p = c.flag_poll + (PART - 1) * 1024 + ((c.lane16 >> 4) & 3);
        poll_eq(p, e);
    }
}
template <int V, int P> __device__ __forceinline__ void epi_piece_padded(Ctx& c) {
    epi_piece<V, P>(c);
    if constexpr ((P & 7) == 2 || (P & 7) == 5) asm volatile("s_nop 0");
}
template <int V, int... P> __device__ __forceinline__ void epi_all(Ctx& c, std::integer_sequence<int, P...>) { (epi_piece_padded<V, P>(c), ...); }

template <int GS> __device__ __forceinline__ void slot(Ctx& c) {
    constexpr bool g1 = GS < G1S;
    constexpr int S = stage_of(GS), R = rel_of(GS), grp = R >> 2, t = R & 3;
    constexpr int fid = g1 ? 36 * S + grp : 324 + 18 * (S - 9) + grp;
    if constexpr (GS == G1S) {
        asm volatile("s_nop 7" ::: "memory");
        epi_all<8>(c, std::make_integer_sequence<int, 64>{});
        store_h<8, 0>(c); store_h<8, 1>(c); store_h<8, 2>(c); store_h<8, 3>(c);
        SCHED();
    }
    if constexpr (S == 0 && t == 0 && (grp & 1) == 0) { wait_vm<wait_for(GS, seq_b(grp >> 1))>(); pin_b<(grp >> 1)>(c); }
    if constexpr (GS == 8 * 144) { wait_vm<wait_for(GS, seq_poll(1))>(); poll_check<1>(c); }
    if constexpr (!g1 && t == 0 && (grp % 9) == 0) { constexpr int kk = 2 * (S - 9) + grp / 9; wait_vm<wait_for(GS, seq_h(kk))>(); pin_b<kk>(c); }
    if constexpr (dma_piece(GS) >= 0 && (dma_piece(GS) & 3) == 0) dma_m0<S + 3, dma_piece(GS)>(c);
    if constexpr (g1) {
        constexpr int ks = grp >> 1, mat = grp & 1;
        mfma<ks, t, (ks == 0)>(c, c.ac[S & 1][mat][t], c.Aq[fid % 6]);
    } else {
        constexpr int kk = 2 * (S - 9) + grp / 9, du = grp % 9;
        mfma<kk, t, (kk == 0)>(c, c.a2[du][t], c.Aq[fid % 6]);
    }
    if constexpr (barpt(GS)) {
        constexpr int n1 = wait_for(GS, seq_dma_last(S + 1));
        constexpr int n2 = (S == 12) ? wait_for(GS, seq_poll(2)) : 63;
        wait_vm<(n1 < n2 ? n1 : n2)>();
        if constexpr (S == 12) poll_check<2>(c);
        __builtin_amdgcn_s_barrier();
        asm volatile("" ::: "memory");
    }
    if constexpr (pub(GS)) {
        wait_vm<wait_for(GS, seq_store_last(pub(GS)))>();
        if (c.lane16 == 0) flag_set(c.flag_pub + (pub(GS) - 1) * 1024, c.epoch);
    }
    if constexpr (poll_issue(GS)) asm volatile("global_load_dwordx4 %0, %1, %2 sc1" : "=v"(c.pollv) : "v"(0u), "s"(c.flag_poll + (poll_issue(GS) - 1) * 1024) : "memory");
    if constexpr (fid + 5 < 486) {
        constexpr int f = fid + 5;
        if constexpr (f < 324) { if constexpr (t == 0) c.Aq[f % 6] = *(const LAS bf16x8*)(c.rd[(f / 36) & 3] + (f % 36) * 1024); }
        else if constexpr (t <= 1) {
            constexpr int fs = (f - 324) % 18;
            const s16x4 h = __builtin_amdgcn_ds_read_tr16_b64_v4i16((LAS s16x4*)(c.rd2[(9 + (f - 324) / 18) & 3] + (fs / 9) * 9216 + (fs % 9) * 32 + t * (16 * 288)));
            const u32x2 hh = __builtin_bit_cast(u32x2, h);
            u32x4 a = __builtin_bit_cast(u32x4, c.Aq[f % 6]);
            a[2 * t] = hh[0]; a[2 * t + 1] = hh[1];
            c.Aq[f % 6] = __builtin_bit_cast(bf16x8, a);
        }
    }
    if constexpr (g1 && S >= 1 && (t == 1 || t == 2) && grp >= 2 && grp < 34) epi_piece<S - 1, (grp - 2) * 2 + t - 1>(c);
    if constexpr (hstore(GS) >= 0) store_h<S - 1, hstore(GS)>(c);
    if constexpr (bload(GS) >= 0) load_b<bload(GS)>(c);
    if constexpr (hload(GS) >= 0) load_h<hload(GS)>(c);
    if constexpr (dma_piece(GS) >= 0) dma_go<S + 3, dma_piece(GS)>(c);
    SCHED();
}
template <int BASE, int... I> __device__ __forceinline__ void run_slots(Ctx& c, std::integer_sequence<int, I...>) { (slot<BASE + I>(c), ...); }
template <int S, int P> __device__ __forceinline__ void dma_one(const Ctx& c) { dma_m0<S, P>(c); asm volatile("s_nop 0"); dma_go<S, P>(c); }
template <int S, int... P> __device__ __forceinline__ void dma_stage(const Ctx& c, std::integer_sequence<int, P...>) { (dma_one<S, P>(c), ...); }
template <int... I> __device__ __forceinline__ void load_b_first(Ctx& c, std::integer_sequence<int, I...>) { (load_b<I>(c), ...); }
}

__device__ __forceinline__ void touch(u32& d, const char* p) { asm volatile("global_load_dword %0, %1, off" : "=v"(d) : "v"(p) : "memory"); }
__global__ __launch_bounds__(256, 1) void k_main(const u16* __restrict__ qin, const u16* __restrict__ kin, const u16* __restrict__ vin, const u16* __restrict__ shift_k,
                                                 const u16* __restrict__ ssm_k, const u16* __restrict__ ssm_D, const u16* __restrict__ wg, const u16* __restrict__ wu,
                                                 const u16* __restrict__ wd, const u16* __restrict__ cvec, const u16* __restrict__ w_mod,
                                                 char* __restrict__ ws, u16* __restrict__ out) {
    using namespace mlp;
    extern __shared__ __attribute__((aligned(16))) char smem[];
    const int tid = threadIdx.x, lane = tid & 63, w = __builtin_amdgcn_readfirstlane(tid >> 6);
    const int b = blockIdx.x, x = b & 7, idx = b >> 3, q = x >> 1;
    const int mt = 4 * (x & 1) + (idx & 3), fcl = idx >> 2, fc = 8 * q + fcl;
    const int mt2 = 8 * (x & 1) + (idx & 7), cc = idx >> 3, tg = w & 1, kh = w >> 1;
    const rsrc_t wsr = __builtin_amdgcn_make_buffer_rsrc(ws, 0, (int)WS_END, 0x00020000);
    STAMP_DECL; STAMP_AT(0);
    const u32 epoch_in = __hip_atomic_load(flag_ptr(ws, F_EPOCH), __ATOMIC_RELAXED, __HIP_MEMORY_SCOPE_AGENT);
    unsigned long long pc_start;
    asm volatile("s_getpc_b64 %0" : "=s"(pc_start));
    u32 epoch;
    {
        const int t0 = (x * 32 + idx) * 8;
        RowsPre rp;
        u32 bg[10] = {0, 0, 0, 0, 0, 0, 0, 0, 0, 0};
        if (b < 192) {
            const int c0 = 72 * x + 3 * idx;
            ConvIn in[3];
            KvIn kin_;
            kv_load(kin_, c0, kin, vin, shift_k);
            conv_load_k(in[0], c0, ssm_k, ssm_D); conv_load_k(in[1], c0 + 1, ssm_k, ssm_D); conv_load_k(in[2], c0 + 2, ssm_k, ssm_D);
            rows_load(rp, t0, qin);
            if (w == 3 && idx < 4) {
                const char* cb = (const char*)(pc_start & ~127ull) + lane * 128;
#pragma unroll
                for (int i = 0; i < 8; ++i) touch(bg[i], cb + (i < 7 || lane < 32 ? i * 8192 : 0));
            }
            kv_compute(kin_, c0, smem + C_XR, C_CH);
            STAMP_AT(1);
            conv_put_k(in[0], smem); conv_put_k(in[1], smem + C_CH); conv_put_k(in[2], smem + 2 * C_CH);
            epoch = __builtin_amdgcn_readfirstlane(epoch_in) + 1u;
            if (w == 3) {
                DRAIN();
#pragma unroll
                for (int i = 0; i < 8; ++i) { const int l = lane + 64 * (i & 3); touch(bg[i], (const char*)(i < 4 ? wg : wu) + (size_t)b * 27648 + (l < 216 ? l : 0) * 128); }
            }
            STAMP_AT(2);
            conv_body(c0, in, smem, wsr STAMP_ARGS);
            STAMP_AT(5);
            poll_eq(flag_ptr(ws, F_MOD + (lane < 54 ? lane : 0)), epoch);
            DRAIN();
#pragma unroll
            for (int i = 0; i < 10; ++i) asm volatile("" :: "v"(bg[i]));
            __syncthreads();
            if (tid < 8) flag_set(flag_ptr(ws, F_CONV + tid * 256 + b), epoch);
            if (w == 3) {
#pragma unroll
                for (int i = 0; i < 4; ++i) { const int l = lane + 64 * i; touch(bg[i], (const char*)wd + (size_t)b * 27648 + (l < 216 ? l : 0) * 128); }
            }
        } else {
            rows_load(rp, t0, qin);
            if (b - 192 < 54) mod_job(b - 192, cvec, w_mod, ws, (float*)(smem + R_MOD), (float*)(smem + R_MOD + 4096));
            epoch = __builtin_amdgcn_readfirstlane(epoch_in) + 1u;
            DRAIN();
            __syncthreads();
            if (tid == 0 && b - 192 < 54) flag_set(flag_ptr(ws, F_MOD + b - 192), epoch);
            STAMP_AT(1);
            if (b - 192 >= 54) { for (int i = 0; i < 40; ++i) __builtin_amdgcn_s_sleep(2); }
            tile_jobs(b - 192, wg, wu, smem, ws, wsr, epoch);
            STAMP_AT(2); STAMP_AT(3); STAMP_AT(4); STAMP_AT(5);
            poll_eq(flag_ptr(ws, F_MOD + (lane < 54 ? lane : 0)), epoch);
        }
        mod_fetch(smem, wsr);
        __syncthreads();
        rows_pre(rp, smem);
        STAMP_AT(6);
        if (w == 0) {
            poll_eq4(wsr, (u32)WS_CNT + (F_CONV + x * 256 + (lane < 48 ? lane : 0) * 4) * 4, epoch);
            rows_fetch(t0, smem, ws);
        }
        __syncthreads();
        STAMP_AT(7);
        rows_main(rp, t0, smem, wsr);
        DRAIN();
#pragma unroll
        for (int i = 0; i < 10; ++i) asm volatile("" :: "v"(bg[i]));
        __syncthreads();
        if (tid == 0) flag_set(flag_ptr(ws, F_ROWS + (t0 >> 6) * 8 + ((t0 >> 3) & 7)), epoch);
        STAMP_AT(8);
    }
    Ctx c;
#pragma unroll
    for (int t = 0; t < 4; ++t) c.hp[t] = (u32x4){0, 0, 0, 0};
    c.lane16 = lane * 16;
#pragma unroll
    for (int t = 0; t < 4; ++t) { c.vo1[t] = lane * 16 + t * 18432; c.vo2[t] = lane * 16 + t * TUS; }
    const LAS char* l0 = (const LAS char*)smem;
#pragma unroll
    for (int i = 0; i < 4; ++i) {
        c.rd[i] = l0 + i * STG + lane * 16; c.rd2[i] = l0 + i * STG + kh * 18432 + (4 * (lane >> 4) + ((lane & 15) >> 2)) * 288 + 8 * (lane & 3);
        asm volatile("" : "+v"(c.rd[i])); asm volatile("" : "+v"(c.rd2[i]));
    }
#pragma unroll
    for (int i = 0; i < 9; ++i) { const int ci = lane + 64 * i, row = (ci * 3641) >> 16, c16 = ci - row * 18; c.voff2[i] = (u32)(row * (D_ * 2) + c16 * 16 - (i & 3) * 1024); }
    c.sub = w & 1;
    c.w1src = ws + WS_WT1 + (size_t)fc * 331776 + w * 9216;
    c.w2src = (const char*)(wd + (size_t)(36 * q + 18 * kh) * 32 * D_ + cc * 144);
    c.bsrc = ws + WS_H2T + (size_t)(16 * mt + 4 * w) * 18432;
    c.hsrc = ws + WS_HT + (size_t)(8 * mt2 + 4 * tg) * TUS + (size_t)((q * 2 + kh) * 18) * 1024;
    c.dmadst = (u32)(uintptr_t)smem + w * 9216;
    c.jj = fcl & 3;
    c.hso = (u32)((16 * mt + 4 * w) * TUS + ((q * 2 + (fcl >> 2)) * 18) * 1024);
    c.hrs = __builtin_amdgcn_make_buffer_rsrc(ws + WS_HT, 0, (int)((size_t)S_ * F_ * 2), 0x00020000);
    c.epoch = epoch;
    c.flag_pub = flag_ptr(ws, F_H + ((((mt * 4 + w) * 4 + q) * 2 + (fcl >> 2))) * 4 + (fcl & 3));
    c.flag_poll = flag_ptr(ws, F_H + (((((mt2 >> 1) * 4 + 2 * (mt2 & 1) + tg) * 4 + q) * 2 + kh)) * 4);
    asm volatile("" ::: "a0", "a1", "a2", "a3", "a4", "a5", "a6", "a7", "a8", "a9", "a10", "a11", "a12", "a13", "a14", "a15", "a16", "a17", "a18", "a19", "a20", "a21", "a22", "a23", "a24", "a25", "a26", "a27", "a28", "a29", "a30", "a31", "a32", "a33", "a34", "a35", "a36", "a37", "a38", "a39", "a40", "a41", "a42", "a43", "a44", "a45", "a46", "a47", "a48", "a49", "a50", "a51", "a52", "a53", "a54", "a55", "a56", "a57", "a58", "a59", "a60", "a61", "a62", "a63", "a64", "a65", "a66", "a67", "a68", "a69", "a70", "a71", "a72", "a73", "a74", "a75", "a76", "a77", "a78", "a79", "a80", "a81", "a82", "a83", "a84", "a85", "a86", "a87", "a88", "a89", "a90", "a91", "a92", "a93", "a94", "a95", "a96", "a97", "a98", "a99", "a100", "a101", "a102", "a103", "a104", "a105", "a106", "a107", "a108", "a109", "a110", "a111", "a112", "a113", "a114", "a115", "a116", "a117", "a118", "a119", "a120", "a121", "a122", "a123", "a124", "a125", "a126", "a127", "a128", "a129", "a130", "a131", "a132", "a133", "a134", "a135", "a136", "a137", "a138", "a139", "a140", "a141", "a142", "a143", "a144", "a145", "a146", "a147", "a148", "a149", "a150", "a151", "a152", "a153", "a154", "a155", "a156", "a157", "a158", "a159", "a160", "a161", "a162", "a163", "a164", "a165", "a166", "a167", "a168", "a169", "a170", "a171", "a172", "a173", "a174", "a175", "a176", "a177", "a178", "a179", "a180", "a181", "a182", "a183", "a184", "a185", "a186", "a187", "a188", "a189", "a190", "a191", "a192", "a193", "a194", "a195", "a196", "a197", "a198", "a199", "a200", "a201", "a202", "a203", "a204", "a205", "a206", "a207", "a208", "a209", "a210", "a211", "a212", "a213", "a214", "a215", "a216", "a217", "a218", "a219", "a220", "a221", "a222", "a223", "a224", "a225", "a226", "a227", "a228", "a229", "a230", "a231", "a232", "a233", "a234", "a235", "a236", "a237", "a238", "a239", "a240", "a241", "a242", "a243", "a244", "a245", "a246", "a247", "a248", "a249", "a250", "a251", "a252", "a253", "a254", "a255");
    poll_eq(flag_ptr(ws, F_TILE + (fc >> 2) * 36 + (lane < 36 ? lane : 0)), epoch);
    dma_stage<0>(c, std::make_integer_sequence<int, 9>{});
    dma_stage<1>(c, std::make_integer_sequence<int, 9>{});
    dma_stage<2>(c, std::make_integer_sequence<int, 9>{});
    poll_eq(flag_ptr(ws, F_ROWS + (4 * mt + w) * 8 + (lane & 7)), epoch);
    STAMP_AT(9);
    load_b_first(c, std::make_integer_sequence<int, 4 * LA>{});
    wait_vm<4 * LA>();
    __builtin_amdgcn_s_barrier();
    asm volatile("" ::: "memory");
#pragma unroll
    for (int f = 0; f < 5; ++f) c.Aq[f] = *(const LAS bf16x8*)(c.rd[0] + f * 1024);
    SCHED();
    STAMP_AT(10);
    run_slots<0>(c, std::make_integer_sequence<int, 144>{});
    STAMP_AT(11);
    run_slots<144>(c, std::make_integer_sequence<int, 144 * 4>{});
    run_slots<144 * 5>(c, std::make_integer_sequence<int, 144 * 4>{});
    STAMP_AT(12);
    run_slots<G1S>(c, std::make_integer_sequence<int, 324>{});
    run_slots<G1S + 324>(c, std::make_integer_sequence<int, 324>{});
    asm volatile("s_nop 7" ::: "memory");
    SCHED();
    STAMP_AT(13);
    const int n = lane & 15, g = lane >> 4;
    u32x2 xr[3][2]; f32x4 gb[3];
#pragma unroll
    for (int i = 0; i < 3; ++i) {
        const int u = (w + 4 * i < 9) ? w + 4 * i : 8, d = 144 * cc + 16 * u + 4 * g;
#pragma unroll
        for (int t = 0; t < 2; ++t) xr[i][t] = __builtin_amdgcn_raw_buffer_load_b64(wsr, (u32)WS_X1 + (u32)((128 * mt2 + 32 * q + 16 * t + n) * D_ + d) * 2, 0, 16);
        gb[i] = *(const f32x4*)(smem + R_MOD + (5 * 576 + d) * 4);
    }
    __builtin_amdgcn_s_barrier();
    {
        char* ex = smem + w * 18432 + lane * 16;
#pragma unroll
        for (int t = 0; t < 2; ++t)
#pragma unroll
            for (int u = 0; u < 9; ++u) *(f32x4*)(ex + (t * 9 + u) * 1024) = kh ? c.a2[u][t] : c.a2[u][2 + t];
    }
    __syncthreads();
    const int j = 2 * tg + kh;
    u32* fred = flag_ptr(ws, F_RED + (mt2 * 4 + cc) * 16);
    const u32 slab = (u32)WS_SLAB + (u32)((mt2 * 4 + cc) * 16) * 9216;
    {
        const char* ey = smem + (w ^ 2) * 18432 + lane * 16;
        char* own = smem + 73728 + lane * 16;
#pragma unroll
        for (int u = 0; u < 9; ++u) {
            const f32x4 r0 = (kh ? c.a2[u][2] : c.a2[u][0]) + *(const f32x4*)(ey + u * 1024);
            const f32x4 r1 = (kh ? c.a2[u][3] : c.a2[u][1]) + *(const f32x4*)(ey + (9 + u) * 1024);
            if (j != q) st16_dev(wsr, slab + (j * 4 + q) * 9216 + u * 1024 + lane * 16, (u32x4){pk2(r0[0], r0[1]), pk2(r0[2], r0[3]), pk2(r1[0], r1[1]), pk2(r1[2], r1[3])});
            else { *(f32x4*)(own + u * 2048) = r0; *(f32x4*)(own + u * 2048 + 1024) = r1; }
        }
    }
    if (j != q) {
        DRAIN();
        if (lane == 0) flag_set(fred + j * 4 + q, epoch);
    }
    STAMP_AT(14);
    poll_eq(fred + q * 4 + (((lane & 3) == q) ? (q ^ 1) : (lane & 3)), epoch);
    __syncthreads();
    u32x4 rem[3][3];
#pragma unroll
    for (int i = 0; i < 3; ++i) {
        const int u = (w + 4 * i < 9) ? w + 4 * i : 8;
#pragma unroll
        for (int k = 0; k < 3; ++k) { const int qq = k + (k >= q ? 1 : 0); rem[i][k] = __builtin_amdgcn_raw_buffer_load_b128(wsr, slab + (q * 4 + qq) * 9216 + u * 1024 + lane * 16, 0, 16); }
    }
#pragma unroll
    for (int i = 0; i < 3; ++i) {
        const int u = w + 4 * i;
        if (u < 9) {
            const int d = 144 * cc + 16 * u + 4 * g;
#pragma unroll
            for (int t = 0; t < 2; ++t) {
                f32x4 s = *(const f32x4*)(smem + 73728 + u * 2048 + t * 1024 + lane * 16);
#pragma unroll
                for (int k = 0; k < 3; ++k) {
                    const u32 a = rem[i][k][2 * t], bq = rem[i][k][2 * t + 1];
                    s += (f32x4){bf_lo(a), bf_hi(a), bf_lo(bq), bf_hi(bq)};
                }
                const float o0 = bf_lo(xr[i][t][0]) + rbf(gb[i][0] * rbf(s[0])), o1 = bf_hi(xr[i][t][0]) + rbf(gb[i][1] * rbf(s[1]));
                const float o2 = bf_lo(xr[i][t][1]) + rbf(gb[i][2] * rbf(s[2])), o3 = bf_hi(xr[i][t][1]) + rbf(gb[i][3] * rbf(s[3]));
                *(u32x2*)(out + (size_t)(128 * mt2 + 32 * q + 16 * t + n) * D_ + d) = (u32x2){pk2(o0, o1), pk2(o2, o3)};
            }
        }
    }
    if (b == 0 && tid == 0) flag_set(flag_ptr(ws, F_EPOCH), epoch);
    STAMP_AT(15);
    STAMP_OUT(4, 16);
}

extern "C" void kernel_launch(void* const* d_in, const int* in_sizes, int n_in, void* d_out, int out_size, void* d_ws, size_t ws_size,
                              hipStream_t stream) {
    const u16* q = (const u16*)d_in[0];
    const u16* k = (const u16*)d_in[1];
    const u16* v = (const u16*)d_in[2];
    const u16* shift_k = (const u16*)d_in[3];
    const u16* ssm_k = (const u16*)d_in[4];
    const u16* ssm_D = (const u16*)d_in[5];
    const u16* wg = (const u16*)d_in[7];
    const u16* wu = (const u16*)d_in[8];
    const u16* wd = (const u16*)d_in[9];
    const u16* c = (const u16*)d_in[10];
    const u16* w_mod = (const u16*)d_in[11];
    char* ws = (char*)d_ws;
    (void)hipFuncSetAttribute((const void*)k_main, hipFuncAttributeMaxDynamicSharedMemorySize, mlp::LDS_BYTES);
    k_main<<<256, 256, mlp::LDS_BYTES, stream>>>(q, k, v, shift_k, ssm_k, ssm_D, wg, wu, wd, c, w_mod, ws, (u16*)d_out);
}
```

```cpp
#include <hip/hip_runtime.h>
#include <stdint.h>
#include <utility>

typedef unsigned short u16;
typedef unsigned int u32;
typedef __bf16 bf16x8 __attribute__((ext_vector_type(8)));
typedef __bf16 bf16x2 __attribute__((ext_vector_type(2)));
typedef float f32x4 __attribute__((ext_vector_type(4)));
typedef float f32x2 __attribute__((ext_vector_type(2)));
typedef u32 u32x4 __attribute__((ext_vector_type(4)));
typedef u32 u32x2 __attribute__((ext_vector_type(2)));
typedef short s16x4 __attribute__((ext_vector_type(4)));
#define LAS __attribute__((address_space(3)))
typedef LAS char* lptr;

constexpr int S_ = 2048, D_ = 576, F_ = 4608;

constexpr size_t WS_MOD = 0;
constexpr size_t WS_CNT = 65536;
constexpr size_t WS_YYT = WS_CNT + 65536;
constexpr size_t WS_X1  = WS_YYT + (size_t)D_ * S_ * 2;
constexpr size_t WS_H2T = WS_X1 + (size_t)D_ * S_ * 2;
constexpr size_t WS_HT  = WS_H2T + (size_t)D_ * S_ * 2;
constexpr size_t WS_WT1 = WS_HT + (size_t)S_ * F_ * 2;
constexpr size_t WS_SLAB = WS_WT1 + (size_t)2 * D_ * F_ * 2;
constexpr size_t WS_END = WS_SLAB + (size_t)16 * 4 * 4 * 4 * 9216;

#define STAMP_DECL
#define STAMP_ARGS
#define STAMP_AT(i)
#define STAMP_OUT(kid, n)
__device__ __forceinline__ float bf_lo(u32 w) { return __uint_as_float(w << 16); }
__device__ __forceinline__ float bf_hi(u32 w) { return __uint_as_float(w & 0xffff0000u); }
__device__ __forceinline__ float bf1(u16 w) { return __uint_as_float((u32)w << 16); }
__device__ __forceinline__ u32 pk2(float lo, float hi) {
    f32x2 v = {lo, hi};
    return __builtin_bit_cast(u32, __builtin_convertvector(v, bf16x2));
}
__device__ __forceinline__ float rbf(float x) { return (float)(__bf16)x; }
__device__ __forceinline__ float sigmoidf_(float x) { return __builtin_amdgcn_rcpf(1.0f + __builtin_amdgcn_exp2f(-1.4426950408889634f * x)); }

constexpr int F_MOD = 256;
constexpr int F_TILE = 512;
constexpr int F_CONV = 1024;
constexpr int F_ROWS = 3072;
constexpr int F_H = 3328;
constexpr int F_RED = 5376;
constexpr int F_EPOCH = 6400;
typedef __amdgpu_buffer_rsrc_t rsrc_t;
__device__ __forceinline__ u32* flag_ptr(char* ws, int idx) { return (u32*)(ws + WS_CNT) + idx; }
__device__ __forceinline__ void flag_set(u32* p, u32 e) { __hip_atomic_store(p, e, __ATOMIC_RELAXED, __HIP_MEMORY_SCOPE_AGENT); }
__device__ __forceinline__ void st16_dev(rsrc_t rs, u32 off, u32x4 v) { __builtin_amdgcn_raw_buffer_store_b128(v, rs, off, 0, 16); }
__device__ __forceinline__ u32x4 ld16_dev(rsrc_t rs, u32 off) { return __builtin_amdgcn_raw_buffer_load_b128(rs, off, 0, 16); }
__device__ __forceinline__ void poll_eq(const u32* p, u32 e) {
    for (int it = 0; it < (1 << 21); ++it) {
        if (__all(__hip_atomic_load(p, __ATOMIC_RELAXED, __HIP_MEMORY_SCOPE_AGENT) == e)) break;
        __builtin_amdgcn_s_sleep(1);
    }
}
__device__ __forceinline__ void poll_eq4(rsrc_t rs, u32 off, u32 e) {
    for (int it = 0; it < (1 << 21); ++it) {
        asm volatile("" ::: "memory");
        const u32x4 v = ld16_dev(rs, off);
        if (__all(v[0] == e && v[1] == e && v[2] == e && v[3] == e)) break;
        __builtin_amdgcn_s_sleep(1);
    }
}
#define DRAIN() asm volatile("s_waitcnt vmcnt(0)" ::: "memory")

#define SCHED() __builtin_amdgcn_sched_barrier(0)
__device__ __forceinline__ float dpp_add(float v, const int ctrl_sel) {
    int r;
    const int iv = __builtin_bit_cast(int, v);
    switch (ctrl_sel) {
        case 0: r = __builtin_amdgcn_update_dpp(0, iv, 0xB1, 0xf, 0xf, true); break;
        case 1: r = __builtin_amdgcn_update_dpp(0, iv, 0x4E, 0xf, 0xf, true); break;
        case 2: r = __builtin_amdgcn_update_dpp(0, iv, 0x141, 0xf, 0xf, true); break;
        default: r = __builtin_amdgcn_update_dpp(0, iv, 0x140, 0xf, 0xf, true); break;
    }
    return v + __builtin_bit_cast(float, r);
}
__device__ __forceinline__ float half_sum(float v) {
    v = dpp_add(v, 0); v = dpp_add(v, 1); v = dpp_add(v, 2); v = dpp_add(v, 3);
    auto r = __builtin_amdgcn_permlane16_swap(__float_as_uint(v), __float_as_uint(v), false, false);
    return __uint_as_float(r[0]) + __uint_as_float(r[1]);
}

struct KvIn { u32x2 kw[8], vw[8]; u32x4 sw; u32x2 sw2; };
__device__ __forceinline__ void kv_load(KvIn& p, int c0, const u16* __restrict__ k, const u16* __restrict__ v, const u16* __restrict__ shift_k) {
    const int cw = c0 & ~1;
#pragma unroll
    for (int i = 0; i < 8; ++i) p.kw[i] = *(const u32x2*)(k + (size_t)(threadIdx.x + 256 * i) * D_ + cw);
#pragma unroll
    for (int i = 0; i < 8; ++i) p.vw[i] = *(const u32x2*)(v + (size_t)(threadIdx.x + 256 * i) * D_ + cw);
    p.sw = (u32x4){*(const u32*)(shift_k + c0 * 4), *(const u32*)(shift_k + c0 * 4 + 2), *(const u32*)(shift_k + c0 * 4 + 4), *(const u32*)(shift_k + c0 * 4 + 6)};
    p.sw2 = (u32x2){*(const u32*)(shift_k + c0 * 4 + 8), *(const u32*)(shift_k + c0 * 4 + 10)};
}
constexpr int KV_RAWV = 257 * 72 + 8;
__device__ __forceinline__ void kv_put(const KvIn& p, char* raw) {
    const int tid = threadIdx.x;
#pragma unroll
    for (int i = 0; i < 8; ++i) {
        const int t = tid + 256 * i, o = 72 * (t >> 3) + 8 * (t & 7);
        *(u32x2*)(raw + 72 + o) = p.kw[i];
        *(u32x2*)(raw + KV_RAWV + o) = p.vw[i];
    }
    if (tid < 8) *(u32x2*)(raw + 8 * tid) = (u32x2){0, 0};
}
__device__ __forceinline__ void kv_compute(const KvIn& p, int c0, const char* raw, char* xr, int stride) {
    const int tid = threadIdx.x;
    u32x2 kw[11], vw[8];
#pragma unroll
    for (int i = 0; i < 11; ++i) kw[i] = *(const u32x2*)(raw + 72 * tid + (i < 3 ? 40 + 8 * i : 72 + 8 * (i - 3)));
#pragma unroll
    for (int i = 0; i < 8; ++i) vw[i] = *(const u32x2*)(raw + KV_RAWV + 72 * tid + 8 * i);
    const bool odd = c0 & 1;
#pragma unroll
    for (int c = 0; c < 3; ++c) {
        const u32 wa = c == 0 ? p.sw[0] : c == 1 ? p.sw[2] : p.sw2[0], wb = c == 0 ? p.sw[1] : c == 1 ? p.sw[3] : p.sw2[1];
        const float w0 = bf_lo(wa), w1 = bf_hi(wa), w2 = bf_lo(wb), w3 = bf_hi(wb);
        float kf[11], r[8];
#define KV_PICK(d) (c == 0 ? (odd ? bf_hi((d)[0]) : bf_lo((d)[0])) : c == 1 ? (odd ? bf_lo((d)[1]) : bf_hi((d)[0])) : (odd ? bf_hi((d)[1]) : bf_lo((d)[1])))
#pragma unroll
        for (int i = 0; i < 11; ++i) kf[i] = KV_PICK(kw[i]);
#pragma unroll
        for (int i = 0; i < 8; ++i) r[i] = (kf[i] * w3 + kf[i + 1] * w2 + kf[i + 2] * w1 + kf[i + 3] * w0) * KV_PICK(vw[i]);
#undef KV_PICK
        *(u32x4*)(xr + c * stride + 2 * (2040 - 8 * tid)) = (u32x4){pk2(r[7], r[6]), pk2(r[5], r[4]), pk2(r[3], r[2]), pk2(r[1], r[0])};
    }
}

__device__ __forceinline__ void mod_job(int job, const u16* __restrict__ c, const u16* __restrict__ w_mod, char* __restrict__ ws, float* sc  , float* red  ) {
    const int tid = threadIdx.x, lane = tid & 63, wid = tid >> 6;
    const int col0 = job * 64, rg = tid >> 3, c8 = tid & 7;
    u32x4 w[18];
#pragma unroll
    for (int i = 0; i < 18; ++i) w[i] = *(const u32x4*)(w_mod + (size_t)(rg + 32 * i) * 3456 + col0 + c8 * 8);
#pragma unroll
    for (int j = 0; j < 3; ++j) { const int r = tid + 256 * j; if (r < 576) { const float x = bf1(c[r]); sc[r] = rbf(x * sigmoidf_(x)); } }
    __syncthreads();
    float acc[8] = {0, 0, 0, 0, 0, 0, 0, 0};
#pragma unroll
    for (int i = 0; i < 18; ++i) {
        const float s = sc[rg + 32 * i];
#pragma unroll
        for (int j = 0; j < 4; ++j) { acc[2 * j] += s * bf_lo(w[i][j]); acc[2 * j + 1] += s * bf_hi(w[i][j]); }
    }
#pragma unroll
    for (int j = 0; j < 8; ++j) {
        acc[j] += __shfl_xor(acc[j], 8); acc[j] += __shfl_xor(acc[j], 16); acc[j] += __shfl_xor(acc[j], 32);
    }
    if (lane < 8) {
#pragma unroll
        for (int j = 0; j < 8; ++j) red[wid * 64 + lane * 8 + j] = acc[j];
    }
    __syncthreads();
    if (tid < 64) {
        float m = rbf((red[tid] + red[64 + tid]) + (red[128 + tid] + red[192 + tid]));
        const int which = col0 / 576;
        if (which == 1 || which == 4) m = rbf(1.0f + m);
        __hip_atomic_store((u32*)(ws + WS_MOD) + col0 + tid, __float_as_uint(m), __ATOMIC_RELAXED, __HIP_MEMORY_SCOPE_AGENT);
    }
}

__device__ __forceinline__ const char* tile_base(int job, const u16* __restrict__ wg, const u16* __restrict__ wu) {
    const int ks = job % 18, mat = (job / 18) & 1, cg = job / 36;
    return (const char*)((mat ? wu : wg) + (size_t)(32 * ks) * F_ + cg * 576);
}
__device__ __forceinline__ void tile_dma(int job, const u16* __restrict__ wg, const u16* __restrict__ wu, char* tile, const u32 (&voff)[9]) {
    const int w = __builtin_amdgcn_readfirstlane(threadIdx.x >> 6);
    const char* base = tile_base(job, wg, wu) + (size_t)w * 8 * (F_ * 2);
    const u32 dst = (u32)(uintptr_t)tile + w * 9216;
#pragma unroll
    for (int i = 0; i < 9; ++i)
        asm volatile("s_mov_b32 m0, %2\n\ts_nop 0\n\tglobal_load_lds_dwordx4 %0, %1" :: "v"(voff[i]), "s"(base), "s"(dst + i * 1024) : "memory");
}
__device__ __forceinline__ void tile_out(int job, const char* tile, rsrc_t wsr) {
    const int lane = threadIdx.x & 63, w = threadIdx.x >> 6;
    const int ks = job % 18, mat = (job / 18) & 1, fc = 4 * (job / 36) + w;
    const int g = lane >> 4, q4 = (lane & 15) >> 2, p = lane & 3;
    const LAS char* rd = (const LAS char*)tile + (4 * g + q4) * 1152 + 8 * p + w * 288;
#pragma unroll
    for (int u = 0; u < 9; ++u) {
        const int pos = (fc & 1) ? (u == 0 ? 4 : u <= 4 ? u - 1 : u) : (u == 8 ? 4 : u < 4 ? u : u + 1);
        const s16x4 lo = __builtin_amdgcn_ds_read_tr16_b64_v4i16((LAS s16x4*)(rd + u * 32));
        const s16x4 hi = __builtin_amdgcn_ds_read_tr16_b64_v4i16((LAS s16x4*)(rd + u * 32 + 16 * 1152));
        typedef short s16x8 __attribute__((ext_vector_type(8)));
        const s16x8 r = __builtin_shufflevector(lo, hi, 0, 1, 2, 3, 4, 5, 6, 7);
        st16_dev(wsr, (u32)WS_WT1 + fc * 331776 + pos * 36864 + (ks * 2 + mat) * 1024 + lane * 16, __builtin_bit_cast(u32x4, r));
    }
}
__device__ __forceinline__ void tile_jobs(int i0, const u16* __restrict__ wg, const u16* __restrict__ wu, char* smem, char* ws, rsrc_t wsr, u32 epoch) {
    const int lane = threadIdx.x & 63;
    u32 voff[9];
#pragma unroll
    for (int i = 0; i < 9; ++i) { const int ci = lane + 64 * i, row = (ci * 58255) >> 22, c16 = ci - row * 72; voff[i] = (u32)(row * (F_ * 2) + c16 * 16); }
#pragma unroll
    for (int j = 0; j < 4; ++j) tile_dma(i0 + 64 * j, wg, wu, smem + j * 36864, voff);
#pragma unroll
    for (int j = 0; j < 4; ++j) {
        asm volatile("s_waitcnt vmcnt(27)" ::: "memory");
        __syncthreads();
        tile_out(i0 + 64 * j, smem + j * 36864, wsr);
    }
    if (i0 < 32) {
        __syncthreads();
        tile_dma(i0 + 256, wg, wu, smem, voff);
        asm volatile("s_waitcnt vmcnt(0)" ::: "memory");
        __syncthreads();
        tile_out(i0 + 256, smem, wsr);
    }
    DRAIN();
    __syncthreads();
    if (threadIdx.x < 5 && i0 + 64 * threadIdx.x < 288) flag_set(flag_ptr(ws, F_TILE + i0 + 64 * threadIdx.x), epoch);
}

constexpr int C_XR = 0;
constexpr int C_K0 = 4608;
constexpr int C_K1 = C_K0 + 4128 + 32;
constexpr int C_CH = C_K1 + 4160;
constexpr int C_RED = 3 * C_CH;
constexpr int C_LDS = C_RED + 3 * 32768;
static_assert(((C_K1 - C_K0) / 4) % 32 == 16, "bank offset");
static_assert(C_CH % 16 == 0 && C_K0 % 16 == 0 && C_K1 % 16 == 0, "align");
template <bool ZERO> __device__ __forceinline__ void mfma_v(f32x4& c, const bf16x8& a, const bf16x8& b) {
    if constexpr (ZERO) asm volatile("v_mfma_f32_16x16x32_bf16 %0, %1, %2, 0" : "=v"(c) : "v"(a), "v"(b));
    else asm volatile("v_mfma_f32_16x16x32_bf16 %0, %1, %2, %0" : "+v"(c) : "v"(a), "v"(b));
}
struct ConvSet { bf16x8 A[8], B[8]; };
template <int J> __device__ __forceinline__ void conv_ld(ConvSet& s, const LAS char* ab, const LAS char* bb) {
    if constexpr (J < 8) {
        const LAS u32* p = (const LAS u32*)(ab + 512 * J);
        u32x4 t = {p[0], p[1], p[2], p[3]};
        s.A[J] = __builtin_bit_cast(bf16x8, t);
    } else s.B[J - 8] = *(const LAS bf16x8*)(bb + 512 * (15 - J));
}
template <int M> struct TriIdx {
    static constexpr int find(bool want_i) { int m = 0; for (int s = 0; s < 8; ++s) for (int j = 0; j <= s; ++j) { if (m == M) return want_i ? s : j; ++m; } return 0; }
    static constexpr int i = find(true), j = find(false);
};
template <int M, bool PF, bool FIRST> __device__ __forceinline__ void conv_slot(f32x4 (&acc)[8], const ConvSet& cur, ConvSet& nxt, const LAS char* ab, const LAS char* bb) {
    mfma_v<(FIRST && TriIdx<M>::j == 0)>(acc[TriIdx<M>::i], cur.A[TriIdx<M>::j], cur.B[TriIdx<M>::i - TriIdx<M>::j]);
    if constexpr (PF && (M & 1) == 0 && M / 2 < 16) conv_ld<((M / 2) & 1) * 8 + (M / 4)>(nxt, ab, bb);
    SCHED();
}
template <bool PF, bool FIRST, int... M> __device__ __forceinline__ void conv_step(f32x4 (&acc)[8], const ConvSet& cur, ConvSet& nxt, const LAS char* ab, const LAS char* bb, std::integer_sequence<int, M...>) {
    (conv_slot<M, PF, FIRST>(acc, cur, nxt, ab, bb), ...);
}
template <int... J> __device__ __forceinline__ void conv_ld_all(ConvSet& s, const LAS char* ab, const LAS char* bb, std::integer_sequence<int, J...>) { (conv_ld<J>(s, ab, bb), ...); }

struct ConvIn { u32x4 k; u32 kn; u16 dv; };
__device__ __forceinline__ void conv_load_k(ConvIn& in, int d, const u16* __restrict__ ssm_k, const u16* __restrict__ ssm_D) {
    const int tid = threadIdx.x;
    in.k = *(const u32x4*)(ssm_k + (size_t)d * S_ + tid * 8);
    in.kn = *(const u32*)(ssm_k + (size_t)d * S_ + (tid < 255 ? tid * 8 + 8 : 0));
    in.dv = ssm_D[d];
}
__device__ __forceinline__ void conv_put_k(const ConvIn& in, char* sm) {
    const int tid = threadIdx.x;
    const u32 kn = tid < 255 ? in.kn : 0u;
    *(u32x4*)(sm + C_K0 + 32 + tid * 16) = in.k;
    *(u32x4*)(sm + C_K1 + 32 + tid * 16) = (u32x4){__builtin_amdgcn_alignbit(in.k[1], in.k[0], 16), __builtin_amdgcn_alignbit(in.k[2], in.k[1], 16),
                                                 __builtin_amdgcn_alignbit(in.k[3], in.k[2], 16), __builtin_amdgcn_alignbit(kn, in.k[3], 16)};
    if (tid < 32) *(u32x4*)(sm + C_XR + 4096 + tid * 16) = (u32x4){0, 0, 0, 0};
    if (tid == 0) {
        *(u32x4*)(sm + C_K0) = (u32x4){0, 0, 0, 0}; *(u32x4*)(sm + C_K0 + 16) = (u32x4){0, 0, 0, 0};
        *(u32x4*)(sm + C_K1) = (u32x4){0, 0, 0, 0}; *(u32x4*)(sm + C_K1 + 16) = (u32x4){0, 0, 0, in.k[0] << 16};
    }
}
__device__ __forceinline__ void conv_finish(int d, const char* sm, const char* red, float Dv, rsrc_t wsr) {
    const int tid = threadIdx.x;
    const int i = tid >> 5, n = tid & 15, gp = (tid >> 4) & 1;
    f32x4 lo = {0.f, 0.f, 0.f, 0.f}, hi = {0.f, 0.f, 0.f, 0.f};
#pragma unroll
    for (int w = 0; w < 4; ++w) {
        lo += *(const f32x4*)(red + (w * 8 + i) * 1024 + (n + 32 * gp) * 16);
        hi += *(const f32x4*)(red + (w * 8 + i) * 1024 + (n + 32 * gp + 16) * 16);
    }
    const int s = 256 * i + 16 * n + 8 * gp;
    const u32x4 xw = *(const u32x4*)(sm + C_XR + 2 * (2040 - s));
    const float o0 = rbf(lo[0]) + rbf(bf_hi(xw[3]) * Dv), o1 = rbf(lo[1]) + rbf(bf_lo(xw[3]) * Dv), o2 = rbf(lo[2]) + rbf(bf_hi(xw[2]) * Dv), o3 = rbf(lo[3]) + rbf(bf_lo(xw[2]) * Dv);
    const float o4 = rbf(hi[0]) + rbf(bf_hi(xw[1]) * Dv), o5 = rbf(hi[1]) + rbf(bf_lo(xw[1]) * Dv), o6 = rbf(hi[2]) + rbf(bf_hi(xw[0]) * Dv), o7 = rbf(hi[3]) + rbf(bf_lo(xw[0]) * Dv);
    st16_dev(wsr, (u32)WS_YYT + (u32)(d * S_ + s) * 2, (u32x4){pk2(o0, o1), pk2(o2, o3), pk2(o4, o5), pk2(o6, o7)});
}
template <class Hook>
__device__ __forceinline__ void conv_body(int ch0, const ConvIn (&in)[3], char* smem, rsrc_t wsr, Hook hook
                                          ) {
    const int lane = threadIdx.x & 63, wid = __builtin_amdgcn_readfirstlane(threadIdx.x >> 6);
    __syncthreads();
    STAMP_AT(3);
    const int n = lane & 15, g = lane >> 4, dd = g >> 1, gh = g & 1;
    const LAS char* l0 = (const LAS char*)smem;
    const LAS char* bb = l0 + C_XR + 480 - 32 * n + 32 * dd + 16 * gh + 128 * wid;
    const LAS char* ab = l0 + ((n & 1) ? C_K0 + 2 * (16 * dd + 8 * gh + n + 1) : C_K1 + 2 * (16 * dd + 8 * gh + n)) + 128 * wid;
    f32x4 acc[3][8];
    ConvSet s0, s1;
    conv_ld_all(s0, ab, bb, std::make_integer_sequence<int, 16>{});
    SCHED();
    constexpr auto SQ = std::make_integer_sequence<int, 36>{};
    conv_step<true, true>(acc[0], s0, s1, ab + 64, bb + 64, SQ);
    conv_step<true, false>(acc[0], s1, s0, ab + C_CH, bb + C_CH, SQ);
    conv_step<true, true>(acc[1], s0, s1, ab + C_CH + 64, bb + C_CH + 64, SQ);
    conv_step<true, false>(acc[1], s1, s0, ab + 2 * C_CH, bb + 2 * C_CH, SQ);
    conv_step<true, true>(acc[2], s0, s1, ab + 2 * C_CH + 64, bb + 2 * C_CH + 64, SQ);
    conv_step<false, false>(acc[2], s1, s0, ab, bb, SQ);
    asm volatile("s_nop 7" ::: "memory");
    STAMP_AT(4);
    hook();
#pragma unroll
    for (int c = 0; c < 3; ++c)
#pragma unroll
        for (int i = 0; i < 8; ++i) *(f32x4*)(smem + C_RED + c * 32768 + (wid * 8 + i) * 1024 + lane * 16) = acc[c][i];
    __syncthreads();
#pragma unroll
    for (int c = 0; c < 3; ++c) conv_finish(ch0 + c, smem + c * C_CH, smem + C_RED + c * 32768, bf1(in[c].dv), wsr);
}

constexpr int R_MOD = 147456;
constexpr int R_YT = 110592;
constexpr int R_HT = R_YT + 8 * 1168;
struct RowsPre { float x[18], gh[18]; u32 xw[9]; };
__device__ __forceinline__ void rows_load(RowsPre& r, int t0, const u16* __restrict__ q) {
    const int tid = threadIdx.x, lane = tid & 63, hl = lane & 31, row = 2 * (tid >> 6) + (lane >> 5);
    const u32* qrow = (const u32*)(q + (size_t)(t0 + row) * D_);
#pragma unroll
    for (int i = 0; i < 9; ++i) r.xw[i] = qrow[hl + 32 * i];
}
struct ModIn { u32x4 mw[4]; };
__device__ __forceinline__ void mod_load(ModIn& m, rsrc_t wsr) {
#pragma unroll
    for (int i = 0; i < 4; ++i) { const int j = threadIdx.x + 256 * i; m.mw[i] = ld16_dev(wsr, (u32)WS_MOD + (j < 864 ? j : 0) * 16); }
}
__device__ __forceinline__ void mod_put(const ModIn& m, char* sm) {
#pragma unroll
    for (int i = 0; i < 4; ++i) { const int j = threadIdx.x + 256 * i; if (j < 864) *(u32x4*)(sm + R_MOD + j * 16) = m.mw[i]; }
}
__device__ __forceinline__ void rows_pre(RowsPre& r, const char* sm) {
    const int hl = threadIdx.x & 31;
#pragma unroll
    for (int i = 0; i < 9; ++i) { r.x[2 * i] = bf_lo(r.xw[i]); r.x[2 * i + 1] = bf_hi(r.xw[i]); }
    float s = 0.f;
#pragma unroll
    for (int i = 0; i < 18; ++i) s += r.x[i];
    const float mean = half_sum(s) * (1.0f / 576.0f);
    float vs = 0.f;
#pragma unroll
    for (int i = 0; i < 18; ++i) { const float dl = r.x[i] - mean; vs += dl * dl; }
    const float rstd = __builtin_amdgcn_rsqf(half_sum(vs) * (1.0f / 576.0f) + 1e-5f);
    const float* mod = (const float*)(sm + R_MOD);
#pragma unroll
    for (int i = 0; i < 9; ++i) {
        const int c = 2 * (hl + 32 * i);
        const f32x2 sa = *(const f32x2*)(mod + c), c1 = *(const f32x2*)(mod + 576 + c), ga = *(const f32x2*)(mod + 2 * 576 + c);
        r.gh[2 * i] = ga[0] * ((r.x[2 * i] - mean) * rstd * c1[0] + sa[0]);
        r.gh[2 * i + 1] = ga[1] * ((r.x[2 * i + 1] - mean) * rstd * c1[1] + sa[1]);
    }
}
__device__ __forceinline__ void rows_fetch(int t0, char* sm, const char* ws) {
    const int lane = threadIdx.x & 63;
    const char* src = ws + WS_YYT + t0 * 2;
    const u32 dst = (u32)(uintptr_t)(sm + R_HT);
#pragma unroll
    for (int i = 0; i < 9; ++i)
        asm volatile("s_mov_b32 m0, %2\n\ts_nop 0\n\tglobal_load_lds_dwordx4 %0, %1 sc1" :: "v"((u32)((lane + 64 * i) * (S_ * 2))), "s"(src), "s"(dst + i * 1024) : "memory");
    asm volatile("s_waitcnt vmcnt(0)" ::: "memory");
}
__device__ __forceinline__ void rows_main(const RowsPre& r, int t0, char* sm, rsrc_t wsr) {
    const int tid = threadIdx.x, lane = tid & 63, hl = lane & 31, row = 2 * (tid >> 6) + (lane >> 5);
    const int cg = tid >> 1, th = tid & 1;
    if (tid < 144) {
        u32x2 yw[8];
#pragma unroll
        for (int c = 0; c < 8; ++c) yw[c] = *(const u32x2*)(sm + R_HT + (8 * cg + c) * 16 + th * 8);
#pragma unroll
        for (int t = 0; t < 4; ++t) {
            u32x4 o;
#pragma unroll
            for (int kk = 0; kk < 4; ++kk) {
                const u32 a = yw[2 * kk][t >> 1], bq = yw[2 * kk + 1][t >> 1];
                o[kk] = (t & 1) ? __builtin_amdgcn_perm(bq, a, 0x07060302u) : __builtin_amdgcn_perm(bq, a, 0x05040100u);
            }
            *(u32x4*)(sm + R_YT + (4 * th + t) * 1168 + cg * 16) = o;
        }
    }
    __syncthreads();
    const float* mod = (const float*)(sm + R_MOD);
    float x1[18];
#pragma unroll
    for (int i = 0; i < 9; ++i) {
        u32* yp = (u32*)(sm + R_YT + row * 1168 + (hl + 32 * i) * 4);
        const u32 yv = *yp;
        const u32 p = pk2(r.x[2 * i] + r.gh[2 * i] * bf_lo(yv), r.x[2 * i + 1] + r.gh[2 * i + 1] * bf_hi(yv));
        *yp = p;
        x1[2 * i] = bf_lo(p); x1[2 * i + 1] = bf_hi(p);
    }
    float s = 0.f;
#pragma unroll
    for (int i = 0; i < 18; ++i) s += x1[i];
    const float mean = half_sum(s) * (1.0f / 576.0f);
    float vs = 0.f;
#pragma unroll
    for (int i = 0; i < 18; ++i) { const float dl = x1[i] - mean; vs += dl * dl; }
    const float rstd = __builtin_amdgcn_rsqf(half_sum(vs) * (1.0f / 576.0f) + 1e-5f);
#pragma unroll
    for (int i = 0; i < 9; ++i) {
        const int c = 2 * (hl + 32 * i);
        const f32x2 sb = *(const f32x2*)(mod + 3 * 576 + c), c1 = *(const f32x2*)(mod + 4 * 576 + c);
        const float h0 = (x1[2 * i] - mean) * rstd * c1[0] + sb[0], h1 = (x1[2 * i + 1] - mean) * rstd * c1[1] + sb[1];
        const int ks = c >> 5, kk = c & 31, gq = (kk & 15) >> 2, j = ((kk >> 4) << 2) | (kk & 3);
        *(u32*)(sm + R_HT + ((ks * 4 + gq) * 8 + row) * 16 + j * 2) = pk2(h0, h1);
    }
    __syncthreads();
    const u32 h2t = (u32)WS_H2T + (u32)(t0 >> 4) * 18 * 1024 + (t0 & 15) * 16, x1g = (u32)WS_X1 + (u32)t0 * D_ * 2;
#pragma unroll
    for (int it = 0; it < 3; ++it) {
        const int i = tid + 256 * it;
        if (i < 576) {
            const int kg = i >> 3, rr8 = i & 7;
            st16_dev(wsr, h2t + (kg >> 2) * 1024 + (kg & 3) * 256 + rr8 * 16, *(const u32x4*)(sm + R_HT + i * 16));
            const int rr = i / 72, cc = i - rr * 72;
            st16_dev(wsr, x1g + rr * 1152 + cc * 16, *(const u32x4*)(sm + R_YT + rr * 1168 + cc * 16));
        }
    }
}

namespace mlp {
constexpr int STG = 36864, LDS_BYTES = 163840;
constexpr int G1S = 9 * 144, G2S = 18 * 36, NS = G1S + G2S;
constexpr int LA = 4;
constexpr int PRO = 9 + 4 * LA + 18;
constexpr int TUS = 147456;

constexpr int F01 = 288;
constexpr int stage_of(int gs) { return gs < F01 ? 0 : gs < G1S ? gs / 144 : 9 + (gs - G1S) / 72; }
constexpr int rel_of(int gs) { return gs < F01 ? gs : gs < G1S ? gs % 144 : (gs - G1S) % 72; }
constexpr int grp_of(int gs) { return rel_of(gs) >> 2; }
constexpr int t_of(int gs) { return rel_of(gs) & 3; }
constexpr bool barpt(int gs) { return gs >= F01 && gs < NS && t_of(gs) == 3 && grp_of(gs) == (gs < G1S ? 3 : 0) && stage_of(gs) < 17; }
constexpr int dma_id(int gs) {
    if (gs >= NS || t_of(gs) != 3) return -1;
    const int g = grp_of(gs), s = stage_of(gs);
    if (gs < F01) return (g >= 36 && g < 72 && (g & 3) == 3) ? 3 * 16 + (g - 36) / 4 : -1;
    if (s == 2) return (g >= 4 && g < 22) ? ((g & 1) ? 5 : 4) * 16 + (g - 4) / 2 : -1;
    if (s + 3 > 17) return -1;
    if (gs < G1S) { constexpr int G[9] = {5, 7, 9, 12, 14, 16, 20, 22, 24}; for (int i = 0; i < 9; ++i) if (G[i] == g) return (s + 3) * 16 + i; return -1; }
    return (g & 1) ? (s + 3) * 16 + (g >> 1) : -1;
}
constexpr int bload(int gs) {
    if (gs >= F01 || t_of(gs) != 1) return -1;
    const int ks = grp_of(gs) >> 2, i = grp_of(gs) & 3;
    return (ks + LA < 18) ? (ks + LA) * 4 + i : -1;
}
constexpr bool unit_stores(int v) { return v == 1 || v == 3 || v == 4 || v == 6 || v == 8; }
constexpr int unit_end(int v) { return v < 2 ? F01 : 144 * (v + 1); }
constexpr int hload(int gs) {
    if (t_of(gs) != 3) return -1;
    const int s = stage_of(gs), g = grp_of(gs);
    if (s == 8) { constexpr int G[20] = {4, 6, 8, 10, 11, 13, 15, 17, 18, 19, 21, 23, 25, 26, 27, 28, 29, 30, 31, 32}; for (int i = 0; i < 20; ++i) if (G[i] == g) return i; return -1; }
    if (s < 9 || g < 2 || g > 16 || (g & 1)) return -1;
    const int i = (g - 2) >> 1;
    if (s == 9) return 20 + i;
    if (s == 10) return 28 + i;
    if (s == 11) return i < 4 ? 36 + i : -1;
    if (s >= 12 && s <= 15) return 40 + 8 * (s - 12) + i;
    return -1;
}
constexpr int pub(int gs) { return (stage_of(gs) == 6 && grp_of(gs) == 27 && t_of(gs) == 3) ? 1 : (stage_of(gs) == 10 && grp_of(gs) == 9 && t_of(gs) == 1) ? 2 : 0; }
constexpr int poll_issue(int gs) { return (stage_of(gs) == 7 && grp_of(gs) == 27 && t_of(gs) == 3) ? 1 : (stage_of(gs) == 11 && grp_of(gs) == 9 && t_of(gs) == 1) ? 2 : 0; }
struct EpiSched { signed char ev[G1S], ep[G1S], sv[G1S], st[G1S]; };
constexpr EpiSched make_epi() {
    EpiSched e{};
    int v = 0, p = 0, pend_v = -1, pend_lo = 0, pend_hi = 0;
    for (int gs = 0; gs < G1S; ++gs) {
        e.ev[gs] = -1; e.ep[gs] = -1; e.sv[gs] = -1; e.st[gs] = -1;
        if (gs < F01) continue;
        const int t = t_of(gs);
        if (t == 0) continue;
        const bool vm = barpt(gs) || dma_id(gs) >= 0 || hload(gs) >= 0 || pub(gs) || poll_issue(gs);
        if (t == 3 && !vm && pend_lo < pend_hi) { e.sv[gs] = (signed char)pend_v; e.st[gs] = (signed char)pend_lo; ++pend_lo; continue; }
        if (t == 3 && vm) continue;
        if (v < 8 && gs >= unit_end(v) + 8 && (pend_lo == pend_hi || pend_v == v || !unit_stores(v) || true)) {
            e.ev[gs] = (signed char)v; e.ep[gs] = (signed char)p;
            ++p;
            if ((p & 15) == 0 && unit_stores(v)) { if (pend_v != v) { pend_v = v; pend_lo = p / 16 - 1; } pend_hi = p / 16; }
            if (p == 64) { p = 0; ++v; }
        }
    }
    return e;
}
constexpr EpiSched ES = make_epi();
constexpr int hstore(int gs) { return gs < G1S ? ES.st[gs] : -1; }
constexpr int epi_last(int v) { for (int gs = G1S - 1; gs >= 0; --gs) if (ES.ev[gs] == v) return gs; return -1; }
constexpr int store_last(int v) { for (int gs = G1S - 1; gs >= 0; --gs) if (ES.sv[gs] == v && ES.st[gs] == 3) return gs; return -1; }
constexpr bool epi_ok() {
    for (int v = 0; v < 8; ++v) {
        if (epi_last(v) < 0 || ES.ep[epi_last(v)] != 63) return false;
        if (v + 3 <= 8 && epi_last(v) >= 144 * (v + 3)) return false;
        if (unit_stores(v) && store_last(v) < 0) return false;
    }
    return store_last(4) + 40 < 6 * 144 + 27 * 4;
}
static_assert(epi_ok(), "epilogue schedule");
constexpr int vm_ops(int gs) { return (pub(gs) ? 1 : 0) + (poll_issue(gs) ? 1 : 0) + (hstore(gs) >= 0) + (bload(gs) >= 0) + (hload(gs) >= 0) + (dma_id(gs) >= 0); }
struct Ledger { int cum[NS + 1]; };
constexpr Ledger make_ledger() {
    Ledger l{}; int n = PRO;
    for (int gs = 0; gs < NS; ++gs) { if (gs == G1S) n += 4; l.cum[gs] = n; n += vm_ops(gs); }
    l.cum[NS] = n; return l;
}
constexpr Ledger LG = make_ledger();
constexpr int seq_in(int gs, int kind) {
    int n = LG.cum[gs];
    if (kind > 0 && pub(gs)) ++n;
    if (kind > 1 && poll_issue(gs)) ++n;
    if (kind > 2 && hstore(gs) >= 0) ++n;
    if (kind > 3 && bload(gs) >= 0) ++n;
    if (kind > 4 && hload(gs) >= 0) ++n;
    return n;
}
constexpr int seq_dma_last(int s) {
    if (s <= 2) return 9 * s + 8;
    for (int gs = 0; gs < NS; ++gs) if (dma_id(gs) == s * 16 + 8) return seq_in(gs, 5);
    return -1;
}
constexpr int seq_b(int ks) {
    if (ks < LA) return 27 + 4 * ks + 3;
    for (int gs = 0; gs < F01; ++gs) if (bload(gs) == ks * 4 + 3) return seq_in(gs, 3);
    return -1;
}
constexpr int seq_h(int kk) {
    for (int gs = 8 * 144; gs < NS; ++gs) if (hload(gs) == kk * 4 + 3) return seq_in(gs, 4);
    return -1;
}
constexpr int seq_poll(int part) {
    for (int gs = 0; gs < NS; ++gs) if (poll_issue(gs) == part) return seq_in(gs, 1);
    return -1;
}
constexpr int seq_store_last(int part) {
    if (part == 2) return LG.cum[G1S] - 1;
    return seq_in(store_last(4), 2);
}
constexpr int clampv(int n) { return n > 63 ? 63 : (n < 0 ? 0 : n); }
constexpr int wait_for(int gs, int seq) { return clampv(LG.cum[gs] - seq - 1); }

template <int N> __device__ __forceinline__ void wait_vm() { asm volatile("s_waitcnt vmcnt(%0)" :: "n"(N) : "memory"); }

struct Ctx {
    bf16x8 Bv[2][4];
    bf16x8 Aq[6];
    f32x4 ac[3][2][4];
    f32x4 a2[9][4];
    float e0, e1;
    u32x4 hp[4];
    u32 vo1[4], vo2[4];
    u32 lane16, epoch;
    u32x4 pollv;
    const LAS char* rd[4]; const LAS char* rd2[4];
    const char* w1src; const char* w2src; const char* bsrc; const char* hsrc;
    u32 voff2[9];
    int sub;
    u32 dmadst;
    u32 hso;
    int jj;
    __amdgpu_buffer_rsrc_t hrs;
    u32* flag_pub; u32* flag_poll;
};

template <int KS, int T, bool ZERO> __device__ __forceinline__ void mfma(Ctx& cx, f32x4& c, const bf16x8& a) {
    constexpr int r = (KS * 4 + T) * 4;
    if constexpr (KS < 16) {
        if constexpr (ZERO) asm volatile("v_mfma_f32_16x16x32_bf16 %0, %1, a[%c2:%c3], 0" : "=v"(c) : "v"(a), "n"(r), "n"(r + 3));
        else asm volatile("v_mfma_f32_16x16x32_bf16 %0, %1, a[%c2:%c3], %0" : "+v"(c) : "v"(a), "n"(r), "n"(r + 3));
    } else {
        asm volatile("v_mfma_f32_16x16x32_bf16 %0, %1, %2, %0" : "+v"(c) : "v"(a), "v"(cx.Bv[KS - 16][T]));
    }
}
template <int KS, int T, bool SC1> __device__ __forceinline__ void gload(Ctx& cx, u32 voff, const char* sb) {
    constexpr int r = (KS * 4 + T) * 4, off = (KS & 3) * 1024;
    if constexpr (KS < 16) {
        if constexpr (SC1) asm volatile("global_load_dwordx4 a[%c2:%c3], %0, %1 offset:%4 sc1" :: "v"(voff), "s"(sb), "n"(r), "n"(r + 3), "n"(off) : "memory");
        else asm volatile("global_load_dwordx4 a[%c2:%c3], %0, %1 offset:%4" :: "v"(voff), "s"(sb), "n"(r), "n"(r + 3), "n"(off) : "memory");
    } else {
        if constexpr (SC1) asm volatile("global_load_dwordx4 %0, %1, %2 offset:%3 sc1" : "=v"(cx.Bv[KS - 16][T]) : "v"(voff), "s"(sb), "n"(off) : "memory");
        else asm volatile("global_load_dwordx4 %0, %1, %2 offset:%3" : "=v"(cx.Bv[KS - 16][T]) : "v"(voff), "s"(sb), "n"(off) : "memory");
    }
}
template <int S, int P> __device__ __forceinline__ void dma_m0(const Ctx& c) {
    asm volatile("s_mov_b32 m0, %0" :: "s"(c.dmadst + (u32)((S & 3) * STG + (P >> 2) * 4096)) : "memory");
}
constexpr int nat_kstep(int ks2) {
    if (ks2 < 10) { constexpr int T[5] = {0, 1, 5, 6, 4}; return 9 * (ks2 / 5) + T[ks2 % 5]; }
    const int jj = (ks2 - 10) >> 1, h = (ks2 - 10) & 1;
    return 9 * (jj >> 1) + ((jj & 1) ? 7 : 2) + h;
}
template <int S, int P> __device__ __forceinline__ void dma_go(const Ctx& c) {
    if constexpr (S < 9) {
        const char* sb = c.w1src + (size_t)S * STG + (P >> 2) * 4096;
        asm volatile("global_load_lds_dwordx4 %0, %1 offset:%2 sc1" :: "v"(c.lane16), "s"(sb), "n"((P & 3) * 1024) : "memory");
    } else {
        constexpr int n0 = nat_kstep(2 * (S - 9)), n1 = nat_kstep(2 * (S - 9) + 1);
        const char* sb = c.w2src + (size_t)(c.sub ? n1 : n0) * (32 * D_ * 2);
        asm volatile("global_load_lds_dwordx4 %0, %1 offset:%2" :: "v"(c.voff2[P]), "s"(sb), "n"((P & 3) * 1024) : "memory");
    }
}
template <int ID> __device__ __forceinline__ void load_b(Ctx& c) {
    constexpr int ks = ID >> 2, t = ID & 3;
    gload<ks, t, true>(c, c.vo1[t], c.bsrc + (ks >> 2) * 4096);
}
template <int ID> __device__ __forceinline__ void load_h(Ctx& c) {
    constexpr int kk = ID >> 2, t = ID & 3;
    gload<kk, t, true>(c, c.vo2[t], c.hsrc + (kk >> 2) * 4096);
}
template <int KS> __device__ __forceinline__ void pin_b(Ctx& c) {
#pragma unroll
    for (int t = 0; t < 4; ++t) { if constexpr (KS >= 16) asm volatile("" : "+v"(c.Bv[KS - 16][t])); }
}
template <int V, int P> __device__ __forceinline__ void epi_piece(Ctx& c) {
    constexpr int pr = P >> 3, st = P & 7, t = pr >> 1, r = 2 * (pr & 1), par = V % 3;
    if constexpr (st == 0) {
        asm volatile("v_mul_f32 %0, 0xbfb8aa3b, %1" : "=v"(c.e0) : "v"(c.ac[par][0][t][r]));
        asm volatile("v_mul_f32 %0, 0xbfb8aa3b, %1" : "=v"(c.e1) : "v"(c.ac[par][0][t][r + 1]));
    } else if constexpr (st == 1) asm volatile("v_exp_f32 %0, %0" : "+v"(c.e0));
    else if constexpr (st == 2) asm volatile("v_exp_f32 %0, %0" : "+v"(c.e1));
    else if constexpr (st == 3) {
        asm volatile("v_add_f32 %0, 1.0, %0" : "+v"(c.e0));
        asm volatile("v_add_f32 %0, 1.0, %0" : "+v"(c.e1));
    } else if constexpr (st == 4) asm volatile("v_rcp_f32 %0, %0" : "+v"(c.e0));
    else if constexpr (st == 5) asm volatile("v_rcp_f32 %0, %0" : "+v"(c.e1));
    else if constexpr (st == 6) {
        asm volatile("v_mul_f32 %0, %0, %1" : "+v"(c.e0) : "v"(c.ac[par][1][t][r]));
        asm volatile("v_mul_f32 %0, %0, %1" : "+v"(c.e1) : "v"(c.ac[par][1][t][r + 1]));
    } else {
        constexpr bool first = (V == 0 || V == 2 || V == 5 || V == 7);
        u32 o;
        asm volatile("v_cvt_pk_bf16_f32 %0, %1, %2" : "=v"(o) : "v"(c.e0), "v"(c.e1));
        c.hp[t][(first ? 0 : 2) + (pr & 1)] = o;
    }
}
template <int V, int T> __device__ __forceinline__ void store_h(Ctx& c) {
    const int m5 = 5 * (c.jj >> 1), ab = c.jj & 1;
    if constexpr (V == 4) {
        const u32x2 d = {c.hp[T][2], c.hp[T][3]};
        __builtin_amdgcn_raw_buffer_store_b64(d, c.hrs, c.vo2[T] + 8 * ab, c.hso + (m5 + 4) * 1024, 16);
    } else {
        const int kstep = (V == 1) ? m5 + 2 * ab : (V == 3) ? m5 + 2 * ab + 1 : (V == 6) ? 10 + 2 * c.jj : 11 + 2 * c.jj;
        __builtin_amdgcn_raw_buffer_store_b128(c.hp[T], c.hrs, c.vo2[T], c.hso + kstep * 1024, 16);
    }
}
template <int PART> __device__ __forceinline__ void poll_check(Ctx& c) {
    asm volatile("" : "+v"(c.pollv));
    const u32 e = c.epoch;
    if (!__all(c.pollv[0] == e && c.pollv[1] == e && c.pollv[2] == e && c.pollv[3] == e)) {
        const u32* p = c.flag_poll + (PART - 1) * 1024 + ((c.lane16 >> 4) & 3);
        poll_eq(p, e);
    }
}
template <int V, int P> __device__ __forceinline__ void epi_piece_padded(Ctx& c) {
    epi_piece<V, P>(c);
    if constexpr ((P & 7) == 2 || (P & 7) == 5) asm volatile("s_nop 0");
}
template <int V, int... P> __device__ __forceinline__ void epi_all(Ctx& c, std::integer_sequence<int, P...>) { (epi_piece_padded<V, P>(c), ...); }

template <int GS> __device__ __forceinline__ void slot(Ctx& c) {
    constexpr bool g1 = GS < G1S;
    constexpr int S = stage_of(GS), R = rel_of(GS), grp = R >> 2, t = R & 3;
    constexpr int fid = GS / 4;
    constexpr bool f01 = GS < F01;
    constexpr int UNIT = f01 ? (grp & 3) >> 1 : S, KS = f01 ? grp >> 2 : grp >> 1, MAT = grp & 1;
    if constexpr (GS == G1S) {
        asm volatile("s_nop 7" ::: "memory");
        epi_all<8>(c, std::make_integer_sequence<int, 64>{});
        store_h<8, 0>(c); store_h<8, 1>(c); store_h<8, 2>(c); store_h<8, 3>(c);
        SCHED();
    }
    if constexpr (f01 && t == 0 && (grp & 3) == 0) { wait_vm<wait_for(GS, seq_b(grp >> 2))>(); pin_b<(grp >> 2)>(c); }
    if constexpr (GS == 8 * 144) { wait_vm<wait_for(GS, seq_poll(1))>(); poll_check<1>(c); }
    if constexpr (!g1 && t == 0 && (grp % 9) == 0) { constexpr int kk = 2 * (S - 9) + grp / 9; wait_vm<wait_for(GS, seq_h(kk))>(); pin_b<kk>(c); }
    if constexpr (dma_id(GS) >= 0) dma_m0<dma_id(GS) / 16, dma_id(GS) % 16>(c);
    if constexpr (g1) {
        mfma<KS, t, (KS == 0)>(c, c.ac[UNIT % 3][MAT][t], c.Aq[fid % 6]);
    } else {
        constexpr int kk = 2 * (S - 9) + grp / 9, du = grp % 9;
        mfma<kk, t, (kk == 0)>(c, c.a2[du][t], c.Aq[fid % 6]);
    }
    if constexpr (barpt(GS)) {
        constexpr int n1 = wait_for(GS, seq_dma_last(S + 1));
        constexpr int n2 = (S == 12) ? wait_for(GS, seq_poll(2)) : 63;
        wait_vm<(n1 < n2 ? n1 : n2)>();
        if constexpr (S == 12) poll_check<2>(c);
        __builtin_amdgcn_s_barrier();
        asm volatile("" ::: "memory");
    }
    if constexpr (pub(GS)) {
        wait_vm<wait_for(GS, seq_store_last(pub(GS)))>();
        if (c.lane16 == 0) flag_set(c.flag_pub + (pub(GS) - 1) * 1024, c.epoch);
    }
    if constexpr (poll_issue(GS)) asm volatile("global_load_dwordx4 %0, %1, %2 sc1" : "=v"(c.pollv) : "v"(0u), "s"(c.flag_poll + (poll_issue(GS) - 1) * 1024) : "memory");
    if constexpr (fid + 5 < 486) {
        constexpr int f = fid + 5;
        if constexpr (f < 72) { if constexpr (t == 0) c.Aq[f % 6] = *(const LAS bf16x8*)(c.rd[(f & 3) >> 1] + (2 * (f >> 2) + (f & 1)) * 1024); }
        else if constexpr (f < 324) { if constexpr (t == 0) c.Aq[f % 6] = *(const LAS bf16x8*)(c.rd[(f / 36) & 3] + (f % 36) * 1024); }
        else if constexpr (t <= 1) {
            constexpr int fs = (f - 324) % 18;
            const s16x4 h = __builtin_amdgcn_ds_read_tr16_b64_v4i16((LAS s16x4*)(c.rd2[(9 + (f - 324) / 18) & 3] + (fs / 9) * 9216 + (fs % 9) * 32 + t * (16 * 288)));
            const u32x2 hh = __builtin_bit_cast(u32x2, h);
            u32x4 a = __builtin_bit_cast(u32x4, c.Aq[f % 6]);
            a[2 * t] = hh[0]; a[2 * t + 1] = hh[1];
            c.Aq[f % 6] = __builtin_bit_cast(bf16x8, a);
        }
    }
    if constexpr (g1) {
        if constexpr (ES.ev[g1 ? GS : 0] >= 0) epi_piece<ES.ev[g1 ? GS : 0], ES.ep[g1 ? GS : 0]>(c);
        if constexpr (ES.sv[g1 ? GS : 0] >= 0) store_h<ES.sv[g1 ? GS : 0], ES.st[g1 ? GS : 0]>(c);
    }
    if constexpr (bload(GS) >= 0) load_b<bload(GS)>(c);
    if constexpr (hload(GS) >= 0) load_h<hload(GS)>(c);
    if constexpr (dma_id(GS) >= 0) dma_go<dma_id(GS) / 16, dma_id(GS) % 16>(c);
    SCHED();
}
template <int BASE, int... I> __device__ __forceinline__ void run_slots(Ctx& c, std::integer_sequence<int, I...>) { (slot<BASE + I>(c), ...); }
template <int S, int P> __device__ __forceinline__ void dma_one(const Ctx& c) { dma_m0<S, P>(c); asm volatile("s_nop 0"); dma_go<S, P>(c); }
template <int S, int... P> __device__ __forceinline__ void dma_stage(const Ctx& c, std::integer_sequence<int, P...>) { (dma_one<S, P>(c), ...); }
template <int... I> __device__ __forceinline__ void load_b_first(Ctx& c, std::integer_sequence<int, I...>) { (load_b<I>(c), ...); }
}

__device__ __forceinline__ void touch(u32& d, const char* p) { asm volatile("global_load_dword %0, %1, off" : "=v"(d) : "v"(p) : "memory"); }
__global__ __launch_bounds__(256, 1) void k_main(const u16* __restrict__ qin, const u16* __restrict__ kin, const u16* __restrict__ vin, const u16* __restrict__ shift_k,
                                                 const u16* __restrict__ ssm_k, const u16* __restrict__ ssm_D, const u16* __restrict__ wg, const u16* __restrict__ wu,
                                                 const u16* __restrict__ wd, const u16* __restrict__ cvec, const u16* __restrict__ w_mod,
                                                 char* __restrict__ ws, u16* __restrict__ out) {
    using namespace mlp;
    extern __shared__ __attribute__((aligned(16))) char smem[];
    const int tid = threadIdx.x, lane = tid & 63, w = __builtin_amdgcn_readfirstlane(tid >> 6);
    const int b = blockIdx.x, x = b & 7, idx = b >> 3, q = x >> 1;
    const int mt = 4 * (x & 1) + (idx & 3), fcl = idx >> 2, fc = 8 * q + fcl;
    const int mt2 = 8 * (x & 1) + (idx & 7), cc = idx >> 3, tg = w & 1, kh = w >> 1;
    const rsrc_t wsr = __builtin_amdgcn_make_buffer_rsrc(ws, 0, (int)WS_END, 0x00020000);
    STAMP_DECL; STAMP_AT(0);
    const u32 epoch_in = __hip_atomic_load(flag_ptr(ws, F_EPOCH), __ATOMIC_RELAXED, __HIP_MEMORY_SCOPE_AGENT);
    unsigned long long pc_start;
    asm volatile("s_getpc_b64 %0" : "=s"(pc_start));
    u32 epoch;
    {
        const int t0 = (x * 32 + idx) * 8;
        RowsPre rp;
        u32 bg[10] = {0, 0, 0, 0, 0, 0, 0, 0, 0, 0};
        if (b < 192) {
            const int c0 = 72 * x + 3 * idx;
            ConvIn in[3];
            KvIn kin_;
            if (w < 3) touch(bg[8], (const char*)(b < 96 ? kin : vin) + (size_t)((b % 96) * 192 + w * 64 + lane) * 128);
            kv_load(kin_, c0, kin, vin, shift_k);
            conv_load_k(in[0], c0, ssm_k, ssm_D); conv_load_k(in[1], c0 + 1, ssm_k, ssm_D); conv_load_k(in[2], c0 + 2, ssm_k, ssm_D);
            rows_load(rp, t0, qin);
            kv_put(kin_, smem + C_RED);
            conv_put_k(in[0], smem); conv_put_k(in[1], smem + C_CH); conv_put_k(in[2], smem + 2 * C_CH);
            __syncthreads();
            STAMP_AT(1);
            kv_compute(kin_, c0, smem + C_RED, smem + C_XR, C_CH);
            epoch = __builtin_amdgcn_readfirstlane(epoch_in) + 1u;
            if (w == 3) {
                DRAIN();
#pragma unroll
                for (int i = 0; i < 8; ++i) { const int l = lane + 64 * (i & 3); touch(bg[i], (const char*)(i < 4 ? wg : wu) + (size_t)b * 27648 + (l < 216 ? l : 0) * 128); }
            }
            STAMP_AT(2);
            ModIn mi;
            const u32* mf = flag_ptr(ws, F_MOD + (lane < 54 ? lane : 0));
            u32 pm;
            asm volatile("global_load_dword %0, %1, off sc1" : "=v"(pm) : "v"(mf) : "memory");
            conv_body(c0, in, smem, wsr, [&]() {
                DRAIN();
                asm volatile("" : "+v"(pm));
                if (!__all(pm == epoch)) poll_eq(mf, epoch);
                mod_load(mi, wsr); } STAMP_ARGS);
            STAMP_AT(5);
            mod_put(mi, smem);
            DRAIN();
#pragma unroll
            for (int i = 0; i < 10; ++i) asm volatile("" :: "v"(bg[i]));
            __syncthreads();
            if (tid < 8) flag_set(flag_ptr(ws, F_CONV + tid * 256 + b), epoch);
            if (w == 3) {
#pragma unroll
                for (int i = 0; i < 4; ++i) { const int l = lane + 64 * i; touch(bg[i], (const char*)wd + (size_t)b * 27648 + (l < 216 ? l : 0) * 128); }
            }
        } else {
            if (w == 3 && idx < 28) {
                const char* cb = (const char*)(pc_start & ~127ull) + lane * 128;
#pragma unroll
                for (int i = 0; i < 8; ++i) touch(bg[i], cb + (i < 7 || lane < 32 ? i * 8192 : 0));
            }
            rows_load(rp, t0, qin);
            if (b - 192 < 54) mod_job(b - 192, cvec, w_mod, ws, (float*)(smem + R_MOD), (float*)(smem + R_MOD + 4096));
            epoch = __builtin_amdgcn_readfirstlane(epoch_in) + 1u;
            DRAIN();
            __syncthreads();
            if (tid == 0 && b - 192 < 54) flag_set(flag_ptr(ws, F_MOD + b - 192), epoch);
            STAMP_AT(1);
            if (b - 192 >= 54) { for (int i = 0; i < 40; ++i) __builtin_amdgcn_s_sleep(2); }
            tile_jobs(b - 192, wg, wu, smem, ws, wsr, epoch);
            STAMP_AT(2); STAMP_AT(3); STAMP_AT(4); STAMP_AT(5);
            poll_eq(flag_ptr(ws, F_MOD + (lane < 54 ? lane : 0)), epoch);
            ModIn mi;
            mod_load(mi, wsr);
            mod_put(mi, smem);
            __syncthreads();
        }
        rows_pre(rp, smem);
        STAMP_AT(6);
        if (w == 0) {
            poll_eq4(wsr, (u32)WS_CNT + (F_CONV + x * 256 + (lane < 48 ? lane : 0) * 4) * 4, epoch);
            rows_fetch(t0, smem, ws);
        }
        __syncthreads();
        STAMP_AT(7);
        rows_main(rp, t0, smem, wsr);
        DRAIN();
#pragma unroll
        for (int i = 0; i < 10; ++i) asm volatile("" :: "v"(bg[i]));
        __syncthreads();
        if (tid == 0) flag_set(flag_ptr(ws, F_ROWS + (t0 >> 6) * 8 + ((t0 >> 3) & 7)), epoch);
        STAMP_AT(8);
    }
    Ctx c;
#pragma unroll
    for (int t = 0; t < 4; ++t) c.hp[t] = (u32x4){0, 0, 0, 0};
    c.lane16 = lane * 16;
#pragma unroll
    for (int t = 0; t < 4; ++t) { c.vo1[t] = lane * 16 + t * 18432; c.vo2[t] = lane * 16 + t * TUS; }
    const LAS char* l0 = (const LAS char*)smem;
#pragma unroll
    for (int i = 0; i < 4; ++i) {
        c.rd[i] = l0 + i * STG + lane * 16; c.rd2[i] = l0 + i * STG + kh * 18432 + (4 * (lane >> 4) + ((lane & 15) >> 2)) * 288 + 8 * (lane & 3);
        asm volatile("" : "+v"(c.rd[i])); asm volatile("" : "+v"(c.rd2[i]));
    }
#pragma unroll
    for (int i = 0; i < 9; ++i) { const int ci = lane + 64 * i, row = (ci * 3641) >> 16, c16 = ci - row * 18; c.voff2[i] = (u32)(row * (D_ * 2) + c16 * 16 - (i & 3) * 1024); }
    c.sub = w & 1;
    c.w1src = ws + WS_WT1 + (size_t)fc * 331776 + w * 9216;
    c.w2src = (const char*)(wd + (size_t)(36 * q + 18 * kh) * 32 * D_ + cc * 144);
    c.bsrc = ws + WS_H2T + (size_t)(16 * mt + 4 * w) * 18432;
    c.hsrc = ws + WS_HT + (size_t)(8 * mt2 + 4 * tg) * TUS + (size_t)((q * 2 + kh) * 18) * 1024;
    c.dmadst = (u32)(uintptr_t)smem + w * 9216;
    c.jj = fcl & 3;
    c.hso = (u32)((16 * mt + 4 * w) * TUS + ((q * 2 + (fcl >> 2)) * 18) * 1024);
    c.hrs = __builtin_amdgcn_make_buffer_rsrc(ws + WS_HT, 0, (int)((size_t)S_ * F_ * 2), 0x00020000);
    c.epoch = epoch;
    c.flag_pub = flag_ptr(ws, F_H + ((((mt * 4 + w) * 4 + q) * 2 + (fcl >> 2))) * 4 + (fcl & 3));
    c.flag_poll = flag_ptr(ws, F_H + (((((mt2 >> 1) * 4 + 2 * (mt2 & 1) + tg) * 4 + q) * 2 + kh)) * 4);
    asm volatile("" ::: "a0", "a1", "a2", "a3", "a4", "a5", "a6", "a7", "a8", "a9", "a10", "a11", "a12", "a13", "a14", "a15", "a16", "a17", "a18", "a19", "a20", "a21", "a22", "a23", "a24", "a25", "a26", "a27", "a28", "a29", "a30", "a31", "a32", "a33", "a34", "a35", "a36", "a37", "a38", "a39", "a40", "a41", "a42", "a43", "a44", "a45", "a46", "a47", "a48", "a49", "a50", "a51", "a52", "a53", "a54", "a55", "a56", "a57", "a58", "a59", "a60", "a61", "a62", "a63", "a64", "a65", "a66", "a67", "a68", "a69", "a70", "a71", "a72", "a73", "a74", "a75", "a76", "a77", "a78", "a79", "a80", "a81", "a82", "a83", "a84", "a85", "a86", "a87", "a88", "a89", "a90", "a91", "a92", "a93", "a94", "a95", "a96", "a97", "a98", "a99", "a100", "a101", "a102", "a103", "a104", "a105", "a106", "a107", "a108", "a109", "a110", "a111", "a112", "a113", "a114", "a115", "a116", "a117", "a118", "a119", "a120", "a121", "a122", "a123", "a124", "a125", "a126", "a127", "a128", "a129", "a130", "a131", "a132", "a133", "a134", "a135", "a136", "a137", "a138", "a139", "a140", "a141", "a142", "a143", "a144", "a145", "a146", "a147", "a148", "a149", "a150", "a151", "a152", "a153", "a154", "a155", "a156", "a157", "a158", "a159", "a160", "a161", "a162", "a163", "a164", "a165", "a166", "a167", "a168", "a169", "a170", "a171", "a172", "a173", "a174", "a175", "a176", "a177", "a178", "a179", "a180", "a181", "a182", "a183", "a184", "a185", "a186", "a187", "a188", "a189", "a190", "a191", "a192", "a193", "a194", "a195", "a196", "a197", "a198", "a199", "a200", "a201", "a202", "a203", "a204", "a205", "a206", "a207", "a208", "a209", "a210", "a211", "a212", "a213", "a214", "a215", "a216", "a217", "a218", "a219", "a220", "a221", "a222", "a223", "a224", "a225", "a226", "a227", "a228", "a229", "a230", "a231", "a232", "a233", "a234", "a235", "a236", "a237", "a238", "a239", "a240", "a241", "a242", "a243", "a244", "a245", "a246", "a247", "a248", "a249", "a250", "a251", "a252", "a253", "a254", "a255");
    poll_eq(flag_ptr(ws, F_TILE + (fc >> 2) * 36 + (lane < 36 ? lane : 0)), epoch);
    dma_stage<0>(c, std::make_integer_sequence<int, 9>{});
    dma_stage<1>(c, std::make_integer_sequence<int, 9>{});
    dma_stage<2>(c, std::make_integer_sequence<int, 9>{});
    poll_eq(flag_ptr(ws, F_ROWS + (4 * mt + w) * 8 + (lane & 7)), epoch);
    STAMP_AT(9);
    load_b_first(c, std::make_integer_sequence<int, 4 * LA>{});
    wait_vm<4 * LA>();
    __builtin_amdgcn_s_barrier();
    asm volatile("" ::: "memory");
#pragma unroll
    for (int f = 0; f < 5; ++f) c.Aq[f] = *(const LAS bf16x8*)(c.rd[(f & 3) >> 1] + (2 * (f >> 2) + (f & 1)) * 1024);
    SCHED();
    STAMP_AT(10);
    run_slots<0>(c, std::make_integer_sequence<int, 288>{});
    STAMP_AT(11);
    run_slots<288>(c, std::make_integer_sequence<int, 144 * 3>{});
    run_slots<144 * 5>(c, std::make_integer_sequence<int, 144 * 4>{});
    STAMP_AT(12);
    run_slots<G1S>(c, std::make_integer_sequence<int, 324>{});
    run_slots<G1S + 324>(c, std::make_integer_sequence<int, 324>{});
    asm volatile("s_nop 7" ::: "memory");
    SCHED();
    STAMP_AT(13);
    const int n = lane & 15, g = lane >> 4;
    u32x2 xr[3][2]; f32x4 gb[3];
#pragma unroll
    for (int i = 0; i < 3; ++i) {
        const int u = (w + 4 * i < 9) ? w + 4 * i : 8, d = 144 * cc + 16 * u + 4 * g;
#pragma unroll
        for (int t = 0; t < 2; ++t) xr[i][t] = __builtin_amdgcn_raw_buffer_load_b64(wsr, (u32)WS_X1 + (u32)((128 * mt2 + 32 * q + 16 * t + n) * D_ + d) * 2, 0, 16);
        gb[i] = *(const f32x4*)(smem + R_MOD + (5 * 576 + d) * 4);
    }
    __builtin_amdgcn_s_barrier();
    {
        char* ex = smem + w * 18432 + lane * 16;
#pragma unroll
        for (int t = 0; t < 2; ++t)
#pragma unroll
            for (int u = 0; u < 9; ++u) *(f32x4*)(ex + (t * 9 + u) * 1024) = kh ? c.a2[u][t] : c.a2[u][2 + t];
    }
    __syncthreads();
    const int j = 2 * tg + kh;
    u32* fred = flag_ptr(ws, F_RED + (mt2 * 4 + cc) * 16);
    const u32 slab = (u32)WS_SLAB + (u32)((mt2 * 4 + cc) * 16) * 9216;
    {
        const char* ey = smem + (w ^ 2) * 18432 + lane * 16;
        char* own = smem + 73728 + lane * 16;
#pragma unroll
        for (int u = 0; u < 9; ++u) {
            const f32x4 r0 = (kh ? c.a2[u][2] : c.a2[u][0]) + *(const f32x4*)(ey + u * 1024);
            const f32x4 r1 = (kh ? c.a2[u][3] : c.a2[u][1]) + *(const f32x4*)(ey + (9 + u) * 1024);
            if (j != q) st16_dev(wsr, slab + (j * 4 + q) * 9216 + u * 1024 + lane * 16, (u32x4){pk2(r0[0], r0[1]), pk2(r0[2], r0[3]), pk2(r1[0], r1[1]), pk2(r1[2], r1[3])});
            else { *(f32x4*)(own + u * 2048) = r0; *(f32x4*)(own + u * 2048 + 1024) = r1; }
        }
    }
    if (j != q) {
        DRAIN();
        if (lane == 0) flag_set(fred + j * 4 + q, epoch);
    }
    STAMP_AT(14);
    poll_eq(fred + q * 4 + (((lane & 3) == q) ? (q ^ 1) : (lane & 3)), epoch);
    __syncthreads();
    u32x4 rem[3][3];
#pragma unroll
    for (int i = 0; i < 3; ++i) {
        const int u = (w + 4 * i < 9) ? w + 4 * i : 8;
#pragma unroll
        for (int k = 0; k < 3; ++k) { const int qq = k + (k >= q ? 1 : 0); rem[i][k] = __builtin_amdgcn_raw_buffer_load_b128(wsr, slab + (q * 4 + qq) * 9216 + u * 1024 + lane * 16, 0, 16); }
    }
#pragma unroll
    for (int i = 0; i < 3; ++i) {
        const int u = w + 4 * i;
        if (u < 9) {
            const int d = 144 * cc + 16 * u + 4 * g;
#pragma unroll
            for (int t = 0; t < 2; ++t) {
                f32x4 s = *(const f32x4*)(smem + 73728 + u * 2048 + t * 1024 + lane * 16);
#pragma unroll
                for (int k = 0; k < 3; ++k) {
                    const u32 a = rem[i][k][2 * t], bq = rem[i][k][2 * t + 1];
                    s += (f32x4){bf_lo(a), bf_hi(a), bf_lo(bq), bf_hi(bq)};
                }
                const float o0 = bf_lo(xr[i][t][0]) + rbf(gb[i][0] * rbf(s[0])), o1 = bf_hi(xr[i][t][0]) + rbf(gb[i][1] * rbf(s[1]));
                const float o2 = bf_lo(xr[i][t][1]) + rbf(gb[i][2] * rbf(s[2])), o3 = bf_hi(xr[i][t][1]) + rbf(gb[i][3] * rbf(s[3]));
                *(u32x2*)(out + (size_t)(128 * mt2 + 32 * q + 16 * t + n) * D_ + d) = (u32x2){pk2(o0, o1), pk2(o2, o3)};
            }
        }
    }
    if (b == 0 && tid == 0) flag_set(flag_ptr(ws, F_EPOCH), epoch);
    STAMP_AT(15);
    STAMP_OUT(4, 16);
}

extern "C" void kernel_launch(void* const* d_in, const int* in_sizes, int n_in, void* d_out, int out_size, void* d_ws, size_t ws_size,
                              hipStream_t stream) {
    const u16* q = (const u16*)d_in[0];
    const u16* k = (const u16*)d_in[1];
    const u16* v = (const u16*)d_in[2];
    const u16* shift_k = (const u16*)d_in[3];
    const u16* ssm_k = (const u16*)d_in[4];
    const u16* ssm_D = (const u16*)d_in[5];
    const u16* wg = (const u16*)d_in[7];
    const u16* wu = (const u16*)d_in[8];
    const u16* wd = (const u16*)d_in[9];
    const u16* c = (const u16*)d_in[10];
    const u16* w_mod = (const u16*)d_in[11];
    char* ws = (char*)d_ws;
    (void)hipFuncSetAttribute((const void*)k_main, hipFuncAttributeMaxDynamicSharedMemorySize, mlp::LDS_BYTES);
    k_main<<<256, 256, mlp::LDS_BYTES, stream>>>(q, k, v, shift_k, ssm_k, ssm_D, wg, wu, wd, c, w_mod, ws, (u16*)d_out);
}
```

```cpp
#include <hip/hip_runtime.h>
#include <stdint.h>
#include <utility>

typedef unsigned short u16;
typedef unsigned int u32;
typedef __bf16 bf16x8 __attribute__((ext_vector_type(8)));
typedef __bf16 bf16x2 __attribute__((ext_vector_type(2)));
typedef float f32x4 __attribute__((ext_vector_type(4)));
typedef float f32x2 __attribute__((ext_vector_type(2)));
typedef u32 u32x4 __attribute__((ext_vector_type(4)));
typedef u32 u32x2 __attribute__((ext_vector_type(2)));
typedef short s16x4 __attribute__((ext_vector_type(4)));
#define LAS __attribute__((address_space(3)))
typedef LAS char* lptr;

constexpr int S_ = 2048, D_ = 576, F_ = 4608;

constexpr size_t WS_MOD = 0;
constexpr size_t WS_CNT = 65536;
constexpr size_t WS_YYT = WS_CNT + 65536;
constexpr size_t WS_X1  = WS_YYT + (size_t)D_ * S_ * 2;
constexpr size_t WS_H2T = WS_X1 + (size_t)D_ * S_ * 2;
constexpr size_t WS_HT  = WS_H2T + (size_t)D_ * S_ * 2;
constexpr size_t WS_WT1 = WS_HT + (size_t)S_ * F_ * 2;
constexpr size_t WS_SLAB = WS_WT1 + (size_t)2 * D_ * F_ * 2;
constexpr size_t WS_END = WS_SLAB + (size_t)16 * 4 * 4 * 4 * 9216;

#define STAMP_DECL
#define STAMP_ARGS
#define STAMP_AT(i)
#define STAMP_OUT(kid, n)
__device__ __forceinline__ float bf_lo(u32 w) { return __uint_as_float(w << 16); }
__device__ __forceinline__ float bf_hi(u32 w) { return __uint_as_float(w & 0xffff0000u); }
__device__ __forceinline__ float bf1(u16 w) { return __uint_as_float((u32)w << 16); }
__device__ __forceinline__ u32 pk2(float lo, float hi) {
    f32x2 v = {lo, hi};
    return __builtin_bit_cast(u32, __builtin_convertvector(v, bf16x2));
}
__device__ __forceinline__ float rbf(float x) { return (float)(__bf16)x; }
__device__ __forceinline__ float sigmoidf_(float x) { return __builtin_amdgcn_rcpf(1.0f + __builtin_amdgcn_exp2f(-1.4426950408889634f * x)); }

constexpr int F_MOD = 256;
constexpr int F_TILE = 512;
constexpr int F_CONV = 1024;
constexpr int F_ROWS = 3072;
constexpr int F_H = 3328;
constexpr int F_RED = 5376;
constexpr int F_EPOCH = 6400;
typedef __amdgpu_buffer_rsrc_t rsrc_t;
__device__ __forceinline__ u32* flag_ptr(char* ws, int idx) { return (u32*)(ws + WS_CNT) + idx; }
__device__ __forceinline__ void flag_set(u32* p, u32 e) { __hip_atomic_store(p, e, __ATOMIC_RELAXED, __HIP_MEMORY_SCOPE_AGENT); }
__device__ __forceinline__ void st16_dev(rsrc_t rs, u32 off, u32x4 v) { __builtin_amdgcn_raw_buffer_store_b128(v, rs, off, 0, 16); }
__device__ __forceinline__ u32x4 ld16_dev(rsrc_t rs, u32 off) { return __builtin_amdgcn_raw_buffer_load_b128(rs, off, 0, 16); }
__device__ __forceinline__ void poll_eq(const u32* p, u32 e) {
    for (int it = 0; it < (1 << 21); ++it) {
        if (__all(__hip_atomic_load(p, __ATOMIC_RELAXED, __HIP_MEMORY_SCOPE_AGENT) == e)) break;
        __builtin_amdgcn_s_sleep(1);
    }
}
__device__ __forceinline__ void poll_eq4(rsrc_t rs, u32 off, u32 e) {
    for (int it = 0; it < (1 << 21); ++it) {
        asm volatile("" ::: "memory");
        const u32x4 v = ld16_dev(rs, off);
        if (__all(v[0] == e && v[1] == e && v[2] == e && v[3] == e)) break;
        __builtin_amdgcn_s_sleep(1);
    }
}
#define DRAIN() asm volatile("s_waitcnt vmcnt(0)" ::: "memory")

#define SCHED() __builtin_amdgcn_sched_barrier(0)
__device__ __forceinline__ float dpp_add(float v, const int ctrl_sel) {
    int r;
    const int iv = __builtin_bit_cast(int, v);
    switch (ctrl_sel) {
        case 0: r = __builtin_amdgcn_update_dpp(0, iv, 0xB1, 0xf, 0xf, true); break;
        case 1: r = __builtin_amdgcn_update_dpp(0, iv, 0x4E, 0xf, 0xf, true); break;
        case 2: r = __builtin_amdgcn_update_dpp(0, iv, 0x141, 0xf, 0xf, true); break;
        default: r = __builtin_amdgcn_update_dpp(0, iv, 0x140, 0xf, 0xf, true); break;
    }
    return v + __builtin_bit_cast(float, r);
}
__device__ __forceinline__ float half_sum(float v) {
    v = dpp_add(v, 0); v = dpp_add(v, 1); v = dpp_add(v, 2); v = dpp_add(v, 3);
    auto r = __builtin_amdgcn_permlane16_swap(__float_as_uint(v), __float_as_uint(v), false, false);
    return __uint_as_float(r[0]) + __uint_as_float(r[1]);
}

struct KvIn { u32x2 kw[8], vw[8]; u32x4 sw; u32x2 sw2; };
__device__ __forceinline__ void kv_load(KvIn& p, int c0, const u16* __restrict__ k, const u16* __restrict__ v, const u16* __restrict__ shift_k) {
    const int cw = c0 & ~1;
#pragma unroll
    for (int i = 0; i < 8; ++i) p.kw[i] = *(const u32x2*)(k + (size_t)(threadIdx.x + 256 * i) * D_ + cw);
#pragma unroll
    for (int i = 0; i < 8; ++i) p.vw[i] = *(const u32x2*)(v + (size_t)(threadIdx.x + 256 * i) * D_ + cw);
    p.sw = (u32x4){*(const u32*)(shift_k + c0 * 4), *(const u32*)(shift_k + c0 * 4 + 2), *(const u32*)(shift_k + c0 * 4 + 4), *(const u32*)(shift_k + c0 * 4 + 6)};
    p.sw2 = (u32x2){*(const u32*)(shift_k + c0 * 4 + 8), *(const u32*)(shift_k + c0 * 4 + 10)};
}
constexpr int KV_RAWV = 257 * 72 + 8;
__device__ __forceinline__ void kv_put(const KvIn& p, char* raw) {
    const int tid = threadIdx.x;
#pragma unroll
    for (int i = 0; i < 8; ++i) {
        const int t = tid + 256 * i, o = 72 * (t >> 3) + 8 * (t & 7);
        *(u32x2*)(raw + 72 + o) = p.kw[i];
        *(u32x2*)(raw + KV_RAWV + o) = p.vw[i];
    }
    if (tid < 8) *(u32x2*)(raw + 8 * tid) = (u32x2){0, 0};
}
__device__ __forceinline__ void kv_compute(const KvIn& p, int c0, const char* raw, char* xr, int stride) {
    const int tid = threadIdx.x;
    u32x2 kw[11], vw[8];
#pragma unroll
    for (int i = 0; i < 11; ++i) kw[i] = *(const u32x2*)(raw + 72 * tid + (i < 3 ? 40 + 8 * i : 72 + 8 * (i - 3)));
#pragma unroll
    for (int i = 0; i < 8; ++i) vw[i] = *(const u32x2*)(raw + KV_RAWV + 72 * tid + 8 * i);
    const bool odd = c0 & 1;
#pragma unroll
    for (int c = 0; c < 3; ++c) {
        const u32 wa = c == 0 ? p.sw[0] : c == 1 ? p.sw[2] : p.sw2[0], wb = c == 0 ? p.sw[1] : c == 1 ? p.sw[3] : p.sw2[1];
        const float w0 = bf_lo(wa), w1 = bf_hi(wa), w2 = bf_lo(wb), w3 = bf_hi(wb);
        float kf[11], r[8];
#define KV_PICK(d) (c == 0 ? (odd ? bf_hi((d)[0]) : bf_lo((d)[0])) : c == 1 ? (odd ? bf_lo((d)[1]) : bf_hi((d)[0])) : (odd ? bf_hi((d)[1]) : bf_lo((d)[1])))
#pragma unroll
        for (int i = 0; i < 11; ++i) kf[i] = KV_PICK(kw[i]);
#pragma unroll
        for (int i = 0; i < 8; ++i) r[i] = (kf[i] * w3 + kf[i + 1] * w2 + kf[i + 2] * w1 + kf[i + 3] * w0) * KV_PICK(vw[i]);
#undef KV_PICK
        *(u32x4*)(xr + c * stride + 2 * (2040 - 8 * tid)) = (u32x4){pk2(r[7], r[6]), pk2(r[5], r[4]), pk2(r[3], r[2]), pk2(r[1], r[0])};
    }
}

__device__ __forceinline__ void mod_job(int job, const u16* __restrict__ c, const u16* __restrict__ w_mod, char* __restrict__ ws, float* sc  , float* red  ) {
    const int tid = threadIdx.x, lane = tid & 63, wid = tid >> 6;
    const int col0 = job * 64, rg = tid >> 3, c8 = tid & 7;
    u32x4 w[18];
#pragma unroll
    for (int i = 0; i < 18; ++i) w[i] = *(const u32x4*)(w_mod + (size_t)(rg + 32 * i) * 3456 + col0 + c8 * 8);
#pragma unroll
    for (int j = 0; j < 3; ++j) { const int r = tid + 256 * j; if (r < 576) { const float x = bf1(c[r]); sc[r] = rbf(x * sigmoidf_(x)); } }
    __syncthreads();
    float acc[8] = {0, 0, 0, 0, 0, 0, 0, 0};
#pragma unroll
    for (int i = 0; i < 18; ++i) {
        const float s = sc[rg + 32 * i];
#pragma unroll
        for (int j = 0; j < 4; ++j) { acc[2 * j] += s * bf_lo(w[i][j]); acc[2 * j + 1] += s * bf_hi(w[i][j]); }
    }
#pragma unroll
    for (int j = 0; j < 8; ++j) {
        acc[j] += __shfl_xor(acc[j], 8); acc[j] += __shfl_xor(acc[j], 16); acc[j] += __shfl_xor(acc[j], 32);
    }
    if (lane < 8) {
#pragma unroll
        for (int j = 0; j < 8; ++j) red[wid * 64 + lane * 8 + j] = acc[j];
    }
    __syncthreads();
    if (tid < 64) {
        float m = rbf((red[tid] + red[64 + tid]) + (red[128 + tid] + red[192 + tid]));
        const int which = col0 / 576;
        if (which == 1 || which == 4) m = rbf(1.0f + m);
        __hip_atomic_store((u32*)(ws + WS_MOD) + col0 + tid, __float_as_uint(m), __ATOMIC_RELAXED, __HIP_MEMORY_SCOPE_AGENT);
    }
}

__device__ __forceinline__ const char* tile_base(int job, const u16* __restrict__ wg, const u16* __restrict__ wu) {
    const int ks = job % 18, mat = (job / 18) & 1, cg = job / 36;
    return (const char*)((mat ? wu : wg) + (size_t)(32 * ks) * F_ + cg * 576);
}
__device__ __forceinline__ void tile_dma(int job, const u16* __restrict__ wg, const u16* __restrict__ wu, char* tile, const u32 (&voff)[9]) {
    const int w = __builtin_amdgcn_readfirstlane(threadIdx.x >> 6);
    const char* base = tile_base(job, wg, wu) + (size_t)w * 8 * (F_ * 2);
    const u32 dst = (u32)(uintptr_t)tile + w * 9216;
#pragma unroll
    for (int i = 0; i < 9; ++i)
        asm volatile("s_mov_b32 m0, %2\n\ts_nop 0\n\tglobal_load_lds_dwordx4 %0, %1" :: "v"(voff[i]), "s"(base), "s"(dst + i * 1024) : "memory");
}
__device__ __forceinline__ void tile_out(int job, const char* tile, rsrc_t wsr) {
    const int lane = threadIdx.x & 63, w = threadIdx.x >> 6;
    const int ks = job % 18, mat = (job / 18) & 1, fc = 4 * (job / 36) + w;
    const int g = lane >> 4, q4 = (lane & 15) >> 2, p = lane & 3;
    const LAS char* rd = (const LAS char*)tile + (4 * g + q4) * 1152 + 8 * p + w * 288;
#pragma unroll
    for (int u = 0; u < 9; ++u) {
        const int pos = (fc & 1) ? (u == 0 ? 4 : u <= 4 ? u - 1 : u) : (u == 8 ? 4 : u < 4 ? u : u + 1);
        const s16x4 lo = __builtin_amdgcn_ds_read_tr16_b64_v4i16((LAS s16x4*)(rd + u * 32));
        const s16x4 hi = __builtin_amdgcn_ds_read_tr16_b64_v4i16((LAS s16x4*)(rd + u * 32 + 16 * 1152));
        typedef short s16x8 __attribute__((ext_vector_type(8)));
        const s16x8 r = __builtin_shufflevector(lo, hi, 0, 1, 2, 3, 4, 5, 6, 7);
        st16_dev(wsr, (u32)WS_WT1 + fc * 331776 + pos * 36864 + (ks * 2 + mat) * 1024 + lane * 16, __builtin_bit_cast(u32x4, r));
    }
}
__device__ __forceinline__ void tile_jobs(int i0, const u16* __restrict__ wg, const u16* __restrict__ wu, char* smem, char* ws, rsrc_t wsr, u32 epoch) {
    const int lane = threadIdx.x & 63;
    u32 voff[9];
#pragma unroll
    for (int i = 0; i < 9; ++i) { const int ci = lane + 64 * i, row = (ci * 58255) >> 22, c16 = ci - row * 72; voff[i] = (u32)(row * (F_ * 2) + c16 * 16); }
#pragma unroll
    for (int j = 0; j < 4; ++j) tile_dma(i0 + 64 * j, wg, wu, smem + j * 36864, voff);
#pragma unroll
    for (int j = 0; j < 4; ++j) {
        asm volatile("s_waitcnt vmcnt(27)" ::: "memory");
        __syncthreads();
        tile_out(i0 + 64 * j, smem + j * 36864, wsr);
    }
    if (i0 < 32) {
        __syncthreads();
        tile_dma(i0 + 256, wg, wu, smem, voff);
        asm volatile("s_waitcnt vmcnt(0)" ::: "memory");
        __syncthreads();
        tile_out(i0 + 256, smem, wsr);
    }
    DRAIN();
    __syncthreads();
    if (threadIdx.x < 5 && i0 + 64 * threadIdx.x < 288) flag_set(flag_ptr(ws, F_TILE + i0 + 64 * threadIdx.x), epoch);
}

constexpr int C_XR = 0;
constexpr int C_K0 = 4608;
constexpr int C_K1 = C_K0 + 4128 + 32;
constexpr int C_CH = C_K1 + 4160;
constexpr int C_RED = 3 * C_CH;
constexpr int C_LDS = C_RED + 3 * 32768;
static_assert(((C_K1 - C_K0) / 4) % 32 == 16, "bank offset");
static_assert(C_CH % 16 == 0 && C_K0 % 16 == 0 && C_K1 % 16 == 0, "align");
template <bool ZERO> __device__ __forceinline__ void mfma_v(f32x4& c, const bf16x8& a, const bf16x8& b) {
    if constexpr (ZERO) asm volatile("v_mfma_f32_16x16x32_bf16 %0, %1, %2, 0" : "=v"(c) : "v"(a), "v"(b));
    else asm volatile("v_mfma_f32_16x16x32_bf16 %0, %1, %2, %0" : "+v"(c) : "v"(a), "v"(b));
}
struct ConvSet { bf16x8 A[8], B[8]; };
template <int J> __device__ __forceinline__ void conv_ld(ConvSet& s, const LAS char* ab, const LAS char* bb) {
    if constexpr (J < 8) {
        const LAS u32* p = (const LAS u32*)(ab + 512 * J);
        u32x4 t = {p[0], p[1], p[2], p[3]};
        s.A[J] = __builtin_bit_cast(bf16x8, t);
    } else s.B[J - 8] = *(const LAS bf16x8*)(bb + 512 * (15 - J));
}
template <int M> struct TriIdx {
    static constexpr int find(bool want_i) { int m = 0; for (int s = 0; s < 8; ++s) for (int j = 0; j <= s; ++j) { if (m == M) return want_i ? s : j; ++m; } return 0; }
    static constexpr int i = find(true), j = find(false);
};
template <int M, bool PF, bool FIRST> __device__ __forceinline__ void conv_slot(f32x4 (&acc)[8], const ConvSet& cur, ConvSet& nxt, const LAS char* ab, const LAS char* bb) {
    mfma_v<(FIRST && TriIdx<M>::j == 0)>(acc[TriIdx<M>::i], cur.A[TriIdx<M>::j], cur.B[TriIdx<M>::i - TriIdx<M>::j]);
    if constexpr (PF && (M & 1) == 0 && M / 2 < 16) conv_ld<((M / 2) & 1) * 8 + (M / 4)>(nxt, ab, bb);
    SCHED();
}
template <bool PF, bool FIRST, int... M> __device__ __forceinline__ void conv_step(f32x4 (&acc)[8], const ConvSet& cur, ConvSet& nxt, const LAS char* ab, const LAS char* bb, std::integer_sequence<int, M...>) {
    (conv_slot<M, PF, FIRST>(acc, cur, nxt, ab, bb), ...);
}
template <int... J> __device__ __forceinline__ void conv_ld_all(ConvSet& s, const LAS char* ab, const LAS char* bb, std::integer_sequence<int, J...>) { (conv_ld<J>(s, ab, bb), ...); }

struct ConvIn { u32x4 k; u32 kn; u16 dv; };
__device__ __forceinline__ void conv_load_k(ConvIn& in, int d, const u16* __restrict__ ssm_k, const u16* __restrict__ ssm_D) {
    const int tid = threadIdx.x;
    in.k = *(const u32x4*)(ssm_k + (size_t)d * S_ + tid * 8);
    in.kn = *(const u32*)(ssm_k + (size_t)d * S_ + (tid < 255 ? tid * 8 + 8 : 0));
    in.dv = ssm_D[d];
}
__device__ __forceinline__ void conv_put_k(const ConvIn& in, char* sm) {
    const int tid = threadIdx.x;
    const u32 kn = tid < 255 ? in.kn : 0u;
    *(u32x4*)(sm + C_K0 + 32 + tid * 16) = in.k;
    *(u32x4*)(sm + C_K1 + 32 + tid * 16) = (u32x4){__builtin_amdgcn_alignbit(in.k[1], in.k[0], 16), __builtin_amdgcn_alignbit(in.k[2], in.k[1], 16),
                                                 __builtin_amdgcn_alignbit(in.k[3], in.k[2], 16), __builtin_amdgcn_alignbit(kn, in.k[3], 16)};
    if (tid < 32) *(u32x4*)(sm + C_XR + 4096 + tid * 16) = (u32x4){0, 0, 0, 0};
    if (tid == 0) {
        *(u32x4*)(sm + C_K0) = (u32x4){0, 0, 0, 0}; *(u32x4*)(sm + C_K0 + 16) = (u32x4){0, 0, 0, 0};
        *(u32x4*)(sm + C_K1) = (u32x4){0, 0, 0, 0}; *(u32x4*)(sm + C_K1 + 16) = (u32x4){0, 0, 0, in.k[0] << 16};
    }
}
__device__ __forceinline__ void conv_finish(int d, const char* sm, const char* red, float Dv, rsrc_t wsr) {
    const int tid = threadIdx.x;
    const int i = tid >> 5, n = tid & 15, gp = (tid >> 4) & 1;
    f32x4 lo = {0.f, 0.f, 0.f, 0.f}, hi = {0.f, 0.f, 0.f, 0.f};
#pragma unroll
    for (int w = 0; w < 4; ++w) {
        lo += *(const f32x4*)(red + (w * 8 + i) * 1024 + (n + 32 * gp) * 16);
        hi += *(const f32x4*)(red + (w * 8 + i) * 1024 + (n + 32 * gp + 16) * 16);
    }
    const int s = 256 * i + 16 * n + 8 * gp;
    const u32x4 xw = *(const u32x4*)(sm + C_XR + 2 * (2040 - s));
    const float o0 = rbf(lo[0]) + rbf(bf_hi(xw[3]) * Dv), o1 = rbf(lo[1]) + rbf(bf_lo(xw[3]) * Dv), o2 = rbf(lo[2]) + rbf(bf_hi(xw[2]) * Dv), o3 = rbf(lo[3]) + rbf(bf_lo(xw[2]) * Dv);
    const float o4 = rbf(hi[0]) + rbf(bf_hi(xw[1]) * Dv), o5 = rbf(hi[1]) + rbf(bf_lo(xw[1]) * Dv), o6 = rbf(hi[2]) + rbf(bf_hi(xw[0]) * Dv), o7 = rbf(hi[3]) + rbf(bf_lo(xw[0]) * Dv);
    st16_dev(wsr, (u32)WS_YYT + (u32)(d * S_ + s) * 2, (u32x4){pk2(o0, o1), pk2(o2, o3), pk2(o4, o5), pk2(o6, o7)});
}
template <class Hook>
__device__ __forceinline__ void conv_body(int ch0, const ConvIn (&in)[3], char* smem, rsrc_t wsr, Hook hook
                                          ) {
    const int lane = threadIdx.x & 63, wid = __builtin_amdgcn_readfirstlane(threadIdx.x >> 6);
    __syncthreads();
    STAMP_AT(3);
    const int n = lane & 15, g = lane >> 4, dd = g >> 1, gh = g & 1;
    const LAS char* l0 = (const LAS char*)smem;
    const LAS char* bb = l0 + C_XR + 480 - 32 * n + 32 * dd + 16 * gh + 128 * wid;
    const LAS char* ab = l0 + ((n & 1) ? C_K0 + 2 * (16 * dd + 8 * gh + n + 1) : C_K1 + 2 * (16 * dd + 8 * gh + n)) + 128 * wid;
    f32x4 acc[3][8];
    ConvSet s0, s1;
    conv_ld_all(s0, ab, bb, std::make_integer_sequence<int, 16>{});
    SCHED();
    constexpr auto SQ = std::make_integer_sequence<int, 36>{};
    conv_step<true, true>(acc[0], s0, s1, ab + 64, bb + 64, SQ);
    conv_step<true, false>(acc[0], s1, s0, ab + C_CH, bb + C_CH, SQ);
    conv_step<true, true>(acc[1], s0, s1, ab + C_CH + 64, bb + C_CH + 64, SQ);
    conv_step<true, false>(acc[1], s1, s0, ab + 2 * C_CH, bb + 2 * C_CH, SQ);
    conv_step<true, true>(acc[2], s0, s1, ab + 2 * C_CH + 64, bb + 2 * C_CH + 64, SQ);
    conv_step<false, false>(acc[2], s1, s0, ab, bb, SQ);
    asm volatile("s_nop 7" ::: "memory");
    STAMP_AT(4);
    hook();
#pragma unroll
    for (int c = 0; c < 3; ++c)
#pragma unroll
        for (int i = 0; i < 8; ++i) *(f32x4*)(smem + C_RED + c * 32768 + (wid * 8 + i) * 1024 + lane * 16) = acc[c][i];
    __syncthreads();
#pragma unroll
    for (int c = 0; c < 3; ++c) conv_finish(ch0 + c, smem + c * C_CH, smem + C_RED + c * 32768, bf1(in[c].dv), wsr);
}

constexpr int R_MOD = 147456;
constexpr int R_YT = 110592;
constexpr int R_HT = R_YT + 8 * 1168;
struct RowsPre { float x[18], gh[18]; u32 xw[9]; };
__device__ __forceinline__ void rows_load(RowsPre& r, int t0, const u16* __restrict__ q) {
    const int tid = threadIdx.x, lane = tid & 63, hl = lane & 31, row = 2 * (tid >> 6) + (lane >> 5);
    const u32* qrow = (const u32*)(q + (size_t)(t0 + row) * D_);
#pragma unroll
    for (int i = 0; i < 9; ++i) r.xw[i] = qrow[hl + 32 * i];
}
struct ModIn { u32x4 mw[4]; };
__device__ __forceinline__ void mod_load(ModIn& m, rsrc_t wsr) {
#pragma unroll
    for (int i = 0; i < 4; ++i) { const int j = threadIdx.x + 256 * i; m.mw[i] = ld16_dev(wsr, (u32)WS_MOD + (j < 864 ? j : 0) * 16); }
}
__device__ __forceinline__ void mod_put(const ModIn& m, char* sm) {
#pragma unroll
    for (int i = 0; i < 4; ++i) { const int j = threadIdx.x + 256 * i; if (j < 864) *(u32x4*)(sm + R_MOD + j * 16) = m.mw[i]; }
}
__device__ __forceinline__ void rows_pre(RowsPre& r, const char* sm) {
    const int hl = threadIdx.x & 31;
#pragma unroll
    for (int i = 0; i < 9; ++i) { r.x[2 * i] = bf_lo(r.xw[i]); r.x[2 * i + 1] = bf_hi(r.xw[i]); }
    float s = 0.f;
#pragma unroll
    for (int i = 0; i < 18; ++i) s += r.x[i];
    const float mean = half_sum(s) * (1.0f / 576.0f);
    float vs = 0.f;
#pragma unroll
    for (int i = 0; i < 18; ++i) { const float dl = r.x[i] - mean; vs += dl * dl; }
    const float rstd = __builtin_amdgcn_rsqf(half_sum(vs) * (1.0f / 576.0f) + 1e-5f);
    const float* mod = (const float*)(sm + R_MOD);
#pragma unroll
    for (int i = 0; i < 9; ++i) {
        const int c = 2 * (hl + 32 * i);
        const f32x2 sa = *(const f32x2*)(mod + c), c1 = *(const f32x2*)(mod + 576 + c), ga = *(const f32x2*)(mod + 2 * 576 + c);
        r.gh[2 * i] = ga[0] * ((r.x[2 * i] - mean) * rstd * c1[0] + sa[0]);
        r.gh[2 * i + 1] = ga[1] * ((r.x[2 * i + 1] - mean) * rstd * c1[1] + sa[1]);
    }
}
__device__ __forceinline__ void rows_fetch(int t0, char* sm, const char* ws) {
    const int lane = threadIdx.x & 63;
    const char* src = ws + WS_YYT + t0 * 2;
    const u32 dst = (u32)(uintptr_t)(sm + R_HT);
#pragma unroll
    for (int i = 0; i < 9; ++i)
        asm volatile("s_mov_b32 m0, %2\n\ts_nop 0\n\tglobal_load_lds_dwordx4 %0, %1 sc1" :: "v"((u32)((lane + 64 * i) * (S_ * 2))), "s"(src), "s"(dst + i * 1024) : "memory");
    asm volatile("s_waitcnt vmcnt(0)" ::: "memory");
}
__device__ __forceinline__ void rows_main(const RowsPre& r, int t0, char* sm, rsrc_t wsr) {
    const int tid = threadIdx.x, lane = tid & 63, hl = lane & 31, row = 2 * (tid >> 6) + (lane >> 5);
    const int cg = tid >> 1, th = tid & 1;
    if (tid < 144) {
        u32x2 yw[8];
#pragma unroll
        for (int c = 0; c < 8; ++c) yw[c] = *(const u32x2*)(sm + R_HT + (8 * cg + c) * 16 + th * 8);
#pragma unroll
        for (int t = 0; t < 4; ++t) {
            u32x4 o;
#pragma unroll
            for (int kk = 0; kk < 4; ++kk) {
                const u32 a = yw[2 * kk][t >> 1], bq = yw[2 * kk + 1][t >> 1];
                o[kk] = (t & 1) ? __builtin_amdgcn_perm(bq, a, 0x07060302u) : __builtin_amdgcn_perm(bq, a, 0x05040100u);
            }
            *(u32x4*)(sm + R_YT + (4 * th + t) * 1168 + cg * 16) = o;
        }
    }
    __syncthreads();
    const float* mod = (const float*)(sm + R_MOD);
    float x1[18];
#pragma unroll
    for (int i = 0; i < 9; ++i) {
        u32* yp = (u32*)(sm + R_YT + row * 1168 + (hl + 32 * i) * 4);
        const u32 yv = *yp;
        const u32 p = pk2(r.x[2 * i] + r.gh[2 * i] * bf_lo(yv), r.x[2 * i + 1] + r.gh[2 * i + 1] * bf_hi(yv));
        *yp = p;
        x1[2 * i] = bf_lo(p); x1[2 * i + 1] = bf_hi(p);
    }
    float s = 0.f;
#pragma unroll
    for (int i = 0; i < 18; ++i) s += x1[i];
    const float mean = half_sum(s) * (1.0f / 576.0f);
    float vs = 0.f;
#pragma unroll
    for (int i = 0; i < 18; ++i) { const float dl = x1[i] - mean; vs += dl * dl; }
    const float rstd = __builtin_amdgcn_rsqf(half_sum(vs) * (1.0f / 576.0f) + 1e-5f);
#pragma unroll
    for (int i = 0; i < 9; ++i) {
        const int c = 2 * (hl + 32 * i);
        const f32x2 sb = *(const f32x2*)(mod + 3 * 576 + c), c1 = *(const f32x2*)(mod + 4 * 576 + c);
        const float h0 = (x1[2 * i] - mean) * rstd * c1[0] + sb[0], h1 = (x1[2 * i + 1] - mean) * rstd * c1[1] + sb[1];
        const int ks = c >> 5, kk = c & 31, gq = (kk & 15) >> 2, j = ((kk >> 4) << 2) | (kk & 3);
        *(u32*)(sm + R_HT + ((ks * 4 + gq) * 8 + row) * 16 + j * 2) = pk2(h0, h1);
    }
    __syncthreads();
    const u32 h2t = (u32)WS_H2T + (u32)(t0 >> 4) * 18 * 1024 + (t0 & 15) * 16, x1g = (u32)WS_X1 + (u32)t0 * D_ * 2;
#pragma unroll
    for (int it = 0; it < 3; ++it) {
        const int i = tid + 256 * it;
        if (i < 576) {
            const int kg = i >> 3, rr8 = i & 7;
            st16_dev(wsr, h2t + (kg >> 2) * 1024 + (kg & 3) * 256 + rr8 * 16, *(const u32x4*)(sm + R_HT + i * 16));
            const int rr = i / 72, cc = i - rr * 72;
            st16_dev(wsr, x1g + rr * 1152 + cc * 16, *(const u32x4*)(sm + R_YT + rr * 1168 + cc * 16));
        }
    }
}

namespace mlp {
constexpr int STG = 36864, LDS_BYTES = 163840;
constexpr int G1S = 9 * 144, G2S = 18 * 36, NS = G1S + G2S;
constexpr int LA = 4;
constexpr int PRO = 9 + 4 * LA + 18;
constexpr int TUS = 147456;

constexpr int stage_of(int gs) { return gs < G1S ? gs / 144 : 9 + (gs - G1S) / 72; }
constexpr int rel_of(int gs) { return gs < G1S ? gs % 144 : (gs - G1S) % 72; }
constexpr int grp_of(int gs) { return rel_of(gs) >> 2; }
constexpr int t_of(int gs) { return rel_of(gs) & 3; }
constexpr bool barpt(int gs) { return gs >= 0 && gs < NS && t_of(gs) == 3 && grp_of(gs) == (gs < G1S ? 3 : 0) && stage_of(gs) < 17; }
constexpr int dma_piece(int gs) {
    if (gs >= NS || t_of(gs) != 3 || stage_of(gs) + 3 > 17) return -1;
    const int g = grp_of(gs);
    if (gs < G1S) { constexpr int G[9] = {5, 7, 9, 12, 14, 16, 20, 22, 24}; for (int i = 0; i < 9; ++i) if (G[i] == g) return i; return -1; }
    return (g & 1) ? g >> 1 : -1;
}
constexpr int bload(int gs) {
    if (gs >= 144 || t_of(gs) == 0 || t_of(gs) == 3) return -1;
    const int ks = grp_of(gs) >> 1, i = (grp_of(gs) & 1) * 2 + t_of(gs) - 1;
    return (ks + LA < 18) ? (ks + LA) * 4 + i : -1;
}
constexpr bool unit_stores(int v) { return v == 1 || v == 3 || v == 4 || v == 6 || v == 8; }
constexpr int hstore(int gs) {
    if (gs >= G1S || gs < 144 || !unit_stores(gs / 144 - 1) || t_of(gs) != 3) return -1;
    const int g = grp_of(gs);
    return (g == 10 || g == 18 || g == 26 || g == 34) ? (g - 10) / 8 : -1;
}
constexpr int hload(int gs) {
    if (t_of(gs) != 3) return -1;
    const int s = stage_of(gs), g = grp_of(gs);
    if (s == 8) { constexpr int G[20] = {4, 6, 8, 10, 11, 13, 15, 17, 18, 19, 21, 23, 25, 26, 27, 28, 29, 30, 31, 32}; for (int i = 0; i < 20; ++i) if (G[i] == g) return i; return -1; }
    if (s < 9 || g < 2 || g > 16 || (g & 1)) return -1;
    const int i = (g - 2) >> 1;
    if (s == 9) return 20 + i;
    if (s == 10) return 28 + i;
    if (s == 11) return i < 4 ? 36 + i : -1;
    if (s >= 12 && s <= 15) return 40 + 8 * (s - 12) + i;
    return -1;
}
constexpr int pub(int gs) { return (stage_of(gs) == 6 && grp_of(gs) == 27 && t_of(gs) == 3) ? 1 : (stage_of(gs) == 10 && grp_of(gs) == 9 && t_of(gs) == 1) ? 2 : 0; }
constexpr int poll_issue(int gs) { return (stage_of(gs) == 7 && grp_of(gs) == 27 && t_of(gs) == 3) ? 1 : (stage_of(gs) == 11 && grp_of(gs) == 9 && t_of(gs) == 1) ? 2 : 0; }
constexpr int vm_ops(int gs) { return (pub(gs) ? 1 : 0) + (poll_issue(gs) ? 1 : 0) + (hstore(gs) >= 0) + (bload(gs) >= 0) + (hload(gs) >= 0) + (dma_piece(gs) >= 0); }
struct Ledger { int cum[NS + 1]; };
constexpr Ledger make_ledger() {
    Ledger l{}; int n = PRO;
    for (int gs = 0; gs < NS; ++gs) { if (gs == G1S) n += 4; l.cum[gs] = n; n += vm_ops(gs); }
    l.cum[NS] = n; return l;
}
constexpr Ledger LG = make_ledger();
constexpr int seq_in(int gs, int kind) {
    int n = LG.cum[gs];
    if (kind > 0 && pub(gs)) ++n;
    if (kind > 1 && poll_issue(gs)) ++n;
    if (kind > 2 && hstore(gs) >= 0) ++n;
    if (kind > 3 && bload(gs) >= 0) ++n;
    if (kind > 4 && hload(gs) >= 0) ++n;
    return n;
}
constexpr int seq_dma_last(int s) {
    if (s <= 2) return 9 * s + 8;
    for (int gs = 0; gs < NS; ++gs) if (stage_of(gs) == s - 3 && dma_piece(gs) == 8) return seq_in(gs, 5);
    return -1;
}
constexpr int seq_b(int ks) {
    if (ks < LA) return 27 + 4 * ks + 3;
    for (int gs = 0; gs < 144; ++gs) if (bload(gs) == ks * 4 + 3) return seq_in(gs, 3);
    return -1;
}
constexpr int seq_h(int kk) {
    for (int gs = 8 * 144; gs < NS; ++gs) if (hload(gs) == kk * 4 + 3) return seq_in(gs, 4);
    return -1;
}
constexpr int seq_poll(int part) {
    for (int gs = 0; gs < NS; ++gs) if (poll_issue(gs) == part) return seq_in(gs, 1);
    return -1;
}
constexpr int seq_store_last(int part) {
    if (part == 2) return LG.cum[G1S] - 1;
    for (int gs = 5 * 144; gs < 6 * 144; ++gs) if (hstore(gs) == 3) return seq_in(gs, 2);
    return -1;
}
constexpr int clampv(int n) { return n > 63 ? 63 : (n < 0 ? 0 : n); }
constexpr int wait_for(int gs, int seq) { return clampv(LG.cum[gs] - seq - 1); }

template <int N> __device__ __forceinline__ void wait_vm() { asm volatile("s_waitcnt vmcnt(%0)" :: "n"(N) : "memory"); }

struct Ctx {
    bf16x8 Bv[2][4];
    bf16x8 Aq[6];
    f32x4 ac[2][2][4];
    f32x4 a2[9][4];
    float e0, e1;
    u32x4 hp[4];
    u32 vo1[4], vo2[4];
    u32 lane16, epoch;
    u32x4 pollv;
    const LAS char* rd[4]; const LAS char* rd2[4];
    const char* w1src; const char* w2src; const char* bsrc; const char* hsrc;
    u32 voff2[9];
    int sub;
    u32 dmadst;
    u32 hso;
    int jj;
    __amdgpu_buffer_rsrc_t hrs;
    u32* flag_pub; u32* flag_poll;
};

template <int KS, int T, bool ZERO> __device__ __forceinline__ void mfma(Ctx& cx, f32x4& c, const bf16x8& a) {
    constexpr int r = (KS * 4 + T) * 4;
    if constexpr (KS < 16) {
        if constexpr (ZERO) asm volatile("v_mfma_f32_16x16x32_bf16 %0, %1, a[%c2:%c3], 0" : "=v"(c) : "v"(a), "n"(r), "n"(r + 3));
        else asm volatile("v_mfma_f32_16x16x32_bf16 %0, %1, a[%c2:%c3], %0" : "+v"(c) : "v"(a), "n"(r), "n"(r + 3));
    } else {
        asm volatile("v_mfma_f32_16x16x32_bf16 %0, %1, %2, %0" : "+v"(c) : "v"(a), "v"(cx.Bv[KS - 16][T]));
    }
}
template <int KS, int T, bool SC1> __device__ __forceinline__ void gload(Ctx& cx, u32 voff, const char* sb) {
    constexpr int r = (KS * 4 + T) * 4, off = (KS & 3) * 1024;
    if constexpr (KS < 16) {
        if constexpr (SC1) asm volatile("global_load_dwordx4 a[%c2:%c3], %0, %1 offset:%4 sc1" :: "v"(voff), "s"(sb), "n"(r), "n"(r + 3), "n"(off) : "memory");
        else asm volatile("global_load_dwordx4 a[%c2:%c3], %0, %1 offset:%4" :: "v"(voff), "s"(sb), "n"(r), "n"(r + 3), "n"(off) : "memory");
    } else {
        if constexpr (SC1) asm volatile("global_load_dwordx4 %0, %1, %2 offset:%3 sc1" : "=v"(cx.Bv[KS - 16][T]) : "v"(voff), "s"(sb), "n"(off) : "memory");
        else asm volatile("global_load_dwordx4 %0, %1, %2 offset:%3" : "=v"(cx.Bv[KS - 16][T]) : "v"(voff), "s"(sb), "n"(off) : "memory");
    }
}
template <int S, int P> __device__ __forceinline__ void dma_m0(const Ctx& c) {
    asm volatile("s_mov_b32 m0, %0" :: "s"(c.dmadst + (u32)((S & 3) * STG + (P >> 2) * 4096)) : "memory");
}
constexpr int nat_kstep(int ks2) {
    if (ks2 < 10) { constexpr int T[5] = {0, 1, 5, 6, 4}; return 9 * (ks2 / 5) + T[ks2 % 5]; }
    const int jj = (ks2 - 10) >> 1, h = (ks2 - 10) & 1;
    return 9 * (jj >> 1) + ((jj & 1) ? 7 : 2) + h;
}
template <int S, int P> __device__ __forceinline__ void dma_go(const Ctx& c) {
    if constexpr (S < 9) {
        const char* sb = c.w1src + (size_t)S * STG + (P >> 2) * 4096;
        asm volatile("global_load_lds_dwordx4 %0, %1 offset:%2 sc1" :: "v"(c.lane16), "s"(sb), "n"((P & 3) * 1024) : "memory");
    } else {
        constexpr int n0 = nat_kstep(2 * (S - 9)), n1 = nat_kstep(2 * (S - 9) + 1);
        const char* sb = c.w2src + (size_t)(c.sub ? n1 : n0) * (32 * D_ * 2);
        asm volatile("global_load_lds_dwordx4 %0, %1 offset:%2" :: "v"(c.voff2[P]), "s"(sb), "n"((P & 3) * 1024) : "memory");
    }
}
template <int ID> __device__ __forceinline__ void load_b(Ctx& c) {
    constexpr int ks = ID >> 2, t = ID & 3;
    gload<ks, t, true>(c, c.vo1[t], c.bsrc + (ks >> 2) * 4096);
}
template <int ID> __device__ __forceinline__ void load_h(Ctx& c) {
    constexpr int kk = ID >> 2, t = ID & 3;
    gload<kk, t, true>(c, c.vo2[t], c.hsrc + (kk >> 2) * 4096);
}
template <int KS> __device__ __forceinline__ void pin_b(Ctx& c) {
#pragma unroll
    for (int t = 0; t < 4; ++t) { if constexpr (KS >= 16) asm volatile("" : "+v"(c.Bv[KS - 16][t])); }
}
template <int V, int P> __device__ __forceinline__ void epi_piece(Ctx& c) {
    constexpr int pr = P >> 3, st = P & 7, t = pr >> 1, r = 2 * (pr & 1), par = V & 1;
    if constexpr (st == 0) {
        asm volatile("v_mul_f32 %0, 0xbfb8aa3b, %1" : "=v"(c.e0) : "v"(c.ac[par][0][t][r]));
        asm volatile("v_mul_f32 %0, 0xbfb8aa3b, %1" : "=v"(c.e1) : "v"(c.ac[par][0][t][r + 1]));
    } else if constexpr (st == 1) asm volatile("v_exp_f32 %0, %0" : "+v"(c.e0));
    else if constexpr (st == 2) asm volatile("v_exp_f32 %0, %0" : "+v"(c.e1));
    else if constexpr (st == 3) {
        asm volatile("v_add_f32 %0, 1.0, %0" : "+v"(c.e0));
        asm volatile("v_add_f32 %0, 1.0, %0" : "+v"(c.e1));
    } else if constexpr (st == 4) asm volatile("v_rcp_f32 %0, %0" : "+v"(c.e0));
    else if constexpr (st == 5) asm volatile("v_rcp_f32 %0, %0" : "+v"(c.e1));
    else if constexpr (st == 6) {
        asm volatile("v_mul_f32 %0, %0, %1" : "+v"(c.e0) : "v"(c.ac[par][1][t][r]));
        asm volatile("v_mul_f32 %0, %0, %1" : "+v"(c.e1) : "v"(c.ac[par][1][t][r + 1]));
    } else {
        constexpr bool first = (V == 0 || V == 2 || V == 5 || V == 7);
        u32 o;
        asm volatile("v_cvt_pk_bf16_f32 %0, %1, %2" : "=v"(o) : "v"(c.e0), "v"(c.e1));
        c.hp[t][(first ? 0 : 2) + (pr & 1)] = o;
    }
}
template <int V, int T> __device__ __forceinline__ void store_h(Ctx& c) {
    const int m5 = 5 * (c.jj >> 1), ab = c.jj & 1;
    if constexpr (V == 4) {
        const u32x2 d = {c.hp[T][2], c.hp[T][3]};
        __builtin_amdgcn_raw_buffer_store_b64(d, c.hrs, c.vo2[T] + 8 * ab, c.hso + (m5 + 4) * 1024, 16);
    } else {
        const int kstep = (V == 1) ? m5 + 2 * ab : (V == 3) ? m5 + 2 * ab + 1 : (V == 6) ? 10 + 2 * c.jj : 11 + 2 * c.jj;
        __builtin_amdgcn_raw_buffer_store_b128(c.hp[T], c.hrs, c.vo2[T], c.hso + kstep * 1024, 16);
    }
}
template <int PART> __device__ __forceinline__ void poll_check(Ctx& c) {
    asm volatile("" : "+v"(c.pollv));
    const u32 e = c.epoch;
    if (!__all(c.pollv[0] == e && c.pollv[1] == e && c.pollv[2] == e && c.pollv[3] == e)) {
        const u32* p = c.flag_poll + (PART - 1) * 1024 + ((c.lane16 >> 4) & 3);
        poll_eq(p, e);
    }
}
template <int V, int P> __device__ __forceinline__ void epi_piece_padded(Ctx& c) {
    epi_piece<V, P>(c);
    if constexpr ((P & 7) == 2 || (P & 7) == 5) asm volatile("s_nop 0");
}
template <int V, int... P> __device__ __forceinline__ void epi_all(Ctx& c, std::integer_sequence<int, P...>) { (epi_piece_padded<V, P>(c), ...); }

template <int GS> __device__ __forceinline__ void slot(Ctx& c) {
    constexpr bool g1 = GS < G1S;
    constexpr int S = stage_of(GS), R = rel_of(GS), grp = R >> 2, t = R & 3;
    constexpr int fid = g1 ? 36 * S + grp : 324 + 18 * (S - 9) + grp;
    if constexpr (GS == G1S) {
        asm volatile("s_nop 7" ::: "memory");
        epi_all<8>(c, std::make_integer_sequence<int, 64>{});
        store_h<8, 0>(c); store_h<8, 1>(c); store_h<8, 2>(c); store_h<8, 3>(c);
        SCHED();
    }
    if constexpr (S == 0 && t == 0 && (grp & 1) == 0) { wait_vm<wait_for(GS, seq_b(grp >> 1))>(); pin_b<(grp >> 1)>(c); }
    if constexpr (GS == 8 * 144) { wait_vm<wait_for(GS, seq_poll(1))>(); poll_check<1>(c); }
    if constexpr (!g1 && t == 0 && (grp % 9) == 0) { constexpr int kk = 2 * (S - 9) + grp / 9; wait_vm<wait_for(GS, seq_h(kk))>(); pin_b<kk>(c); }
    if constexpr (dma_piece(GS) >= 0 && (dma_piece(GS) & 3) == 0) dma_m0<S + 3, dma_piece(GS)>(c);
    if constexpr (g1) {
        constexpr int ks = grp >> 1, mat = grp & 1;
        mfma<ks, t, (ks == 0)>(c, c.ac[S & 1][mat][t], c.Aq[fid % 6]);
    } else {
        constexpr int kk = 2 * (S - 9) + grp / 9, du = grp % 9;
        mfma<kk, t, (kk == 0)>(c, c.a2[du][t], c.Aq[fid % 6]);
    }
    if constexpr (barpt(GS)) {
        constexpr int n1 = wait_for(GS, seq_dma_last(S + 1));
        constexpr int n2 = (S == 12) ? wait_for(GS, seq_poll(2)) : 63;
        wait_vm<(n1 < n2 ? n1 : n2)>();
        if constexpr (S == 12) poll_check<2>(c);
        __builtin_amdgcn_s_barrier();
        asm volatile("" ::: "memory");
    }
    if constexpr (pub(GS)) {
        wait_vm<wait_for(GS, seq_store_last(pub(GS)))>();
        if (c.lane16 == 0) flag_set(c.flag_pub + (pub(GS) - 1) * 1024, c.epoch);
    }
    if constexpr (poll_issue(GS)) asm volatile("global_load_dwordx4 %0, %1, %2 sc1" : "=v"(c.pollv) : "v"(0u), "s"(c.flag_poll + (poll_issue(GS) - 1) * 1024) : "memory");
    if constexpr (fid + 5 < 486) {
        constexpr int f = fid + 5;
        if constexpr (f < 324) { if constexpr (t == 0) c.Aq[f % 6] = *(const LAS bf16x8*)(c.rd[(f / 36) & 3] + (f % 36) * 1024); }
        else if constexpr (t <= 1) {
            constexpr int fs = (f - 324) % 18;
            const s16x4 h = __builtin_amdgcn_ds_read_tr16_b64_v4i16((LAS s16x4*)(c.rd2[(9 + (f - 324) / 18) & 3] + (fs / 9) * 9216 + (fs % 9) * 32 + t * (16 * 288)));
            const u32x2 hh = __builtin_bit_cast(u32x2, h);
            u32x4 a = __builtin_bit_cast(u32x4, c.Aq[f % 6]);
            a[2 * t] = hh[0]; a[2 * t + 1] = hh[1];
            c.Aq[f % 6] = __builtin_bit_cast(bf16x8, a);
        }
    }
    if constexpr (g1 && S >= 1 && (t == 1 || t == 2) && grp >= 2 && grp < 34) epi_piece<S - 1, (grp - 2) * 2 + t - 1>(c);
    if constexpr (hstore(GS) >= 0) store_h<S - 1, hstore(GS)>(c);
    if constexpr (bload(GS) >= 0) load_b<bload(GS)>(c);
    if constexpr (hload(GS) >= 0) load_h<hload(GS)>(c);
    if constexpr (dma_piece(GS) >= 0) dma_go<S + 3, dma_piece(GS)>(c);
    SCHED();
}
template <int BASE, int... I> __device__ __forceinline__ void run_slots(Ctx& c, std::integer_sequence<int, I...>) { (slot<BASE + I>(c), ...); }
template <int S, int P> __device__ __forceinline__ void dma_one(const Ctx& c) { dma_m0<S, P>(c); asm volatile("s_nop 0"); dma_go<S, P>(c); }
template <int S, int... P> __device__ __forceinline__ void dma_stage(const Ctx& c, std::integer_sequence<int, P...>) { (dma_one<S, P>(c), ...); }
template <int... I> __device__ __forceinline__ void load_b_first(Ctx& c, std::integer_sequence<int, I...>) { (load_b<I>(c), ...); }
}

__device__ __forceinline__ void touch(u32& d, const char* p) { asm volatile("global_load_dword %0, %1, off" : "=v"(d) : "v"(p) : "memory"); }
__global__ __launch_bounds__(256, 1) void k_main(const u16* __restrict__ qin, const u16* __restrict__ kin, const u16* __restrict__ vin, const u16* __restrict__ shift_k,
                                                 const u16* __restrict__ ssm_k, const u16* __restrict__ ssm_D, const u16* __restrict__ wg, const u16* __restrict__ wu,
                                                 const u16* __restrict__ wd, const u16* __restrict__ cvec, const u16* __restrict__ w_mod,
                                                 char* __restrict__ ws, u16* __restrict__ out) {
    using namespace mlp;
    extern __shared__ __attribute__((aligned(16))) char smem[];
    const int tid = threadIdx.x, lane = tid & 63, w = __builtin_amdgcn_readfirstlane(tid >> 6);
    const int b = blockIdx.x, x = b & 7, idx = b >> 3, q = x >> 1;
    const int mt = 4 * (x & 1) + (idx & 3), fcl = idx >> 2, fc = 8 * q + fcl;
    const int mt2 = 8 * (x & 1) + (idx & 7), cc = idx >> 3, tg = w & 1, kh = w >> 1;
    const rsrc_t wsr = __builtin_amdgcn_make_buffer_rsrc(ws, 0, (int)WS_END, 0x00020000);
    STAMP_DECL; STAMP_AT(0);
    const u32 epoch_in = __hip_atomic_load(flag_ptr(ws, F_EPOCH), __ATOMIC_RELAXED, __HIP_MEMORY_SCOPE_AGENT);
    unsigned long long pc_start;
    asm volatile("s_getpc_b64 %0" : "=s"(pc_start));
    u32 epoch;
    {
        const int t0 = (x * 32 + idx) * 8;
        RowsPre rp;
        u32 bg[10] = {0, 0, 0, 0, 0, 0, 0, 0, 0, 0};
        if (b < 192) {
            const int c0 = 72 * x + 3 * idx;
            ConvIn in[3];
            KvIn kin_;
            if (w < 3) touch(bg[8], (const char*)(b < 96 ? kin : vin) + (size_t)((b % 96) * 192 + w * 64 + lane) * 128);
            kv_load(kin_, c0, kin, vin, shift_k);
            conv_load_k(in[0], c0, ssm_k, ssm_D); conv_load_k(in[1], c0 + 1, ssm_k, ssm_D); conv_load_k(in[2], c0 + 2, ssm_k, ssm_D);
            rows_load(rp, t0, qin);
            kv_put(kin_, smem + C_RED);
            conv_put_k(in[0], smem); conv_put_k(in[1], smem + C_CH); conv_put_k(in[2], smem + 2 * C_CH);
            __syncthreads();
            STAMP_AT(1);
            kv_compute(kin_, c0, smem + C_RED, smem + C_XR, C_CH);
            epoch = __builtin_amdgcn_readfirstlane(epoch_in) + 1u;
            if (w == 3) {
                DRAIN();
#pragma unroll
                for (int i = 0; i < 8; ++i) { const int l = lane + 64 * (i & 3); touch(bg[i], (const char*)(i < 4 ? wg : wu) + (size_t)b * 27648 + (l < 216 ? l : 0) * 128); }
            }
            STAMP_AT(2);
            ModIn mi;
            const u32* mf = flag_ptr(ws, F_MOD + (lane < 54 ? lane : 0));
            u32 pm;
            asm volatile("global_load_dword %0, %1, off sc1" : "=v"(pm) : "v"(mf) : "memory");
            conv_body(c0, in, smem, wsr, [&]() {
                DRAIN();
                asm volatile("" : "+v"(pm));
                if (!__all(pm == epoch)) poll_eq(mf, epoch);
                mod_load(mi, wsr); } STAMP_ARGS);
            STAMP_AT(5);
            mod_put(mi, smem);
            DRAIN();
#pragma unroll
            for (int i = 0; i < 10; ++i) asm volatile("" :: "v"(bg[i]));
            __syncthreads();
            if (tid < 8) flag_set(flag_ptr(ws, F_CONV + tid * 256 + b), epoch);
            if (w == 3) {
#pragma unroll
                for (int i = 0; i < 4; ++i) { const int l = lane + 64 * i; touch(bg[i], (const char*)wd + (size_t)b * 27648 + (l < 216 ? l : 0) * 128); }
            }
        } else {
            if (w == 3 && idx < 28) {
                const char* cb = (const char*)(pc_start & ~127ull) + lane * 128;
#pragma unroll
                for (int i = 0; i < 8; ++i) touch(bg[i], cb + (i < 7 || lane < 32 ? i * 8192 : 0));
            }
            rows_load(rp, t0, qin);
            if (b - 192 < 54) mod_job(b - 192, cvec, w_mod, ws, (float*)(smem + R_MOD), (float*)(smem + R_MOD + 4096));
            epoch = __builtin_amdgcn_readfirstlane(epoch_in) + 1u;
            DRAIN();
            __syncthreads();
            if (tid == 0 && b - 192 < 54) flag_set(flag_ptr(ws, F_MOD + b - 192), epoch);
            STAMP_AT(1);
            if (b - 192 >= 54) { for (int i = 0; i < 40; ++i) __builtin_amdgcn_s_sleep(2); }
            tile_jobs(b - 192, wg, wu, smem, ws, wsr, epoch);
            STAMP_AT(2); STAMP_AT(3); STAMP_AT(4); STAMP_AT(5);
            poll_eq(flag_ptr(ws, F_MOD + (lane < 54 ? lane : 0)), epoch);
            ModIn mi;
            mod_load(mi, wsr);
            mod_put(mi, smem);
            __syncthreads();
        }
        rows_pre(rp, smem);
        STAMP_AT(6);
        if (w == 0) {
            poll_eq4(wsr, (u32)WS_CNT + (F_CONV + x * 256 + (lane < 48 ? lane : 0) * 4) * 4, epoch);
            rows_fetch(t0, smem, ws);
        }
        __syncthreads();
        STAMP_AT(7);
        rows_main(rp, t0, smem, wsr);
        DRAIN();
#pragma unroll
        for (int i = 0; i < 10; ++i) asm volatile("" :: "v"(bg[i]));
        __syncthreads();
        if (tid == 0) flag_set(flag_ptr(ws, F_ROWS + (t0 >> 6) * 8 + ((t0 >> 3) & 7)), epoch);
        STAMP_AT(8);
    }
    Ctx c;
#pragma unroll
    for (int t = 0; t < 4; ++t) c.hp[t] = (u32x4){0, 0, 0, 0};
    c.lane16 = lane * 16;
#pragma unroll
    for (int t = 0; t < 4; ++t) { c.vo1[t] = lane * 16 + t * 18432; c.vo2[t] = lane * 16 + t * TUS; }
    const LAS char* l0 = (const LAS char*)smem;
#pragma unroll
    for (int i = 0; i < 4; ++i) {
        c.rd[i] = l0 + i * STG + lane * 16; c.rd2[i] = l0 + i * STG + kh * 18432 + (4 * (lane >> 4) + ((lane & 15) >> 2)) * 288 + 8 * (lane & 3);
        asm volatile("" : "+v"(c.rd[i])); asm volatile("" : "+v"(c.rd2[i]));
    }
#pragma unroll
    for (int i = 0; i < 9; ++i) { const int ci = lane + 64 * i, row = (ci * 3641) >> 16, c16 = ci - row * 18; c.voff2[i] = (u32)(row * (D_ * 2) + c16 * 16 - (i & 3) * 1024); }
    c.sub = w & 1;
    c.w1src = ws + WS_WT1 + (size_t)fc * 331776 + w * 9216;
    c.w2src = (const char*)(wd + (size_t)(36 * q + 18 * kh) * 32 * D_ + cc * 144);
    c.bsrc = ws + WS_H2T + (size_t)(16 * mt + 4 * w) * 18432;
    c.hsrc = ws + WS_HT + (size_t)(8 * mt2 + 4 * tg) * TUS + (size_t)((q * 2 + kh) * 18) * 1024;
    c.dmadst = (u32)(uintptr_t)smem + w * 9216;
    c.jj = fcl & 3;
    c.hso = (u32)((16 * mt + 4 * w) * TUS + ((q * 2 + (fcl >> 2)) * 18) * 1024);
    c.hrs = __builtin_amdgcn_make_buffer_rsrc(ws + WS_HT, 0, (int)((size_t)S_ * F_ * 2), 0x00020000);
    c.epoch = epoch;
    c.flag_pub = flag_ptr(ws, F_H + ((((mt * 4 + w) * 4 + q) * 2 + (fcl >> 2))) * 4 + (fcl & 3));
    c.flag_poll = flag_ptr(ws, F_H + (((((mt2 >> 1) * 4 + 2 * (mt2 & 1) + tg) * 4 + q) * 2 + kh)) * 4);
    asm volatile("" ::: "a0", "a1", "a2", "a3", "a4", "a5", "a6", "a7", "a8", "a9", "a10", "a11", "a12", "a13", "a14", "a15", "a16", "a17", "a18", "a19", "a20", "a21", "a22", "a23", "a24", "a25", "a26", "a27", "a28", "a29", "a30", "a31", "a32", "a33", "a34", "a35", "a36", "a37", "a38", "a39", "a40", "a41", "a42", "a43", "a44", "a45", "a46", "a47", "a48", "a49", "a50", "a51", "a52", "a53", "a54", "a55", "a56", "a57", "a58", "a59", "a60", "a61", "a62", "a63", "a64", "a65", "a66", "a67", "a68", "a69", "a70", "a71", "a72", "a73", "a74", "a75", "a76", "a77", "a78", "a79", "a80", "a81", "a82", "a83", "a84", "a85", "a86", "a87", "a88", "a89", "a90", "a91", "a92", "a93", "a94", "a95", "a96", "a97", "a98", "a99", "a100", "a101", "a102", "a103", "a104", "a105", "a106", "a107", "a108", "a109", "a110", "a111", "a112", "a113", "a114", "a115", "a116", "a117", "a118", "a119", "a120", "a121", "a122", "a123", "a124", "a125", "a126", "a127", "a128", "a129", "a130", "a131", "a132", "a133", "a134", "a135", "a136", "a137", "a138", "a139", "a140", "a141", "a142", "a143", "a144", "a145", "a146", "a147", "a148", "a149", "a150", "a151", "a152", "a153", "a154", "a155", "a156", "a157", "a158", "a159", "a160", "a161", "a162", "a163", "a164", "a165", "a166", "a167", "a168", "a169", "a170", "a171", "a172", "a173", "a174", "a175", "a176", "a177", "a178", "a179", "a180", "a181", "a182", "a183", "a184", "a185", "a186", "a187", "a188", "a189", "a190", "a191", "a192", "a193", "a194", "a195", "a196", "a197", "a198", "a199", "a200", "a201", "a202", "a203", "a204", "a205", "a206", "a207", "a208", "a209", "a210", "a211", "a212", "a213", "a214", "a215", "a216", "a217", "a218", "a219", "a220", "a221", "a222", "a223", "a224", "a225", "a226", "a227", "a228", "a229", "a230", "a231", "a232", "a233", "a234", "a235", "a236", "a237", "a238", "a239", "a240", "a241", "a242", "a243", "a244", "a245", "a246", "a247", "a248", "a249", "a250", "a251", "a252", "a253", "a254", "a255");
    poll_eq(flag_ptr(ws, F_TILE + (fc >> 2) * 36 + (lane < 36 ? lane : 0)), epoch);
    dma_stage<0>(c, std::make_integer_sequence<int, 9>{});
    dma_stage<1>(c, std::make_integer_sequence<int, 9>{});
    dma_stage<2>(c, std::make_integer_sequence<int, 9>{});
    poll_eq(flag_ptr(ws, F_ROWS + (4 * mt + w) * 8 + (lane & 7)), epoch);
    STAMP_AT(9);
    load_b_first(c, std::make_integer_sequence<int, 4 * LA>{});
    wait_vm<4 * LA>();
    __builtin_amdgcn_s_barrier();
    asm volatile("" ::: "memory");
#pragma unroll
    for (int f = 0; f < 5; ++f) c.Aq[f] = *(const LAS bf16x8*)(c.rd[0] + f * 1024);
    SCHED();
    STAMP_AT(10);
    run_slots<0>(c, std::make_integer_sequence<int, 144>{});
    STAMP_AT(11);
    run_slots<144>(c, std::make_integer_sequence<int, 144 * 4>{});
    run_slots<144 * 5>(c, std::make_integer_sequence<int, 144 * 4>{});
    STAMP_AT(12);
    run_slots<G1S>(c, std::make_integer_sequence<int, 324>{});
    run_slots<G1S + 324>(c, std::make_integer_sequence<int, 324>{});
    asm volatile("s_nop 7" ::: "memory");
    SCHED();
    STAMP_AT(13);
    const int n = lane & 15, g = lane >> 4;
    u32x2 xr[3][2]; f32x4 gb[3];
#pragma unroll
    for (int i = 0; i < 3; ++i) {
        const int u = (w + 4 * i < 9) ? w + 4 * i : 8, d = 144 * cc + 16 * u + 4 * g;
#pragma unroll
        for (int t = 0; t < 2; ++t) xr[i][t] = __builtin_amdgcn_raw_buffer_load_b64(wsr, (u32)WS_X1 + (u32)((128 * mt2 + 32 * q + 16 * t + n) * D_ + d) * 2, 0, 16);
        gb[i] = *(const f32x4*)(smem + R_MOD + (5 * 576 + d) * 4);
    }
    __builtin_amdgcn_s_barrier();
    {
        char* ex = smem + w * 18432 + lane * 16;
#pragma unroll
        for (int t = 0; t < 2; ++t)
#pragma unroll
            for (int u = 0; u < 9; ++u) *(f32x4*)(ex + (t * 9 + u) * 1024) = kh ? c.a2[u][t] : c.a2[u][2 + t];
    }
    __syncthreads();
    const int j = 2 * tg + kh;
    u32* fred = flag_ptr(ws, F_RED + (mt2 * 4 + cc) * 16);
    const u32 slab = (u32)WS_SLAB + (u32)((mt2 * 4 + cc) * 16) * 9216;
    {
        const char* ey = smem + (w ^ 2) * 18432 + lane * 16;
        char* own = smem + 73728 + lane * 16;
#pragma unroll
        for (int u = 0; u < 9; ++u) {
            const f32x4 r0 = (kh ? c.a2[u][2] : c.a2[u][0]) + *(const f32x4*)(ey + u * 1024);
            const f32x4 r1 = (kh ? c.a2[u][3] : c.a2[u][1]) + *(const f32x4*)(ey + (9 + u) * 1024);
            if (j != q) st16_dev(wsr, slab + (j * 4 + q) * 9216 + u * 1024 + lane * 16, (u32x4){pk2(r0[0], r0[1]), pk2(r0[2], r0[3]), pk2(r1[0], r1[1]), pk2(r1[2], r1[3])});
            else { *(f32x4*)(own + u * 2048) = r0; *(f32x4*)(own + u * 2048 + 1024) = r1; }
        }
    }
    if (j != q) {
        DRAIN();
        if (lane == 0) flag_set(fred + j * 4 + q, epoch);
    }
    STAMP_AT(14);
    poll_eq(fred + q * 4 + (((lane & 3) == q) ? (q ^ 1) : (lane & 3)), epoch);
    __syncthreads();
    u32x4 rem[3][3];
#pragma unroll
    for (int i = 0; i < 3; ++i) {
        const int u = (w + 4 * i < 9) ? w + 4 * i : 8;
#pragma unroll
        for (int k = 0; k < 3; ++k) { const int qq = k + (k >= q ? 1 : 0); rem[i][k] = __builtin_amdgcn_raw_buffer_load_b128(wsr, slab + (q * 4 + qq) * 9216 + u * 1024 + lane * 16, 0, 16); }
    }
#pragma unroll
    for (int i = 0; i < 3; ++i) {
        const int u = w + 4 * i;
        if (u < 9) {
            const int d = 144 * cc + 16 * u + 4 * g;
#pragma unroll
            for (int t = 0; t < 2; ++t) {
                f32x4 s = *(const f32x4*)(smem + 73728 + u * 2048 + t * 1024 + lane * 16);
#pragma unroll
                for (int k = 0; k < 3; ++k) {
                    const u32 a = rem[i][k][2 * t], bq = rem[i][k][2 * t + 1];
                    s += (f32x4){bf_lo(a), bf_hi(a), bf_lo(bq), bf_hi(bq)};
                }
                const float o0 = bf_lo(xr[i][t][0]) + rbf(gb[i][0] * rbf(s[0])), o1 = bf_hi(xr[i][t][0]) + rbf(gb[i][1] * rbf(s[1]));
                const float o2 = bf_lo(xr[i][t][1]) + rbf(gb[i][2] * rbf(s[2])), o3 = bf_hi(xr[i][t][1]) + rbf(gb[i][3] * rbf(s[3]));
                *(u32x2*)(out + (size_t)(128 * mt2 + 32 * q + 16 * t + n) * D_ + d) = (u32x2){pk2(o0, o1), pk2(o2, o3)};
            }
        }
    }
    if (b == 0 && tid == 0) flag_set(flag_ptr(ws, F_EPOCH), epoch);
    STAMP_AT(15);
    STAMP_OUT(4, 16);
}

extern "C" void kernel_launch(void* const* d_in, const int* in_sizes, int n_in, void* d_out, int out_size, void* d_ws, size_t ws_size,
                              hipStream_t stream) {
    const u16* q = (const u16*)d_in[0];
    const u16* k = (const u16*)d_in[1];
    const u16* v = (const u16*)d_in[2];
    const u16* shift_k = (const u16*)d_in[3];
    const u16* ssm_k = (const u16*)d_in[4];
    const u16* ssm_D = (const u16*)d_in[5];
    const u16* wg = (const u16*)d_in[7];
    const u16* wu = (const u16*)d_in[8];
    const u16* wd = (const u16*)d_in[9];
    const u16* c = (const u16*)d_in[10];
    const u16* w_mod = (const u16*)d_in[11];
    char* ws = (char*)d_ws;
    (void)hipFuncSetAttribute((const void*)k_main, hipFuncAttributeMaxDynamicSharedMemorySize, mlp::LDS_BYTES);
    k_main<<<256, 256, mlp::LDS_BYTES, stream>>>(q, k, v, shift_k, ssm_k, ssm_D, wg, wu, wd, c, w_mod, ws, (u16*)d_out);
}
```

```cpp
#include <hip/hip_runtime.h>
#include <stdint.h>
#include <utility>

typedef unsigned short u16;
typedef unsigned int u32;
typedef __bf16 bf16x8 __attribute__((ext_vector_type(8)));
typedef __bf16 bf16x2 __attribute__((ext_vector_type(2)));
typedef float f32x4 __attribute__((ext_vector_type(4)));
typedef float f32x2 __attribute__((ext_vector_type(2)));
typedef u32 u32x4 __attribute__((ext_vector_type(4)));
typedef u32 u32x2 __attribute__((ext_vector_type(2)));
typedef short s16x4 __attribute__((ext_vector_type(4)));
#define LAS __attribute__((address_space(3)))
typedef LAS char* lptr;

constexpr int S_ = 2048, D_ = 576, F_ = 4608;

constexpr size_t WS_MOD = 0;
constexpr size_t WS_CNT = 65536;
constexpr size_t WS_YYT = WS_CNT + 65536;
constexpr size_t WS_X1  = WS_YYT + (size_t)D_ * S_ * 2;
constexpr size_t WS_H2T = WS_X1 + (size_t)D_ * S_ * 2;
constexpr size_t WS_HT  = WS_H2T + (size_t)D_ * S_ * 2;
constexpr size_t WS_WT1 = WS_HT + (size_t)S_ * F_ * 2;
constexpr size_t WS_SLAB = WS_WT1 + (size_t)2 * D_ * F_ * 2;
constexpr size_t WS_END = WS_SLAB + (size_t)16 * 4 * 4 * 4 * 9216;

#define STAMP_DECL
#define STAMP_ARGS
#define STAMP_AT(i)
#define STAMP_OUT(kid, n)
__device__ __forceinline__ float bf_lo(u32 w) { return __uint_as_float(w << 16); }
__device__ __forceinline__ float bf_hi(u32 w) { return __uint_as_float(w & 0xffff0000u); }
__device__ __forceinline__ float bf1(u16 w) { return __uint_as_float((u32)w << 16); }
__device__ __forceinline__ u32 pk2(float lo, float hi) {
    f32x2 v = {lo, hi};
    return __builtin_bit_cast(u32, __builtin_convertvector(v, bf16x2));
}
__device__ __forceinline__ float rbf(float x) { return (float)(__bf16)x; }
__device__ __forceinline__ float sigmoidf_(float x) { return __builtin_amdgcn_rcpf(1.0f + __builtin_amdgcn_exp2f(-1.4426950408889634f * x)); }

constexpr int F_MOD = 256;
constexpr int F_TILE = 512;
constexpr int F_CONV = 1024;
constexpr int F_ROWS = 3072;
constexpr int F_H = 3328;
constexpr int F_RED = 5376;
constexpr int F_EPOCH = 6400;
typedef __amdgpu_buffer_rsrc_t rsrc_t;
__device__ __forceinline__ u32* flag_ptr(char* ws, int idx) { return (u32*)(ws + WS_CNT) + idx; }
__device__ __forceinline__ void flag_set(u32* p, u32 e) { __hip_atomic_store(p, e, __ATOMIC_RELAXED, __HIP_MEMORY_SCOPE_AGENT); }
__device__ __forceinline__ void st16_dev(rsrc_t rs, u32 off, u32x4 v) { __builtin_amdgcn_raw_buffer_store_b128(v, rs, off, 0, 16); }
__device__ __forceinline__ u32x4 ld16_dev(rsrc_t rs, u32 off) { return __builtin_amdgcn_raw_buffer_load_b128(rs, off, 0, 16); }
__device__ __forceinline__ void poll_eq(const u32* p, u32 e) {
    for (int it = 0; it < (1 << 21); ++it) {
        if (__all(__hip_atomic_load(p, __ATOMIC_RELAXED, __HIP_MEMORY_SCOPE_AGENT) == e)) break;
        __builtin_amdgcn_s_sleep(1);
    }
}
__device__ __forceinline__ void poll_eq4(rsrc_t rs, u32 off, u32 e) {
    for (int it = 0; it < (1 << 21); ++it) {
        asm volatile("" ::: "memory");
        const u32x4 v = ld16_dev(rs, off);
        if (__all(v[0] == e && v[1] == e && v[2] == e && v[3] == e)) break;
        __builtin_amdgcn_s_sleep(1);
    }
}
#define DRAIN() asm volatile("s_waitcnt vmcnt(0)" ::: "memory")

#define SCHED() __builtin_amdgcn_sched_barrier(0)
__device__ __forceinline__ float dpp_add(float v, const int ctrl_sel) {
    int r;
    const int iv = __builtin_bit_cast(int, v);
    switch (ctrl_sel) {
        case 0: r = __builtin_amdgcn_update_dpp(0, iv, 0xB1, 0xf, 0xf, true); break;
        case 1: r = __builtin_amdgcn_update_dpp(0, iv, 0x4E, 0xf, 0xf, true); break;
        case 2: r = __builtin_amdgcn_update_dpp(0, iv, 0x141, 0xf, 0xf, true); break;
        default: r = __builtin_amdgcn_update_dpp(0, iv, 0x140, 0xf, 0xf, true); break;
    }
    return v + __builtin_bit_cast(float, r);
}
__device__ __forceinline__ float half_sum(float v) {
    v = dpp_add(v, 0); v = dpp_add(v, 1); v = dpp_add(v, 2); v = dpp_add(v, 3);
    auto r = __builtin_amdgcn_permlane16_swap(__float_as_uint(v), __float_as_uint(v), false, false);
    return __uint_as_float(r[0]) + __uint_as_float(r[1]);
}

struct KvIn { u32x2 kw[8], vw[8]; u32x4 sw; u32x2 sw2; };
__device__ __forceinline__ void kv_load(KvIn& p, int c0, const u16* __restrict__ k, const u16* __restrict__ v, const u16* __restrict__ shift_k) {
    const int cw = c0 & ~1;
#pragma unroll
    for (int i = 0; i < 8; ++i) p.kw[i] = *(const u32x2*)(k + (size_t)(threadIdx.x + 256 * i) * D_ + cw);
#pragma unroll
    for (int i = 0; i < 8; ++i) p.vw[i] = *(const u32x2*)(v + (size_t)(threadIdx.x + 256 * i) * D_ + cw);
    p.sw = (u32x4){*(const u32*)(shift_k + c0 * 4), *(const u32*)(shift_k + c0 * 4 + 2), *(const u32*)(shift_k + c0 * 4 + 4), *(const u32*)(shift_k + c0 * 4 + 6)};
    p.sw2 = (u32x2){*(const u32*)(shift_k + c0 * 4 + 8), *(const u32*)(shift_k + c0 * 4 + 10)};
}
constexpr int KV_RAWV = 257 * 72 + 8;
__device__ __forceinline__ void kv_put(const KvIn& p, char* raw) {
    const int tid = threadIdx.x;
#pragma unroll
    for (int i = 0; i < 8; ++i) {
        const int t = tid + 256 * i, o = 72 * (t >> 3) + 8 * (t & 7);
        *(u32x2*)(raw + 72 + o) = p.kw[i];
        *(u32x2*)(raw + KV_RAWV + o) = p.vw[i];
    }
    if (tid < 8) *(u32x2*)(raw + 8 * tid) = (u32x2){0, 0};
}
__device__ __forceinline__ void kv_compute(const KvIn& p, int c0, const char* raw, char* xr, int stride) {
    const int tid = threadIdx.x;
    u32x2 kw[11], vw[8];
#pragma unroll
    for (int i = 0; i < 11; ++i) kw[i] = *(const u32x2*)(raw + 72 * tid + (i < 3 ? 40 + 8 * i : 72 + 8 * (i - 3)));
#pragma unroll
    for (int i = 0; i < 8; ++i) vw[i] = *(const u32x2*)(raw + KV_RAWV + 72 * tid + 8 * i);
    const bool odd = c0 & 1;
#pragma unroll
    for (int c = 0; c < 3; ++c) {
        const u32 wa = c == 0 ? p.sw[0] : c == 1 ? p.sw[2] : p.sw2[0], wb = c == 0 ? p.sw[1] : c == 1 ? p.sw[3] : p.sw2[1];
        const float w0 = bf_lo(wa), w1 = bf_hi(wa), w2 = bf_lo(wb), w3 = bf_hi(wb);
        float kf[11], r[8];
#define KV_PICK(d) (c == 0 ? (odd ? bf_hi((d)[0]) : bf_lo((d)[0])) : c == 1 ? (odd ? bf_lo((d)[1]) : bf_hi((d)[0])) : (odd ? bf_hi((d)[1]) : bf_lo((d)[1])))
#pragma unroll
        for (int i = 0; i < 11; ++i) kf[i] = KV_PICK(kw[i]);
#pragma unroll
        for (int i = 0; i < 8; ++i) r[i] = (kf[i] * w3 + kf[i + 1] * w2 + kf[i + 2] * w1 + kf[i + 3] * w0) * KV_PICK(vw[i]);
#undef KV_PICK
        *(u32x4*)(xr + c * stride + 2 * (2040 - 8 * tid)) = (u32x4){pk2(r[7], r[6]), pk2(r[5], r[4]), pk2(r[3], r[2]), pk2(r[1], r[0])};
    }
}

__device__ __forceinline__ void mod_job(int job, const u16* __restrict__ c, const u16* __restrict__ w_mod, char* __restrict__ ws, float* sc  , float* red  ) {
    const int tid = threadIdx.x, lane = tid & 63, wid = tid >> 6;
    const int col0 = job * 64, rg = tid >> 3, c8 = tid & 7;
    u32x4 w[18];
#pragma unroll
    for (int i = 0; i < 18; ++i) w[i] = *(const u32x4*)(w_mod + (size_t)(rg + 32 * i) * 3456 + col0 + c8 * 8);
#pragma unroll
    for (int j = 0; j < 3; ++j) { const int r = tid + 256 * j; if (r < 576) { const float x = bf1(c[r]); sc[r] = rbf(x * sigmoidf_(x)); } }
    __syncthreads();
    float acc[8] = {0, 0, 0, 0, 0, 0, 0, 0};
#pragma unroll
    for (int i = 0; i < 18; ++i) {
        const float s = sc[rg + 32 * i];
#pragma unroll
        for (int j = 0; j < 4; ++j) { acc[2 * j] += s * bf_lo(w[i][j]); acc[2 * j + 1] += s * bf_hi(w[i][j]); }
    }
#pragma unroll
    for (int j = 0; j < 8; ++j) {
        acc[j] += __shfl_xor(acc[j], 8); acc[j] += __shfl_xor(acc[j], 16); acc[j] += __shfl_xor(acc[j], 32);
    }
    if (lane < 8) {
#pragma unroll
        for (int j = 0; j < 8; ++j) red[wid * 64 + lane * 8 + j] = acc[j];
    }
    __syncthreads();
    if (tid < 64) {
        float m = rbf((red[tid] + red[64 + tid]) + (red[128 + tid] + red[192 + tid]));
        const int which = col0 / 576;
        if (which == 1 || which == 4) m = rbf(1.0f + m);
        __hip_atomic_store((u32*)(ws + WS_MOD) + col0 + tid, __float_as_uint(m), __ATOMIC_RELAXED, __HIP_MEMORY_SCOPE_AGENT);
    }
}

__device__ __forceinline__ const char* tile_base(int job, const u16* __restrict__ wg, const u16* __restrict__ wu) {
    const int ks = job % 18, mat = (job / 18) & 1, cg = job / 36;
    return (const char*)((mat ? wu : wg) + (size_t)(32 * ks) * F_ + cg * 576);
}
__device__ __forceinline__ void tile_dma(int job, const u16* __restrict__ wg, const u16* __restrict__ wu, char* tile, const u32 (&voff)[9]) {
    const int w = __builtin_amdgcn_readfirstlane(threadIdx.x >> 6);
    const char* base = tile_base(job, wg, wu) + (size_t)w * 8 * (F_ * 2);
    const u32 dst = (u32)(uintptr_t)tile + w * 9216;
#pragma unroll
    for (int i = 0; i < 9; ++i)
        asm volatile("s_mov_b32 m0, %2\n\ts_nop 0\n\tglobal_load_lds_dwordx4 %0, %1" :: "v"(voff[i]), "s"(base), "s"(dst + i * 1024) : "memory");
}
__device__ __forceinline__ void tile_out(int job, const char* tile, rsrc_t wsr) {
    const int lane = threadIdx.x & 63, w = threadIdx.x >> 6;
    const int ks = job % 18, mat = (job / 18) & 1, fc = 4 * (job / 36) + w;
    const int g = lane >> 4, q4 = (lane & 15) >> 2, p = lane & 3;
    const LAS char* rd = (const LAS char*)tile + (4 * g + q4) * 1152 + 8 * p + w * 288;
#pragma unroll
    for (int u = 0; u < 9; ++u) {
        const int pos = (fc & 1) ? (u == 0 ? 4 : u <= 4 ? u - 1 : u) : (u == 8 ? 4 : u < 4 ? u : u + 1);
        const s16x4 lo = __builtin_amdgcn_ds_read_tr16_b64_v4i16((LAS s16x4*)(rd + u * 32));
        const s16x4 hi = __builtin_amdgcn_ds_read_tr16_b64_v4i16((LAS s16x4*)(rd + u * 32 + 16 * 1152));
        typedef short s16x8 __attribute__((ext_vector_type(8)));
        const s16x8 r = __builtin_shufflevector(lo, hi, 0, 1, 2, 3, 4, 5, 6, 7);
        st16_dev(wsr, (u32)WS_WT1 + fc * 331776 + pos * 36864 + (ks * 2 + mat) * 1024 + lane * 16, __builtin_bit_cast(u32x4, r));
    }
}
__device__ __forceinline__ void tile_jobs(int i0, const u16* __restrict__ wg, const u16* __restrict__ wu, char* smem, char* ws, rsrc_t wsr, u32 epoch) {
    const int lane = threadIdx.x & 63;
    u32 voff[9];
#pragma unroll
    for (int i = 0; i < 9; ++i) { const int ci = lane + 64 * i, row = (ci * 58255) >> 22, c16 = ci - row * 72; voff[i] = (u32)(row * (F_ * 2) + c16 * 16); }
#pragma unroll
    for (int j = 0; j < 4; ++j) tile_dma(i0 + 64 * j, wg, wu, smem + j * 36864, voff);
#pragma unroll
    for (int j = 0; j < 4; ++j) {
        asm volatile("s_waitcnt vmcnt(27)" ::: "memory");
        __syncthreads();
        tile_out(i0 + 64 * j, smem + j * 36864, wsr);
    }
    if (i0 < 32) {
        __syncthreads();
        tile_dma(i0 + 256, wg, wu, smem, voff);
        asm volatile("s_waitcnt vmcnt(0)" ::: "memory");
        __syncthreads();
        tile_out(i0 + 256, smem, wsr);
    }
    DRAIN();
    __syncthreads();
    if (threadIdx.x < 5 && i0 + 64 * threadIdx.x < 288) flag_set(flag_ptr(ws, F_TILE + i0 + 64 * threadIdx.x), epoch);
}

constexpr int C_XR = 0;
constexpr int C_K0 = 4608;
constexpr int C_K1 = C_K0 + 4128 + 32;
constexpr int C_CH = C_K1 + 4160;
constexpr int C_RED = 3 * C_CH;
constexpr int C_LDS = C_RED + 3 * 32768;
static_assert(((C_K1 - C_K0) / 4) % 32 == 16, "bank offset");
static_assert(C_CH % 16 == 0 && C_K0 % 16 == 0 && C_K1 % 16 == 0, "align");
template <bool ZERO> __device__ __forceinline__ void mfma_v(f32x4& c, const bf16x8& a, const bf16x8& b) {
    if constexpr (ZERO) asm volatile("v_mfma_f32_16x16x32_bf16 %0, %1, %2, 0" : "=v"(c) : "v"(a), "v"(b));
    else asm volatile("v_mfma_f32_16x16x32_bf16 %0, %1, %2, %0" :: "v"(c), "v"(a), "v"(b));
}
struct ConvSet { bf16x8 A[8], B[8]; };
template <int J> __device__ __forceinline__ void conv_ld(ConvSet& s, const LAS char* ab, const LAS char* bb) {
    if constexpr (J < 8) {
        const LAS u32* p = (const LAS u32*)(ab + 512 * J);
        u32x4 t = {p[0], p[1], p[2], p[3]};
        s.A[J] = __builtin_bit_cast(bf16x8, t);
    } else s.B[J - 8] = *(const LAS bf16x8*)(bb + 512 * (15 - J));
}
template <int M> struct TriIdx {
    static constexpr int find(bool want_i) { int m = 0; for (int s = 0; s < 8; ++s) for (int j = 0; j <= s; ++j) { if (m == M) return want_i ? s : j; ++m; } return 0; }
    static constexpr int i = find(true), j = find(false);
};
template <int M, bool PF, bool FIRST> __device__ __forceinline__ void conv_slot(f32x4 (&acc)[8], const ConvSet& cur, ConvSet& nxt, const LAS char* ab, const LAS char* bb) {
    mfma_v<(FIRST && TriIdx<M>::j == 0)>(acc[TriIdx<M>::i], cur.A[TriIdx<M>::j], cur.B[TriIdx<M>::i - TriIdx<M>::j]);
    if constexpr (PF && (M & 1) == 0 && M / 2 < 16) conv_ld<((M / 2) & 1) * 8 + (M / 4)>(nxt, ab, bb);
    SCHED();
}
template <bool PF, bool FIRST, int... M> __device__ __forceinline__ void conv_step(f32x4 (&acc)[8], const ConvSet& cur, ConvSet& nxt, const LAS char* ab, const LAS char* bb, std::integer_sequence<int, M...>) {
    (conv_slot<M, PF, FIRST>(acc, cur, nxt, ab, bb), ...);
}
template <int... J> __device__ __forceinline__ void conv_ld_all(ConvSet& s, const LAS char* ab, const LAS char* bb, std::integer_sequence<int, J...>) { (conv_ld<J>(s, ab, bb), ...); }

struct ConvIn { u32x4 k; u32 kn; u16 dv; };
__device__ __forceinline__ void conv_load_k(ConvIn& in, int d, const u16* __restrict__ ssm_k, const u16* __restrict__ ssm_D) {
    const int tid = threadIdx.x;
    in.k = *(const u32x4*)(ssm_k + (size_t)d * S_ + tid * 8);
    in.kn = *(const u32*)(ssm_k + (size_t)d * S_ + (tid < 255 ? tid * 8 + 8 : 0));
    in.dv = ssm_D[d];
}
__device__ __forceinline__ void conv_put_k(const ConvIn& in, char* sm) {
    const int tid = threadIdx.x;
    const u32 kn = tid < 255 ? in.kn : 0u;
    *(u32x4*)(sm + C_K0 + 32 + tid * 16) = in.k;
    *(u32x4*)(sm + C_K1 + 32 + tid * 16) = (u32x4){__builtin_amdgcn_alignbit(in.k[1], in.k[0], 16), __builtin_amdgcn_alignbit(in.k[2], in.k[1], 16),
                                                 __builtin_amdgcn_alignbit(in.k[3], in.k[2], 16), __builtin_amdgcn_alignbit(kn, in.k[3], 16)};
    if (tid < 32) *(u32x4*)(sm + C_XR + 4096 + tid * 16) = (u32x4){0, 0, 0, 0};
    if (tid == 0) {
        *(u32x4*)(sm + C_K0) = (u32x4){0, 0, 0, 0}; *(u32x4*)(sm + C_K0 + 16) = (u32x4){0, 0, 0, 0};
        *(u32x4*)(sm + C_K1) = (u32x4){0, 0, 0, 0}; *(u32x4*)(sm + C_K1 + 16) = (u32x4){0, 0, 0, in.k[0] << 16};
    }
}
__device__ __forceinline__ void conv_finish(int d, const char* sm, const char* red, float Dv, rsrc_t wsr) {
    const int tid = threadIdx.x;
    const int i = tid >> 5, n = tid & 15, gp = (tid >> 4) & 1;
    f32x4 lo = {0.f, 0.f, 0.f, 0.f}, hi = {0.f, 0.f, 0.f, 0.f};
#pragma unroll
    for (int w = 0; w < 4; ++w) {
        lo += *(const f32x4*)(red + (w * 8 + i) * 1024 + (n + 32 * gp) * 16);
        hi += *(const f32x4*)(red + (w * 8 + i) * 1024 + (n + 32 * gp + 16) * 16);
    }
    const int s = 256 * i + 16 * n + 8 * gp;
    const u32x4 xw = *(const u32x4*)(sm + C_XR + 2 * (2040 - s));
    const float o0 = rbf(lo[0]) + rbf(bf_hi(xw[3]) * Dv), o1 = rbf(lo[1]) + rbf(bf_lo(xw[3]) * Dv), o2 = rbf(lo[2]) + rbf(bf_hi(xw[2]) * Dv), o3 = rbf(lo[3]) + rbf(bf_lo(xw[2]) * Dv);
    const float o4 = rbf(hi[0]) + rbf(bf_hi(xw[1]) * Dv), o5 = rbf(hi[1]) + rbf(bf_lo(xw[1]) * Dv), o6 = rbf(hi[2]) + rbf(bf_hi(xw[0]) * Dv), o7 = rbf(hi[3]) + rbf(bf_lo(xw[0]) * Dv);
    st16_dev(wsr, (u32)WS_YYT + (u32)(d * S_ + s) * 2, (u32x4){pk2(o0, o1), pk2(o2, o3), pk2(o4, o5), pk2(o6, o7)});
}
template <class Hook>
__device__ __forceinline__ void conv_body(int ch0, const ConvIn (&in)[3], char* smem, rsrc_t wsr, Hook hook
                                          ) {
    const int lane = threadIdx.x & 63, wid = __builtin_amdgcn_readfirstlane(threadIdx.x >> 6);
    __syncthreads();
    STAMP_AT(3);
    const int n = lane & 15, g = lane >> 4, dd = g >> 1, gh = g & 1;
    const LAS char* l0 = (const LAS char*)smem;
    const LAS char* bb = l0 + C_XR + 480 - 32 * n + 32 * dd + 16 * gh + 128 * wid;
    const LAS char* ab = l0 + ((n & 1) ? C_K0 + 2 * (16 * dd + 8 * gh + n + 1) : C_K1 + 2 * (16 * dd + 8 * gh + n)) + 128 * wid;
    f32x4 acc[3][8];
    ConvSet s0, s1;
    conv_ld_all(s0, ab, bb, std::make_integer_sequence<int, 16>{});
    SCHED();
    constexpr auto SQ = std::make_integer_sequence<int, 36>{};
    conv_step<true, true>(acc[0], s0, s1, ab + 64, bb + 64, SQ);
    conv_step<true, false>(acc[0], s1, s0, ab + C_CH, bb + C_CH, SQ);
    conv_step<true, true>(acc[1], s0, s1, ab + C_CH + 64, bb + C_CH + 64, SQ);
    conv_step<true, false>(acc[1], s1, s0, ab + 2 * C_CH, bb + 2 * C_CH, SQ);
    conv_step<true, true>(acc[2], s0, s1, ab + 2 * C_CH + 64, bb + 2 * C_CH + 64, SQ);
    conv_step<false, false>(acc[2], s1, s0, ab, bb, SQ);
    asm volatile("s_nop 7" ::: "memory");
#pragma unroll
    for (int c = 0; c < 3; ++c)
#pragma unroll
        for (int i = 0; i < 8; ++i) asm volatile("" : "+v"(acc[c][i]));
    STAMP_AT(4);
    hook();
#pragma unroll
    for (int c = 0; c < 3; ++c)
#pragma unroll
        for (int i = 0; i < 8; ++i) *(f32x4*)(smem + C_RED + c * 32768 + (wid * 8 + i) * 1024 + lane * 16) = acc[c][i];
    __syncthreads();
#pragma unroll
    for (int c = 0; c < 3; ++c) conv_finish(ch0 + c, smem + c * C_CH, smem + C_RED + c * 32768, bf1(in[c].dv), wsr);
}

constexpr int R_MOD = 147456;
constexpr int R_YT = 110592;
constexpr int R_HT = R_YT + 8 * 1168;
struct RowsPre { float x[18], gh[18]; u32 xw[9]; };
__device__ __forceinline__ void rows_load(RowsPre& r, int t0, const u16* __restrict__ q) {
    const int tid = threadIdx.x, lane = tid & 63, hl = lane & 31, row = 2 * (tid >> 6) + (lane >> 5);
    const u32* qrow = (const u32*)(q + (size_t)(t0 + row) * D_);
#pragma unroll
    for (int i = 0; i < 9; ++i) r.xw[i] = qrow[hl + 32 * i];
}
struct ModIn { u32x4 mw[4]; };
__device__ __forceinline__ void mod_load(ModIn& m, rsrc_t wsr) {
#pragma unroll
    for (int i = 0; i < 4; ++i) { const int j = threadIdx.x + 256 * i; m.mw[i] = ld16_dev(wsr, (u32)WS_MOD + (j < 864 ? j : 0) * 16); }
}
__device__ __forceinline__ void mod_put(const ModIn& m, char* sm) {
#pragma unroll
    for (int i = 0; i < 4; ++i) { const int j = threadIdx.x + 256 * i; if (j < 864) *(u32x4*)(sm + R_MOD + j * 16) = m.mw[i]; }
}
__device__ __forceinline__ void rows_pre(RowsPre& r, const char* sm) {
    const int hl = threadIdx.x & 31;
#pragma unroll
    for (int i = 0; i < 9; ++i) { r.x[2 * i] = bf_lo(r.xw[i]); r.x[2 * i + 1] = bf_hi(r.xw[i]); }
    float s = 0.f;
#pragma unroll
    for (int i = 0; i < 18; ++i) s += r.x[i];
    const float mean = half_sum(s) * (1.0f / 576.0f);
    float vs = 0.f;
#pragma unroll
    for (int i = 0; i < 18; ++i) { const float dl = r.x[i] - mean; vs += dl * dl; }
    const float rstd = __builtin_amdgcn_rsqf(half_sum(vs) * (1.0f / 576.0f) + 1e-5f);
    const float* mod = (const float*)(sm + R_MOD);
#pragma unroll
    for (int i = 0; i < 9; ++i) {
        const int c = 2 * (hl + 32 * i);
        const f32x2 sa = *(const f32x2*)(mod + c), c1 = *(const f32x2*)(mod + 576 + c), ga = *(const f32x2*)(mod + 2 * 576 + c);
        r.gh[2 * i] = ga[0] * ((r.x[2 * i] - mean) * rstd * c1[0] + sa[0]);
        r.gh[2 * i + 1] = ga[1] * ((r.x[2 * i + 1] - mean) * rstd * c1[1] + sa[1]);
    }
}
__device__ __forceinline__ void rows_fetch(int t0, char* sm, const char* ws) {
    const int lane = threadIdx.x & 63;
    const char* src = ws + WS_YYT + t0 * 2;
    const u32 dst = (u32)(uintptr_t)(sm + R_HT);
#pragma unroll
    for (int i = 0; i < 9; ++i)
        asm volatile("s_mov_b32 m0, %2\n\ts_nop 0\n\tglobal_load_lds_dwordx4 %0, %1 sc1" :: "v"((u32)((lane + 64 * i) * (S_ * 2))), "s"(src), "s"(dst + i * 1024) : "memory");
    asm volatile("s_waitcnt vmcnt(0)" ::: "memory");
}
__device__ __forceinline__ void rows_main(const RowsPre& r, int t0, char* sm, rsrc_t wsr) {
    const int tid = threadIdx.x, lane = tid & 63, hl = lane & 31, row = 2 * (tid >> 6) + (lane >> 5);
    const int cg = tid >> 1, th = tid & 1;
    if (tid < 144) {
        u32x2 yw[8];
#pragma unroll
        for (int c = 0; c < 8; ++c) yw[c] = *(const u32x2*)(sm + R_HT + (8 * cg + c) * 16 + th * 8);
#pragma unroll
        for (int t = 0; t < 4; ++t) {
            u32x4 o;
#pragma unroll
            for (int kk = 0; kk < 4; ++kk) {
                const u32 a = yw[2 * kk][t >> 1], bq = yw[2 * kk + 1][t >> 1];
                o[kk] = (t & 1) ? __builtin_amdgcn_perm(bq, a, 0x07060302u) : __builtin_amdgcn_perm(bq, a, 0x05040100u);
            }
            *(u32x4*)(sm + R_YT + (4 * th + t) * 1168 + cg * 16) = o;
        }
    }
    __syncthreads();
    const float* mod = (const float*)(sm + R_MOD);
    float x1[18];
#pragma unroll
    for (int i = 0; i < 9; ++i) {
        u32* yp = (u32*)(sm + R_YT + row * 1168 + (hl + 32 * i) * 4);
        const u32 yv = *yp;
        const u32 p = pk2(r.x[2 * i] + r.gh[2 * i] * bf_lo(yv), r.x[2 * i + 1] + r.gh[2 * i + 1] * bf_hi(yv));
        *yp = p;
        x1[2 * i] = bf_lo(p); x1[2 * i + 1] = bf_hi(p);
    }
    float s = 0.f;
#pragma unroll
    for (int i = 0; i < 18; ++i) s += x1[i];
    const float mean = half_sum(s) * (1.0f / 576.0f);
    float vs = 0.f;
#pragma unroll
    for (int i = 0; i < 18; ++i) { const float dl = x1[i] - mean; vs += dl * dl; }
    const float rstd = __builtin_amdgcn_rsqf(half_sum(vs) * (1.0f / 576.0f) + 1e-5f);
#pragma unroll
    for (int i = 0; i < 9; ++i) {
        const int c = 2 * (hl + 32 * i);
        const f32x2 sb = *(const f32x2*)(mod + 3 * 576 + c), c1 = *(const f32x2*)(mod + 4 * 576 + c);
        const float h0 = (x1[2 * i] - mean) * rstd * c1[0] + sb[0], h1 = (x1[2 * i + 1] - mean) * rstd * c1[1] + sb[1];
        const int ks = c >> 5, kk = c & 31, gq = (kk & 15) >> 2, j = ((kk >> 4) << 2) | (kk & 3);
        *(u32*)(sm + R_HT + ((ks * 4 + gq) * 8 + row) * 16 + j * 2) = pk2(h0, h1);
    }
    __syncthreads();
    const u32 h2t = (u32)WS_H2T + (u32)(t0 >> 4) * 18 * 1024 + (t0 & 15) * 16, x1g = (u32)WS_X1 + (u32)t0 * D_ * 2;
#pragma unroll
    for (int it = 0; it < 3; ++it) {
        const int i = tid + 256 * it;
        if (i < 576) {
            const int kg = i >> 3, rr8 = i & 7;
            st16_dev(wsr, h2t + (kg >> 2) * 1024 + (kg & 3) * 256 + rr8 * 16, *(const u32x4*)(sm + R_HT + i * 16));
            const int rr = i / 72, cc = i - rr * 72;
            st16_dev(wsr, x1g + rr * 1152 + cc * 16, *(const u32x4*)(sm + R_YT + rr * 1168 + cc * 16));
        }
    }
}

namespace mlp {
constexpr int STG = 36864, LDS_BYTES = 163840;
constexpr int G1S = 9 * 144, G2S = 18 * 36, NS = G1S + G2S;
constexpr int LA = 4;
constexpr int PRO = 9 + 4 * LA + 18;
constexpr int TUS = 147456;

constexpr int F01 = 288;
constexpr int stage_of(int gs) { return gs < F01 ? 0 : gs < G1S ? gs / 144 : 9 + (gs - G1S) / 72; }
constexpr int rel_of(int gs) { return gs < F01 ? gs : gs < G1S ? gs % 144 : (gs - G1S) % 72; }
constexpr int grp_of(int gs) { return rel_of(gs) >> 2; }
constexpr int t_of(int gs) { return rel_of(gs) & 3; }
constexpr bool barpt(int gs) { return gs >= F01 && gs < NS && t_of(gs) == 3 && grp_of(gs) == (gs < G1S ? 3 : 0) && stage_of(gs) < 17; }
constexpr int dma_id(int gs) {
    if (gs >= NS || t_of(gs) != 3) return -1;
    const int g = grp_of(gs), s = stage_of(gs);
    if (gs < F01) return (g >= 36 && g < 72 && (g & 3) == 3) ? 3 * 16 + (g - 36) / 4 : -1;
    if (s == 2) return (g >= 4 && g < 22) ? ((g & 1) ? 5 : 4) * 16 + (g - 4) / 2 : -1;
    if (s + 3 > 17) return -1;
    if (s == 8) return (g >= 4 && g < 13) ? 11 * 16 + g - 4 : -1;
    if (gs < G1S) { constexpr int G[9] = {5, 7, 9, 12, 14, 16, 20, 22, 24}; for (int i = 0; i < 9; ++i) if (G[i] == g) return (s + 3) * 16 + i; return -1; }
    return (g & 1) ? (s + 3) * 16 + (g >> 1) : -1;
}
constexpr int bload(int gs) {
    if (gs >= F01 || t_of(gs) != 1) return -1;
    const int ks = 2 * (gs / 32) + LA + (((gs % 32) >> 2) >> 2), i = (gs >> 2) & 3;
    return ks < 18 ? ks * 4 + i : -1;
}
constexpr bool unit_stores(int v) { return v == 1 || v == 3 || v == 4 || v == 6 || v == 8; }
constexpr int unit_end(int v) { return v < 2 ? F01 : 144 * (v + 1); }
constexpr int hload(int gs) {
    if (t_of(gs) != 3) return -1;
    const int s = stage_of(gs), g = grp_of(gs);
    if (s == 8) return (g >= 13 && g < 33) ? g - 13 : -1;
    if (s < 9 || g < 2 || g > 16 || (g & 1)) return -1;
    const int i = (g - 2) >> 1;
    if (s == 9) return 20 + i;
    if (s == 10) return 28 + i;
    if (s == 11) return i < 4 ? 36 + i : -1;
    if (s >= 12 && s <= 15) return 40 + 8 * (s - 12) + i;
    return -1;
}
constexpr int pub(int gs) { return (stage_of(gs) == 6 && grp_of(gs) == 27 && t_of(gs) == 3) ? 1 : (stage_of(gs) == 10 && grp_of(gs) == 9 && t_of(gs) == 1) ? 2 : 0; }
constexpr int poll_issue(int gs) { return (stage_of(gs) == 7 && grp_of(gs) == 27 && t_of(gs) == 3) ? 1 : (stage_of(gs) == 11 && grp_of(gs) == 9 && t_of(gs) == 1) ? 2 : 0; }
struct EpiSched { signed char ev[G1S], ep[G1S], sv[G1S], st[G1S]; };
constexpr EpiSched make_epi() {
    EpiSched e{};
    int v = 0, p = 0, pend_v = -1, pend_lo = 0, pend_hi = 0;
    for (int gs = 0; gs < G1S; ++gs) {
        e.ev[gs] = -1; e.ep[gs] = -1; e.sv[gs] = -1; e.st[gs] = -1;
        if (gs < F01) continue;
        const int t = t_of(gs);
        if (t == 0) continue;
        const bool vm = barpt(gs) || dma_id(gs) >= 0 || hload(gs) >= 0 || pub(gs) || poll_issue(gs);
        if (t == 3 && !vm && pend_lo < pend_hi) { e.sv[gs] = (signed char)pend_v; e.st[gs] = (signed char)pend_lo; ++pend_lo; continue; }
        if (t == 3 && vm) continue;
        if (v < 8 && gs >= unit_end(v) + 8 && (pend_lo == pend_hi || pend_v == v || !unit_stores(v) || true)) {
            e.ev[gs] = (signed char)v; e.ep[gs] = (signed char)p;
            ++p;
            if ((p & 15) == 0 && unit_stores(v)) { if (pend_v != v) { pend_v = v; pend_lo = p / 16 - 1; } pend_hi = p / 16; }
            if (p == 64) { p = 0; ++v; }
        }
    }
    return e;
}
constexpr EpiSched ES = make_epi();
constexpr int hstore(int gs) { return gs < G1S ? ES.st[gs] : -1; }
constexpr int epi_last(int v) { for (int gs = G1S - 1; gs >= 0; --gs) if (ES.ev[gs] == v) return gs; return -1; }
constexpr int store_last(int v) { for (int gs = G1S - 1; gs >= 0; --gs) if (ES.sv[gs] == v && ES.st[gs] == 3) return gs; return -1; }
constexpr bool epi_ok() {
    for (int v = 0; v < 8; ++v) {
        if (epi_last(v) < 0 || ES.ep[epi_last(v)] != 63) return false;
        if (v + 3 <= 8 && epi_last(v) >= 144 * (v + 3)) return false;
        if (unit_stores(v) && store_last(v) < 0) return false;
    }
    return store_last(4) + 40 < 6 * 144 + 27 * 4;
}
static_assert(epi_ok(), "epilogue schedule");
constexpr int vm_ops(int gs) { return (pub(gs) ? 1 : 0) + (poll_issue(gs) ? 1 : 0) + (hstore(gs) >= 0) + (bload(gs) >= 0) + (hload(gs) >= 0) + (dma_id(gs) >= 0); }
struct Ledger { int cum[NS + 1]; };
constexpr Ledger make_ledger() {
    Ledger l{}; int n = PRO;
    for (int gs = 0; gs < NS; ++gs) { if (gs == G1S) n += 4; l.cum[gs] = n; n += vm_ops(gs); }
    l.cum[NS] = n; return l;
}
constexpr Ledger LG = make_ledger();
constexpr int seq_in(int gs, int kind) {
    int n = LG.cum[gs];
    if (kind > 0 && pub(gs)) ++n;
    if (kind > 1 && poll_issue(gs)) ++n;
    if (kind > 2 && hstore(gs) >= 0) ++n;
    if (kind > 3 && bload(gs) >= 0) ++n;
    if (kind > 4 && hload(gs) >= 0) ++n;
    return n;
}
constexpr int seq_dma_last(int s) {
    if (s <= 2) return 9 * s + 8;
    for (int gs = 0; gs < NS; ++gs) if (dma_id(gs) == s * 16 + 8) return seq_in(gs, 5);
    return -1;
}
constexpr int seq_b(int ks) {
    if (ks < LA) return 27 + 4 * ks + 3;
    for (int gs = 0; gs < F01; ++gs) if (bload(gs) == ks * 4 + 3) return seq_in(gs, 3);
    return -1;
}
constexpr int seq_h(int kk) {
    for (int gs = 8 * 144; gs < NS; ++gs) if (hload(gs) == kk * 4 + 3) return seq_in(gs, 4);
    return -1;
}
constexpr int seq_poll(int part) {
    for (int gs = 0; gs < NS; ++gs) if (poll_issue(gs) == part) return seq_in(gs, 1);
    return -1;
}
constexpr int seq_store_last(int part) {
    if (part == 2) return LG.cum[G1S] - 1;
    return seq_in(store_last(4), 2);
}
struct Work { int unit, mat, k, t, n; };
constexpr Work work_of(int gs) {
    if (gs < F01) { const int p = gs / 32, um = (gs % 32) / 8, r = gs % 8; return {um >> 1, um & 1, 2 * p + (r & 1), r >> 1, 8 * p + 2 * um + (r & 1)}; }
    if (gs < G1S) { const int u = gs / 144, bi = (gs % 144) / 24, r = gs % 24; return {u, bi & 1, 6 * (bi >> 1) + r % 6, r / 6, 36 * u + 6 * bi + r % 6}; }
    const int s = (gs - G1S) / 72, r = (gs - G1S) % 72, du = r / 8, r8 = r % 8;
    return {du, 0, 2 * s + (r8 & 1), r8 >> 1, 324 + 18 * s + 2 * du + (r8 & 1)};
}
constexpr int NFRAG = 486, RQ = 12, FR0 = 8;
constexpr int frag_first(int n) {
    if (n < 72) return 32 * (n / 8) + 8 * ((n % 8) >> 1) + (n & 1);
    if (n < 324) return 144 * (n / 36) + 24 * ((n % 36) / 6) + n % 6;
    return G1S + 72 * ((n - 324) / 18) + 8 * (((n - 324) % 18) >> 1) + (n & 1);
}
constexpr int frag_last(int n) { return frag_first(n) + ((n >= 72 && n < 324) ? 18 : 6); }
constexpr int frag_stage(int n) { return n < 72 ? (n % 8) >> 2 : n < 324 ? n / 36 : 9 + (n - 324) / 18; }
constexpr int frag_off(int n) {
    if (n < 72) return (2 * (2 * (n / 8) + (n & 1)) + (((n % 8) >> 1) & 1)) * 1024;
    if (n < 324) { const int bi = (n % 36) / 6; return (2 * (6 * (bi >> 1) + n % 6) + (bi & 1)) * 1024; }
    return (n & 1) * 9216 + (((n - 324) % 18) >> 1) * 32;
}
constexpr int stage_ready(int s) {
    if (s <= 2) return 0;
    for (int gs = 0; gs < NS; ++gs) if (barpt(gs) && stage_of(gs) == s - 1) return gs + 1;
    return NS;
}
struct PfSched { short id[NS]; };
constexpr PfSched make_pf() {
    PfSched t{};
    int m = FR0, rdy = stage_ready(frag_stage(FR0));
    for (int gs = 0; gs < NS; ++gs) {
        t.id[gs] = -1;
        if ((gs & 3) != 0 || m >= NFRAG) continue;
        if (m >= RQ && frag_last(m - RQ) >= gs) continue;
        if (rdy > gs) continue;
        t.id[gs] = (short)m;
        ++m;
        if (m < NFRAG && frag_stage(m) != frag_stage(m - 1)) rdy = stage_ready(frag_stage(m));
    }
    return t;
}
constexpr PfSched PF = make_pf();
constexpr bool pf_ok() {
    int cnt = FR0;
    for (int gs = 0; gs < NS; ++gs) if (PF.id[gs] >= 0) { if (PF.id[gs] != cnt || frag_first(cnt) - gs < 8) return false; ++cnt; }
    return cnt == NFRAG;
}
static_assert(pf_ok(), "fragment prefetch schedule");
constexpr int clampv(int n) { return n > 63 ? 63 : (n < 0 ? 0 : n); }
constexpr int wait_for(int gs, int seq) { return clampv(LG.cum[gs] - seq - 1); }

template <int N> __device__ __forceinline__ void wait_vm() { asm volatile("s_waitcnt vmcnt(%0)" :: "n"(N) : "memory"); }

struct Ctx {
    bf16x8 Bv[2][4];
    bf16x8 Aq[12];
    f32x4 ac[3][2][4];
    f32x4 a2[9][4];
    float e0, e1;
    u32x4 hp[4];
    u32 vo1[4], vo2[4];
    u32 lane16, epoch;
    u32x4 pollv;
    const LAS char* rd[4]; const LAS char* rd2[4];
    const char* w1src; const char* w2src; const char* bsrc; const char* hsrc;
    u32 voff2[9];
    int sub;
    u32 dmadst;
    u32 hso;
    int jj;
    __amdgpu_buffer_rsrc_t hrs;
    u32* flag_pub; u32* flag_poll;
};

template <int KS, int T, bool ZERO> __device__ __forceinline__ void mfma(Ctx& cx, f32x4& c, const bf16x8& a) {
    constexpr int r = (KS * 4 + T) * 4;
    if constexpr (KS < 16) {
        if constexpr (ZERO) asm volatile("v_mfma_f32_16x16x32_bf16 %0, %1, a[%c2:%c3], 0" : "=v"(c) : "v"(a), "n"(r), "n"(r + 3));
        else asm volatile("v_mfma_f32_16x16x32_bf16 %0, %1, a[%c2:%c3], %0" :: "v"(c), "v"(a), "n"(r), "n"(r + 3));
    } else {
        asm volatile("v_mfma_f32_16x16x32_bf16 %0, %1, %2, %0" :: "v"(c), "v"(a), "v"(cx.Bv[KS - 16][T]));
    }
}
template <int KS, int T, bool SC1> __device__ __forceinline__ void gload(Ctx& cx, u32 voff, const char* sb) {
    constexpr int r = (KS * 4 + T) * 4, off = (KS & 3) * 1024;
    if constexpr (KS < 16) {
        if constexpr (SC1) asm volatile("global_load_dwordx4 a[%c2:%c3], %0, %1 offset:%4 sc1" :: "v"(voff), "s"(sb), "n"(r), "n"(r + 3), "n"(off) : "memory");
        else asm volatile("global_load_dwordx4 a[%c2:%c3], %0, %1 offset:%4" :: "v"(voff), "s"(sb), "n"(r), "n"(r + 3), "n"(off) : "memory");
    } else {
        if constexpr (SC1) asm volatile("global_load_dwordx4 %0, %1, %2 offset:%3 sc1" : "=v"(cx.Bv[KS - 16][T]) : "v"(voff), "s"(sb), "n"(off) : "memory");
        else asm volatile("global_load_dwordx4 %0, %1, %2 offset:%3" : "=v"(cx.Bv[KS - 16][T]) : "v"(voff), "s"(sb), "n"(off) : "memory");
    }
}
template <int S, int P> __device__ __forceinline__ void dma_m0(const Ctx& c) {
    asm volatile("s_mov_b32 m0, %0" :: "s"(c.dmadst + (u32)((S & 3) * STG + (P >> 2) * 4096)) : "memory");
}
constexpr int nat_kstep(int ks2) {
    if (ks2 < 10) { constexpr int T[5] = {0, 1, 5, 6, 4}; return 9 * (ks2 / 5) + T[ks2 % 5]; }
    const int jj = (ks2 - 10) >> 1, h = (ks2 - 10) & 1;
    return 9 * (jj >> 1) + ((jj & 1) ? 7 : 2) + h;
}
template <int S, int P> __device__ __forceinline__ void dma_go(const Ctx& c) {
    if constexpr (S < 9) {
        const char* sb = c.w1src + (size_t)S * STG + (P >> 2) * 4096;
        asm volatile("global_load_lds_dwordx4 %0, %1 offset:%2 sc1" :: "v"(c.lane16), "s"(sb), "n"((P & 3) * 1024) : "memory");
    } else {
        constexpr int n0 = nat_kstep(2 * (S - 9)), n1 = nat_kstep(2 * (S - 9) + 1);
        const char* sb = c.w2src + (size_t)(c.sub ? n1 : n0) * (32 * D_ * 2);
        asm volatile("global_load_lds_dwordx4 %0, %1 offset:%2" :: "v"(c.voff2[P]), "s"(sb), "n"((P & 3) * 1024) : "memory");
    }
}
template <int ID> __device__ __forceinline__ void load_b(Ctx& c) {
    constexpr int ks = ID >> 2, t = ID & 3;
    gload<ks, t, true>(c, c.vo1[t], c.bsrc + (ks >> 2) * 4096);
}
template <int ID> __device__ __forceinline__ void load_h(Ctx& c) {
    constexpr int kk = ID >> 2, t = ID & 3;
    gload<kk, t, true>(c, c.vo2[t], c.hsrc + (kk >> 2) * 4096);
}
template <int KS> __device__ __forceinline__ void pin_b(Ctx& c) {
#pragma unroll
    for (int t = 0; t < 4; ++t) { if constexpr (KS >= 16) asm volatile("" : "+v"(c.Bv[KS - 16][t])); }
}
template <int V, int P> __device__ __forceinline__ void epi_piece(Ctx& c) {
    constexpr int pr = P >> 3, st = P & 7, t = pr >> 1, r = 2 * (pr & 1), par = V % 3;
    if constexpr (st == 0) {
        asm volatile("v_mul_f32 %0, 0xbfb8aa3b, %1" : "=v"(c.e0) : "v"(c.ac[par][0][t][r]));
        asm volatile("v_mul_f32 %0, 0xbfb8aa3b, %1" : "=v"(c.e1) : "v"(c.ac[par][0][t][r + 1]));
    } else if constexpr (st == 1) asm volatile("v_exp_f32 %0, %0" :: "v"(c.e0));
    else if constexpr (st == 2) asm volatile("v_exp_f32 %0, %0" :: "v"(c.e1));
    else if constexpr (st == 3) {
        asm volatile("v_add_f32 %0, 1.0, %0" :: "v"(c.e0));
        asm volatile("v_add_f32 %0, 1.0, %0" :: "v"(c.e1));
    } else if constexpr (st == 4) asm volatile("v_rcp_f32 %0, %0" :: "v"(c.e0));
    else if constexpr (st == 5) asm volatile("v_rcp_f32 %0, %0" :: "v"(c.e1));
    else if constexpr (st == 6) {
        asm volatile("v_mul_f32 %0, %0, %1" :: "v"(c.e0), "v"(c.ac[par][1][t][r]));
        asm volatile("v_mul_f32 %0, %0, %1" :: "v"(c.e1), "v"(c.ac[par][1][t][r + 1]));
    } else {
        constexpr bool first = (V == 0 || V == 2 || V == 5 || V == 7);
        u32 o;
        asm volatile("v_cvt_pk_bf16_f32 %0, %1, %2" : "=v"(o) : "v"(c.e0), "v"(c.e1));
        c.hp[t][(first ? 0 : 2) + (pr & 1)] = o;
    }
}
template <int V, int T> __device__ __forceinline__ void store_h(Ctx& c) {
    const int m5 = 5 * (c.jj >> 1), ab = c.jj & 1;
    if constexpr (V == 4) {
        const u32x2 d = {c.hp[T][2], c.hp[T][3]};
        __builtin_amdgcn_raw_buffer_store_b64(d, c.hrs, c.vo2[T] + 8 * ab, c.hso + (m5 + 4) * 1024, 16);
    } else {
        const int kstep = (V == 1) ? m5 + 2 * ab : (V == 3) ? m5 + 2 * ab + 1 : (V == 6) ? 10 + 2 * c.jj : 11 + 2 * c.jj;
        __builtin_amdgcn_raw_buffer_store_b128(c.hp[T], c.hrs, c.vo2[T], c.hso + kstep * 1024, 16);
    }
}
template <int PART> __device__ __forceinline__ void poll_check(Ctx& c) {
    asm volatile("" : "+v"(c.pollv));
    const u32 e = c.epoch;
    if (!__all(c.pollv[0] == e && c.pollv[1] == e && c.pollv[2] == e && c.pollv[3] == e)) {
        const u32* p = c.flag_poll + (PART - 1) * 1024 + ((c.lane16 >> 4) & 3);
        poll_eq(p, e);
    }
}
template <int V, int P> __device__ __forceinline__ void epi_piece_padded(Ctx& c) {
    epi_piece<V, P>(c);
    if constexpr ((P & 7) == 2 || (P & 7) == 5) asm volatile("s_nop 0");
}
template <int V, int... P> __device__ __forceinline__ void epi_all(Ctx& c, std::integer_sequence<int, P...>) { (epi_piece_padded<V, P>(c), ...); }

template <int GS> __device__ __forceinline__ void slot(Ctx& c) {
    constexpr bool g1 = GS < G1S;
    constexpr int S = stage_of(GS), R = rel_of(GS), grp = R >> 2, t = R & 3;
    constexpr Work W = work_of(GS);
    constexpr bool f01 = GS < F01;
    if constexpr (GS == G1S) {
        asm volatile("s_nop 7" ::: "memory");
        epi_all<8>(c, std::make_integer_sequence<int, 64>{});
        store_h<8, 0>(c); store_h<8, 1>(c); store_h<8, 2>(c); store_h<8, 3>(c);
        SCHED();
    }
    if constexpr (f01 && GS % 32 == 0) { wait_vm<wait_for(GS, seq_b(2 * (GS / 32) + 1))>(); pin_b<2 * (GS / 32)>(c); pin_b<2 * (GS / 32) + 1>(c); }
    if constexpr (GS == 8 * 144) { wait_vm<wait_for(GS, seq_poll(1))>(); poll_check<1>(c); }
    if constexpr (!g1 && R == 0) { wait_vm<wait_for(GS, seq_h(2 * (S - 9) + 1))>(); pin_b<2 * (S - 9)>(c); pin_b<2 * (S - 9) + 1>(c); }
    if constexpr (dma_id(GS) >= 0) dma_m0<dma_id(GS) / 16, dma_id(GS) % 16>(c);
    if constexpr (g1) mfma<W.k, W.t, (W.k == 0)>(c, c.ac[W.unit % 3][W.mat][W.t], c.Aq[W.n % RQ]);
    else mfma<W.k, W.t, (W.k == 0)>(c, c.a2[W.unit][W.t], c.Aq[W.n % RQ]);
    if constexpr (barpt(GS)) {
        constexpr int n1 = wait_for(GS, seq_dma_last(S + 1));
        constexpr int n2 = (S == 12) ? wait_for(GS, seq_poll(2)) : 63;
        wait_vm<(n1 < n2 ? n1 : n2)>();
        if constexpr (S == 12) poll_check<2>(c);
        __builtin_amdgcn_s_barrier();
        asm volatile("" ::: "memory");
    }
    if constexpr (pub(GS)) {
        wait_vm<wait_for(GS, seq_store_last(pub(GS)))>();
        if (c.lane16 == 0) flag_set(c.flag_pub + (pub(GS) - 1) * 1024, c.epoch);
    }
    if constexpr (poll_issue(GS)) asm volatile("global_load_dwordx4 %0, %1, %2 sc1" : "=v"(c.pollv) : "v"(0u), "s"(c.flag_poll + (poll_issue(GS) - 1) * 1024) : "memory");
    {
        constexpr int f = PF.id[GS], fh = (GS & 3) == 1 ? PF.id[GS - ((GS & 3) == 1 ? 1 : 0)] : -1;
        if constexpr (f >= 0 && f < 324) c.Aq[f % RQ] = *(const LAS bf16x8*)(c.rd[frag_stage(f) & 3] + frag_off(f));
        constexpr int ft = f >= 324 ? f : fh >= 324 ? fh : -1;
        if constexpr (ft >= 0) {
            constexpr int half = f >= 324 ? 0 : 1;
            const s16x4 h = __builtin_amdgcn_ds_read_tr16_b64_v4i16((LAS s16x4*)(c.rd2[frag_stage(ft) & 3] + frag_off(ft) + half * (16 * 288)));
            const u32x2 hh = __builtin_bit_cast(u32x2, h);
            u32x4 a = __builtin_bit_cast(u32x4, c.Aq[ft % RQ]);
            a[2 * half] = hh[0]; a[2 * half + 1] = hh[1];
            c.Aq[ft % RQ] = __builtin_bit_cast(bf16x8, a);
        }
    }
    if constexpr (g1) {
        if constexpr (ES.ev[g1 ? GS : 0] >= 0) epi_piece<ES.ev[g1 ? GS : 0], ES.ep[g1 ? GS : 0]>(c);
        if constexpr (ES.sv[g1 ? GS : 0] >= 0) store_h<ES.sv[g1 ? GS : 0], ES.st[g1 ? GS : 0]>(c);
    }
    if constexpr (bload(GS) >= 0) load_b<bload(GS)>(c);
    if constexpr (hload(GS) >= 0) load_h<hload(GS)>(c);
    if constexpr (dma_id(GS) >= 0) dma_go<dma_id(GS) / 16, dma_id(GS) % 16>(c);
    SCHED();
}
template <int BASE, int... I> __device__ __forceinline__ void run_slots(Ctx& c, std::integer_sequence<int, I...>) { (slot<BASE + I>(c), ...); }
template <int S, int P> __device__ __forceinline__ void dma_one(const Ctx& c) { dma_m0<S, P>(c); asm volatile("s_nop 0"); dma_go<S, P>(c); }
template <int S, int... P> __device__ __forceinline__ void dma_stage(const Ctx& c, std::integer_sequence<int, P...>) { (dma_one<S, P>(c), ...); }
template <int... I> __device__ __forceinline__ void load_b_first(Ctx& c, std::integer_sequence<int, I...>) { (load_b<I>(c), ...); }
}

__device__ __forceinline__ void touch(u32& d, const char* p) { asm volatile("global_load_dword %0, %1, off" : "=v"(d) : "v"(p) : "memory"); }
__global__ __launch_bounds__(256, 1) void k_main(const u16* __restrict__ qin, const u16* __restrict__ kin, const u16* __restrict__ vin, const u16* __restrict__ shift_k,
                                                 const u16* __restrict__ ssm_k, const u16* __restrict__ ssm_D, const u16* __restrict__ wg, const u16* __restrict__ wu,
                                                 const u16* __restrict__ wd, const u16* __restrict__ cvec, const u16* __restrict__ w_mod,
                                                 char* __restrict__ ws, u16* __restrict__ out) {
    using namespace mlp;
    extern __shared__ __attribute__((aligned(16))) char smem[];
    const int tid = threadIdx.x, lane = tid & 63, w = __builtin_amdgcn_readfirstlane(tid >> 6);
    const int b = blockIdx.x, x = b & 7, idx = b >> 3, q = x >> 1;
    const int mt = 4 * (x & 1) + (idx & 3), fcl = idx >> 2, fc = 8 * q + fcl;
    const int mt2 = 8 * (x & 1) + (idx & 7), cc = idx >> 3, tg = w & 1, kh = w >> 1;
    const rsrc_t wsr = __builtin_amdgcn_make_buffer_rsrc(ws, 0, (int)WS_END, 0x00020000);
    STAMP_DECL; STAMP_AT(0);
    const u32 epoch_in = __hip_atomic_load(flag_ptr(ws, F_EPOCH), __ATOMIC_RELAXED, __HIP_MEMORY_SCOPE_AGENT);
    unsigned long long pc_start;
    asm volatile("s_getpc_b64 %0" : "=s"(pc_start));
    u32 epoch;
    {
        const int t0 = (x * 32 + idx) * 8;
        RowsPre rp;
        u32 bg[10] = {0, 0, 0, 0, 0, 0, 0, 0, 0, 0};
        if (b < 192) {
            const int c0 = 72 * x + 3 * idx;
            ConvIn in[3];
            KvIn kin_;
            if (w < 3) touch(bg[8], (const char*)(b < 96 ? kin : vin) + (size_t)((b % 96) * 192 + w * 64 + lane) * 128);
            kv_load(kin_, c0, kin, vin, shift_k);
            conv_load_k(in[0], c0, ssm_k, ssm_D); conv_load_k(in[1], c0 + 1, ssm_k, ssm_D); conv_load_k(in[2], c0 + 2, ssm_k, ssm_D);
            rows_load(rp, t0, qin);
            kv_put(kin_, smem + C_RED);
            conv_put_k(in[0], smem); conv_put_k(in[1], smem + C_CH); conv_put_k(in[2], smem + 2 * C_CH);
            __syncthreads();
            STAMP_AT(1);
            kv_compute(kin_, c0, smem + C_RED, smem + C_XR, C_CH);
            epoch = __builtin_amdgcn_readfirstlane(epoch_in) + 1u;
            if (w == 3) {
                DRAIN();
#pragma unroll
                for (int i = 0; i < 8; ++i) { const int l = lane + 64 * (i & 3); touch(bg[i], (const char*)(i < 4 ? wg : wu) + (size_t)b * 27648 + (l < 216 ? l : 0) * 128); }
            }
            STAMP_AT(2);
            ModIn mi;
            const u32* mf = flag_ptr(ws, F_MOD + (lane < 54 ? lane : 0));
            u32 pm;
            asm volatile("global_load_dword %0, %1, off sc1" : "=v"(pm) : "v"(mf) : "memory");
            conv_body(c0, in, smem, wsr, [&]() {
                DRAIN();
                asm volatile("" : "+v"(pm));
                if (!__all(pm == epoch)) poll_eq(mf, epoch);
                mod_load(mi, wsr); } STAMP_ARGS);
            STAMP_AT(5);
            mod_put(mi, smem);
            DRAIN();
#pragma unroll
            for (int i = 0; i < 10; ++i) asm volatile("" :: "v"(bg[i]));
            __syncthreads();
            if (tid < 8) flag_set(flag_ptr(ws, F_CONV + tid * 256 + b), epoch);
            if (w == 3) {
#pragma unroll
                for (int i = 0; i < 4; ++i) { const int l = lane + 64 * i; touch(bg[i], (const char*)wd + (size_t)b * 27648 + (l < 216 ? l : 0) * 128); }
            }
        } else {
            if (w == 3 && idx < 28) {
                const char* cb = (const char*)(pc_start & ~127ull) + lane * 128;
#pragma unroll
                for (int i = 0; i < 8; ++i) touch(bg[i], cb + (i < 7 || lane < 32 ? i * 8192 : 0));
            }
            if (w == 3 && idx >= 28) {
#pragma unroll
                for (int i = 0; i < 3; ++i) { const size_t o = (size_t)(((idx - 28) * 3 + i) * 64 + lane) << 16; touch(bg[i], ws + (o < WS_END ? o : 0)); }
                touch(bg[3], (const char*)out + (size_t)(lane < 36 ? lane : 0) * 65536);
            }
            rows_load(rp, t0, qin);
            if (b - 192 < 54) mod_job(b - 192, cvec, w_mod, ws, (float*)(smem + R_MOD), (float*)(smem + R_MOD + 4096));
            epoch = __builtin_amdgcn_readfirstlane(epoch_in) + 1u;
            DRAIN();
            __syncthreads();
            if (tid == 0 && b - 192 < 54) flag_set(flag_ptr(ws, F_MOD + b - 192), epoch);
            STAMP_AT(1);
            if (b - 192 >= 54) { for (int i = 0; i < 40; ++i) __builtin_amdgcn_s_sleep(2); }
            tile_jobs(b - 192, wg, wu, smem, ws, wsr, epoch);
            STAMP_AT(2); STAMP_AT(3); STAMP_AT(4); STAMP_AT(5);
            poll_eq(flag_ptr(ws, F_MOD + (lane < 54 ? lane : 0)), epoch);
            ModIn mi;
            mod_load(mi, wsr);
            mod_put(mi, smem);
            __syncthreads();
        }
        rows_pre(rp, smem);
        STAMP_AT(6);
        if (w == 0) {
            poll_eq4(wsr, (u32)WS_CNT + (F_CONV + x * 256 + (lane < 48 ? lane : 0) * 4) * 4, epoch);
            rows_fetch(t0, smem, ws);
        }
        __syncthreads();
        STAMP_AT(7);
        rows_main(rp, t0, smem, wsr);
        DRAIN();
#pragma unroll
        for (int i = 0; i < 10; ++i) asm volatile("" :: "v"(bg[i]));
        __syncthreads();
        if (tid == 0) flag_set(flag_ptr(ws, F_ROWS + (t0 >> 6) * 8 + ((t0 >> 3) & 7)), epoch);
        STAMP_AT(8);
    }
    Ctx c;
#pragma unroll
    for (int t = 0; t < 4; ++t) c.hp[t] = (u32x4){0, 0, 0, 0};
    c.lane16 = lane * 16;
#pragma unroll
    for (int t = 0; t < 4; ++t) { c.vo1[t] = lane * 16 + t * 18432; c.vo2[t] = lane * 16 + t * TUS; }
    const LAS char* l0 = (const LAS char*)smem;
#pragma unroll
    for (int i = 0; i < 4; ++i) {
        c.rd[i] = l0 + i * STG + lane * 16; c.rd2[i] = l0 + i * STG + kh * 18432 + (4 * (lane >> 4) + ((lane & 15) >> 2)) * 288 + 8 * (lane & 3);
        asm volatile("" : "+v"(c.rd[i])); asm volatile("" : "+v"(c.rd2[i]));
    }
#pragma unroll
    for (int i = 0; i < 9; ++i) { const int ci = lane + 64 * i, row = (ci * 3641) >> 16, c16 = ci - row * 18; c.voff2[i] = (u32)(row * (D_ * 2) + c16 * 16 - (i & 3) * 1024); }
    c.sub = w & 1;
    c.w1src = ws + WS_WT1 + (size_t)fc * 331776 + w * 9216;
    c.w2src = (const char*)(wd + (size_t)(36 * q + 18 * kh) * 32 * D_ + cc * 144);
    c.bsrc = ws + WS_H2T + (size_t)(16 * mt + 4 * w) * 18432;
    c.hsrc = ws + WS_HT + (size_t)(8 * mt2 + 4 * tg) * TUS + (size_t)((q * 2 + kh) * 18) * 1024;
    c.dmadst = (u32)(uintptr_t)smem + w * 9216;
    c.jj = fcl & 3;
    c.hso = (u32)((16 * mt + 4 * w) * TUS + ((q * 2 + (fcl >> 2)) * 18) * 1024);
    c.hrs = __builtin_amdgcn_make_buffer_rsrc(ws + WS_HT, 0, (int)((size_t)S_ * F_ * 2), 0x00020000);
    c.epoch = epoch;
    c.flag_pub = flag_ptr(ws, F_H + ((((mt * 4 + w) * 4 + q) * 2 + (fcl >> 2))) * 4 + (fcl & 3));
    c.flag_poll = flag_ptr(ws, F_H + (((((mt2 >> 1) * 4 + 2 * (mt2 & 1) + tg) * 4 + q) * 2 + kh)) * 4);
    asm volatile("" ::: "a0", "a1", "a2", "a3", "a4", "a5", "a6", "a7", "a8", "a9", "a10", "a11", "a12", "a13", "a14", "a15", "a16", "a17", "a18", "a19", "a20", "a21", "a22", "a23", "a24", "a25", "a26", "a27", "a28", "a29", "a30", "a31", "a32", "a33", "a34", "a35", "a36", "a37", "a38", "a39", "a40", "a41", "a42", "a43", "a44", "a45", "a46", "a47", "a48", "a49", "a50", "a51", "a52", "a53", "a54", "a55", "a56", "a57", "a58", "a59", "a60", "a61", "a62", "a63", "a64", "a65", "a66", "a67", "a68", "a69", "a70", "a71", "a72", "a73", "a74", "a75", "a76", "a77", "a78", "a79", "a80", "a81", "a82", "a83", "a84", "a85", "a86", "a87", "a88", "a89", "a90", "a91", "a92", "a93", "a94", "a95", "a96", "a97", "a98", "a99", "a100", "a101", "a102", "a103", "a104", "a105", "a106", "a107", "a108", "a109", "a110", "a111", "a112", "a113", "a114", "a115", "a116", "a117", "a118", "a119", "a120", "a121", "a122", "a123", "a124", "a125", "a126", "a127", "a128", "a129", "a130", "a131", "a132", "a133", "a134", "a135", "a136", "a137", "a138", "a139", "a140", "a141", "a142", "a143", "a144", "a145", "a146", "a147", "a148", "a149", "a150", "a151", "a152", "a153", "a154", "a155", "a156", "a157", "a158", "a159", "a160", "a161", "a162", "a163", "a164", "a165", "a166", "a167", "a168", "a169", "a170", "a171", "a172", "a173", "a174", "a175", "a176", "a177", "a178", "a179", "a180", "a181", "a182", "a183", "a184", "a185", "a186", "a187", "a188", "a189", "a190", "a191", "a192", "a193", "a194", "a195", "a196", "a197", "a198", "a199", "a200", "a201", "a202", "a203", "a204", "a205", "a206", "a207", "a208", "a209", "a210", "a211", "a212", "a213", "a214", "a215", "a216", "a217", "a218", "a219", "a220", "a221", "a222", "a223", "a224", "a225", "a226", "a227", "a228", "a229", "a230", "a231", "a232", "a233", "a234", "a235", "a236", "a237", "a238", "a239", "a240", "a241", "a242", "a243", "a244", "a245", "a246", "a247", "a248", "a249", "a250", "a251", "a252", "a253", "a254", "a255");
    poll_eq(flag_ptr(ws, F_TILE + (fc >> 2) * 36 + (lane < 36 ? lane : 0)), epoch);
    dma_stage<0>(c, std::make_integer_sequence<int, 9>{});
    dma_stage<1>(c, std::make_integer_sequence<int, 9>{});
    dma_stage<2>(c, std::make_integer_sequence<int, 9>{});
    poll_eq(flag_ptr(ws, F_ROWS + (4 * mt + w) * 8 + (lane & 7)), epoch);
    STAMP_AT(9);
    load_b_first(c, std::make_integer_sequence<int, 4 * LA>{});
    wait_vm<4 * LA>();
    __builtin_amdgcn_s_barrier();
    asm volatile("" ::: "memory");
#pragma unroll
    for (int f = 0; f < FR0; ++f) c.Aq[f] = *(const LAS bf16x8*)(c.rd[frag_stage(f)] + frag_off(f));
    SCHED();
    STAMP_AT(10);
    run_slots<0>(c, std::make_integer_sequence<int, 288>{});
    STAMP_AT(11);
    run_slots<288>(c, std::make_integer_sequence<int, 144 * 3>{});
    run_slots<144 * 5>(c, std::make_integer_sequence<int, 144 * 4>{});
    STAMP_AT(12);
    run_slots<G1S>(c, std::make_integer_sequence<int, 324>{});
    run_slots<G1S + 324>(c, std::make_integer_sequence<int, 324>{});
    asm volatile("s_nop 7" ::: "memory");
#pragma unroll
    for (int u = 0; u < 9; ++u)
#pragma unroll
        for (int t = 0; t < 4; ++t) asm volatile("" : "+v"(c.a2[u][t]));
    SCHED();
    STAMP_AT(13);
    const int n = lane & 15, g = lane >> 4;
    u32x2 xr[3][2]; f32x4 gb[3];
#pragma unroll
    for (int i = 0; i < 3; ++i) {
        const int u = (w + 4 * i < 9) ? w + 4 * i : 8, d = 144 * cc + 16 * u + 4 * g;
#pragma unroll
        for (int t = 0; t < 2; ++t) xr[i][t] = __builtin_amdgcn_raw_buffer_load_b64(wsr, (u32)WS_X1 + (u32)((128 * mt2 + 32 * q + 16 * t + n) * D_ + d) * 2, 0, 16);
        gb[i] = *(const f32x4*)(smem + R_MOD + (5 * 576 + d) * 4);
    }
    __builtin_amdgcn_s_barrier();
    {
        char* ex = smem + w * 18432 + lane * 16;
#pragma unroll
        for (int t = 0; t < 2; ++t)
#pragma unroll
            for (int u = 0; u < 9; ++u) *(f32x4*)(ex + (t * 9 + u) * 1024) = kh ? c.a2[u][t] : c.a2[u][2 + t];
    }
    __syncthreads();
    const int j = 2 * tg + kh;
    u32* fred = flag_ptr(ws, F_RED + (mt2 * 4 + cc) * 16);
    const u32 slab = (u32)WS_SLAB + (u32)((mt2 * 4 + cc) * 16) * 9216;
    {
        const char* ey = smem + (w ^ 2) * 18432 + lane * 16;
        char* own = smem + 73728 + lane * 16;
#pragma unroll
        for (int u = 0; u < 9; ++u) {
            const f32x4 r0 = (kh ? c.a2[u][2] : c.a2[u][0]) + *(const f32x4*)(ey + u * 1024);
            const f32x4 r1 = (kh ? c.a2[u][3] : c.a2[u][1]) + *(const f32x4*)(ey + (9 + u) * 1024);
            if (j != q) st16_dev(wsr, slab + (j * 4 + q) * 9216 + u * 1024 + lane * 16, (u32x4){pk2(r0[0], r0[1]), pk2(r0[2], r0[3]), pk2(r1[0], r1[1]), pk2(r1[2], r1[3])});
            else { *(f32x4*)(own + u * 2048) = r0; *(f32x4*)(own + u * 2048 + 1024) = r1; }
        }
    }
    if (j != q) {
        DRAIN();
        if (lane == 0) flag_set(fred + j * 4 + q, epoch);
    }
    STAMP_AT(14);
    poll_eq(fred + q * 4 + (((lane & 3) == q) ? (q ^ 1) : (lane & 3)), epoch);
    __syncthreads();
    u32x4 rem[3][3];
#pragma unroll
    for (int i = 0; i < 3; ++i) {
        const int u = (w + 4 * i < 9) ? w + 4 * i : 8;
#pragma unroll
        for (int k = 0; k < 3; ++k) { const int qq = k + (k >= q ? 1 : 0); rem[i][k] = __builtin_amdgcn_raw_buffer_load_b128(wsr, slab + (q * 4 + qq) * 9216 + u * 1024 + lane * 16, 0, 16); }
    }
#pragma unroll
    for (int i = 0; i < 3; ++i) {
        const int u = w + 4 * i;
        if (u < 9) {
            const int d = 144 * cc + 16 * u + 4 * g;
#pragma unroll
            for (int t = 0; t < 2; ++t) {
                f32x4 s = *(const f32x4*)(smem + 73728 + u * 2048 + t * 1024 + lane * 16);
#pragma unroll
                for (int k = 0; k < 3; ++k) {
                    const u32 a = rem[i][k][2 * t], bq = rem[i][k][2 * t + 1];
                    s += (f32x4){bf_lo(a), bf_hi(a), bf_lo(bq), bf_hi(bq)};
                }
                const float o0 = bf_lo(xr[i][t][0]) + rbf(gb[i][0] * rbf(s[0])), o1 = bf_hi(xr[i][t][0]) + rbf(gb[i][1] * rbf(s[1]));
                const float o2 = bf_lo(xr[i][t][1]) + rbf(gb[i][2] * rbf(s[2])), o3 = bf_hi(xr[i][t][1]) + rbf(gb[i][3] * rbf(s[3]));
                *(u32x2*)(out + (size_t)(128 * mt2 + 32 * q + 16 * t + n) * D_ + d) = (u32x2){pk2(o0, o1), pk2(o2, o3)};
            }
        }
    }
    if (b == 0 && tid == 0) flag_set(flag_ptr(ws, F_EPOCH), epoch);
    STAMP_AT(15);
    STAMP_OUT(4, 16);
}

extern "C" void kernel_launch(void* const* d_in, const int* in_sizes, int n_in, void* d_out, int out_size, void* d_ws, size_t ws_size,
                              hipStream_t stream) {
    const u16* q = (const u16*)d_in[0];
    const u16* k = (const u16*)d_in[1];
    const u16* v = (const u16*)d_in[2];
    const u16* shift_k = (const u16*)d_in[3];
    const u16* ssm_k = (const u16*)d_in[4];
    const u16* ssm_D = (const u16*)d_in[5];
    const u16* wg = (const u16*)d_in[7];
    const u16* wu = (const u16*)d_in[8];
    const u16* wd = (const u16*)d_in[9];
    const u16* c = (const u16*)d_in[10];
    const u16* w_mod = (const u16*)d_in[11];
    char* ws = (char*)d_ws;
    (void)hipFuncSetAttribute((const void*)k_main, hipFuncAttributeMaxDynamicSharedMemorySize, mlp::LDS_BYTES);
    k_main<<<256, 256, mlp::LDS_BYTES, stream>>>(q, k, v, shift_k, ssm_k, ssm_D, wg, wu, wd, c, w_mod, ws, (u16*)d_out);
}
```

```cpp
#include <hip/hip_runtime.h>
#include <stdint.h>
#include <utility>

typedef unsigned short u16;
typedef unsigned int u32;
typedef __bf16 bf16x8 __attribute__((ext_vector_type(8)));
typedef __bf16 bf16x2 __attribute__((ext_vector_type(2)));
typedef float f32x4 __attribute__((ext_vector_type(4)));
typedef float f32x2 __attribute__((ext_vector_type(2)));
typedef u32 u32x4 __attribute__((ext_vector_type(4)));
typedef u32 u32x2 __attribute__((ext_vector_type(2)));
typedef short s16x4 __attribute__((ext_vector_type(4)));
#define LAS __attribute__((address_space(3)))
typedef LAS char* lptr;

constexpr int S_ = 2048, D_ = 576, F_ = 4608;

constexpr size_t WS_MOD = 0;
constexpr size_t WS_CNT = 65536;
constexpr size_t WS_YYT = WS_CNT + 65536;
constexpr size_t WS_X1  = WS_YYT + (size_t)D_ * S_ * 2;
constexpr size_t WS_H2T = WS_X1 + (size_t)D_ * S_ * 2;
constexpr size_t WS_HT  = WS_H2T + (size_t)D_ * S_ * 2;
constexpr size_t WS_WT1 = WS_HT + (size_t)S_ * F_ * 2;
constexpr size_t WS_SLAB = WS_WT1 + (size_t)2 * D_ * F_ * 2;
constexpr size_t WS_END = WS_SLAB + (size_t)16 * 4 * 4 * 4 * 9216;

#define STAMP_DECL
#define STAMP_ARGS
#define STAMP_AT(i)
#define STAMP_OUT(kid, n)
__device__ __forceinline__ float bf_lo(u32 w) { return __uint_as_float(w << 16); }
__device__ __forceinline__ float bf_hi(u32 w) { return __uint_as_float(w & 0xffff0000u); }
__device__ __forceinline__ float bf1(u16 w) { return __uint_as_float((u32)w << 16); }
__device__ __forceinline__ u32 pk2(float lo, float hi) {
    f32x2 v = {lo, hi};
    return __builtin_bit_cast(u32, __builtin_convertvector(v, bf16x2));
}
__device__ __forceinline__ float rbf(float x) { return (float)(__bf16)x; }
__device__ __forceinline__ float sigmoidf_(float x) { return __builtin_amdgcn_rcpf(1.0f + __builtin_amdgcn_exp2f(-1.4426950408889634f * x)); }

constexpr int F_MOD = 256;
constexpr int F_TILE = 512;
constexpr int F_CONV = 1024;
constexpr int F_ROWS = 3072;
constexpr int F_H = 3328;
constexpr int F_RED = 5376;
constexpr int F_EPOCH = 6400;
typedef __amdgpu_buffer_rsrc_t rsrc_t;
__device__ __forceinline__ u32* flag_ptr(char* ws, int idx) { return (u32*)(ws + WS_CNT) + idx; }
__device__ __forceinline__ void flag_set(u32* p, u32 e) { __hip_atomic_store(p, e, __ATOMIC_RELAXED, __HIP_MEMORY_SCOPE_AGENT); }
__device__ __forceinline__ void st16_dev(rsrc_t rs, u32 off, u32x4 v) { __builtin_amdgcn_raw_buffer_store_b128(v, rs, off, 0, 16); }
__device__ __forceinline__ u32x4 ld16_dev(rsrc_t rs, u32 off) { return __builtin_amdgcn_raw_buffer_load_b128(rs, off, 0, 16); }
__device__ __forceinline__ void poll_eq(const u32* p, u32 e) {
    for (int it = 0; it < (1 << 21); ++it) {
        if (__all(__hip_atomic_load(p, __ATOMIC_RELAXED, __HIP_MEMORY_SCOPE_AGENT) == e)) break;
        __builtin_amdgcn_s_sleep(1);
    }
}
__device__ __forceinline__ void poll_eq4(rsrc_t rs, u32 off, u32 e) {
    for (int it = 0; it < (1 << 21); ++it) {
        asm volatile("" ::: "memory");
        const u32x4 v = ld16_dev(rs, off);
        if (__all(v[0] == e && v[1] == e && v[2] == e && v[3] == e)) break;
        __builtin_amdgcn_s_sleep(1);
    }
}
#define DRAIN() asm volatile("s_waitcnt vmcnt(0)" ::: "memory")

#define SCHED() __builtin_amdgcn_sched_barrier(0)
__device__ __forceinline__ float dpp_add(float v, const int ctrl_sel) {
    int r;
    const int iv = __builtin_bit_cast(int, v);
    switch (ctrl_sel) {
        case 0: r = __builtin_amdgcn_update_dpp(0, iv, 0xB1, 0xf, 0xf, true); break;
        case 1: r = __builtin_amdgcn_update_dpp(0, iv, 0x4E, 0xf, 0xf, true); break;
        case 2: r = __builtin_amdgcn_update_dpp(0, iv, 0x141, 0xf, 0xf, true); break;
        default: r = __builtin_amdgcn_update_dpp(0, iv, 0x140, 0xf, 0xf, true); break;
    }
    return v + __builtin_bit_cast(float, r);
}
__device__ __forceinline__ float half_sum(float v) {
    v = dpp_add(v, 0); v = dpp_add(v, 1); v = dpp_add(v, 2); v = dpp_add(v, 3);
    auto r = __builtin_amdgcn_permlane16_swap(__float_as_uint(v), __float_as_uint(v), false, false);
    return __uint_as_float(r[0]) + __uint_as_float(r[1]);
}

struct KvIn { u32x2 kw[8], vw[8]; u32x4 sw; u32x2 sw2; };
__device__ __forceinline__ void kv_load(KvIn& p, int c0, const u16* __restrict__ k, const u16* __restrict__ v, const u16* __restrict__ shift_k) {
    const int cw = c0 & ~1;
#pragma unroll
    for (int i = 0; i < 8; ++i) p.kw[i] = *(const u32x2*)(k + (size_t)(threadIdx.x + 256 * i) * D_ + cw);
#pragma unroll
    for (int i = 0; i < 8; ++i) p.vw[i] = *(const u32x2*)(v + (size_t)(threadIdx.x + 256 * i) * D_ + cw);
    p.sw = (u32x4){*(const u32*)(shift_k + c0 * 4), *(const u32*)(shift_k + c0 * 4 + 2), *(const u32*)(shift_k + c0 * 4 + 4), *(const u32*)(shift_k + c0 * 4 + 6)};
    p.sw2 = (u32x2){*(const u32*)(shift_k + c0 * 4 + 8), *(const u32*)(shift_k + c0 * 4 + 10)};
}
constexpr int KV_RAWV = 257 * 72 + 8;
__device__ __forceinline__ void kv_put(const KvIn& p, char* raw) {
    const int tid = threadIdx.x;
#pragma unroll
    for (int i = 0; i < 8; ++i) {
        const int t = tid + 256 * i, o = 72 * (t >> 3) + 8 * (t & 7);
        *(u32x2*)(raw + 72 + o) = p.kw[i];
        *(u32x2*)(raw + KV_RAWV + o) = p.vw[i];
    }
    if (tid < 8) *(u32x2*)(raw + 8 * tid) = (u32x2){0, 0};
}
__device__ __forceinline__ void kv_compute(const KvIn& p, int c0, const char* raw, char* xr, int stride) {
    const int tid = threadIdx.x;
    u32x2 kw[11], vw[8];
#pragma unroll
    for (int i = 0; i < 11; ++i) kw[i] = *(const u32x2*)(raw + 72 * tid + (i < 3 ? 40 + 8 * i : 72 + 8 * (i - 3)));
#pragma unroll
    for (int i = 0; i < 8; ++i) vw[i] = *(const u32x2*)(raw + KV_RAWV + 72 * tid + 8 * i);
    const bool odd = c0 & 1;
#pragma unroll
    for (int c = 0; c < 3; ++c) {
        const u32 wa = c == 0 ? p.sw[0] : c == 1 ? p.sw[2] : p.sw2[0], wb = c == 0 ? p.sw[1] : c == 1 ? p.sw[3] : p.sw2[1];
        const float w0 = bf_lo(wa), w1 = bf_hi(wa), w2 = bf_lo(wb), w3 = bf_hi(wb);
        float kf[11], r[8];
#define KV_PICK(d) (c == 0 ? (odd ? bf_hi((d)[0]) : bf_lo((d)[0])) : c == 1 ? (odd ? bf_lo((d)[1]) : bf_hi((d)[0])) : (odd ? bf_hi((d)[1]) : bf_lo((d)[1])))
#pragma unroll
        for (int i = 0; i < 11; ++i) kf[i] = KV_PICK(kw[i]);
#pragma unroll
        for (int i = 0; i < 8; ++i) r[i] = (kf[i] * w3 + kf[i + 1] * w2 + kf[i + 2] * w1 + kf[i + 3] * w0) * KV_PICK(vw[i]);
#undef KV_PICK
        *(u32x4*)(xr + c * stride + 2 * (2040 - 8 * tid)) = (u32x4){pk2(r[7], r[6]), pk2(r[5], r[4]), pk2(r[3], r[2]), pk2(r[1], r[0])};
    }
}

__device__ __forceinline__ void mod_job(int job, const u16* __restrict__ c, const u16* __restrict__ w_mod, char* __restrict__ ws, float* sc  , float* red  ) {
    const int tid = threadIdx.x, lane = tid & 63, wid = tid >> 6;
    const int col0 = job * 64, rg = tid >> 3, c8 = tid & 7;
    u32x4 w[18];
#pragma unroll
    for (int i = 0; i < 18; ++i) w[i] = *(const u32x4*)(w_mod + (size_t)(rg + 32 * i) * 3456 + col0 + c8 * 8);
#pragma unroll
    for (int j = 0; j < 3; ++j) { const int r = tid + 256 * j; if (r < 576) { const float x = bf1(c[r]); sc[r] = rbf(x * sigmoidf_(x)); } }
    __syncthreads();
    float acc[8] = {0, 0, 0, 0, 0, 0, 0, 0};
#pragma unroll
    for (int i = 0; i < 18; ++i) {
        const float s = sc[rg + 32 * i];
#pragma unroll
        for (int j = 0; j < 4; ++j) { acc[2 * j] += s * bf_lo(w[i][j]); acc[2 * j + 1] += s * bf_hi(w[i][j]); }
    }
#pragma unroll
    for (int j = 0; j < 8; ++j) {
        acc[j] += __shfl_xor(acc[j], 8); acc[j] += __shfl_xor(acc[j], 16); acc[j] += __shfl_xor(acc[j], 32);
    }
    if (lane < 8) {
#pragma unroll
        for (int j = 0; j < 8; ++j) red[wid * 64 + lane * 8 + j] = acc[j];
    }
    __syncthreads();
    if (tid < 64) {
        float m = rbf((red[tid] + red[64 + tid]) + (red[128 + tid] + red[192 + tid]));
        const int which = col0 / 576;
        if (which == 1 || which == 4) m = rbf(1.0f + m);
        __hip_atomic_store((u32*)(ws + WS_MOD) + col0 + tid, __float_as_uint(m), __ATOMIC_RELAXED, __HIP_MEMORY_SCOPE_AGENT);
    }
}

__device__ __forceinline__ const char* tile_base(int job, const u16* __restrict__ wg, const u16* __restrict__ wu) {
    const int ks = job % 18, mat = (job / 18) & 1, cg = job / 36;
    return (const char*)((mat ? wu : wg) + (size_t)(32 * ks) * F_ + cg * 576);
}
__device__ __forceinline__ void tile_dma(int job, const u16* __restrict__ wg, const u16* __restrict__ wu, char* tile, const u32 (&voff)[9]) {
    const int w = __builtin_amdgcn_readfirstlane(threadIdx.x >> 6);
    const char* base = tile_base(job, wg, wu) + (size_t)w * 8 * (F_ * 2);
    const u32 dst = (u32)(uintptr_t)tile + w * 9216;
#pragma unroll
    for (int i = 0; i < 9; ++i)
        asm volatile("s_mov_b32 m0, %2\n\ts_nop 0\n\tglobal_load_lds_dwordx4 %0, %1" :: "v"(voff[i]), "s"(base), "s"(dst + i * 1024) : "memory");
}
__device__ __forceinline__ void tile_out(int job, const char* tile, rsrc_t wsr) {
    const int lane = threadIdx.x & 63, w = threadIdx.x >> 6;
    const int ks = job % 18, mat = (job / 18) & 1, fc = 4 * (job / 36) + w;
    const int g = lane >> 4, q4 = (lane & 15) >> 2, p = lane & 3;
    const LAS char* rd = (const LAS char*)tile + (4 * g + q4) * 1152 + 8 * p + w * 288;
#pragma unroll
    for (int u = 0; u < 9; ++u) {
        const int pos = (fc & 1) ? (u == 0 ? 4 : u <= 4 ? u - 1 : u) : (u == 8 ? 4 : u < 4 ? u : u + 1);
        const s16x4 lo = __builtin_amdgcn_ds_read_tr16_b64_v4i16((LAS s16x4*)(rd + u * 32));
        const s16x4 hi = __builtin_amdgcn_ds_read_tr16_b64_v4i16((LAS s16x4*)(rd + u * 32 + 16 * 1152));
        typedef short s16x8 __attribute__((ext_vector_type(8)));
        const s16x8 r = __builtin_shufflevector(lo, hi, 0, 1, 2, 3, 4, 5, 6, 7);
        st16_dev(wsr, (u32)WS_WT1 + fc * 331776 + pos * 36864 + (ks * 2 + mat) * 1024 + lane * 16, __builtin_bit_cast(u32x4, r));
    }
}
__device__ __forceinline__ void tile_jobs(int i0, const u16* __restrict__ wg, const u16* __restrict__ wu, char* smem, char* ws, rsrc_t wsr, u32 epoch) {
    const int lane = threadIdx.x & 63;
    u32 voff[9];
#pragma unroll
    for (int i = 0; i < 9; ++i) { const int ci = lane + 64 * i, row = (ci * 58255) >> 22, c16 = ci - row * 72; voff[i] = (u32)(row * (F_ * 2) + c16 * 16); }
#pragma unroll
    for (int j = 0; j < 4; ++j) tile_dma(i0 + 64 * j, wg, wu, smem + j * 36864, voff);
#pragma unroll
    for (int j = 0; j < 4; ++j) {
        asm volatile("s_waitcnt vmcnt(27)" ::: "memory");
        __syncthreads();
        tile_out(i0 + 64 * j, smem + j * 36864, wsr);
    }
    if (i0 < 32) {
        __syncthreads();
        tile_dma(i0 + 256, wg, wu, smem, voff);
        asm volatile("s_waitcnt vmcnt(0)" ::: "memory");
        __syncthreads();
        tile_out(i0 + 256, smem, wsr);
    }
    DRAIN();
    __syncthreads();
    if (threadIdx.x < 5 && i0 + 64 * threadIdx.x < 288) flag_set(flag_ptr(ws, F_TILE + i0 + 64 * threadIdx.x), epoch);
}

constexpr int C_XR = 0;
constexpr int C_K0 = 4608;
constexpr int C_K1 = C_K0 + 4128 + 32;
constexpr int C_CH = C_K1 + 4160;
constexpr int C_RED = 3 * C_CH;
constexpr int C_LDS = C_RED + 3 * 32768;
static_assert(((C_K1 - C_K0) / 4) % 32 == 16, "bank offset");
static_assert(C_CH % 16 == 0 && C_K0 % 16 == 0 && C_K1 % 16 == 0, "align");
template <bool ZERO> __device__ __forceinline__ void mfma_v(f32x4& c, const bf16x8& a, const bf16x8& b) {
    if constexpr (ZERO) asm volatile("v_mfma_f32_16x16x32_bf16 %0, %1, %2, 0" : "=v"(c) : "v"(a), "v"(b));
    else asm volatile("v_mfma_f32_16x16x32_bf16 %0, %1, %2, %0" :: "v"(c), "v"(a), "v"(b));
}
struct ConvSet { bf16x8 A[8], B[8]; };
template <int J> __device__ __forceinline__ void conv_ld(ConvSet& s, const LAS char* ab, const LAS char* bb) {
    if constexpr (J < 8) {
        const LAS u32* p = (const LAS u32*)(ab + 512 * J);
        u32x4 t = {p[0], p[1], p[2], p[3]};
        s.A[J] = __builtin_bit_cast(bf16x8, t);
    } else s.B[J - 8] = *(const LAS bf16x8*)(bb + 512 * (15 - J));
}
template <int M> struct TriIdx {
    static constexpr int find(bool want_i) { int m = 0; for (int s = 0; s < 8; ++s) for (int j = 0; j <= s; ++j) { if (m == M) return want_i ? s : j; ++m; } return 0; }
    static constexpr int i = find(true), j = find(false);
};
template <int M, bool PF, bool FIRST> __device__ __forceinline__ void conv_slot(f32x4 (&acc)[8], const ConvSet& cur, ConvSet& nxt, const LAS char* ab, const LAS char* bb) {
    mfma_v<(FIRST && TriIdx<M>::j == 0)>(acc[TriIdx<M>::i], cur.A[TriIdx<M>::j], cur.B[TriIdx<M>::i - TriIdx<M>::j]);
    if constexpr (PF && (M & 1) == 0 && M / 2 < 16) conv_ld<((M / 2) & 1) * 8 + (M / 4)>(nxt, ab, bb);
    SCHED();
}
template <bool PF, bool FIRST, int... M> __device__ __forceinline__ void conv_step(f32x4 (&acc)[8], const ConvSet& cur, ConvSet& nxt, const LAS char* ab, const LAS char* bb, std::integer_sequence<int, M...>) {
    (conv_slot<M, PF, FIRST>(acc, cur, nxt, ab, bb), ...);
}
template <int... J> __device__ __forceinline__ void conv_ld_all(ConvSet& s, const LAS char* ab, const LAS char* bb, std::integer_sequence<int, J...>) { (conv_ld<J>(s, ab, bb), ...); }

struct ConvIn { u32x4 k; u32 kn; u16 dv; };
__device__ __forceinline__ void conv_load_k(ConvIn& in, int d, const u16* __restrict__ ssm_k, const u16* __restrict__ ssm_D) {
    const int tid = threadIdx.x;
    in.k = *(const u32x4*)(ssm_k + (size_t)d * S_ + tid * 8);
    in.kn = *(const u32*)(ssm_k + (size_t)d * S_ + (tid < 255 ? tid * 8 + 8 : 0));
    in.dv = ssm_D[d];
}
__device__ __forceinline__ void conv_put_k(const ConvIn& in, char* sm) {
    const int tid = threadIdx.x;
    const u32 kn = tid < 255 ? in.kn : 0u;
    *(u32x4*)(sm + C_K0 + 32 + tid * 16) = in.k;
    *(u32x4*)(sm + C_K1 + 32 + tid * 16) = (u32x4){__builtin_amdgcn_alignbit(in.k[1], in.k[0], 16), __builtin_amdgcn_alignbit(in.k[2], in.k[1], 16),
                                                 __builtin_amdgcn_alignbit(in.k[3], in.k[2], 16), __builtin_amdgcn_alignbit(kn, in.k[3], 16)};
    if (tid < 32) *(u32x4*)(sm + C_XR + 4096 + tid * 16) = (u32x4){0, 0, 0, 0};
    if (tid == 0) {
        *(u32x4*)(sm + C_K0) = (u32x4){0, 0, 0, 0}; *(u32x4*)(sm + C_K0 + 16) = (u32x4){0, 0, 0, 0};
        *(u32x4*)(sm + C_K1) = (u32x4){0, 0, 0, 0}; *(u32x4*)(sm + C_K1 + 16) = (u32x4){0, 0, 0, in.k[0] << 16};
    }
}
__device__ __forceinline__ void conv_finish(int d, const char* sm, const char* red, float Dv, rsrc_t wsr) {
    const int tid = threadIdx.x;
    const int i = tid >> 5, n = tid & 15, gp = (tid >> 4) & 1;
    f32x4 lo = {0.f, 0.f, 0.f, 0.f}, hi = {0.f, 0.f, 0.f, 0.f};
#pragma unroll
    for (int w = 0; w < 4; ++w) {
        lo += *(const f32x4*)(red + (w * 8 + i) * 1024 + (n + 32 * gp) * 16);
        hi += *(const f32x4*)(red + (w * 8 + i) * 1024 + (n + 32 * gp + 16) * 16);
    }
    const int s = 256 * i + 16 * n + 8 * gp;
    const u32x4 xw = *(const u32x4*)(sm + C_XR + 2 * (2040 - s));
    const float o0 = rbf(lo[0]) + rbf(bf_hi(xw[3]) * Dv), o1 = rbf(lo[1]) + rbf(bf_lo(xw[3]) * Dv), o2 = rbf(lo[2]) + rbf(bf_hi(xw[2]) * Dv), o3 = rbf(lo[3]) + rbf(bf_lo(xw[2]) * Dv);
    const float o4 = rbf(hi[0]) + rbf(bf_hi(xw[1]) * Dv), o5 = rbf(hi[1]) + rbf(bf_lo(xw[1]) * Dv), o6 = rbf(hi[2]) + rbf(bf_hi(xw[0]) * Dv), o7 = rbf(hi[3]) + rbf(bf_lo(xw[0]) * Dv);
    st16_dev(wsr, (u32)WS_YYT + (u32)(d * S_ + s) * 2, (u32x4){pk2(o0, o1), pk2(o2, o3), pk2(o4, o5), pk2(o6, o7)});
}
template <class Hook>
__device__ __forceinline__ void conv_body(int ch0, const ConvIn (&in)[3], char* smem, rsrc_t wsr, Hook hook
                                          ) {
    const int lane = threadIdx.x & 63, wid = __builtin_amdgcn_readfirstlane(threadIdx.x >> 6);
    __syncthreads();
    STAMP_AT(3);
    const int n = lane & 15, g = lane >> 4, dd = g >> 1, gh = g & 1;
    const LAS char* l0 = (const LAS char*)smem;
    const LAS char* bb = l0 + C_XR + 480 - 32 * n + 32 * dd + 16 * gh + 128 * wid;
    const LAS char* ab = l0 + ((n & 1) ? C_K0 + 2 * (16 * dd + 8 * gh + n + 1) : C_K1 + 2 * (16 * dd + 8 * gh + n)) + 128 * wid;
    f32x4 acc[3][8];
    ConvSet s0, s1;
    conv_ld_all(s0, ab, bb, std::make_integer_sequence<int, 16>{});
    SCHED();
    constexpr auto SQ = std::make_integer_sequence<int, 36>{};
    conv_step<true, true>(acc[0], s0, s1, ab + 64, bb + 64, SQ);
    conv_step<true, false>(acc[0], s1, s0, ab + C_CH, bb + C_CH, SQ);
    conv_step<true, true>(acc[1], s0, s1, ab + C_CH + 64, bb + C_CH + 64, SQ);
    conv_step<true, false>(acc[1], s1, s0, ab + 2 * C_CH, bb + 2 * C_CH, SQ);
    conv_step<true, true>(acc[2], s0, s1, ab + 2 * C_CH + 64, bb + 2 * C_CH + 64, SQ);
    conv_step<false, false>(acc[2], s1, s0, ab, bb, SQ);
    asm volatile("s_nop 7" ::: "memory");
#pragma unroll
    for (int c = 0; c < 3; ++c)
#pragma unroll
        for (int i = 0; i < 8; ++i) asm volatile("" : "+v"(acc[c][i]));
    STAMP_AT(4);
    hook();
#pragma unroll
    for (int c = 0; c < 3; ++c)
#pragma unroll
        for (int i = 0; i < 8; ++i) *(f32x4*)(smem + C_RED + c * 32768 + (wid * 8 + i) * 1024 + lane * 16) = acc[c][i];
    __syncthreads();
#pragma unroll
    for (int c = 0; c < 3; ++c) conv_finish(ch0 + c, smem + c * C_CH, smem + C_RED + c * 32768, bf1(in[c].dv), wsr);
}

constexpr int R_MOD = 147456;
constexpr int R_YT = 110592;
constexpr int R_HT = R_YT + 8 * 1168;
struct RowsPre { float x[18], gh[18]; u32 xw[9]; };
__device__ __forceinline__ void rows_load(RowsPre& r, int t0, const u16* __restrict__ q) {
    const int tid = threadIdx.x, lane = tid & 63, hl = lane & 31, row = 2 * (tid >> 6) + (lane >> 5);
    const u32* qrow = (const u32*)(q + (size_t)(t0 + row) * D_);
#pragma unroll
    for (int i = 0; i < 9; ++i) r.xw[i] = qrow[hl + 32 * i];
}
struct ModIn { u32x4 mw[4]; };
__device__ __forceinline__ void mod_load(ModIn& m, rsrc_t wsr) {
#pragma unroll
    for (int i = 0; i < 4; ++i) { const int j = threadIdx.x + 256 * i; m.mw[i] = ld16_dev(wsr, (u32)WS_MOD + (j < 864 ? j : 0) * 16); }
}
__device__ __forceinline__ void mod_put(const ModIn& m, char* sm) {
#pragma unroll
    for (int i = 0; i < 4; ++i) { const int j = threadIdx.x + 256 * i; if (j < 864) *(u32x4*)(sm + R_MOD + j * 16) = m.mw[i]; }
}
__device__ __forceinline__ void rows_pre(RowsPre& r, const char* sm) {
    const int hl = threadIdx.x & 31;
#pragma unroll
    for (int i = 0; i < 9; ++i) { r.x[2 * i] = bf_lo(r.xw[i]); r.x[2 * i + 1] = bf_hi(r.xw[i]); }
    float s = 0.f;
#pragma unroll
    for (int i = 0; i < 18; ++i) s += r.x[i];
    const float mean = half_sum(s) * (1.0f / 576.0f);
    float vs = 0.f;
#pragma unroll
    for (int i = 0; i < 18; ++i) { const float dl = r.x[i] - mean; vs += dl * dl; }
    const float rstd = __builtin_amdgcn_rsqf(half_sum(vs) * (1.0f / 576.0f) + 1e-5f);
    const float* mod = (const float*)(sm + R_MOD);
#pragma unroll
    for (int i = 0; i < 9; ++i) {
        const int c = 2 * (hl + 32 * i);
        const f32x2 sa = *(const f32x2*)(mod + c), c1 = *(const f32x2*)(mod + 576 + c), ga = *(const f32x2*)(mod + 2 * 576 + c);
        r.gh[2 * i] = ga[0] * ((r.x[2 * i] - mean) * rstd * c1[0] + sa[0]);
        r.gh[2 * i + 1] = ga[1] * ((r.x[2 * i + 1] - mean) * rstd * c1[1] + sa[1]);
    }
}
__device__ __forceinline__ void rows_fetch(int t0, char* sm, const char* ws) {
    const int lane = threadIdx.x & 63;
    const char* src = ws + WS_YYT + t0 * 2;
    const u32 dst = (u32)(uintptr_t)(sm + R_HT);
#pragma unroll
    for (int i = 0; i < 9; ++i)
        asm volatile("s_mov_b32 m0, %2\n\ts_nop 0\n\tglobal_load_lds_dwordx4 %0, %1 sc1" :: "v"((u32)((lane + 64 * i) * (S_ * 2))), "s"(src), "s"(dst + i * 1024) : "memory");
    asm volatile("s_waitcnt vmcnt(0)" ::: "memory");
}
__device__ __forceinline__ void rows_main(const RowsPre& r, int t0, char* sm, rsrc_t wsr) {
    const int tid = threadIdx.x, lane = tid & 63, hl = lane & 31, row = 2 * (tid >> 6) + (lane >> 5);
    const int cg = tid >> 1, th = tid & 1;
    if (tid < 144) {
        u32x2 yw[8];
#pragma unroll
        for (int c = 0; c < 8; ++c) yw[c] = *(const u32x2*)(sm + R_HT + (8 * cg + c) * 16 + th * 8);
#pragma unroll
        for (int t = 0; t < 4; ++t) {
            u32x4 o;
#pragma unroll
            for (int kk = 0; kk < 4; ++kk) {
                const u32 a = yw[2 * kk][t >> 1], bq = yw[2 * kk + 1][t >> 1];
                o[kk] = (t & 1) ? __builtin_amdgcn_perm(bq, a, 0x07060302u) : __builtin_amdgcn_perm(bq, a, 0x05040100u);
            }
            *(u32x4*)(sm + R_YT + (4 * th + t) * 1168 + cg * 16) = o;
        }
    }
    __syncthreads();
    const float* mod = (const float*)(sm + R_MOD);
    float x1[18];
#pragma unroll
    for (int i = 0; i < 9; ++i) {
        u32* yp = (u32*)(sm + R_YT + row * 1168 + (hl + 32 * i) * 4);
        const u32 yv = *yp;
        const u32 p = pk2(r.x[2 * i] + r.gh[2 * i] * bf_lo(yv), r.x[2 * i + 1] + r.gh[2 * i + 1] * bf_hi(yv));
        *yp = p;
        x1[2 * i] = bf_lo(p); x1[2 * i + 1] = bf_hi(p);
    }
    float s = 0.f;
#pragma unroll
    for (int i = 0; i < 18; ++i) s += x1[i];
    const float mean = half_sum(s) * (1.0f / 576.0f);
    float vs = 0.f;
#pragma unroll
    for (int i = 0; i < 18; ++i) { const float dl = x1[i] - mean; vs += dl * dl; }
    const float rstd = __builtin_amdgcn_rsqf(half_sum(vs) * (1.0f / 576.0f) + 1e-5f);
#pragma unroll
    for (int i = 0; i < 9; ++i) {
        const int c = 2 * (hl + 32 * i);
        const f32x2 sb = *(const f32x2*)(mod + 3 * 576 + c), c1 = *(const f32x2*)(mod + 4 * 576 + c);
        const float h0 = (x1[2 * i] - mean) * rstd * c1[0] + sb[0], h1 = (x1[2 * i + 1] - mean) * rstd * c1[1] + sb[1];
        const int ks = c >> 5, kk = c & 31, gq = (kk & 15) >> 2, j = ((kk >> 4) << 2) | (kk & 3);
        *(u32*)(sm + R_HT + ((ks * 4 + gq) * 8 + row) * 16 + j * 2) = pk2(h0, h1);
    }
    __syncthreads();
    const u32 h2t = (u32)WS_H2T + (u32)(t0 >> 4) * 18 * 1024 + (t0 & 15) * 16, x1g = (u32)WS_X1 + (u32)t0 * D_ * 2;
#pragma unroll
    for (int it = 0; it < 3; ++it) {
        const int i = tid + 256 * it;
        if (i < 576) {
            const int kg = i >> 3, rr8 = i & 7;
            st16_dev(wsr, h2t + (kg >> 2) * 1024 + (kg & 3) * 256 + rr8 * 16, *(const u32x4*)(sm + R_HT + i * 16));
            const int rr = i / 72, cc = i - rr * 72;
            st16_dev(wsr, x1g + rr * 1152 + cc * 16, *(const u32x4*)(sm + R_YT + rr * 1168 + cc * 16));
        }
    }
}

namespace mlp {
constexpr int STG = 36864, LDS_BYTES = 163840;
constexpr int G1S = 9 * 144, G2S = 18 * 36, NS = G1S + G2S;
constexpr int LA = 4;
constexpr int PRO = 9 + 4 * LA + 18;
constexpr int TUS = 147456;

constexpr int F01 = 288;
constexpr int stage_of(int gs) { return gs < F01 ? 0 : gs < G1S ? gs / 144 : 9 + (gs - G1S) / 72; }
constexpr int rel_of(int gs) { return gs < F01 ? gs : gs < G1S ? gs % 144 : (gs - G1S) % 72; }
constexpr int grp_of(int gs) { return rel_of(gs) >> 2; }
constexpr int t_of(int gs) { return rel_of(gs) & 3; }
constexpr bool barpt(int gs) { return gs >= F01 && gs < NS && t_of(gs) == 3 && grp_of(gs) == (gs < G1S ? 3 : 0) && stage_of(gs) < 17; }
constexpr int dma_id(int gs) {
    if (gs >= NS || t_of(gs) != 3) return -1;
    const int g = grp_of(gs), s = stage_of(gs);
    if (gs < F01) return (g >= 36 && g < 72 && (g & 3) == 3) ? 3 * 16 + (g - 36) / 4 : -1;
    if (s == 2) return (g >= 4 && g < 22) ? ((g & 1) ? 5 : 4) * 16 + (g - 4) / 2 : -1;
    if (s + 3 > 17) return -1;
    if (s == 8) return (g >= 4 && g < 13) ? 11 * 16 + g - 4 : -1;
    if (gs < G1S) { constexpr int G[9] = {5, 7, 9, 12, 14, 16, 20, 22, 24}; for (int i = 0; i < 9; ++i) if (G[i] == g) return (s + 3) * 16 + i; return -1; }
    return (g & 1) ? (s + 3) * 16 + (g >> 1) : -1;
}
constexpr int bload(int gs) {
    if (gs >= F01 || t_of(gs) != 1) return -1;
    const int ks = 2 * (gs / 32) + LA + (((gs % 32) >> 2) >> 2), i = (gs >> 2) & 3;
    return ks < 18 ? ks * 4 + i : -1;
}
constexpr bool unit_stores(int v) { return v == 1 || v == 3 || v == 4 || v == 6 || v == 8; }
constexpr int unit_end(int v) { return v < 2 ? F01 : 144 * (v + 1); }
constexpr int hload(int gs) {
    if (t_of(gs) != 3) return -1;
    const int s = stage_of(gs), g = grp_of(gs);
    if (s == 8) return (g >= 13 && g < 33) ? g - 13 : -1;
    if (s < 9 || g < 2 || g > 16 || (g & 1)) return -1;
    const int i = (g - 2) >> 1;
    if (s == 9) return 20 + i;
    if (s == 10) return 28 + i;
    if (s == 11) return i < 4 ? 36 + i : -1;
    if (s >= 12 && s <= 15) return 40 + 8 * (s - 12) + i;
    return -1;
}
constexpr int pub(int gs) { return (stage_of(gs) == 6 && grp_of(gs) == 27 && t_of(gs) == 3) ? 1 : (stage_of(gs) == 10 && grp_of(gs) == 9 && t_of(gs) == 1) ? 2 : 0; }
constexpr int poll_issue(int gs) { return (stage_of(gs) == 7 && grp_of(gs) == 27 && t_of(gs) == 3) ? 1 : (stage_of(gs) == 11 && grp_of(gs) == 9 && t_of(gs) == 1) ? 2 : 0; }
struct EpiSched { signed char ev[G1S], ep[G1S], sv[G1S], st[G1S]; };
constexpr EpiSched make_epi() {
    EpiSched e{};
    int v = 0, p = 0, pend_v = -1, pend_lo = 0, pend_hi = 0;
    for (int gs = 0; gs < G1S; ++gs) {
        e.ev[gs] = -1; e.ep[gs] = -1; e.sv[gs] = -1; e.st[gs] = -1;
        if (gs < F01) continue;
        const int t = t_of(gs);
        if (t == 0) continue;
        const bool vm = barpt(gs) || dma_id(gs) >= 0 || hload(gs) >= 0 || pub(gs) || poll_issue(gs);
        if (t == 3 && !vm && pend_lo < pend_hi) { e.sv[gs] = (signed char)pend_v; e.st[gs] = (signed char)pend_lo; ++pend_lo; continue; }
        if (t == 3 && vm) continue;
        if (v < 8 && gs >= unit_end(v) + 8 && (pend_lo == pend_hi || pend_v == v || !unit_stores(v) || true)) {
            e.ev[gs] = (signed char)v; e.ep[gs] = (signed char)p;
            ++p;
            if ((p & 15) == 0 && unit_stores(v)) { if (pend_v != v) { pend_v = v; pend_lo = p / 16 - 1; } pend_hi = p / 16; }
            if (p == 64) { p = 0; ++v; }
        }
    }
    return e;
}
constexpr EpiSched ES = make_epi();
constexpr int hstore(int gs) { return gs < G1S ? ES.st[gs] : -1; }
constexpr int epi_last(int v) { for (int gs = G1S - 1; gs >= 0; --gs) if (ES.ev[gs] == v) return gs; return -1; }
constexpr int store_last(int v) { for (int gs = G1S - 1; gs >= 0; --gs) if (ES.sv[gs] == v && ES.st[gs] == 3) return gs; return -1; }
constexpr bool epi_ok() {
    for (int v = 0; v < 8; ++v) {
        if (epi_last(v) < 0 || ES.ep[epi_last(v)] != 63) return false;
        if (v + 3 <= 8 && epi_last(v) >= 144 * (v + 3)) return false;
        if (unit_stores(v) && store_last(v) < 0) return false;
    }
    return store_last(4) + 40 < 6 * 144 + 27 * 4;
}
static_assert(epi_ok(), "epilogue schedule");
constexpr int vm_ops(int gs) { return (pub(gs) ? 1 : 0) + (poll_issue(gs) ? 1 : 0) + (hstore(gs) >= 0) + (bload(gs) >= 0) + (hload(gs) >= 0) + (dma_id(gs) >= 0); }
struct Ledger { int cum[NS + 1]; };
constexpr Ledger make_ledger() {
    Ledger l{}; int n = PRO;
    for (int gs = 0; gs < NS; ++gs) { if (gs == G1S) n += 4; l.cum[gs] = n; n += vm_ops(gs); }
    l.cum[NS] = n; return l;
}
constexpr Ledger LG = make_ledger();
constexpr int seq_in(int gs, int kind) {
    int n = LG.cum[gs];
    if (kind > 0 && pub(gs)) ++n;
    if (kind > 1 && poll_issue(gs)) ++n;
    if (kind > 2 && hstore(gs) >= 0) ++n;
    if (kind > 3 && bload(gs) >= 0) ++n;
    if (kind > 4 && hload(gs) >= 0) ++n;
    return n;
}
constexpr int seq_dma_last(int s) {
    if (s <= 2) return 9 * s + 8;
    for (int gs = 0; gs < NS; ++gs) if (dma_id(gs) == s * 16 + 8) return seq_in(gs, 5);
    return -1;
}
constexpr int seq_b(int ks) {
    if (ks < LA) return 27 + 4 * ks + 3;
    for (int gs = 0; gs < F01; ++gs) if (bload(gs) == ks * 4 + 3) return seq_in(gs, 3);
    return -1;
}
constexpr int seq_h(int kk) {
    for (int gs = 8 * 144; gs < NS; ++gs) if (hload(gs) == kk * 4 + 3) return seq_in(gs, 4);
    return -1;
}
constexpr int seq_poll(int part) {
    for (int gs = 0; gs < NS; ++gs) if (poll_issue(gs) == part) return seq_in(gs, 1);
    return -1;
}
constexpr int seq_store_last(int part) {
    if (part == 2) return LG.cum[G1S] - 1;
    return seq_in(store_last(4), 2);
}
struct Work { int unit, mat, k, t, n; };
constexpr Work work_of(int gs) {
    if (gs < F01) { const int p = gs / 32, um = (gs % 32) / 8, r = gs % 8; return {um >> 1, um & 1, 2 * p + (r & 1), r >> 1, 8 * p + 2 * um + (r & 1)}; }
    if (gs < G1S) { const int u = gs / 144, bi = (gs % 144) / 24, r = gs % 24; return {u, bi & 1, 6 * (bi >> 1) + r % 6, r / 6, 36 * u + 6 * bi + r % 6}; }
    const int s = (gs - G1S) / 72, r = (gs - G1S) % 72, du = r / 8, r8 = r % 8;
    return {du, 0, 2 * s + (r8 & 1), r8 >> 1, 324 + 18 * s + 2 * du + (r8 & 1)};
}
constexpr int NFRAG = 486, RQ = 12, FR0 = 8;
constexpr int frag_first(int n) {
    if (n < 72) return 32 * (n / 8) + 8 * ((n % 8) >> 1) + (n & 1);
    if (n < 324) return 144 * (n / 36) + 24 * ((n % 36) / 6) + n % 6;
    return G1S + 72 * ((n - 324) / 18) + 8 * (((n - 324) % 18) >> 1) + (n & 1);
}
constexpr int frag_last(int n) { return frag_first(n) + ((n >= 72 && n < 324) ? 18 : 6); }
constexpr int frag_stage(int n) { return n < 72 ? (n % 8) >> 2 : n < 324 ? n / 36 : 9 + (n - 324) / 18; }
constexpr int frag_off(int n) {
    if (n < 72) return (2 * (2 * (n / 8) + (n & 1)) + (((n % 8) >> 1) & 1)) * 1024;
    if (n < 324) { const int bi = (n % 36) / 6; return (2 * (6 * (bi >> 1) + n % 6) + (bi & 1)) * 1024; }
    return (n & 1) * 9216 + (((n - 324) % 18) >> 1) * 32;
}
constexpr int stage_ready(int s) {
    if (s <= 2) return 0;
    for (int gs = 0; gs < NS; ++gs) if (barpt(gs) && stage_of(gs) == s - 1) return gs + 1;
    return NS;
}
struct PfSched { short id[NS]; };
constexpr PfSched make_pf() {
    PfSched t{};
    int m = FR0, rdy = stage_ready(frag_stage(FR0));
    for (int gs = 0; gs < NS; ++gs) {
        t.id[gs] = -1;
        if ((gs & 3) != 0 || m >= NFRAG) continue;
        if (m >= RQ && frag_last(m - RQ) >= gs) continue;
        if (rdy > gs) continue;
        t.id[gs] = (short)m;
        ++m;
        if (m < NFRAG && frag_stage(m) != frag_stage(m - 1)) rdy = stage_ready(frag_stage(m));
    }
    return t;
}
constexpr PfSched PF = make_pf();
constexpr bool pf_ok() {
    int cnt = FR0;
    for (int gs = 0; gs < NS; ++gs) if (PF.id[gs] >= 0) { if (PF.id[gs] != cnt || frag_first(cnt) - gs < 8) return false; ++cnt; }
    return cnt == NFRAG;
}
static_assert(pf_ok(), "fragment prefetch schedule");
constexpr int clampv(int n) { return n > 63 ? 63 : (n < 0 ? 0 : n); }
constexpr int wait_for(int gs, int seq) { return clampv(LG.cum[gs] - seq - 1); }

template <int N> __device__ __forceinline__ void wait_vm() { asm volatile("s_waitcnt vmcnt(%0)" :: "n"(N) : "memory"); }

struct Ctx {
    bf16x8 Bv[2][4];
    bf16x8 Aq[12];
    f32x4 ac[3][2][4];
    f32x4 a2[9][4];
    float e0, e1;
    u32x4 hp[4];
    u32 vo1[4], vo2[4], vo3[4];
    u32 lane16, epoch;
    u32x4 pollv;
    const LAS char* rd[4]; const LAS char* rd2[4];
    const char* w1src; const char* w2src; const char* bsrc; const char* hsrc;
    u32 voff2[9];
    int sub;
    u32 dmadst;
    u32 hso;
    int jj;
    __amdgpu_buffer_rsrc_t hrs;
    u32* flag_pub; u32* flag_poll;
};

template <int KS, int T, bool ZERO> __device__ __forceinline__ void mfma(Ctx& cx, f32x4& c, const bf16x8& a) {
    constexpr int r = (KS * 4 + T) * 4;
    if constexpr (KS < 16) {
        if constexpr (ZERO) asm volatile("v_mfma_f32_16x16x32_bf16 %0, %1, a[%c2:%c3], 0" : "=v"(c) : "v"(a), "n"(r), "n"(r + 3));
        else asm volatile("v_mfma_f32_16x16x32_bf16 %0, %1, a[%c2:%c3], %0" :: "v"(c), "v"(a), "n"(r), "n"(r + 3));
    } else {
        asm volatile("v_mfma_f32_16x16x32_bf16 %0, %1, %2, %0" :: "v"(c), "v"(a), "v"(cx.Bv[KS - 16][T]));
    }
}
template <int KS, int T, bool SC1> __device__ __forceinline__ void gload(Ctx& cx, u32 voff, const char* sb) {
    constexpr int r = (KS * 4 + T) * 4, off = (KS & 3) * 1024;
    if constexpr (KS < 16) {
        if constexpr (SC1) asm volatile("global_load_dwordx4 a[%c2:%c3], %0, %1 offset:%4 sc1" :: "v"(voff), "s"(sb), "n"(r), "n"(r + 3), "n"(off) : "memory");
        else asm volatile("global_load_dwordx4 a[%c2:%c3], %0, %1 offset:%4" :: "v"(voff), "s"(sb), "n"(r), "n"(r + 3), "n"(off) : "memory");
    } else {
        if constexpr (SC1) asm volatile("global_load_dwordx4 %0, %1, %2 offset:%3 sc1" : "=v"(cx.Bv[KS - 16][T]) : "v"(voff), "s"(sb), "n"(off) : "memory");
        else asm volatile("global_load_dwordx4 %0, %1, %2 offset:%3" : "=v"(cx.Bv[KS - 16][T]) : "v"(voff), "s"(sb), "n"(off) : "memory");
    }
}
template <int S, int P> __device__ __forceinline__ void dma_m0(const Ctx& c) {
    asm volatile("s_mov_b32 m0, %0" :: "s"(c.dmadst + (u32)((S & 3) * STG + (P >> 2) * 4096)) : "memory");
}
constexpr int nat_kstep(int ks2) {
    if (ks2 < 10) { constexpr int T[5] = {0, 1, 5, 6, 4}; return 9 * (ks2 / 5) + T[ks2 % 5]; }
    const int jj = (ks2 - 10) >> 1, h = (ks2 - 10) & 1;
    return 9 * (jj >> 1) + ((jj & 1) ? 7 : 2) + h;
}
template <int S, int P> __device__ __forceinline__ void dma_go(const Ctx& c) {
    if constexpr (S < 9) {
        const char* sb = c.w1src + (size_t)S * STG + (P >> 2) * 4096;
        asm volatile("global_load_lds_dwordx4 %0, %1 offset:%2 sc1" :: "v"(c.lane16), "s"(sb), "n"((P & 3) * 1024) : "memory");
    } else {
        constexpr int n0 = nat_kstep(2 * (S - 9)), n1 = nat_kstep(2 * (S - 9) + 1);
        const char* sb = c.w2src + (size_t)(c.sub ? n1 : n0) * (32 * D_ * 2);
        asm volatile("global_load_lds_dwordx4 %0, %1 offset:%2" :: "v"(c.voff2[P]), "s"(sb), "n"((P & 3) * 1024) : "memory");
    }
}
template <int ID> __device__ __forceinline__ void load_b(Ctx& c) {
    constexpr int ks = ID >> 2, t = ID & 3;
    gload<ks, t, true>(c, c.vo1[t], c.bsrc + (ks >> 2) * 4096);
}
template <int ID> __device__ __forceinline__ void load_h(Ctx& c) {
    constexpr int kk = ID >> 2, t = ID & 3;
    gload<kk, t, true>(c, c.vo3[t], c.hsrc + (kk >> 2) * 4096);
}
template <int KS> __device__ __forceinline__ void pin_b(Ctx& c) {
#pragma unroll
    for (int t = 0; t < 4; ++t) { if constexpr (KS >= 16) asm volatile("" : "+v"(c.Bv[KS - 16][t])); }
}
template <int V, int P> __device__ __forceinline__ void epi_piece(Ctx& c) {
    constexpr int pr = P >> 3, st = P & 7, t = pr >> 1, r = 2 * (pr & 1), par = V % 3;
    if constexpr (st == 0) {
        asm volatile("v_mul_f32 %0, 0xbfb8aa3b, %1" : "=v"(c.e0) : "v"(c.ac[par][0][t][r]));
        asm volatile("v_mul_f32 %0, 0xbfb8aa3b, %1" : "=v"(c.e1) : "v"(c.ac[par][0][t][r + 1]));
    } else if constexpr (st == 1) asm volatile("v_exp_f32 %0, %0" :: "v"(c.e0));
    else if constexpr (st == 2) asm volatile("v_exp_f32 %0, %0" :: "v"(c.e1));
    else if constexpr (st == 3) {
        asm volatile("v_add_f32 %0, 1.0, %0" :: "v"(c.e0));
        asm volatile("v_add_f32 %0, 1.0, %0" :: "v"(c.e1));
    } else if constexpr (st == 4) asm volatile("v_rcp_f32 %0, %0" :: "v"(c.e0));
    else if constexpr (st == 5) asm volatile("v_rcp_f32 %0, %0" :: "v"(c.e1));
    else if constexpr (st == 6) {
        asm volatile("v_mul_f32 %0, %0, %1" :: "v"(c.e0), "v"(c.ac[par][1][t][r]));
        asm volatile("v_mul_f32 %0, %0, %1" :: "v"(c.e1), "v"(c.ac[par][1][t][r + 1]));
    } else {
        constexpr bool first = (V == 0 || V == 2 || V == 5 || V == 7);
        u32 o;
        asm volatile("v_cvt_pk_bf16_f32 %0, %1, %2" : "=v"(o) : "v"(c.e0), "v"(c.e1));
        c.hp[t][(first ? 0 : 2) + (pr & 1)] = o;
    }
}
template <int V, int T> __device__ __forceinline__ void store_h(Ctx& c) {
    const int m5 = 5 * (c.jj >> 1), ab = c.jj & 1;
    if constexpr (V == 4) {
        const u32x2 d = {c.hp[T][2], c.hp[T][3]};
        __builtin_amdgcn_raw_buffer_store_b64(d, c.hrs, c.vo2[T] + 8 * ab, c.hso + (m5 + 4) * 1024, 16);
    } else {
        const int kstep = (V == 1) ? m5 + 2 * ab : (V == 3) ? m5 + 2 * ab + 1 : (V == 6) ? 10 + 2 * c.jj : 11 + 2 * c.jj;
        __builtin_amdgcn_raw_buffer_store_b128(c.hp[T], c.hrs, c.vo2[T], c.hso + kstep * 1024, 16);
    }
}
template <int PART> __device__ __forceinline__ void poll_check(Ctx& c) {
    asm volatile("" : "+v"(c.pollv));
    const u32 e = c.epoch;
    if (!__all(c.pollv[0] == e && c.pollv[1] == e && c.pollv[2] == e && c.pollv[3] == e)) {
        const u32* p = c.flag_poll + (PART - 1) * 1024 + ((c.lane16 >> 4) & 3);
        poll_eq(p, e);
    }
}
template <int V, int P> __device__ __forceinline__ void epi_piece_padded(Ctx& c) {
    epi_piece<V, P>(c);
    if constexpr ((P & 7) == 2 || (P & 7) == 5) asm volatile("s_nop 0");
}
template <int V, int... P> __device__ __forceinline__ void epi_all(Ctx& c, std::integer_sequence<int, P...>) { (epi_piece_padded<V, P>(c), ...); }

template <int GS> __device__ __forceinline__ void slot(Ctx& c) {
    constexpr bool g1 = GS < G1S;
    constexpr int S = stage_of(GS), R = rel_of(GS), grp = R >> 2, t = R & 3;
    constexpr Work W = work_of(GS);
    constexpr bool f01 = GS < F01;
    if constexpr (GS == G1S) {
        asm volatile("s_nop 7" ::: "memory");
        epi_all<8>(c, std::make_integer_sequence<int, 64>{});
        store_h<8, 0>(c); store_h<8, 1>(c); store_h<8, 2>(c); store_h<8, 3>(c);
        SCHED();
    }
    if constexpr (f01 && GS % 32 == 0) { wait_vm<wait_for(GS, seq_b(2 * (GS / 32) + 1))>(); pin_b<2 * (GS / 32)>(c); pin_b<2 * (GS / 32) + 1>(c); }
    if constexpr (GS == 8 * 144) { wait_vm<wait_for(GS, seq_poll(1))>(); poll_check<1>(c); }
    if constexpr (!g1 && R == 0) { wait_vm<wait_for(GS, seq_h(2 * (S - 9) + 1))>(); pin_b<2 * (S - 9)>(c); pin_b<2 * (S - 9) + 1>(c); }
    if constexpr (dma_id(GS) >= 0) dma_m0<dma_id(GS) / 16, dma_id(GS) % 16>(c);
    if constexpr (g1) mfma<W.k, W.t, (W.k == 0)>(c, c.ac[W.unit % 3][W.mat][W.t], c.Aq[W.n % RQ]);
    else mfma<W.k, W.t, (W.k == 0)>(c, c.a2[W.unit][W.t], c.Aq[W.n % RQ]);
    if constexpr (barpt(GS)) {
        constexpr int n1 = wait_for(GS, seq_dma_last(S + 1));
        constexpr int n2 = (S == 12) ? wait_for(GS, seq_poll(2)) : 63;
        wait_vm<(n1 < n2 ? n1 : n2)>();
        if constexpr (S == 12) poll_check<2>(c);
        __builtin_amdgcn_s_barrier();
        asm volatile("" ::: "memory");
    }
    if constexpr (pub(GS)) {
        wait_vm<wait_for(GS, seq_store_last(pub(GS)))>();
        if (c.lane16 == 0) flag_set(c.flag_pub + (pub(GS) - 1) * 1024, c.epoch);
    }
    if constexpr (poll_issue(GS)) asm volatile("global_load_dwordx4 %0, %1, %2 sc1" : "=v"(c.pollv) : "v"(0u), "s"(c.flag_poll + (poll_issue(GS) - 1) * 1024) : "memory");
    {
        constexpr int f = PF.id[GS], fh = (GS & 3) == 1 ? PF.id[GS - ((GS & 3) == 1 ? 1 : 0)] : -1;
        if constexpr (f >= 0 && f < 324) c.Aq[f % RQ] = *(const LAS bf16x8*)(c.rd[frag_stage(f) & 3] + frag_off(f));
        constexpr int ft = f >= 324 ? f : fh >= 324 ? fh : -1;
        if constexpr (ft >= 0) {
            constexpr int half = f >= 324 ? 0 : 1;
            const s16x4 h = __builtin_amdgcn_ds_read_tr16_b64_v4i16((LAS s16x4*)(c.rd2[frag_stage(ft) & 3] + frag_off(ft) + half * (16 * 288)));
            const u32x2 hh = __builtin_bit_cast(u32x2, h);
            u32x4 a = __builtin_bit_cast(u32x4, c.Aq[ft % RQ]);
            a[2 * half] = hh[0]; a[2 * half + 1] = hh[1];
            c.Aq[ft % RQ] = __builtin_bit_cast(bf16x8, a);
        }
    }
    if constexpr (g1) {
        if constexpr (ES.ev[g1 ? GS : 0] >= 0) epi_piece<ES.ev[g1 ? GS : 0], ES.ep[g1 ? GS : 0]>(c);
        if constexpr (ES.sv[g1 ? GS : 0] >= 0) store_h<ES.sv[g1 ? GS : 0], ES.st[g1 ? GS : 0]>(c);
    }
    if constexpr (bload(GS) >= 0) load_b<bload(GS)>(c);
    if constexpr (hload(GS) >= 0) load_h<hload(GS)>(c);
    if constexpr (dma_id(GS) >= 0) dma_go<dma_id(GS) / 16, dma_id(GS) % 16>(c);
    SCHED();
}
template <int BASE, int... I> __device__ __forceinline__ void run_slots(Ctx& c, std::integer_sequence<int, I...>) { (slot<BASE + I>(c), ...); }
template <int S, int P> __device__ __forceinline__ void dma_one(const Ctx& c) { dma_m0<S, P>(c); asm volatile("s_nop 0"); dma_go<S, P>(c); }
template <int S, int... P> __device__ __forceinline__ void dma_stage(const Ctx& c, std::integer_sequence<int, P...>) { (dma_one<S, P>(c), ...); }
template <int... I> __device__ __forceinline__ void load_b_first(Ctx& c, std::integer_sequence<int, I...>) { (load_b<I>(c), ...); }
}

__device__ __forceinline__ void touch(u32& d, const char* p) { asm volatile("global_load_dword %0, %1, off" : "=v"(d) : "v"(p) : "memory"); }
__global__ __launch_bounds__(256, 1) void k_main(const u16* __restrict__ qin, const u16* __restrict__ kin, const u16* __restrict__ vin, const u16* __restrict__ shift_k,
                                                 const u16* __restrict__ ssm_k, const u16* __restrict__ ssm_D, const u16* __restrict__ wg, const u16* __restrict__ wu,
                                                 const u16* __restrict__ wd, const u16* __restrict__ cvec, const u16* __restrict__ w_mod,
                                                 char* __restrict__ ws, u16* __restrict__ out) {
    using namespace mlp;
    extern __shared__ __attribute__((aligned(16))) char smem[];
    const int tid = threadIdx.x, lane = tid & 63, w = __builtin_amdgcn_readfirstlane(tid >> 6);
    const int b = blockIdx.x, x = b & 7, idx = b >> 3, q = x >> 1;
    const int mt = 4 * (x & 1) + (idx & 3), fcl = idx >> 2, fc = 8 * q + fcl;
    const int mt2 = 8 * (x & 1) + (idx & 7), cc = idx >> 3, tg = w & 1, kh = w >> 1;
    const rsrc_t wsr = __builtin_amdgcn_make_buffer_rsrc(ws, 0, (int)WS_END, 0x00020000);
    STAMP_DECL; STAMP_AT(0);
    const u32 epoch_in = __hip_atomic_load(flag_ptr(ws, F_EPOCH), __ATOMIC_RELAXED, __HIP_MEMORY_SCOPE_AGENT);
    unsigned long long pc_start;
    asm volatile("s_getpc_b64 %0" : "=s"(pc_start));
    u32 epoch;
    {
        const int t0 = (x * 32 + idx) * 8;
        RowsPre rp;
        u32 bg[10] = {0, 0, 0, 0, 0, 0, 0, 0, 0, 0};
        if (b < 192) {
            const int c0 = 72 * x + 3 * idx;
            ConvIn in[3];
            KvIn kin_;
            if (w < 3) touch(bg[8], (const char*)(b < 96 ? kin : vin) + (size_t)((b % 96) * 192 + w * 64 + lane) * 128);
            kv_load(kin_, c0, kin, vin, shift_k);
            conv_load_k(in[0], c0, ssm_k, ssm_D); conv_load_k(in[1], c0 + 1, ssm_k, ssm_D); conv_load_k(in[2], c0 + 2, ssm_k, ssm_D);
            rows_load(rp, t0, qin);
            kv_put(kin_, smem + C_RED);
            conv_put_k(in[0], smem); conv_put_k(in[1], smem + C_CH); conv_put_k(in[2], smem + 2 * C_CH);
            __syncthreads();
            STAMP_AT(1);
            kv_compute(kin_, c0, smem + C_RED, smem + C_XR, C_CH);
            epoch = __builtin_amdgcn_readfirstlane(epoch_in) + 1u;
            if (w == 3) {
                DRAIN();
#pragma unroll
                for (int i = 0; i < 8; ++i) { const int l = lane + 64 * (i & 3); touch(bg[i], (const char*)(i < 4 ? wg : wu) + (size_t)b * 27648 + (l < 216 ? l : 0) * 128); }
            }
            STAMP_AT(2);
            ModIn mi;
            const u32* mf = flag_ptr(ws, F_MOD + (lane < 54 ? lane : 0));
            u32 pm;
            asm volatile("global_load_dword %0, %1, off sc1" : "=v"(pm) : "v"(mf) : "memory");
            conv_body(c0, in, smem, wsr, [&]() {
                DRAIN();
                asm volatile("" : "+v"(pm));
                if (!__all(pm == epoch)) poll_eq(mf, epoch);
                mod_load(mi, wsr); } STAMP_ARGS);
            STAMP_AT(5);
            mod_put(mi, smem);
            DRAIN();
#pragma unroll
            for (int i = 0; i < 10; ++i) asm volatile("" :: "v"(bg[i]));
            __syncthreads();
            if (tid < 8) flag_set(flag_ptr(ws, F_CONV + tid * 256 + b), epoch);
            if (w == 3) {
#pragma unroll
                for (int i = 0; i < 4; ++i) { const int l = lane + 64 * i; touch(bg[i], (const char*)wd + (size_t)b * 27648 + (l < 216 ? l : 0) * 128); }
            }
        } else {
            if (w == 3 && idx < 28) {
                const char* cb = (const char*)(pc_start & ~127ull) + lane * 128;
#pragma unroll
                for (int i = 0; i < 8; ++i) touch(bg[i], cb + (i < 7 || lane < 32 ? i * 8192 : 0));
            }
            if (w == 3 && idx >= 28) {
#pragma unroll
                for (int i = 0; i < 3; ++i) { const size_t o = (size_t)(((idx - 28) * 3 + i) * 64 + lane) << 16; touch(bg[i], ws + (o < WS_END ? o : 0)); }
                touch(bg[3], (const char*)out + (size_t)(lane < 36 ? lane : 0) * 65536);
            }
            for (int i = 0; i < 40; ++i) __builtin_amdgcn_s_sleep(2);
            rows_load(rp, t0, qin);
            if (b - 192 < 54) mod_job(b - 192, cvec, w_mod, ws, (float*)(smem + R_MOD), (float*)(smem + R_MOD + 4096));
            epoch = __builtin_amdgcn_readfirstlane(epoch_in) + 1u;
            DRAIN();
            __syncthreads();
            if (tid == 0 && b - 192 < 54) flag_set(flag_ptr(ws, F_MOD + b - 192), epoch);
            STAMP_AT(1);
            tile_jobs(b - 192, wg, wu, smem, ws, wsr, epoch);
            STAMP_AT(2); STAMP_AT(3); STAMP_AT(4); STAMP_AT(5);
            poll_eq(flag_ptr(ws, F_MOD + (lane < 54 ? lane : 0)), epoch);
            ModIn mi;
            mod_load(mi, wsr);
            mod_put(mi, smem);
            __syncthreads();
        }
        rows_pre(rp, smem);
        STAMP_AT(6);
        if (w == 0) {
            poll_eq4(wsr, (u32)WS_CNT + (F_CONV + x * 256 + (lane < 48 ? lane : 0) * 4) * 4, epoch);
            rows_fetch(t0, smem, ws);
        }
        __syncthreads();
        STAMP_AT(7);
        rows_main(rp, t0, smem, wsr);
        DRAIN();
#pragma unroll
        for (int i = 0; i < 10; ++i) asm volatile("" :: "v"(bg[i]));
        __syncthreads();
        if (tid == 0) flag_set(flag_ptr(ws, F_ROWS + (t0 >> 6) * 8 + ((t0 >> 3) & 7)), epoch);
        STAMP_AT(8);
    }
    Ctx c;
#pragma unroll
    for (int t = 0; t < 4; ++t) c.hp[t] = (u32x4){0, 0, 0, 0};
    c.lane16 = lane * 16;
#pragma unroll
    for (int t = 0; t < 4; ++t) { c.vo1[t] = lane * 16 + t * 18432; c.vo2[t] = lane * 16 + t * TUS; c.vo3[t] = lane * 16 + ((t + 2 * kh) & 3) * TUS; }
    const LAS char* l0 = (const LAS char*)smem;
#pragma unroll
    for (int i = 0; i < 4; ++i) {
        c.rd[i] = l0 + i * STG + lane * 16; c.rd2[i] = l0 + i * STG + kh * 18432 + (4 * (lane >> 4) + ((lane & 15) >> 2)) * 288 + 8 * (lane & 3);
        asm volatile("" : "+v"(c.rd[i])); asm volatile("" : "+v"(c.rd2[i]));
    }
#pragma unroll
    for (int i = 0; i < 9; ++i) { const int ci = lane + 64 * i, row = (ci * 3641) >> 16, c16 = ci - row * 18; c.voff2[i] = (u32)(row * (D_ * 2) + c16 * 16 - (i & 3) * 1024); }
    c.sub = w & 1;
    c.w1src = ws + WS_WT1 + (size_t)fc * 331776 + w * 9216;
    c.w2src = (const char*)(wd + (size_t)(36 * q + 18 * kh) * 32 * D_ + cc * 144);
    c.bsrc = ws + WS_H2T + (size_t)(16 * mt + 4 * w) * 18432;
    c.hsrc = ws + WS_HT + (size_t)(8 * mt2 + 4 * tg) * TUS + (size_t)((q * 2 + kh) * 18) * 1024;
    c.dmadst = (u32)(uintptr_t)smem + w * 9216;
    c.jj = fcl & 3;
    c.hso = (u32)((16 * mt + 4 * w) * TUS + ((q * 2 + (fcl >> 2)) * 18) * 1024);
    c.hrs = __builtin_amdgcn_make_buffer_rsrc(ws + WS_HT, 0, (int)((size_t)S_ * F_ * 2), 0x00020000);
    c.epoch = epoch;
    c.flag_pub = flag_ptr(ws, F_H + ((((mt * 4 + w) * 4 + q) * 2 + (fcl >> 2))) * 4 + (fcl & 3));
    c.flag_poll = flag_ptr(ws, F_H + (((((mt2 >> 1) * 4 + 2 * (mt2 & 1) + tg) * 4 + q) * 2 + kh)) * 4);
    asm volatile("" ::: "a0", "a1", "a2", "a3", "a4", "a5", "a6", "a7", "a8", "a9", "a10", "a11", "a12", "a13", "a14", "a15", "a16", "a17", "a18", "a19", "a20", "a21", "a22", "a23", "a24", "a25", "a26", "a27", "a28", "a29", "a30", "a31", "a32", "a33", "a34", "a35", "a36", "a37", "a38", "a39", "a40", "a41", "a42", "a43", "a44", "a45", "a46", "a47", "a48", "a49", "a50", "a51", "a52", "a53", "a54", "a55", "a56", "a57", "a58", "a59", "a60", "a61", "a62", "a63", "a64", "a65", "a66", "a67", "a68", "a69", "a70", "a71", "a72", "a73", "a74", "a75", "a76", "a77", "a78", "a79", "a80", "a81", "a82", "a83", "a84", "a85", "a86", "a87", "a88", "a89", "a90", "a91", "a92", "a93", "a94", "a95", "a96", "a97", "a98", "a99", "a100", "a101", "a102", "a103", "a104", "a105", "a106", "a107", "a108", "a109", "a110", "a111", "a112", "a113", "a114", "a115", "a116", "a117", "a118", "a119", "a120", "a121", "a122", "a123", "a124", "a125", "a126", "a127", "a128", "a129", "a130", "a131", "a132", "a133", "a134", "a135", "a136", "a137", "a138", "a139", "a140", "a141", "a142", "a143", "a144", "a145", "a146", "a147", "a148", "a149", "a150", "a151", "a152", "a153", "a154", "a155", "a156", "a157", "a158", "a159", "a160", "a161", "a162", "a163", "a164", "a165", "a166", "a167", "a168", "a169", "a170", "a171", "a172", "a173", "a174", "a175", "a176", "a177", "a178", "a179", "a180", "a181", "a182", "a183", "a184", "a185", "a186", "a187", "a188", "a189", "a190", "a191", "a192", "a193", "a194", "a195", "a196", "a197", "a198", "a199", "a200", "a201", "a202", "a203", "a204", "a205", "a206", "a207", "a208", "a209", "a210", "a211", "a212", "a213", "a214", "a215", "a216", "a217", "a218", "a219", "a220", "a221", "a222", "a223", "a224", "a225", "a226", "a227", "a228", "a229", "a230", "a231", "a232", "a233", "a234", "a235", "a236", "a237", "a238", "a239", "a240", "a241", "a242", "a243", "a244", "a245", "a246", "a247", "a248", "a249", "a250", "a251", "a252", "a253", "a254", "a255");
    poll_eq(flag_ptr(ws, F_TILE + (fc >> 2) * 36 + (lane < 36 ? lane : 0)), epoch);
    dma_stage<0>(c, std::make_integer_sequence<int, 9>{});
    dma_stage<1>(c, std::make_integer_sequence<int, 9>{});
    dma_stage<2>(c, std::make_integer_sequence<int, 9>{});
    poll_eq(flag_ptr(ws, F_ROWS + (4 * mt + w) * 8 + (lane & 7)), epoch);
    STAMP_AT(9);
    load_b_first(c, std::make_integer_sequence<int, 4 * LA>{});
    wait_vm<4 * LA>();
    __builtin_amdgcn_s_barrier();
    asm volatile("" ::: "memory");
#pragma unroll
    for (int f = 0; f < FR0; ++f) c.Aq[f] = *(const LAS bf16x8*)(c.rd[frag_stage(f)] + frag_off(f));
    SCHED();
    STAMP_AT(10);
    run_slots<0>(c, std::make_integer_sequence<int, 288>{});
    STAMP_AT(11);
    run_slots<288>(c, std::make_integer_sequence<int, 144 * 3>{});
    run_slots<144 * 5>(c, std::make_integer_sequence<int, 144 * 4>{});
    STAMP_AT(12);
    run_slots<G1S>(c, std::make_integer_sequence<int, 324>{});
    run_slots<G1S + 324>(c, std::make_integer_sequence<int, 324>{});
    asm volatile("s_nop 7" ::: "memory");
#pragma unroll
    for (int u = 0; u < 9; ++u)
#pragma unroll
        for (int t = 0; t < 4; ++t) asm volatile("" : "+v"(c.a2[u][t]));
    SCHED();
    STAMP_AT(13);
    const int n = lane & 15, g = lane >> 4;
    u32x2 xr[3][2]; f32x4 gb[3];
#pragma unroll
    for (int i = 0; i < 3; ++i) {
        const int u = (w + 4 * i < 9) ? w + 4 * i : 8, d = 144 * cc + 16 * u + 4 * g;
#pragma unroll
        for (int t = 0; t < 2; ++t) xr[i][t] = __builtin_amdgcn_raw_buffer_load_b64(wsr, (u32)WS_X1 + (u32)((128 * mt2 + 32 * q + 16 * t + n) * D_ + d) * 2, 0, 16);
        gb[i] = *(const f32x4*)(smem + R_MOD + (5 * 576 + d) * 4);
    }
    {
        char* ex = smem + 73728 + w * 18432 + lane * 16;
#pragma unroll
        for (int t = 0; t < 2; ++t)
#pragma unroll
            for (int u = 0; u < 9; ++u) *(f32x4*)(ex + (t * 9 + u) * 1024) = c.a2[u][2 + t];
    }
    __syncthreads();
    const int j = 2 * tg + kh;
    u32* fred = flag_ptr(ws, F_RED + (mt2 * 4 + cc) * 16);
    const u32 slab = (u32)WS_SLAB + (u32)((mt2 * 4 + cc) * 16) * 9216;
    {
        const char* ey = smem + 73728 + (w ^ 2) * 18432 + lane * 16;
        char* own = smem + lane * 16;
#pragma unroll
        for (int u = 0; u < 9; ++u) {
            const f32x4 r0 = c.a2[u][0] + *(const f32x4*)(ey + u * 1024);
            const f32x4 r1 = c.a2[u][1] + *(const f32x4*)(ey + (9 + u) * 1024);
            if (j != q) st16_dev(wsr, slab + (j * 4 + q) * 9216 + u * 1024 + lane * 16, (u32x4){pk2(r0[0], r0[1]), pk2(r0[2], r0[3]), pk2(r1[0], r1[1]), pk2(r1[2], r1[3])});
            else { *(f32x4*)(own + u * 2048) = r0; *(f32x4*)(own + u * 2048 + 1024) = r1; }
        }
    }
    if (j != q) {
        DRAIN();
        if (lane == 0) flag_set(fred + j * 4 + q, epoch);
    }
    STAMP_AT(14);
    poll_eq(fred + q * 4 + (((lane & 3) == q) ? (q ^ 1) : (lane & 3)), epoch);
    __syncthreads();
    u32x4 rem[3][3];
#pragma unroll
    for (int i = 0; i < 3; ++i) {
        const int u = (w + 4 * i < 9) ? w + 4 * i : 8;
#pragma unroll
        for (int k = 0; k < 3; ++k) { const int qq = k + (k >= q ? 1 : 0); rem[i][k] = __builtin_amdgcn_raw_buffer_load_b128(wsr, slab + (q * 4 + qq) * 9216 + u * 1024 + lane * 16, 0, 16); }
    }
#pragma unroll
    for (int i = 0; i < 3; ++i) {
        const int u = w + 4 * i;
        if (u < 9) {
            const int d = 144 * cc + 16 * u + 4 * g;
#pragma unroll
            for (int t = 0; t < 2; ++t) {
                f32x4 s = *(const f32x4*)(smem + u * 2048 + t * 1024 + lane * 16);
#pragma unroll
                for (int k = 0; k < 3; ++k) {
                    const u32 a = rem[i][k][2 * t], bq = rem[i][k][2 * t + 1];
                    s += (f32x4){bf_lo(a), bf_hi(a), bf_lo(bq), bf_hi(bq)};
                }
                const float o0 = bf_lo(xr[i][t][0]) + rbf(gb[i][0] * rbf(s[0])), o1 = bf_hi(xr[i][t][0]) + rbf(gb[i][1] * rbf(s[1]));
                const float o2 = bf_lo(xr[i][t][1]) + rbf(gb[i][2] * rbf(s[2])), o3 = bf_hi(xr[i][t][1]) + rbf(gb[i][3] * rbf(s[3]));
                *(u32x2*)(out + (size_t)(128 * mt2 + 32 * q + 16 * t + n) * D_ + d) = (u32x2){pk2(o0, o1), pk2(o2, o3)};
            }
        }
    }
    if (b == 0 && tid == 0) flag_set(flag_ptr(ws, F_EPOCH), epoch);
    STAMP_AT(15);
    STAMP_OUT(4, 16);
}

extern "C" void kernel_launch(void* const* d_in, const int* in_sizes, int n_in, void* d_out, int out_size, void* d_ws, size_t ws_size,
                              hipStream_t stream) {
    const u16* q = (const u16*)d_in[0];
    const u16* k = (const u16*)d_in[1];
    const u16* v = (const u16*)d_in[2];
    const u16* shift_k = (const u16*)d_in[3];
    const u16* ssm_k = (const u16*)d_in[4];
    const u16* ssm_D = (const u16*)d_in[5];
    const u16* wg = (const u16*)d_in[7];
    const u16* wu = (const u16*)d_in[8];
    const u16* wd = (const u16*)d_in[9];
    const u16* c = (const u16*)d_in[10];
    const u16* w_mod = (const u16*)d_in[11];
    char* ws = (char*)d_ws;
    (void)hipFuncSetAttribute((const void*)k_main, hipFuncAttributeMaxDynamicSharedMemorySize, mlp::LDS_BYTES);
    k_main<<<256, 256, mlp::LDS_BYTES, stream>>>(q, k, v, shift_k, ssm_k, ssm_D, wg, wu, wd, c, w_mod, ws, (u16*)d_out);
}
```

```cpp
#include <hip/hip_runtime.h>
#include <stdint.h>
#include <utility>

typedef unsigned short u16;
typedef unsigned int u32;
typedef __bf16 bf16x8 __attribute__((ext_vector_type(8)));
typedef __bf16 bf16x2 __attribute__((ext_vector_type(2)));
typedef float f32x4 __attribute__((ext_vector_type(4)));
typedef float f32x2 __attribute__((ext_vector_type(2)));
typedef u32 u32x4 __attribute__((ext_vector_type(4)));
typedef u32 u32x2 __attribute__((ext_vector_type(2)));
typedef short s16x4 __attribute__((ext_vector_type(4)));
#define LAS __attribute__((address_space(3)))
typedef LAS char* lptr;

constexpr int S_ = 2048, D_ = 576, F_ = 4608;

constexpr size_t WS_MOD = 0;
constexpr size_t WS_CNT = 65536;
constexpr size_t WS_YYT = WS_CNT + 65536;
constexpr size_t WS_X1  = WS_YYT + (size_t)D_ * S_ * 2;
constexpr size_t WS_H2T = WS_X1 + (size_t)D_ * S_ * 2;
constexpr size_t WS_HT  = WS_H2T + (size_t)D_ * S_ * 2;
constexpr size_t WS_WT1 = WS_HT + (size_t)S_ * F_ * 2;
constexpr size_t WS_SLAB = WS_WT1 + (size_t)2 * D_ * F_ * 2;
constexpr size_t WS_END = WS_SLAB + (size_t)16 * 4 * 4 * 4 * 9216;

#define STAMP_DECL
#define STAMP_ARGS
#define STAMP_AT(i)
#define STAMP_OUT(kid, n)
__device__ __forceinline__ float bf_lo(u32 w) { return __uint_as_float(w << 16); }
__device__ __forceinline__ float bf_hi(u32 w) { return __uint_as_float(w & 0xffff0000u); }
__device__ __forceinline__ float bf1(u16 w) { return __uint_as_float((u32)w << 16); }
__device__ __forceinline__ u32 pk2(float lo, float hi) {
    f32x2 v = {lo, hi};
    return __builtin_bit_cast(u32, __builtin_convertvector(v, bf16x2));
}
__device__ __forceinline__ float rbf(float x) { return (float)(__bf16)x; }
__device__ __forceinline__ float sigmoidf_(float x) { return __builtin_amdgcn_rcpf(1.0f + __builtin_amdgcn_exp2f(-1.4426950408889634f * x)); }

constexpr int F_MOD = 256;
constexpr int F_TILE = 512;
constexpr int F_CONV = 1024;
constexpr int F_ROWS = 3072;
constexpr int F_H = 3328;
constexpr int F_EPOCH = 6400;
typedef __amdgpu_buffer_rsrc_t rsrc_t;
__device__ __forceinline__ u32* flag_ptr(char* ws, int idx) { return (u32*)(ws + WS_CNT) + idx; }
__device__ __forceinline__ void flag_set(u32* p, u32 e) { __hip_atomic_store(p, e, __ATOMIC_RELAXED, __HIP_MEMORY_SCOPE_AGENT); }
__device__ __forceinline__ void st16_dev(rsrc_t rs, u32 off, u32x4 v) { __builtin_amdgcn_raw_buffer_store_b128(v, rs, off, 0, 16); }
__device__ __forceinline__ u32x4 ld16_dev(rsrc_t rs, u32 off) { return __builtin_amdgcn_raw_buffer_load_b128(rs, off, 0, 16); }
__device__ __forceinline__ void poll_eq(const u32* p, u32 e) {
    for (int it = 0; it < (1 << 21); ++it) {
        if (__all(__hip_atomic_load(p, __ATOMIC_RELAXED, __HIP_MEMORY_SCOPE_AGENT) == e)) break;
        __builtin_amdgcn_s_sleep(1);
    }
}
__device__ __forceinline__ void poll_eq4(rsrc_t rs, u32 off, u32 e) {
    for (int it = 0; it < (1 << 21); ++it) {
        asm volatile("" ::: "memory");
        const u32x4 v = ld16_dev(rs, off);
        if (__all(v[0] == e && v[1] == e && v[2] == e && v[3] == e)) break;
        __builtin_amdgcn_s_sleep(1);
    }
}
#define DRAIN() asm volatile("s_waitcnt vmcnt(0)" ::: "memory")

#define SCHED() __builtin_amdgcn_sched_barrier(0)
__device__ __forceinline__ float dpp_add(float v, const int ctrl_sel) {
    int r;
    const int iv = __builtin_bit_cast(int, v);
    switch (ctrl_sel) {
        case 0: r = __builtin_amdgcn_update_dpp(0, iv, 0xB1, 0xf, 0xf, true); break;
        case 1: r = __builtin_amdgcn_update_dpp(0, iv, 0x4E, 0xf, 0xf, true); break;
        case 2: r = __builtin_amdgcn_update_dpp(0, iv, 0x141, 0xf, 0xf, true); break;
        default: r = __builtin_amdgcn_update_dpp(0, iv, 0x140, 0xf, 0xf, true); break;
    }
    return v + __builtin_bit_cast(float, r);
}
__device__ __forceinline__ float half_sum(float v) {
    v = dpp_add(v, 0); v = dpp_add(v, 1); v = dpp_add(v, 2); v = dpp_add(v, 3);
    auto r = __builtin_amdgcn_permlane16_swap(__float_as_uint(v), __float_as_uint(v), false, false);
    return __uint_as_float(r[0]) + __uint_as_float(r[1]);
}

struct KvIn { u32x2 kw[8], vw[8]; u32x4 sw; u32x2 sw2; };
__device__ __forceinline__ void kv_load(KvIn& p, int c0, const u16* __restrict__ k, const u16* __restrict__ v, const u16* __restrict__ shift_k) {
    const int cw = c0 & ~1;
#pragma unroll
    for (int i = 0; i < 8; ++i) p.kw[i] = *(const u32x2*)(k + (size_t)(threadIdx.x + 256 * i) * D_ + cw);
#pragma unroll
    for (int i = 0; i < 8; ++i) p.vw[i] = *(const u32x2*)(v + (size_t)(threadIdx.x + 256 * i) * D_ + cw);
    p.sw = (u32x4){*(const u32*)(shift_k + c0 * 4), *(const u32*)(shift_k + c0 * 4 + 2), *(const u32*)(shift_k + c0 * 4 + 4), *(const u32*)(shift_k + c0 * 4 + 6)};
    p.sw2 = (u32x2){*(const u32*)(shift_k + c0 * 4 + 8), *(const u32*)(shift_k + c0 * 4 + 10)};
}
constexpr int KV_RAWV = 257 * 72 + 8;
__device__ __forceinline__ void kv_put(const KvIn& p, char* raw) {
    const int tid = threadIdx.x;
#pragma unroll
    for (int i = 0; i < 8; ++i) {
        const int t = tid + 256 * i, o = 72 * (t >> 3) + 8 * (t & 7);
        *(u32x2*)(raw + 72 + o) = p.kw[i];
        *(u32x2*)(raw + KV_RAWV + o) = p.vw[i];
    }
    if (tid < 8) *(u32x2*)(raw + 8 * tid) = (u32x2){0, 0};
}
__device__ __forceinline__ void kv_compute(const KvIn& p, int c0, const char* raw, char* xr, int stride) {
    const int tid = threadIdx.x;
    u32x2 kw[11], vw[8];
#pragma unroll
    for (int i = 0; i < 11; ++i) kw[i] = *(const u32x2*)(raw + 72 * tid + (i < 3 ? 40 + 8 * i : 72 + 8 * (i - 3)));
#pragma unroll
    for (int i = 0; i < 8; ++i) vw[i] = *(const u32x2*)(raw + KV_RAWV + 72 * tid + 8 * i);
    const bool odd = c0 & 1;
#pragma unroll
    for (int c = 0; c < 3; ++c) {
        const u32 wa = c == 0 ? p.sw[0] : c == 1 ? p.sw[2] : p.sw2[0], wb = c == 0 ? p.sw[1] : c == 1 ? p.sw[3] : p.sw2[1];
        const float w0 = bf_lo(wa), w1 = bf_hi(wa), w2 = bf_lo(wb), w3 = bf_hi(wb);
        float kf[11], r[8];
#define KV_PICK(d) (c == 0 ? (odd ? bf_hi((d)[0]) : bf_lo((d)[0])) : c == 1 ? (odd ? bf_lo((d)[1]) : bf_hi((d)[0])) : (odd ? bf_hi((d)[1]) : bf_lo((d)[1])))
#pragma unroll
        for (int i = 0; i < 11; ++i) kf[i] = KV_PICK(kw[i]);
#pragma unroll
        for (int i = 0; i < 8; ++i) r[i] = (kf[i] * w3 + kf[i + 1] * w2 + kf[i + 2] * w1 + kf[i + 3] * w0) * KV_PICK(vw[i]);
#undef KV_PICK
        *(u32x4*)(xr + c * stride + 2 * (2040 - 8 * tid)) = (u32x4){pk2(r[7], r[6]), pk2(r[5], r[4]), pk2(r[3], r[2]), pk2(r[1], r[0])};
    }
}

__device__ __forceinline__ void mod_job(int job, const u16* __restrict__ c, const u16* __restrict__ w_mod, char* __restrict__ ws, float* sc  , float* red  ) {
    const int tid = threadIdx.x, lane = tid & 63, wid = tid >> 6;
    const int col0 = job * 64, rg = tid >> 3, c8 = tid & 7;
    u32x4 w[18];
#pragma unroll
    for (int i = 0; i < 18; ++i) w[i] = *(const u32x4*)(w_mod + (size_t)(rg + 32 * i) * 3456 + col0 + c8 * 8);
#pragma unroll
    for (int j = 0; j < 3; ++j) { const int r = tid + 256 * j; if (r < 576) { const float x = bf1(c[r]); sc[r] = rbf(x * sigmoidf_(x)); } }
    __syncthreads();
    float acc[8] = {0, 0, 0, 0, 0, 0, 0, 0};
#pragma unroll
    for (int i = 0; i < 18; ++i) {
        const float s = sc[rg + 32 * i];
#pragma unroll
        for (int j = 0; j < 4; ++j) { acc[2 * j] += s * bf_lo(w[i][j]); acc[2 * j + 1] += s * bf_hi(w[i][j]); }
    }
#pragma unroll
    for (int j = 0; j < 8; ++j) {
        acc[j] += __shfl_xor(acc[j], 8); acc[j] += __shfl_xor(acc[j], 16); acc[j] += __shfl_xor(acc[j], 32);
    }
    if (lane < 8) {
#pragma unroll
        for (int j = 0; j < 8; ++j) red[wid * 64 + lane * 8 + j] = acc[j];
    }
    __syncthreads();
    if (tid < 64) {
        float m = rbf((red[tid] + red[64 + tid]) + (red[128 + tid] + red[192 + tid]));
        const int which = col0 / 576;
        if (which == 1 || which == 4) m = rbf(1.0f + m);
        __hip_atomic_store((u32*)(ws + WS_MOD) + col0 + tid, __float_as_uint(m), __ATOMIC_RELAXED, __HIP_MEMORY_SCOPE_AGENT);
    }
}

__device__ __forceinline__ const char* tile_base(int job, const u16* __restrict__ wg, const u16* __restrict__ wu) {
    const int ks = job % 18, mat = (job / 18) & 1, cg = job / 36;
    return (const char*)((mat ? wu : wg) + (size_t)(32 * ks) * F_ + cg * 576);
}
__device__ __forceinline__ void tile_dma(int job, const u16* __restrict__ wg, const u16* __restrict__ wu, char* tile, const u32 (&voff)[9]) {
    const int w = __builtin_amdgcn_readfirstlane(threadIdx.x >> 6);
    const char* base = tile_base(job, wg, wu) + (size_t)w * 8 * (F_ * 2);
    const u32 dst = (u32)(uintptr_t)tile + w * 9216;
#pragma unroll
    for (int i = 0; i < 9; ++i)
        asm volatile("s_mov_b32 m0, %2\n\ts_nop 0\n\tglobal_load_lds_dwordx4 %0, %1" :: "v"(voff[i]), "s"(base), "s"(dst + i * 1024) : "memory");
}
__device__ __forceinline__ void tile_out(int job, const char* tile, rsrc_t wsr) {
    const int lane = threadIdx.x & 63, w = threadIdx.x >> 6;
    const int ks = job % 18, mat = (job / 18) & 1, fc = 4 * (job / 36) + w;
    const int g = lane >> 4, q4 = (lane & 15) >> 2, p = lane & 3;
    const LAS char* rd = (const LAS char*)tile + (4 * g + q4) * 1152 + 8 * p + w * 288;
#pragma unroll
    for (int u = 0; u < 9; ++u) {
        const int pos = (fc & 1) ? (u == 0 ? 4 : u <= 4 ? u - 1 : u) : (u == 8 ? 4 : u < 4 ? u : u + 1);
        const s16x4 lo = __builtin_amdgcn_ds_read_tr16_b64_v4i16((LAS s16x4*)(rd + u * 32));
        const s16x4 hi = __builtin_amdgcn_ds_read_tr16_b64_v4i16((LAS s16x4*)(rd + u * 32 + 16 * 1152));
        typedef short s16x8 __attribute__((ext_vector_type(8)));
        const s16x8 r = __builtin_shufflevector(lo, hi, 0, 1, 2, 3, 4, 5, 6, 7);
        st16_dev(wsr, (u32)WS_WT1 + fc * 331776 + pos * 36864 + (ks * 2 + mat) * 1024 + lane * 16, __builtin_bit_cast(u32x4, r));
    }
}
__device__ __forceinline__ void tile_jobs(int i0, const u16* __restrict__ wg, const u16* __restrict__ wu, char* smem, char* ws, rsrc_t wsr, u32 epoch) {
    const int lane = threadIdx.x & 63;
    u32 voff[9];
#pragma unroll
    for (int i = 0; i < 9; ++i) { const int ci = lane + 64 * i, row = (ci * 58255) >> 22, c16 = ci - row * 72; voff[i] = (u32)(row * (F_ * 2) + c16 * 16); }
#pragma unroll
    for (int j = 0; j < 4; ++j) tile_dma(i0 + 64 * j, wg, wu, smem + j * 36864, voff);
#pragma unroll
    for (int j = 0; j < 4; ++j) {
        asm volatile("s_waitcnt vmcnt(27)" ::: "memory");
        __syncthreads();
        tile_out(i0 + 64 * j, smem + j * 36864, wsr);
    }
    if (i0 < 32) {
        __syncthreads();
        tile_dma(i0 + 256, wg, wu, smem, voff);
        asm volatile("s_waitcnt vmcnt(0)" ::: "memory");
        __syncthreads();
        tile_out(i0 + 256, smem, wsr);
    }
    DRAIN();
    __syncthreads();
    if (threadIdx.x < 5 && i0 + 64 * threadIdx.x < 288) flag_set(flag_ptr(ws, F_TILE + i0 + 64 * threadIdx.x), epoch);
}

constexpr int C_XR = 0;
constexpr int C_K0 = 4608;
constexpr int C_K1 = C_K0 + 4128 + 32;
constexpr int C_CH = C_K1 + 4160;
constexpr int C_RED = 3 * C_CH;
constexpr int C_LDS = C_RED + 3 * 32768;
static_assert(((C_K1 - C_K0) / 4) % 32 == 16, "bank offset");
static_assert(C_CH % 16 == 0 && C_K0 % 16 == 0 && C_K1 % 16 == 0, "align");
template <bool ZERO> __device__ __forceinline__ void mfma_v(f32x4& c, const bf16x8& a, const bf16x8& b) {
    if constexpr (ZERO) asm volatile("v_mfma_f32_16x16x32_bf16 %0, %1, %2, 0" : "=v"(c) : "v"(a), "v"(b));
    else asm volatile("v_mfma_f32_16x16x32_bf16 %0, %1, %2, %0" :: "v"(c), "v"(a), "v"(b));
}
struct ConvSet { bf16x8 A[8], B[8]; };
template <int J> __device__ __forceinline__ void conv_ld(ConvSet& s, const LAS char* ab, const LAS char* bb) {
    if constexpr (J < 8) {
        const LAS u32* p = (const LAS u32*)(ab + 512 * J);
        u32x4 t = {p[0], p[1], p[2], p[3]};
        s.A[J] = __builtin_bit_cast(bf16x8, t);
    } else s.B[J - 8] = *(const LAS bf16x8*)(bb + 512 * (15 - J));
}
template <int M> struct TriIdx {
    static constexpr int find(bool want_i) { int m = 0; for (int s = 0; s < 8; ++s) for (int j = 0; j <= s; ++j) { if (m == M) return want_i ? s : j; ++m; } return 0; }
    static constexpr int i = find(true), j = find(false);
};
template <int M, bool PF, bool FIRST> __device__ __forceinline__ void conv_slot(f32x4 (&acc)[8], const ConvSet& cur, ConvSet& nxt, const LAS char* ab, const LAS char* bb) {
    mfma_v<(FIRST && TriIdx<M>::j == 0)>(acc[TriIdx<M>::i], cur.A[TriIdx<M>::j], cur.B[TriIdx<M>::i - TriIdx<M>::j]);
    if constexpr (PF && (M & 1) == 0 && M / 2 < 16) conv_ld<((M / 2) & 1) * 8 + (M / 4)>(nxt, ab, bb);
    SCHED();
}
template <bool PF, bool FIRST, int... M> __device__ __forceinline__ void conv_step(f32x4 (&acc)[8], const ConvSet& cur, ConvSet& nxt, const LAS char* ab, const LAS char* bb, std::integer_sequence<int, M...>) {
    (conv_slot<M, PF, FIRST>(acc, cur, nxt, ab, bb), ...);
}
template <int... J> __device__ __forceinline__ void conv_ld_all(ConvSet& s, const LAS char* ab, const LAS char* bb, std::integer_sequence<int, J...>) { (conv_ld<J>(s, ab, bb), ...); }

struct ConvIn { u32x4 k; u32 kn; u16 dv; };
__device__ __forceinline__ void conv_load_k(ConvIn& in, int d, const u16* __restrict__ ssm_k, const u16* __restrict__ ssm_D) {
    const int tid = threadIdx.x;
    in.k = *(const u32x4*)(ssm_k + (size_t)d * S_ + tid * 8);
    in.kn = *(const u32*)(ssm_k + (size_t)d * S_ + (tid < 255 ? tid * 8 + 8 : 0));
    in.dv = ssm_D[d];
}
__device__ __forceinline__ void conv_put_k(const ConvIn& in, char* sm) {
    const int tid = threadIdx.x;
    const u32 kn = tid < 255 ? in.kn : 0u;
    *(u32x4*)(sm + C_K0 + 32 + tid * 16) = in.k;
    *(u32x4*)(sm + C_K1 + 32 + tid * 16) = (u32x4){__builtin_amdgcn_alignbit(in.k[1], in.k[0], 16), __builtin_amdgcn_alignbit(in.k[2], in.k[1], 16),
                                                 __builtin_amdgcn_alignbit(in.k[3], in.k[2], 16), __builtin_amdgcn_alignbit(kn, in.k[3], 16)};
    if (tid < 32) *(u32x4*)(sm + C_XR + 4096 + tid * 16) = (u32x4){0, 0, 0, 0};
    if (tid == 0) {
        *(u32x4*)(sm + C_K0) = (u32x4){0, 0, 0, 0}; *(u32x4*)(sm + C_K0 + 16) = (u32x4){0, 0, 0, 0};
        *(u32x4*)(sm + C_K1) = (u32x4){0, 0, 0, 0}; *(u32x4*)(sm + C_K1 + 16) = (u32x4){0, 0, 0, in.k[0] << 16};
    }
}
__device__ __forceinline__ void conv_finish(int d, const char* sm, const char* red, float Dv, rsrc_t wsr) {
    const int tid = threadIdx.x;
    const int i = tid >> 5, n = tid & 15, gp = (tid >> 4) & 1;
    f32x4 lo = {0.f, 0.f, 0.f, 0.f}, hi = {0.f, 0.f, 0.f, 0.f};
#pragma unroll
    for (int w = 0; w < 4; ++w) {
        lo += *(const f32x4*)(red + (w * 8 + i) * 1024 + (n + 32 * gp) * 16);
        hi += *(const f32x4*)(red + (w * 8 + i) * 1024 + (n + 32 * gp + 16) * 16);
    }
    const int s = 256 * i + 16 * n + 8 * gp;
    const u32x4 xw = *(const u32x4*)(sm + C_XR + 2 * (2040 - s));
    const float o0 = rbf(lo[0]) + rbf(bf_hi(xw[3]) * Dv), o1 = rbf(lo[1]) + rbf(bf_lo(xw[3]) * Dv), o2 = rbf(lo[2]) + rbf(bf_hi(xw[2]) * Dv), o3 = rbf(lo[3]) + rbf(bf_lo(xw[2]) * Dv);
    const float o4 = rbf(hi[0]) + rbf(bf_hi(xw[1]) * Dv), o5 = rbf(hi[1]) + rbf(bf_lo(xw[1]) * Dv), o6 = rbf(hi[2]) + rbf(bf_hi(xw[0]) * Dv), o7 = rbf(hi[3]) + rbf(bf_lo(xw[0]) * Dv);
    st16_dev(wsr, (u32)WS_YYT + (u32)(d * S_ + s) * 2, (u32x4){pk2(o0, o1), pk2(o2, o3), pk2(o4, o5), pk2(o6, o7)});
}
template <class Hook>
__device__ __forceinline__ void conv_body(int ch0, const ConvIn (&in)[3], char* smem, rsrc_t wsr, Hook hook
                                          ) {
    const int lane = threadIdx.x & 63, wid = __builtin_amdgcn_readfirstlane(threadIdx.x >> 6);
    __syncthreads();
    STAMP_AT(3);
    const int n = lane & 15, g = lane >> 4, dd = g >> 1, gh = g & 1;
    const LAS char* l0 = (const LAS char*)smem;
    const LAS char* bb = l0 + C_XR + 480 - 32 * n + 32 * dd + 16 * gh + 128 * wid;
    const LAS char* ab = l0 + ((n & 1) ? C_K0 + 2 * (16 * dd + 8 * gh + n + 1) : C_K1 + 2 * (16 * dd + 8 * gh + n)) + 128 * wid;
    f32x4 acc[3][8];
    ConvSet s0, s1;
    conv_ld_all(s0, ab, bb, std::make_integer_sequence<int, 16>{});
    SCHED();
    constexpr auto SQ = std::make_integer_sequence<int, 36>{};
    conv_step<true, true>(acc[0], s0, s1, ab + 64, bb + 64, SQ);
    conv_step<true, false>(acc[0], s1, s0, ab + C_CH, bb + C_CH, SQ);
    conv_step<true, true>(acc[1], s0, s1, ab + C_CH + 64, bb + C_CH + 64, SQ);
    conv_step<true, false>(acc[1], s1, s0, ab + 2 * C_CH, bb + 2 * C_CH, SQ);
    conv_step<true, true>(acc[2], s0, s1, ab + 2 * C_CH + 64, bb + 2 * C_CH + 64, SQ);
    conv_step<false, false>(acc[2], s1, s0, ab, bb, SQ);
    asm volatile("s_nop 7" ::: "memory");
#pragma unroll
    for (int c = 0; c < 3; ++c)
#pragma unroll
        for (int i = 0; i < 8; ++i) asm volatile("" : "+v"(acc[c][i]));
    STAMP_AT(4);
    hook();
#pragma unroll
    for (int c = 0; c < 3; ++c)
#pragma unroll
        for (int i = 0; i < 8; ++i) *(f32x4*)(smem + C_RED + c * 32768 + (wid * 8 + i) * 1024 + lane * 16) = acc[c][i];
    __syncthreads();
#pragma unroll
    for (int c = 0; c < 3; ++c) conv_finish(ch0 + c, smem + c * C_CH, smem + C_RED + c * 32768, bf1(in[c].dv), wsr);
}

constexpr int R_MOD = 147456;
constexpr int R_YT = 110592;
constexpr int R_HT = R_YT + 8 * 1168;
struct RowsPre { float x[18], gh[18]; u32 xw[9]; };
__device__ __forceinline__ void rows_load(RowsPre& r, int t0, const u16* __restrict__ q) {
    const int tid = threadIdx.x, lane = tid & 63, hl = lane & 31, row = 2 * (tid >> 6) + (lane >> 5);
    const u32* qrow = (const u32*)(q + (size_t)(t0 + row) * D_);
#pragma unroll
    for (int i = 0; i < 9; ++i) r.xw[i] = qrow[hl + 32 * i];
}
struct ModIn { u32x4 mw[4]; };
__device__ __forceinline__ void mod_load(ModIn& m, rsrc_t wsr) {
#pragma unroll
    for (int i = 0; i < 4; ++i) { const int j = threadIdx.x + 256 * i; m.mw[i] = ld16_dev(wsr, (u32)WS_MOD + (j < 864 ? j : 0) * 16); }
}
__device__ __forceinline__ void mod_put(const ModIn& m, char* sm) {
#pragma unroll
    for (int i = 0; i < 4; ++i) { const int j = threadIdx.x + 256 * i; if (j < 864) *(u32x4*)(sm + R_MOD + j * 16) = m.mw[i]; }
}
__device__ __forceinline__ void rows_pre(RowsPre& r, const char* sm) {
    const int hl = threadIdx.x & 31;
#pragma unroll
    for (int i = 0; i < 9; ++i) { r.x[2 * i] = bf_lo(r.xw[i]); r.x[2 * i + 1] = bf_hi(r.xw[i]); }
    float s = 0.f;
#pragma unroll
    for (int i = 0; i < 18; ++i) s += r.x[i];
    const float mean = half_sum(s) * (1.0f / 576.0f);
    float vs = 0.f;
#pragma unroll
    for (int i = 0; i < 18; ++i) { const float dl = r.x[i] - mean; vs += dl * dl; }
    const float rstd = __builtin_amdgcn_rsqf(half_sum(vs) * (1.0f / 576.0f) + 1e-5f);
    const float* mod = (const float*)(sm + R_MOD);
#pragma unroll
    for (int i = 0; i < 9; ++i) {
        const int c = 2 * (hl + 32 * i);
        const f32x2 sa = *(const f32x2*)(mod + c), c1 = *(const f32x2*)(mod + 576 + c), ga = *(const f32x2*)(mod + 2 * 576 + c);
        r.gh[2 * i] = ga[0] * ((r.x[2 * i] - mean) * rstd * c1[0] + sa[0]);
        r.gh[2 * i + 1] = ga[1] * ((r.x[2 * i + 1] - mean) * rstd * c1[1] + sa[1]);
    }
}
__device__ __forceinline__ void rows_fetch(int t0, char* sm, const char* ws) {
    const int lane = threadIdx.x & 63;
    const char* src = ws + WS_YYT + t0 * 2;
    const u32 dst = (u32)(uintptr_t)(sm + R_HT);
#pragma unroll
    for (int i = 0; i < 9; ++i)
        asm volatile("s_mov_b32 m0, %2\n\ts_nop 0\n\tglobal_load_lds_dwordx4 %0, %1 sc1" :: "v"((u32)((lane + 64 * i) * (S_ * 2))), "s"(src), "s"(dst + i * 1024) : "memory");
    asm volatile("s_waitcnt vmcnt(0)" ::: "memory");
}
__device__ __forceinline__ void rows_main(const RowsPre& r, int t0, char* sm, rsrc_t wsr) {
    const int tid = threadIdx.x, lane = tid & 63, hl = lane & 31, row = 2 * (tid >> 6) + (lane >> 5);
    const int cg = tid >> 1, th = tid & 1;
    if (tid < 144) {
        u32x2 yw[8];
#pragma unroll
        for (int c = 0; c < 8; ++c) yw[c] = *(const u32x2*)(sm + R_HT + (8 * cg + c) * 16 + th * 8);
#pragma unroll
        for (int t = 0; t < 4; ++t) {
            u32x4 o;
#pragma unroll
            for (int kk = 0; kk < 4; ++kk) {
                const u32 a = yw[2 * kk][t >> 1], bq = yw[2 * kk + 1][t >> 1];
                o[kk] = (t & 1) ? __builtin_amdgcn_perm(bq, a, 0x07060302u) : __builtin_amdgcn_perm(bq, a, 0x05040100u);
            }
            *(u32x4*)(sm + R_YT + (4 * th + t) * 1168 + cg * 16) = o;
        }
    }
    __syncthreads();
    const float* mod = (const float*)(sm + R_MOD);
    float x1[18];
#pragma unroll
    for (int i = 0; i < 9; ++i) {
        u32* yp = (u32*)(sm + R_YT + row * 1168 + (hl + 32 * i) * 4);
        const u32 yv = *yp;
        const u32 p = pk2(r.x[2 * i] + r.gh[2 * i] * bf_lo(yv), r.x[2 * i + 1] + r.gh[2 * i + 1] * bf_hi(yv));
        *yp = p;
        x1[2 * i] = bf_lo(p); x1[2 * i + 1] = bf_hi(p);
    }
    float s = 0.f;
#pragma unroll
    for (int i = 0; i < 18; ++i) s += x1[i];
    const float mean = half_sum(s) * (1.0f / 576.0f);
    float vs = 0.f;
#pragma unroll
    for (int i = 0; i < 18; ++i) { const float dl = x1[i] - mean; vs += dl * dl; }
    const float rstd = __builtin_amdgcn_rsqf(half_sum(vs) * (1.0f / 576.0f) + 1e-5f);
#pragma unroll
    for (int i = 0; i < 9; ++i) {
        const int c = 2 * (hl + 32 * i);
        const f32x2 sb = *(const f32x2*)(mod + 3 * 576 + c), c1 = *(const f32x2*)(mod + 4 * 576 + c);
        const float h0 = (x1[2 * i] - mean) * rstd * c1[0] + sb[0], h1 = (x1[2 * i + 1] - mean) * rstd * c1[1] + sb[1];
        const int ks = c >> 5, kk = c & 31, gq = (kk & 15) >> 2, j = ((kk >> 4) << 2) | (kk & 3);
        *(u32*)(sm + R_HT + ((ks * 4 + gq) * 8 + row) * 16 + j * 2) = pk2(h0, h1);
    }
    __syncthreads();
    const u32 h2t = (u32)WS_H2T + (u32)(t0 >> 4) * 18 * 1024 + (t0 & 15) * 16, x1g = (u32)WS_X1 + (u32)t0 * D_ * 2;
#pragma unroll
    for (int it = 0; it < 3; ++it) {
        const int i = tid + 256 * it;
        if (i < 576) {
            const int kg = i >> 3, rr8 = i & 7;
            st16_dev(wsr, h2t + (kg >> 2) * 1024 + (kg & 3) * 256 + rr8 * 16, *(const u32x4*)(sm + R_HT + i * 16));
            const int rr = i / 72, cc = i - rr * 72;
            st16_dev(wsr, x1g + rr * 1152 + cc * 16, *(const u32x4*)(sm + R_YT + rr * 1168 + cc * 16));
        }
    }
}

namespace mlp {
constexpr int STG = 36864, LDS_BYTES = 163840;
constexpr int G1S = 9 * 144, G2S = 18 * 36, NS = G1S + G2S;
constexpr int LA = 4;
constexpr int PRO = 9 + 4 * LA + 18;
constexpr int TUS = 147456;

constexpr int F01 = 288;
constexpr int stage_of(int gs) { return gs < F01 ? 0 : gs < G1S ? gs / 144 : 9 + (gs - G1S) / 72; }
constexpr int rel_of(int gs) { return gs < F01 ? gs : gs < G1S ? gs % 144 : (gs - G1S) % 72; }
constexpr int grp_of(int gs) { return rel_of(gs) >> 2; }
constexpr int t_of(int gs) { return rel_of(gs) & 3; }
constexpr bool barpt(int gs) { return gs >= F01 && gs < NS && t_of(gs) == 3 && grp_of(gs) == (gs < G1S ? 3 : 0) && stage_of(gs) < 17; }
constexpr int dma_id(int gs) {
    if (gs >= NS || t_of(gs) != 3) return -1;
    const int g = grp_of(gs), s = stage_of(gs);
    if (gs < F01) return (g >= 36 && g < 72 && (g & 3) == 3) ? 3 * 16 + (g - 36) / 4 : -1;
    if (s == 2) return (g >= 4 && g < 22) ? ((g & 1) ? 5 : 4) * 16 + (g - 4) / 2 : -1;
    if (s + 3 > 17) return -1;
    if (s == 8) return (g >= 4 && g < 13) ? 11 * 16 + g - 4 : -1;
    if (gs < G1S) { constexpr int G[9] = {5, 7, 9, 12, 14, 16, 20, 22, 24}; for (int i = 0; i < 9; ++i) if (G[i] == g) return (s + 3) * 16 + i; return -1; }
    return (g & 1) ? (s + 3) * 16 + (g >> 1) : -1;
}
constexpr int bload(int gs) {
    if (gs >= F01 || t_of(gs) != 1) return -1;
    const int ks = 2 * (gs / 32) + LA + (((gs % 32) >> 2) >> 2), i = (gs >> 2) & 3;
    return ks < 18 ? ks * 4 + i : -1;
}
constexpr bool unit_stores(int v) { return v == 1 || v == 3 || v == 4 || v == 6 || v == 8; }
constexpr int unit_end(int v) { return v < 2 ? F01 : 144 * (v + 1); }
constexpr int hload(int gs) {
    if (t_of(gs) != 3) return -1;
    const int s = stage_of(gs), g = grp_of(gs);
    if (s == 8) return (g >= 13 && g < 33) ? g - 13 : -1;
    if (s < 9 || g < 2 || g > 16 || (g & 1)) return -1;
    const int i = (g - 2) >> 1;
    if (s == 9) return 20 + i;
    if (s == 10) return 28 + i;
    if (s == 11) return i < 4 ? 36 + i : -1;
    if (s >= 12 && s <= 15) return 40 + 8 * (s - 12) + i;
    return -1;
}
constexpr int pub(int gs) { return (stage_of(gs) == 6 && grp_of(gs) == 27 && t_of(gs) == 3) ? 1 : (stage_of(gs) == 10 && grp_of(gs) == 9 && t_of(gs) == 1) ? 2 : 0; }
constexpr int poll_issue(int gs) { return (stage_of(gs) == 7 && grp_of(gs) == 27 && t_of(gs) == 3) ? 1 : (stage_of(gs) == 11 && grp_of(gs) == 9 && t_of(gs) == 1) ? 2 : 0; }
struct EpiSched { signed char ev[G1S], ep[G1S], sv[G1S], st[G1S]; };
constexpr EpiSched make_epi() {
    EpiSched e{};
    int v = 0, p = 0, pend_v = -1, pend_lo = 0, pend_hi = 0;
    for (int gs = 0; gs < G1S; ++gs) {
        e.ev[gs] = -1; e.ep[gs] = -1; e.sv[gs] = -1; e.st[gs] = -1;
        if (gs < F01) continue;
        const int t = t_of(gs);
        if (t == 0) continue;
        const bool vm = barpt(gs) || dma_id(gs) >= 0 || hload(gs) >= 0 || pub(gs) || poll_issue(gs);
        if (t == 3 && !vm && pend_lo < pend_hi) { e.sv[gs] = (signed char)pend_v; e.st[gs] = (signed char)pend_lo; ++pend_lo; continue; }
        if (t == 3 && vm) continue;
        if (v < 8 && gs >= unit_end(v) + 8 && (pend_lo == pend_hi || pend_v == v || !unit_stores(v) || true)) {
            e.ev[gs] = (signed char)v; e.ep[gs] = (signed char)p;
            ++p;
            if ((p & 15) == 0 && unit_stores(v)) { if (pend_v != v) { pend_v = v; pend_lo = p / 16 - 1; } pend_hi = p / 16; }
            if (p == 64) { p = 0; ++v; }
        }
    }
    return e;
}
constexpr EpiSched ES = make_epi();
constexpr int hstore(int gs) { return gs < G1S ? ES.st[gs] : -1; }
constexpr int epi_last(int v) { for (int gs = G1S - 1; gs >= 0; --gs) if (ES.ev[gs] == v) return gs; return -1; }
constexpr int store_last(int v) { for (int gs = G1S - 1; gs >= 0; --gs) if (ES.sv[gs] == v && ES.st[gs] == 3) return gs; return -1; }
constexpr bool epi_ok() {
    for (int v = 0; v < 8; ++v) {
        if (epi_last(v) < 0 || ES.ep[epi_last(v)] != 63) return false;
        if (v + 3 <= 8 && epi_last(v) >= 144 * (v + 3)) return false;
        if (unit_stores(v) && store_last(v) < 0) return false;
    }
    return store_last(4) + 40 < 6 * 144 + 27 * 4;
}
static_assert(epi_ok(), "epilogue schedule");
constexpr int vm_ops(int gs) { return (pub(gs) ? 1 : 0) + (poll_issue(gs) ? 1 : 0) + (hstore(gs) >= 0) + (bload(gs) >= 0) + (hload(gs) >= 0) + (dma_id(gs) >= 0); }
struct Ledger { int cum[NS + 1]; };
constexpr Ledger make_ledger() {
    Ledger l{}; int n = PRO;
    for (int gs = 0; gs < NS; ++gs) { if (gs == G1S) n += 4; l.cum[gs] = n; n += vm_ops(gs); }
    l.cum[NS] = n; return l;
}
constexpr Ledger LG = make_ledger();
constexpr int seq_in(int gs, int kind) {
    int n = LG.cum[gs];
    if (kind > 0 && pub(gs)) ++n;
    if (kind > 1 && poll_issue(gs)) ++n;
    if (kind > 2 && hstore(gs) >= 0) ++n;
    if (kind > 3 && bload(gs) >= 0) ++n;
    if (kind > 4 && hload(gs) >= 0) ++n;
    return n;
}
constexpr int seq_dma_last(int s) {
    if (s <= 2) return 9 * s + 8;
    for (int gs = 0; gs < NS; ++gs) if (dma_id(gs) == s * 16 + 8) return seq_in(gs, 5);
    return -1;
}
constexpr int seq_b(int ks) {
    if (ks < LA) return 27 + 4 * ks + 3;
    for (int gs = 0; gs < F01; ++gs) if (bload(gs) == ks * 4 + 3) return seq_in(gs, 3);
    return -1;
}
constexpr int seq_h(int kk) {
    for (int gs = 8 * 144; gs < NS; ++gs) if (hload(gs) == kk * 4 + 3) return seq_in(gs, 4);
    return -1;
}
constexpr int seq_poll(int part) {
    for (int gs = 0; gs < NS; ++gs) if (poll_issue(gs) == part) return seq_in(gs, 1);
    return -1;
}
constexpr int seq_store_last(int part) {
    if (part == 2) return LG.cum[G1S] - 1;
    return seq_in(store_last(4), 2);
}
struct Work { int unit, mat, k, t, n; };
constexpr Work work_of(int gs) {
    if (gs < F01) { const int p = gs / 32, um = (gs % 32) / 8, r = gs % 8; return {um >> 1, um & 1, 2 * p + (r & 1), r >> 1, 8 * p + 2 * um + (r & 1)}; }
    if (gs < G1S) { const int u = gs / 144, bi = (gs % 144) / 24, r = gs % 24; return {u, bi & 1, 6 * (bi >> 1) + r % 6, r / 6, 36 * u + 6 * bi + r % 6}; }
    const int s = (gs - G1S) / 72, r = (gs - G1S) % 72, du = r / 8, r8 = r % 8;
    return {du, 0, 2 * s + (r8 & 1), r8 >> 1, 324 + 18 * s + 2 * du + (r8 & 1)};
}
constexpr int NFRAG = 486, RQ = 12, FR0 = 8;
constexpr int frag_first(int n) {
    if (n < 72) return 32 * (n / 8) + 8 * ((n % 8) >> 1) + (n & 1);
    if (n < 324) return 144 * (n / 36) + 24 * ((n % 36) / 6) + n % 6;
    return G1S + 72 * ((n - 324) / 18) + 8 * (((n - 324) % 18) >> 1) + (n & 1);
}
constexpr int frag_last(int n) { return frag_first(n) + ((n >= 72 && n < 324) ? 18 : 6); }
constexpr int frag_stage(int n) { return n < 72 ? (n % 8) >> 2 : n < 324 ? n / 36 : 9 + (n - 324) / 18; }
constexpr int frag_off(int n) {
    if (n < 72) return (2 * (2 * (n / 8) + (n & 1)) + (((n % 8) >> 1) & 1)) * 1024;
    if (n < 324) { const int bi = (n % 36) / 6; return (2 * (6 * (bi >> 1) + n % 6) + (bi & 1)) * 1024; }
    return (n & 1) * 9216 + (((n - 324) % 18) >> 1) * 32;
}
constexpr int stage_ready(int s) {
    if (s <= 2) return 0;
    for (int gs = 0; gs < NS; ++gs) if (barpt(gs) && stage_of(gs) == s - 1) return gs + 1;
    return NS;
}
struct PfSched { short id[NS]; };
constexpr PfSched make_pf() {
    PfSched t{};
    int m = FR0, rdy = stage_ready(frag_stage(FR0));
    for (int gs = 0; gs < NS; ++gs) {
        t.id[gs] = -1;
        if ((gs & 3) != 0 || m >= NFRAG) continue;
        if (m >= RQ && frag_last(m - RQ) >= gs) continue;
        if (rdy > gs) continue;
        t.id[gs] = (short)m;
        ++m;
        if (m < NFRAG && frag_stage(m) != frag_stage(m - 1)) rdy = stage_ready(frag_stage(m));
    }
    return t;
}
constexpr PfSched PF = make_pf();
constexpr bool pf_ok() {
    int cnt = FR0;
    for (int gs = 0; gs < NS; ++gs) if (PF.id[gs] >= 0) { if (PF.id[gs] != cnt || frag_first(cnt) - gs < 8) return false; ++cnt; }
    return cnt == NFRAG;
}
static_assert(pf_ok(), "fragment prefetch schedule");
constexpr int clampv(int n) { return n > 63 ? 63 : (n < 0 ? 0 : n); }
constexpr int wait_for(int gs, int seq) { return clampv(LG.cum[gs] - seq - 1); }

template <int N> __device__ __forceinline__ void wait_vm() { asm volatile("s_waitcnt vmcnt(%0)" :: "n"(N) : "memory"); }

struct Ctx {
    bf16x8 Bv[2][4];
    bf16x8 Aq[12];
    f32x4 ac[3][2][4];
    f32x4 a2[9][4];
    float e0, e1;
    u32x4 hp[4];
    u32 vo1[4], vo2[4], vo3[4];
    u32 lane16, epoch;
    u32x4 pollv;
    const LAS char* rd[4]; const LAS char* rd2[4];
    const char* w1src; const char* w2src; const char* bsrc; const char* hsrc;
    u32 voff2[9];
    int sub;
    u32 dmadst;
    u32 hso;
    int jj;
    __amdgpu_buffer_rsrc_t hrs;
    u32* flag_pub; u32* flag_poll;
};

template <int KS, int T, bool ZERO> __device__ __forceinline__ void mfma(Ctx& cx, f32x4& c, const bf16x8& a) {
    constexpr int r = (KS * 4 + T) * 4;
    if constexpr (KS < 16) {
        if constexpr (ZERO) asm volatile("v_mfma_f32_16x16x32_bf16 %0, %1, a[%c2:%c3], 0" : "=v"(c) : "v"(a), "n"(r), "n"(r + 3));
        else asm volatile("v_mfma_f32_16x16x32_bf16 %0, %1, a[%c2:%c3], %0" :: "v"(c), "v"(a), "n"(r), "n"(r + 3));
    } else {
        asm volatile("v_mfma_f32_16x16x32_bf16 %0, %1, %2, %0" :: "v"(c), "v"(a), "v"(cx.Bv[KS - 16][T]));
    }
}
template <int KS, int T, bool SC1> __device__ __forceinline__ void gload(Ctx& cx, u32 voff, const char* sb) {
    constexpr int r = (KS * 4 + T) * 4, off = (KS & 3) * 1024;
    if constexpr (KS < 16) {
        if constexpr (SC1) asm volatile("global_load_dwordx4 a[%c2:%c3], %0, %1 offset:%4 sc1" :: "v"(voff), "s"(sb), "n"(r), "n"(r + 3), "n"(off) : "memory");
        else asm volatile("global_load_dwordx4 a[%c2:%c3], %0, %1 offset:%4" :: "v"(voff), "s"(sb), "n"(r), "n"(r + 3), "n"(off) : "memory");
    } else {
        if constexpr (SC1) asm volatile("global_load_dwordx4 %0, %1, %2 offset:%3 sc1" : "=v"(cx.Bv[KS - 16][T]) : "v"(voff), "s"(sb), "n"(off) : "memory");
        else asm volatile("global_load_dwordx4 %0, %1, %2 offset:%3" : "=v"(cx.Bv[KS - 16][T]) : "v"(voff), "s"(sb), "n"(off) : "memory");
    }
}
template <int S, int P> __device__ __forceinline__ void dma_m0(const Ctx& c) {
    asm volatile("s_mov_b32 m0, %0" :: "s"(c.dmadst + (u32)((S & 3) * STG + (P >> 2) * 4096)) : "memory");
}
constexpr int nat_kstep(int ks2) {
    if (ks2 < 10) { constexpr int T[5] = {0, 1, 5, 6, 4}; return 9 * (ks2 / 5) + T[ks2 % 5]; }
    const int jj = (ks2 - 10) >> 1, h = (ks2 - 10) & 1;
    return 9 * (jj >> 1) + ((jj & 1) ? 7 : 2) + h;
}
template <int S, int P> __device__ __forceinline__ void dma_go(const Ctx& c) {
    if constexpr (S < 9) {
        const char* sb = c.w1src + (size_t)S * STG + (P >> 2) * 4096;
        asm volatile("global_load_lds_dwordx4 %0, %1 offset:%2 sc1" :: "v"(c.lane16), "s"(sb), "n"((P & 3) * 1024) : "memory");
    } else {
        constexpr int n0 = nat_kstep(2 * (S - 9)), n1 = nat_kstep(2 * (S - 9) + 1);
        const char* sb = c.w2src + (size_t)(c.sub ? n1 : n0) * (32 * D_ * 2);
        asm volatile("global_load_lds_dwordx4 %0, %1 offset:%2" :: "v"(c.voff2[P]), "s"(sb), "n"((P & 3) * 1024) : "memory");
    }
}
template <int ID> __device__ __forceinline__ void load_b(Ctx& c) {
    constexpr int ks = ID >> 2, t = ID & 3;
    gload<ks, t, true>(c, c.vo1[t], c.bsrc + (ks >> 2) * 4096);
}
template <int ID> __device__ __forceinline__ void load_h(Ctx& c) {
    constexpr int kk = ID >> 2, t = ID & 3;
    gload<kk, t, true>(c, c.vo3[t], c.hsrc + (kk >> 2) * 4096);
}
template <int KS> __device__ __forceinline__ void pin_b(Ctx& c) {
#pragma unroll
    for (int t = 0; t < 4; ++t) { if constexpr (KS >= 16) asm volatile("" : "+v"(c.Bv[KS - 16][t])); }
}
template <int V, int P> __device__ __forceinline__ void epi_piece(Ctx& c) {
    constexpr int pr = P >> 3, st = P & 7, t = pr >> 1, r = 2 * (pr & 1), par = V % 3;
    if constexpr (st == 0) {
        asm volatile("v_mul_f32 %0, 0xbfb8aa3b, %1" : "=v"(c.e0) : "v"(c.ac[par][0][t][r]));
        asm volatile("v_mul_f32 %0, 0xbfb8aa3b, %1" : "=v"(c.e1) : "v"(c.ac[par][0][t][r + 1]));
    } else if constexpr (st == 1) asm volatile("v_exp_f32 %0, %0" :: "v"(c.e0));
    else if constexpr (st == 2) asm volatile("v_exp_f32 %0, %0" :: "v"(c.e1));
    else if constexpr (st == 3) {
        asm volatile("v_add_f32 %0, 1.0, %0" :: "v"(c.e0));
        asm volatile("v_add_f32 %0, 1.0, %0" :: "v"(c.e1));
    } else if constexpr (st == 4) asm volatile("v_rcp_f32 %0, %0" :: "v"(c.e0));
    else if constexpr (st == 5) asm volatile("v_rcp_f32 %0, %0" :: "v"(c.e1));
    else if constexpr (st == 6) {
        asm volatile("v_mul_f32 %0, %0, %1" :: "v"(c.e0), "v"(c.ac[par][1][t][r]));
        asm volatile("v_mul_f32 %0, %0, %1" :: "v"(c.e1), "v"(c.ac[par][1][t][r + 1]));
    } else {
        constexpr bool first = (V == 0 || V == 2 || V == 5 || V == 7);
        u32 o;
        asm volatile("v_cvt_pk_bf16_f32 %0, %1, %2" : "=v"(o) : "v"(c.e0), "v"(c.e1));
        c.hp[t][(first ? 0 : 2) + (pr & 1)] = o;
    }
}
template <int V, int T> __device__ __forceinline__ void store_h(Ctx& c) {
    const int m5 = 5 * (c.jj >> 1), ab = c.jj & 1;
    if constexpr (V == 4) {
        const u32x2 d = {c.hp[T][2], c.hp[T][3]};
        __builtin_amdgcn_raw_buffer_store_b64(d, c.hrs, c.vo2[T] + 8 * ab, c.hso + (m5 + 4) * 1024, 16);
    } else {
        const int kstep = (V == 1) ? m5 + 2 * ab : (V == 3) ? m5 + 2 * ab + 1 : (V == 6) ? 10 + 2 * c.jj : 11 + 2 * c.jj;
        __builtin_amdgcn_raw_buffer_store_b128(c.hp[T], c.hrs, c.vo2[T], c.hso + kstep * 1024, 16);
    }
}
template <int PART> __device__ __forceinline__ void poll_check(Ctx& c) {
    asm volatile("" : "+v"(c.pollv));
    const u32 e = c.epoch;
    if (!__all(c.pollv[0] == e && c.pollv[1] == e && c.pollv[2] == e && c.pollv[3] == e)) {
        const u32* p = c.flag_poll + (PART - 1) * 1024 + ((c.lane16 >> 4) & 3);
        poll_eq(p, e);
    }
}
template <int V, int P> __device__ __forceinline__ void epi_piece_padded(Ctx& c) {
    epi_piece<V, P>(c);
    if constexpr ((P & 7) == 2 || (P & 7) == 5) asm volatile("s_nop 0");
}
template <int V, int... P> __device__ __forceinline__ void epi_all(Ctx& c, std::integer_sequence<int, P...>) { (epi_piece_padded<V, P>(c), ...); }

template <int GS> __device__ __forceinline__ void slot(Ctx& c) {
    constexpr bool g1 = GS < G1S;
    constexpr int S = stage_of(GS), R = rel_of(GS), grp = R >> 2, t = R & 3;
    constexpr Work W = work_of(GS);
    constexpr bool f01 = GS < F01;
    if constexpr (GS == G1S) {
        asm volatile("s_nop 7" ::: "memory");
        epi_all<8>(c, std::make_integer_sequence<int, 64>{});
        store_h<8, 0>(c); store_h<8, 1>(c); store_h<8, 2>(c); store_h<8, 3>(c);
        SCHED();
    }
    if constexpr (f01 && GS % 32 == 0) { wait_vm<wait_for(GS, seq_b(2 * (GS / 32) + 1))>(); pin_b<2 * (GS / 32)>(c); pin_b<2 * (GS / 32) + 1>(c); }
    if constexpr (GS == 8 * 144) { wait_vm<wait_for(GS, seq_poll(1))>(); poll_check<1>(c); }
    if constexpr (!g1 && R == 0) { wait_vm<wait_for(GS, seq_h(2 * (S - 9) + 1))>(); pin_b<2 * (S - 9)>(c); pin_b<2 * (S - 9) + 1>(c); }
    if constexpr (dma_id(GS) >= 0) dma_m0<dma_id(GS) / 16, dma_id(GS) % 16>(c);
    if constexpr (g1) mfma<W.k, W.t, (W.k == 0)>(c, c.ac[W.unit % 3][W.mat][W.t], c.Aq[W.n % RQ]);
    else mfma<W.k, W.t, (W.k == 0)>(c, c.a2[W.unit][W.t], c.Aq[W.n % RQ]);
    if constexpr (barpt(GS)) {
        constexpr int n1 = wait_for(GS, seq_dma_last(S + 1));
        constexpr int n2 = (S == 12) ? wait_for(GS, seq_poll(2)) : 63;
        wait_vm<(n1 < n2 ? n1 : n2)>();
        if constexpr (S == 12) poll_check<2>(c);
        __builtin_amdgcn_s_barrier();
        asm volatile("" ::: "memory");
    }
    if constexpr (pub(GS)) {
        wait_vm<wait_for(GS, seq_store_last(pub(GS)))>();
        if (c.lane16 == 0) flag_set(c.flag_pub + (pub(GS) - 1) * 1024, c.epoch);
    }
    if constexpr (poll_issue(GS)) asm volatile("global_load_dwordx4 %0, %1, %2 sc1" : "=v"(c.pollv) : "v"(0u), "s"(c.flag_poll + (poll_issue(GS) - 1) * 1024) : "memory");
    {
        constexpr int f = PF.id[GS], fh = (GS & 3) == 1 ? PF.id[GS - ((GS & 3) == 1 ? 1 : 0)] : -1;
        if constexpr (f >= 0 && f < 324) c.Aq[f % RQ] = *(const LAS bf16x8*)(c.rd[frag_stage(f) & 3] + frag_off(f));
        constexpr int ft = f >= 324 ? f : fh >= 324 ? fh : -1;
        if constexpr (ft >= 0) {
            constexpr int half = f >= 324 ? 0 : 1;
            const s16x4 h = __builtin_amdgcn_ds_read_tr16_b64_v4i16((LAS s16x4*)(c.rd2[frag_stage(ft) & 3] + frag_off(ft) + half * (16 * 288)));
            const u32x2 hh = __builtin_bit_cast(u32x2, h);
            u32x4 a = __builtin_bit_cast(u32x4, c.Aq[ft % RQ]);
            a[2 * half] = hh[0]; a[2 * half + 1] = hh[1];
            c.Aq[ft % RQ] = __builtin_bit_cast(bf16x8, a);
        }
    }
    if constexpr (g1) {
        if constexpr (ES.ev[g1 ? GS : 0] >= 0) epi_piece<ES.ev[g1 ? GS : 0], ES.ep[g1 ? GS : 0]>(c);
        if constexpr (ES.sv[g1 ? GS : 0] >= 0) store_h<ES.sv[g1 ? GS : 0], ES.st[g1 ? GS : 0]>(c);
    }
    if constexpr (bload(GS) >= 0) load_b<bload(GS)>(c);
    if constexpr (hload(GS) >= 0) load_h<hload(GS)>(c);
    if constexpr (dma_id(GS) >= 0) dma_go<dma_id(GS) / 16, dma_id(GS) % 16>(c);
    SCHED();
}
template <int BASE, int... I> __device__ __forceinline__ void run_slots(Ctx& c, std::integer_sequence<int, I...>) { (slot<BASE + I>(c), ...); }
template <int S, int P> __device__ __forceinline__ void dma_one(const Ctx& c) { dma_m0<S, P>(c); asm volatile("s_nop 0"); dma_go<S, P>(c); }
template <int S, int... P> __device__ __forceinline__ void dma_stage(const Ctx& c, std::integer_sequence<int, P...>) { (dma_one<S, P>(c), ...); }
template <int... I> __device__ __forceinline__ void load_b_first(Ctx& c, std::integer_sequence<int, I...>) { (load_b<I>(c), ...); }
}

constexpr u32 SLAB_EMPTY = 0xFFFFFFFFu;
__device__ __forceinline__ void slab_clear(rsrc_t wsr, u32 slab_q  , int q) {
#pragma unroll
    for (int i = 0; i < 7; ++i) {
        const int c = threadIdx.x + 256 * i, s3 = c / 576, qq = s3 + (s3 >= q ? 1 : 0);
        if (c < 1728) st16_dev(wsr, slab_q + qq * 9216 + (c - s3 * 576) * 16, (u32x4){SLAB_EMPTY, SLAB_EMPTY, SLAB_EMPTY, SLAB_EMPTY});
    }
}
__device__ __forceinline__ void touch(u32& d, const char* p) { asm volatile("global_load_dword %0, %1, off" : "=v"(d) : "v"(p) : "memory"); }
__global__ __launch_bounds__(256, 1) void k_main(const u16* __restrict__ qin, const u16* __restrict__ kin, const u16* __restrict__ vin, const u16* __restrict__ shift_k,
                                                 const u16* __restrict__ ssm_k, const u16* __restrict__ ssm_D, const u16* __restrict__ wg, const u16* __restrict__ wu,
                                                 const u16* __restrict__ wd, const u16* __restrict__ cvec, const u16* __restrict__ w_mod,
                                                 char* __restrict__ ws, u16* __restrict__ out) {
    using namespace mlp;
    extern __shared__ __attribute__((aligned(16))) char smem[];
    const int tid = threadIdx.x, lane = tid & 63, w = __builtin_amdgcn_readfirstlane(tid >> 6);
    const int b = blockIdx.x, x = b & 7, idx = b >> 3, q = x >> 1;
    const int mt = 4 * (x & 1) + (idx & 3), fcl = idx >> 2, fc = 8 * q + fcl;
    const int mt2 = 8 * (x & 1) + (idx & 7), cc = idx >> 3, tg = w & 1, kh = w >> 1;
    const rsrc_t wsr = __builtin_amdgcn_make_buffer_rsrc(ws, 0, (int)WS_END, 0x00020000);
    STAMP_DECL; STAMP_AT(0);
    const u32 epoch_in = __hip_atomic_load(flag_ptr(ws, F_EPOCH), __ATOMIC_RELAXED, __HIP_MEMORY_SCOPE_AGENT);
    unsigned long long pc_start;
    asm volatile("s_getpc_b64 %0" : "=s"(pc_start));
    u32 epoch;
    {
        const int t0 = (x * 32 + idx) * 8;
        RowsPre rp;
        u32 bg[10] = {0, 0, 0, 0, 0, 0, 0, 0, 0, 0};
        if (b < 192) {
            const int c0 = 72 * x + 3 * idx;
            ConvIn in[3];
            KvIn kin_;
            if (w < 3) touch(bg[8], (const char*)(b < 96 ? kin : vin) + (size_t)((b % 96) * 192 + w * 64 + lane) * 128);
            kv_load(kin_, c0, kin, vin, shift_k);
            slab_clear(wsr, (u32)WS_SLAB + (u32)((mt2 * 4 + cc) * 16 + q * 4) * 9216, q);
            conv_load_k(in[0], c0, ssm_k, ssm_D); conv_load_k(in[1], c0 + 1, ssm_k, ssm_D); conv_load_k(in[2], c0 + 2, ssm_k, ssm_D);
            rows_load(rp, t0, qin);
            kv_put(kin_, smem + C_RED);
            conv_put_k(in[0], smem); conv_put_k(in[1], smem + C_CH); conv_put_k(in[2], smem + 2 * C_CH);
            __syncthreads();
            STAMP_AT(1);
            kv_compute(kin_, c0, smem + C_RED, smem + C_XR, C_CH);
            epoch = __builtin_amdgcn_readfirstlane(epoch_in) + 1u;
            if (w == 3) {
                DRAIN();
#pragma unroll
                for (int i = 0; i < 8; ++i) { const int l = lane + 64 * (i & 3); touch(bg[i], (const char*)(i < 4 ? wg : wu) + (size_t)b * 27648 + (l < 216 ? l : 0) * 128); }
            }
            STAMP_AT(2);
            ModIn mi;
            const u32* mf = flag_ptr(ws, F_MOD + (lane < 54 ? lane : 0));
            u32 pm;
            asm volatile("global_load_dword %0, %1, off sc1" : "=v"(pm) : "v"(mf) : "memory");
            conv_body(c0, in, smem, wsr, [&]() {
                DRAIN();
                asm volatile("" : "+v"(pm));
                if (!__all(pm == epoch)) poll_eq(mf, epoch);
                mod_load(mi, wsr); } STAMP_ARGS);
            STAMP_AT(5);
            mod_put(mi, smem);
            DRAIN();
#pragma unroll
            for (int i = 0; i < 10; ++i) asm volatile("" :: "v"(bg[i]));
            __syncthreads();
            if (tid < 8) flag_set(flag_ptr(ws, F_CONV + tid * 256 + b), epoch);
            if (w == 3) {
#pragma unroll
                for (int i = 0; i < 4; ++i) { const int l = lane + 64 * i; touch(bg[i], (const char*)wd + (size_t)b * 27648 + (l < 216 ? l : 0) * 128); }
            }
        } else {
            if (w == 3 && idx < 28) {
                const char* cb = (const char*)(pc_start & ~127ull) + lane * 128;
#pragma unroll
                for (int i = 0; i < 8; ++i) touch(bg[i], cb + (i < 7 || lane < 32 ? i * 8192 : 0));
            }
            if (w == 3 && idx >= 28) {
#pragma unroll
                for (int i = 0; i < 3; ++i) { const size_t o = (size_t)(((idx - 28) * 3 + i) * 64 + lane) << 16; touch(bg[i], ws + (o < WS_END ? o : 0)); }
                touch(bg[3], (const char*)out + (size_t)(lane < 36 ? lane : 0) * 65536);
            }
            slab_clear(wsr, (u32)WS_SLAB + (u32)((mt2 * 4 + cc) * 16 + q * 4) * 9216, q);
            rows_load(rp, t0, qin);
            if (b - 192 < 54) mod_job(b - 192, cvec, w_mod, ws, (float*)(smem + R_MOD), (float*)(smem + R_MOD + 4096));
            epoch = __builtin_amdgcn_readfirstlane(epoch_in) + 1u;
            DRAIN();
            __syncthreads();
            if (tid == 0 && b - 192 < 54) flag_set(flag_ptr(ws, F_MOD + b - 192), epoch);
            if (tid >= 64 && tid < 72) flag_set(flag_ptr(ws, F_CONV + (tid - 64) * 256 + b), epoch);
            STAMP_AT(1);
            if (b - 192 >= 54) { for (int i = 0; i < 40; ++i) __builtin_amdgcn_s_sleep(2); }
            tile_jobs(b - 192, wg, wu, smem, ws, wsr, epoch);
            STAMP_AT(2); STAMP_AT(3); STAMP_AT(4); STAMP_AT(5);
            poll_eq(flag_ptr(ws, F_MOD + (lane < 54 ? lane : 0)), epoch);
            ModIn mi;
            mod_load(mi, wsr);
            mod_put(mi, smem);
            __syncthreads();
        }
        rows_pre(rp, smem);
        STAMP_AT(6);
        if (w == 0) {
            poll_eq4(wsr, (u32)WS_CNT + (F_CONV + x * 256 + lane * 4) * 4, epoch);
            rows_fetch(t0, smem, ws);
        }
        __syncthreads();
        STAMP_AT(7);
        rows_main(rp, t0, smem, wsr);
        DRAIN();
#pragma unroll
        for (int i = 0; i < 10; ++i) asm volatile("" :: "v"(bg[i]));
        __syncthreads();
        if (tid == 0) flag_set(flag_ptr(ws, F_ROWS + (t0 >> 6) * 8 + ((t0 >> 3) & 7)), epoch);
        STAMP_AT(8);
    }
    Ctx c;
#pragma unroll
    for (int t = 0; t < 4; ++t) c.hp[t] = (u32x4){0, 0, 0, 0};
    c.lane16 = lane * 16;
#pragma unroll
    for (int t = 0; t < 4; ++t) { c.vo1[t] = lane * 16 + t * 18432; c.vo2[t] = lane * 16 + t * TUS; c.vo3[t] = lane * 16 + ((t + 2 * kh) & 3) * TUS; }
    const LAS char* l0 = (const LAS char*)smem;
#pragma unroll
    for (int i = 0; i < 4; ++i) {
        c.rd[i] = l0 + i * STG + lane * 16; c.rd2[i] = l0 + i * STG + kh * 18432 + (4 * (lane >> 4) + ((lane & 15) >> 2)) * 288 + 8 * (lane & 3);
        asm volatile("" : "+v"(c.rd[i])); asm volatile("" : "+v"(c.rd2[i]));
    }
#pragma unroll
    for (int i = 0; i < 9; ++i) { const int ci = lane + 64 * i, row = (ci * 3641) >> 16, c16 = ci - row * 18; c.voff2[i] = (u32)(row * (D_ * 2) + c16 * 16 - (i & 3) * 1024); }
    c.sub = w & 1;
    c.w1src = ws + WS_WT1 + (size_t)fc * 331776 + w * 9216;
    c.w2src = (const char*)(wd + (size_t)(36 * q + 18 * kh) * 32 * D_ + cc * 144);
    c.bsrc = ws + WS_H2T + (size_t)(16 * mt + 4 * w) * 18432;
    c.hsrc = ws + WS_HT + (size_t)(8 * mt2 + 4 * tg) * TUS + (size_t)((q * 2 + kh) * 18) * 1024;
    c.dmadst = (u32)(uintptr_t)smem + w * 9216;
    c.jj = fcl & 3;
    c.hso = (u32)((16 * mt + 4 * w) * TUS + ((q * 2 + (fcl >> 2)) * 18) * 1024);
    c.hrs = __builtin_amdgcn_make_buffer_rsrc(ws + WS_HT, 0, (int)((size_t)S_ * F_ * 2), 0x00020000);
    c.epoch = epoch;
    c.flag_pub = flag_ptr(ws, F_H + ((((mt * 4 + w) * 4 + q) * 2 + (fcl >> 2))) * 4 + (fcl & 3));
    c.flag_poll = flag_ptr(ws, F_H + (((((mt2 >> 1) * 4 + 2 * (mt2 & 1) + tg) * 4 + q) * 2 + kh)) * 4);
    asm volatile("" ::: "a0", "a1", "a2", "a3", "a4", "a5", "a6", "a7", "a8", "a9", "a10", "a11", "a12", "a13", "a14", "a15", "a16", "a17", "a18", "a19", "a20", "a21", "a22", "a23", "a24", "a25", "a26", "a27", "a28", "a29", "a30", "a31", "a32", "a33", "a34", "a35", "a36", "a37", "a38", "a39", "a40", "a41", "a42", "a43", "a44", "a45", "a46", "a47", "a48", "a49", "a50", "a51", "a52", "a53", "a54", "a55", "a56", "a57", "a58", "a59", "a60", "a61", "a62", "a63", "a64", "a65", "a66", "a67", "a68", "a69", "a70", "a71", "a72", "a73", "a74", "a75", "a76", "a77", "a78", "a79", "a80", "a81", "a82", "a83", "a84", "a85", "a86", "a87", "a88", "a89", "a90", "a91", "a92", "a93", "a94", "a95", "a96", "a97", "a98", "a99", "a100", "a101", "a102", "a103", "a104", "a105", "a106", "a107", "a108", "a109", "a110", "a111", "a112", "a113", "a114", "a115", "a116", "a117", "a118", "a119", "a120", "a121", "a122", "a123", "a124", "a125", "a126", "a127", "a128", "a129", "a130", "a131", "a132", "a133", "a134", "a135", "a136", "a137", "a138", "a139", "a140", "a141", "a142", "a143", "a144", "a145", "a146", "a147", "a148", "a149", "a150", "a151", "a152", "a153", "a154", "a155", "a156", "a157", "a158", "a159", "a160", "a161", "a162", "a163", "a164", "a165", "a166", "a167", "a168", "a169", "a170", "a171", "a172", "a173", "a174", "a175", "a176", "a177", "a178", "a179", "a180", "a181", "a182", "a183", "a184", "a185", "a186", "a187", "a188", "a189", "a190", "a191", "a192", "a193", "a194", "a195", "a196", "a197", "a198", "a199", "a200", "a201", "a202", "a203", "a204", "a205", "a206", "a207", "a208", "a209", "a210", "a211", "a212", "a213", "a214", "a215", "a216", "a217", "a218", "a219", "a220", "a221", "a222", "a223", "a224", "a225", "a226", "a227", "a228", "a229", "a230", "a231", "a232", "a233", "a234", "a235", "a236", "a237", "a238", "a239", "a240", "a241", "a242", "a243", "a244", "a245", "a246", "a247", "a248", "a249", "a250", "a251", "a252", "a253", "a254", "a255");
    poll_eq(flag_ptr(ws, F_TILE + (fc >> 2) * 36 + (lane < 36 ? lane : 0)), epoch);
    dma_stage<0>(c, std::make_integer_sequence<int, 9>{});
    dma_stage<1>(c, std::make_integer_sequence<int, 9>{});
    dma_stage<2>(c, std::make_integer_sequence<int, 9>{});
    poll_eq(flag_ptr(ws, F_ROWS + (4 * mt + w) * 8 + (lane & 7)), epoch);
    STAMP_AT(9);
    load_b_first(c, std::make_integer_sequence<int, 4 * LA>{});
    wait_vm<4 * LA>();
    __builtin_amdgcn_s_barrier();
    asm volatile("" ::: "memory");
#pragma unroll
    for (int f = 0; f < FR0; ++f) c.Aq[f] = *(const LAS bf16x8*)(c.rd[frag_stage(f)] + frag_off(f));
    SCHED();
    STAMP_AT(10);
    run_slots<0>(c, std::make_integer_sequence<int, 288>{});
    STAMP_AT(11);
    run_slots<288>(c, std::make_integer_sequence<int, 144 * 3>{});
    run_slots<144 * 5>(c, std::make_integer_sequence<int, 144 * 4>{});
    STAMP_AT(12);
    run_slots<G1S>(c, std::make_integer_sequence<int, 324>{});
    run_slots<G1S + 324>(c, std::make_integer_sequence<int, 324>{});
    asm volatile("s_nop 7" ::: "memory");
#pragma unroll
    for (int u = 0; u < 9; ++u)
#pragma unroll
        for (int t = 0; t < 4; ++t) asm volatile("" : "+v"(c.a2[u][t]));
    SCHED();
    STAMP_AT(13);
    const int n = lane & 15, g = lane >> 4;
    u32x2 xr[3][2]; f32x4 gb[3];
#pragma unroll
    for (int i = 0; i < 3; ++i) {
        const int u = (w + 4 * i < 9) ? w + 4 * i : 8, d = 144 * cc + 16 * u + 4 * g;
#pragma unroll
        for (int t = 0; t < 2; ++t) xr[i][t] = __builtin_amdgcn_raw_buffer_load_b64(wsr, (u32)WS_X1 + (u32)((128 * mt2 + 32 * q + 16 * t + n) * D_ + d) * 2, 0, 16);
        gb[i] = *(const f32x4*)(smem + R_MOD + (5 * 576 + d) * 4);
    }
    {
        char* ex = smem + 73728 + w * 18432 + lane * 16;
#pragma unroll
        for (int t = 0; t < 2; ++t)
#pragma unroll
            for (int u = 0; u < 9; ++u) *(f32x4*)(ex + (t * 9 + u) * 1024) = c.a2[u][2 + t];
    }
    __syncthreads();
    const int j = 2 * tg + kh;
    const u32 slab = (u32)WS_SLAB + (u32)((mt2 * 4 + cc) * 16) * 9216;
    {
        const char* ey = smem + 73728 + (w ^ 2) * 18432 + lane * 16;
        char* own = smem + lane * 16;
#pragma unroll
        for (int u = 0; u < 9; ++u) {
            const f32x4 r0 = c.a2[u][0] + *(const f32x4*)(ey + u * 1024);
            const f32x4 r1 = c.a2[u][1] + *(const f32x4*)(ey + (9 + u) * 1024);
            if (j != q) st16_dev(wsr, slab + (j * 4 + q) * 9216 + u * 1024 + lane * 16, (u32x4){pk2(r0[0], r0[1]), pk2(r0[2], r0[3]), pk2(r1[0], r1[1]), pk2(r1[2], r1[3])});
            else { *(f32x4*)(own + u * 2048) = r0; *(f32x4*)(own + u * 2048 + 1024) = r1; }
        }
    }
    STAMP_AT(14);
    __syncthreads();
    u32x4 rem[3][3];
    for (int it = 0; it < (1 << 16); ++it) {
        asm volatile("" ::: "memory");
        bool ok = true;
#pragma unroll
        for (int i = 0; i < 3; ++i) {
            const int u = (w + 4 * i < 9) ? w + 4 * i : 8;
#pragma unroll
            for (int k = 0; k < 3; ++k) { const int qq = k + (k >= q ? 1 : 0); rem[i][k] = __builtin_amdgcn_raw_buffer_load_b128(wsr, slab + (q * 4 + qq) * 9216 + u * 1024 + lane * 16, 0, 16); }
        }
#pragma unroll
        for (int i = 0; i < 3; ++i)
#pragma unroll
            for (int k = 0; k < 3; ++k) ok = ok && rem[i][k][0] != SLAB_EMPTY && rem[i][k][1] != SLAB_EMPTY && rem[i][k][2] != SLAB_EMPTY && rem[i][k][3] != SLAB_EMPTY;
        if (__all(ok)) break;
        __builtin_amdgcn_s_sleep(1);
    }
#pragma unroll
    for (int i = 0; i < 3; ++i) {
        const int u = w + 4 * i;
        if (u < 9) {
            const int d = 144 * cc + 16 * u + 4 * g;
#pragma unroll
            for (int t = 0; t < 2; ++t) {
                f32x4 s = *(const f32x4*)(smem + u * 2048 + t * 1024 + lane * 16);
#pragma unroll
                for (int k = 0; k < 3; ++k) {
                    const u32 a = rem[i][k][2 * t], bq = rem[i][k][2 * t + 1];
                    s += (f32x4){bf_lo(a), bf_hi(a), bf_lo(bq), bf_hi(bq)};
                }
                const float o0 = bf_lo(xr[i][t][0]) + rbf(gb[i][0] * rbf(s[0])), o1 = bf_hi(xr[i][t][0]) + rbf(gb[i][1] * rbf(s[1]));
                const float o2 = bf_lo(xr[i][t][1]) + rbf(gb[i][2] * rbf(s[2])), o3 = bf_hi(xr[i][t][1]) + rbf(gb[i][3] * rbf(s[3]));
                *(u32x2*)(out + (size_t)(128 * mt2 + 32 * q + 16 * t + n) * D_ + d) = (u32x2){pk2(o0, o1), pk2(o2, o3)};
            }
        }
    }
    if (b == 0 && tid == 0) flag_set(flag_ptr(ws, F_EPOCH), epoch);
    STAMP_AT(15);
    STAMP_OUT(4, 16);
}

extern "C" void kernel_launch(void* const* d_in, const int* in_sizes, int n_in, void* d_out, int out_size, void* d_ws, size_t ws_size,
                              hipStream_t stream) {
    const u16* q = (const u16*)d_in[0];
    const u16* k = (const u16*)d_in[1];
    const u16* v = (const u16*)d_in[2];
    const u16* shift_k = (const u16*)d_in[3];
    const u16* ssm_k = (const u16*)d_in[4];
    const u16* ssm_D = (const u16*)d_in[5];
    const u16* wg = (const u16*)d_in[7];
    const u16* wu = (const u16*)d_in[8];
    const u16* wd = (const u16*)d_in[9];
    const u16* c = (const u16*)d_in[10];
    const u16* w_mod = (const u16*)d_in[11];
    char* ws = (char*)d_ws;
    (void)hipFuncSetAttribute((const void*)k_main, hipFuncAttributeMaxDynamicSharedMemorySize, mlp::LDS_BYTES);
    k_main<<<256, 256, mlp::LDS_BYTES, stream>>>(q, k, v, shift_k, ssm_k, ssm_D, wg, wu, wd, c, w_mod, ws, (u16*)d_out);
}
```

```cpp
#include <hip/hip_runtime.h>
#include <stdint.h>
#include <utility>

typedef unsigned short u16;
typedef unsigned int u32;
typedef __bf16 bf16x8 __attribute__((ext_vector_type(8)));
typedef __bf16 bf16x2 __attribute__((ext_vector_type(2)));
typedef float f32x4 __attribute__((ext_vector_type(4)));
typedef float f32x2 __attribute__((ext_vector_type(2)));
typedef u32 u32x4 __attribute__((ext_vector_type(4)));
typedef u32 u32x2 __attribute__((ext_vector_type(2)));
typedef short s16x4 __attribute__((ext_vector_type(4)));
#define LAS __attribute__((address_space(3)))
typedef LAS char* lptr;

constexpr int S_ = 2048, D_ = 576, F_ = 4608;

constexpr size_t WS_MOD = 0;
constexpr size_t WS_CNT = 65536;
constexpr size_t WS_YYT = WS_CNT + 65536;
constexpr size_t WS_X1  = WS_YYT + (size_t)D_ * S_ * 2;
constexpr size_t WS_H2T = WS_X1 + (size_t)D_ * S_ * 2;
constexpr size_t WS_HT  = WS_H2T + (size_t)D_ * S_ * 2;
constexpr size_t WS_WT1 = WS_HT + (size_t)S_ * F_ * 2;
constexpr size_t WS_SLAB = WS_WT1 + (size_t)2 * D_ * F_ * 2;
constexpr size_t WS_END = WS_SLAB + (size_t)16 * 4 * 4 * 4 * 9216;

#define STAMP_DECL
#define STAMP_ARGS
#define STAMP_AT(i)
#define STAMP_OUT(kid, n)
__device__ __forceinline__ float bf_lo(u32 w) { return __uint_as_float(w << 16); }
__device__ __forceinline__ float bf_hi(u32 w) { return __uint_as_float(w & 0xffff0000u); }
__device__ __forceinline__ float bf1(u16 w) { return __uint_as_float((u32)w << 16); }
__device__ __forceinline__ u32 pk2(float lo, float hi) {
    f32x2 v = {lo, hi};
    return __builtin_bit_cast(u32, __builtin_convertvector(v, bf16x2));
}
__device__ __forceinline__ float rbf(float x) { return (float)(__bf16)x; }
__device__ __forceinline__ float sigmoidf_(float x) { return __builtin_amdgcn_rcpf(1.0f + __builtin_amdgcn_exp2f(-1.4426950408889634f * x)); }

constexpr int F_MOD = 256;
constexpr int F_TILE = 512;
constexpr int F_CONV = 1024;
constexpr int F_ROWS = 3072;
constexpr int F_H = 3328;
constexpr int F_RED = 5376;
constexpr int F_EPOCH = 6400;
typedef __amdgpu_buffer_rsrc_t rsrc_t;
__device__ __forceinline__ u32* flag_ptr(char* ws, int idx) { return (u32*)(ws + WS_CNT) + idx; }
__device__ __forceinline__ void flag_set(u32* p, u32 e) { __hip_atomic_store(p, e, __ATOMIC_RELAXED, __HIP_MEMORY_SCOPE_AGENT); }
__device__ __forceinline__ void st16_dev(rsrc_t rs, u32 off, u32x4 v) { __builtin_amdgcn_raw_buffer_store_b128(v, rs, off, 0, 16); }
__device__ __forceinline__ u32x4 ld16_dev(rsrc_t rs, u32 off) { return __builtin_amdgcn_raw_buffer_load_b128(rs, off, 0, 16); }
__device__ __forceinline__ void poll_eq(const u32* p, u32 e) {
    for (int it = 0; it < (1 << 21); ++it) {
        if (__all(__hip_atomic_load(p, __ATOMIC_RELAXED, __HIP_MEMORY_SCOPE_AGENT) == e)) break;
        __builtin_amdgcn_s_sleep(1);
    }
}
__device__ __forceinline__ void poll_eq4(rsrc_t rs, u32 off, u32 e) {
    for (int it = 0; it < (1 << 21); ++it) {
        asm volatile("" ::: "memory");
        const u32x4 v = ld16_dev(rs, off);
        if (__all(v[0] == e && v[1] == e && v[2] == e && v[3] == e)) break;
        __builtin_amdgcn_s_sleep(1);
    }
}
#define DRAIN() asm volatile("s_waitcnt vmcnt(0)" ::: "memory")

#define SCHED() __builtin_amdgcn_sched_barrier(0)
__device__ __forceinline__ float dpp_add(float v, const int ctrl_sel) {
    int r;
    const int iv = __builtin_bit_cast(int, v);
    switch (ctrl_sel) {
        case 0: r = __builtin_amdgcn_update_dpp(0, iv, 0xB1, 0xf, 0xf, true); break;
        case 1: r = __builtin_amdgcn_update_dpp(0, iv, 0x4E, 0xf, 0xf, true); break;
        case 2: r = __builtin_amdgcn_update_dpp(0, iv, 0x141, 0xf, 0xf, true); break;
        default: r = __builtin_amdgcn_update_dpp(0, iv, 0x140, 0xf, 0xf, true); break;
    }
    return v + __builtin_bit_cast(float, r);
}
__device__ __forceinline__ float half_sum(float v) {
    v = dpp_add(v, 0); v = dpp_add(v, 1); v = dpp_add(v, 2); v = dpp_add(v, 3);
    auto r = __builtin_amdgcn_permlane16_swap(__float_as_uint(v), __float_as_uint(v), false, false);
    return __uint_as_float(r[0]) + __uint_as_float(r[1]);
}

struct KvIn { u32x2 kw[8], vw[8]; u32x4 sw; u32x2 sw2; };
__device__ __forceinline__ void kv_load(KvIn& p, int c0, const u16* __restrict__ k, const u16* __restrict__ v, const u16* __restrict__ shift_k) {
    const int cw = c0 & ~1;
#pragma unroll
    for (int i = 0; i < 8; ++i) p.kw[i] = *(const u32x2*)(k + (size_t)(threadIdx.x + 256 * i) * D_ + cw);
#pragma unroll
    for (int i = 0; i < 8; ++i) p.vw[i] = *(const u32x2*)(v + (size_t)(threadIdx.x + 256 * i) * D_ + cw);
    p.sw = (u32x4){*(const u32*)(shift_k + c0 * 4), *(const u32*)(shift_k + c0 * 4 + 2), *(const u32*)(shift_k + c0 * 4 + 4), *(const u32*)(shift_k + c0 * 4 + 6)};
    p.sw2 = (u32x2){*(const u32*)(shift_k + c0 * 4 + 8), *(const u32*)(shift_k + c0 * 4 + 10)};
}
constexpr int KV_RAWV = 257 * 72 + 8;
__device__ __forceinline__ void kv_put(const KvIn& p, char* raw) {
    const int tid = threadIdx.x;
#pragma unroll
    for (int i = 0; i < 8; ++i) {
        const int t = tid + 256 * i, o = 72 * (t >> 3) + 8 * (t & 7);
        *(u32x2*)(raw + 72 + o) = p.kw[i];
        *(u32x2*)(raw + KV_RAWV + o) = p.vw[i];
    }
    if (tid < 8) *(u32x2*)(raw + 8 * tid) = (u32x2){0, 0};
}
__device__ __forceinline__ void kv_compute(const KvIn& p, int c0, const char* raw, char* xr, int stride) {
    const int tid = threadIdx.x;
    u32x2 kw[11], vw[8];
#pragma unroll
    for (int i = 0; i < 11; ++i) kw[i] = *(const u32x2*)(raw + 72 * tid + (i < 3 ? 40 + 8 * i : 72 + 8 * (i - 3)));
#pragma unroll
    for (int i = 0; i < 8; ++i) vw[i] = *(const u32x2*)(raw + KV_RAWV + 72 * tid + 8 * i);
    const bool odd = c0 & 1;
#pragma unroll
    for (int c = 0; c < 3; ++c) {
        const u32 wa = c == 0 ? p.sw[0] : c == 1 ? p.sw[2] : p.sw2[0], wb = c == 0 ? p.sw[1] : c == 1 ? p.sw[3] : p.sw2[1];
        const float w0 = bf_lo(wa), w1 = bf_hi(wa), w2 = bf_lo(wb), w3 = bf_hi(wb);
        float kf[11], r[8];
#define KV_PICK(d) (c == 0 ? (odd ? bf_hi((d)[0]) : bf_lo((d)[0])) : c == 1 ? (odd ? bf_lo((d)[1]) : bf_hi((d)[0])) : (odd ? bf_hi((d)[1]) : bf_lo((d)[1])))
#pragma unroll
        for (int i = 0; i < 11; ++i) kf[i] = KV_PICK(kw[i]);
#pragma unroll
        for (int i = 0; i < 8; ++i) r[i] = (kf[i] * w3 + kf[i + 1] * w2 + kf[i + 2] * w1 + kf[i + 3] * w0) * KV_PICK(vw[i]);
#undef KV_PICK
        *(u32x4*)(xr + c * stride + 2 * (2040 - 8 * tid)) = (u32x4){pk2(r[7], r[6]), pk2(r[5], r[4]), pk2(r[3], r[2]), pk2(r[1], r[0])};
    }
}

__device__ __forceinline__ void mod_job(int job, const u16* __restrict__ c, const u16* __restrict__ w_mod, char* __restrict__ ws, float* sc  , float* red  ) {
    const int tid = threadIdx.x, lane = tid & 63, wid = tid >> 6;
    const int col0 = job * 64, rg = tid >> 3, c8 = tid & 7;
    u32x4 w[18];
#pragma unroll
    for (int i = 0; i < 18; ++i) w[i] = *(const u32x4*)(w_mod + (size_t)(rg + 32 * i) * 3456 + col0 + c8 * 8);
#pragma unroll
    for (int j = 0; j < 3; ++j) { const int r = tid + 256 * j; if (r < 576) { const float x = bf1(c[r]); sc[r] = rbf(x * sigmoidf_(x)); } }
    __syncthreads();
    float acc[8] = {0, 0, 0, 0, 0, 0, 0, 0};
#pragma unroll
    for (int i = 0; i < 18; ++i) {
        const float s = sc[rg + 32 * i];
#pragma unroll
        for (int j = 0; j < 4; ++j) { acc[2 * j] += s * bf_lo(w[i][j]); acc[2 * j + 1] += s * bf_hi(w[i][j]); }
    }
#pragma unroll
    for (int j = 0; j < 8; ++j) {
        acc[j] += __shfl_xor(acc[j], 8); acc[j] += __shfl_xor(acc[j], 16); acc[j] += __shfl_xor(acc[j], 32);
    }
    if (lane < 8) {
#pragma unroll
        for (int j = 0; j < 8; ++j) red[wid * 64 + lane * 8 + j] = acc[j];
    }
    __syncthreads();
    if (tid < 64) {
        float m = rbf((red[tid] + red[64 + tid]) + (red[128 + tid] + red[192 + tid]));
        const int which = col0 / 576;
        if (which == 1 || which == 4) m = rbf(1.0f + m);
        __hip_atomic_store((u32*)(ws + WS_MOD) + col0 + tid, __float_as_uint(m), __ATOMIC_RELAXED, __HIP_MEMORY_SCOPE_AGENT);
    }
}

__device__ __forceinline__ const char* tile_base(int job, const u16* __restrict__ wg, const u16* __restrict__ wu) {
    const int ks = job % 18, mat = (job / 18) & 1, cg = job / 36;
    return (const char*)((mat ? wu : wg) + (size_t)(32 * ks) * F_ + cg * 576);
}
__device__ __forceinline__ void tile_dma(int job, const u16* __restrict__ wg, const u16* __restrict__ wu, char* tile, const u32 (&voff)[9]) {
    const int w = __builtin_amdgcn_readfirstlane(threadIdx.x >> 6);
    const char* base = tile_base(job, wg, wu) + (size_t)w * 8 * (F_ * 2);
    const u32 dst = (u32)(uintptr_t)tile + w * 9216;
#pragma unroll
    for (int i = 0; i < 9; ++i)
        asm volatile("s_mov_b32 m0, %2\n\ts_nop 0\n\tglobal_load_lds_dwordx4 %0, %1" :: "v"(voff[i]), "s"(base), "s"(dst + i * 1024) : "memory");
}
__device__ __forceinline__ void tile_out(int job, const char* tile, rsrc_t wsr) {
    const int lane = threadIdx.x & 63, w = threadIdx.x >> 6;
    const int ks = job % 18, mat = (job / 18) & 1, fc = 4 * (job / 36) + w;
    const int g = lane >> 4, q4 = (lane & 15) >> 2, p = lane & 3;
    const LAS char* rd = (const LAS char*)tile + (4 * g + q4) * 1152 + 8 * p + w * 288;
#pragma unroll
    for (int u = 0; u < 9; ++u) {
        const int pos = (fc & 1) ? (u == 0 ? 4 : u <= 4 ? u - 1 : u) : (u == 8 ? 4 : u < 4 ? u : u + 1);
        const s16x4 lo = __builtin_amdgcn_ds_read_tr16_b64_v4i16((LAS s16x4*)(rd + u * 32));
        const s16x4 hi = __builtin_amdgcn_ds_read_tr16_b64_v4i16((LAS s16x4*)(rd + u * 32 + 16 * 1152));
        typedef short s16x8 __attribute__((ext_vector_type(8)));
        const s16x8 r = __builtin_shufflevector(lo, hi, 0, 1, 2, 3, 4, 5, 6, 7);
        st16_dev(wsr, (u32)WS_WT1 + fc * 331776 + pos * 36864 + (ks * 2 + mat) * 1024 + lane * 16, __builtin_bit_cast(u32x4, r));
    }
}
__device__ __forceinline__ void tile_jobs(int i0, const u16* __restrict__ wg, const u16* __restrict__ wu, char* smem, char* ws, rsrc_t wsr, u32 epoch) {
    const int lane = threadIdx.x & 63;
    u32 voff[9];
#pragma unroll
    for (int i = 0; i < 9; ++i) { const int ci = lane + 64 * i, row = (ci * 58255) >> 22, c16 = ci - row * 72; voff[i] = (u32)(row * (F_ * 2) + c16 * 16); }
#pragma unroll
    for (int j = 0; j < 4; ++j) tile_dma(i0 + 64 * j, wg, wu, smem + j * 36864, voff);
#pragma unroll
    for (int j = 0; j < 4; ++j) {
        asm volatile("s_waitcnt vmcnt(27)" ::: "memory");
        __syncthreads();
        tile_out(i0 + 64 * j, smem + j * 36864, wsr);
    }
    if (i0 < 32) {
        __syncthreads();
        tile_dma(i0 + 256, wg, wu, smem, voff);
        asm volatile("s_waitcnt vmcnt(0)" ::: "memory");
        __syncthreads();
        tile_out(i0 + 256, smem, wsr);
    }
    DRAIN();
    __syncthreads();
    if (threadIdx.x < 5 && i0 + 64 * threadIdx.x < 288) flag_set(flag_ptr(ws, F_TILE + i0 + 64 * threadIdx.x), epoch);
}

constexpr int C_XR = 0;
constexpr int C_K0 = 4608;
constexpr int C_K1 = C_K0 + 4128 + 32;
constexpr int C_CH = C_K1 + 4160;
constexpr int C_RED = 3 * C_CH;
constexpr int C_LDS = C_RED + 3 * 32768;
static_assert(((C_K1 - C_K0) / 4) % 32 == 16, "bank offset");
static_assert(C_CH % 16 == 0 && C_K0 % 16 == 0 && C_K1 % 16 == 0, "align");
template <bool ZERO> __device__ __forceinline__ void mfma_v(f32x4& c, const bf16x8& a, const bf16x8& b) {
    if constexpr (ZERO) asm volatile("v_mfma_f32_16x16x32_bf16 %0, %1, %2, 0" : "=v"(c) : "v"(a), "v"(b));
    else asm volatile("v_mfma_f32_16x16x32_bf16 %0, %1, %2, %0" :: "v"(c), "v"(a), "v"(b));
}
struct ConvSet { bf16x8 A[8], B[8]; };
template <int J> __device__ __forceinline__ void conv_ld(ConvSet& s, const LAS char* ab, const LAS char* bb) {
    if constexpr (J < 8) {
        const LAS u32* p = (const LAS u32*)(ab + 512 * J);
        u32x4 t = {p[0], p[1], p[2], p[3]};
        s.A[J] = __builtin_bit_cast(bf16x8, t);
    } else s.B[J - 8] = *(const LAS bf16x8*)(bb + 512 * (15 - J));
}
template <int M> struct TriIdx {
    static constexpr int find(bool want_i) { int m = 0; for (int s = 0; s < 8; ++s) for (int j = 0; j <= s; ++j) { if (m == M) return want_i ? s : j; ++m; } return 0; }
    static constexpr int i = find(true), j = find(false);
};
template <int M, bool PF, bool FIRST> __device__ __forceinline__ void conv_slot(f32x4 (&acc)[8], const ConvSet& cur, ConvSet& nxt, const LAS char* ab, const LAS char* bb) {
    mfma_v<(FIRST && TriIdx<M>::j == 0)>(acc[TriIdx<M>::i], cur.A[TriIdx<M>::j], cur.B[TriIdx<M>::i - TriIdx<M>::j]);
    if constexpr (PF && (M & 1) == 0 && M / 2 < 16) conv_ld<((M / 2) & 1) * 8 + (M / 4)>(nxt, ab, bb);
    SCHED();
}
template <bool PF, bool FIRST, int... M> __device__ __forceinline__ void conv_step(f32x4 (&acc)[8], const ConvSet& cur, ConvSet& nxt, const LAS char* ab, const LAS char* bb, std::integer_sequence<int, M...>) {
    (conv_slot<M, PF, FIRST>(acc, cur, nxt, ab, bb), ...);
}
template <int... J> __device__ __forceinline__ void conv_ld_all(ConvSet& s, const LAS char* ab, const LAS char* bb, std::integer_sequence<int, J...>) { (conv_ld<J>(s, ab, bb), ...); }

struct ConvIn { u32x4 k; u32 kn; u16 dv; };
__device__ __forceinline__ void conv_load_k(ConvIn& in, int d, const u16* __restrict__ ssm_k, const u16* __restrict__ ssm_D) {
    const int tid = threadIdx.x;
    in.k = *(const u32x4*)(ssm_k + (size_t)d * S_ + tid * 8);
    in.kn = *(const u32*)(ssm_k + (size_t)d * S_ + (tid < 255 ? tid * 8 + 8 : 0));
    in.dv = ssm_D[d];
}
__device__ __forceinline__ void conv_put_k(const ConvIn& in, char* sm) {
    const int tid = threadIdx.x;
    const u32 kn = tid < 255 ? in.kn : 0u;
    *(u32x4*)(sm + C_K0 + 32 + tid * 16) = in.k;
    *(u32x4*)(sm + C_K1 + 32 + tid * 16) = (u32x4){__builtin_amdgcn_alignbit(in.k[1], in.k[0], 16), __builtin_amdgcn_alignbit(in.k[2], in.k[1], 16),
                                                 __builtin_amdgcn_alignbit(in.k[3], in.k[2], 16), __builtin_amdgcn_alignbit(kn, in.k[3], 16)};
    if (tid < 32) *(u32x4*)(sm + C_XR + 4096 + tid * 16) = (u32x4){0, 0, 0, 0};
    if (tid == 0) {
        *(u32x4*)(sm + C_K0) = (u32x4){0, 0, 0, 0}; *(u32x4*)(sm + C_K0 + 16) = (u32x4){0, 0, 0, 0};
        *(u32x4*)(sm + C_K1) = (u32x4){0, 0, 0, 0}; *(u32x4*)(sm + C_K1 + 16) = (u32x4){0, 0, 0, in.k[0] << 16};
    }
}
__device__ __forceinline__ void conv_finish(int d, const char* sm, const char* red, float Dv, rsrc_t wsr) {
    const int tid = threadIdx.x;
    const int i = tid >> 5, n = tid & 15, gp = (tid >> 4) & 1;
    f32x4 lo = {0.f, 0.f, 0.f, 0.f}, hi = {0.f, 0.f, 0.f, 0.f};
#pragma unroll
    for (int w = 0; w < 4; ++w) {
        lo += *(const f32x4*)(red + (w * 8 + i) * 1024 + (n + 32 * gp) * 16);
        hi += *(const f32x4*)(red + (w * 8 + i) * 1024 + (n + 32 * gp + 16) * 16);
    }
    const int s = 256 * i + 16 * n + 8 * gp;
    const u32x4 xw = *(const u32x4*)(sm + C_XR + 2 * (2040 - s));
    const float o0 = rbf(lo[0]) + rbf(bf_hi(xw[3]) * Dv), o1 = rbf(lo[1]) + rbf(bf_lo(xw[3]) * Dv), o2 = rbf(lo[2]) + rbf(bf_hi(xw[2]) * Dv), o3 = rbf(lo[3]) + rbf(bf_lo(xw[2]) * Dv);
    const float o4 = rbf(hi[0]) + rbf(bf_hi(xw[1]) * Dv), o5 = rbf(hi[1]) + rbf(bf_lo(xw[1]) * Dv), o6 = rbf(hi[2]) + rbf(bf_hi(xw[0]) * Dv), o7 = rbf(hi[3]) + rbf(bf_lo(xw[0]) * Dv);
    st16_dev(wsr, (u32)WS_YYT + (u32)(d * S_ + s) * 2, (u32x4){pk2(o0, o1), pk2(o2, o3), pk2(o4, o5), pk2(o6, o7)});
}
template <class Hook>
__device__ __forceinline__ void conv_body(int ch0, const ConvIn (&in)[3], char* smem, rsrc_t wsr, Hook hook
                                          ) {
    const int lane = threadIdx.x & 63, wid = __builtin_amdgcn_readfirstlane(threadIdx.x >> 6);
    __syncthreads();
    STAMP_AT(3);
    const int n = lane & 15, g = lane >> 4, dd = g >> 1, gh = g & 1;
    const LAS char* l0 = (const LAS char*)smem;
    const LAS char* bb = l0 + C_XR + 480 - 32 * n + 32 * dd + 16 * gh + 128 * wid;
    const LAS char* ab = l0 + ((n & 1) ? C_K0 + 2 * (16 * dd + 8 * gh + n + 1) : C_K1 + 2 * (16 * dd + 8 * gh + n)) + 128 * wid;
    f32x4 acc[3][8];
    ConvSet s0, s1;
    conv_ld_all(s0, ab, bb, std::make_integer_sequence<int, 16>{});
    SCHED();
    constexpr auto SQ = std::make_integer_sequence<int, 36>{};
    conv_step<true, true>(acc[0], s0, s1, ab + 64, bb + 64, SQ);
    conv_step<true, false>(acc[0], s1, s0, ab + C_CH, bb + C_CH, SQ);
    conv_step<true, true>(acc[1], s0, s1, ab + C_CH + 64, bb + C_CH + 64, SQ);
    conv_step<true, false>(acc[1], s1, s0, ab + 2 * C_CH, bb + 2 * C_CH, SQ);
    conv_step<true, true>(acc[2], s0, s1, ab + 2 * C_CH + 64, bb + 2 * C_CH + 64, SQ);
    conv_step<false, false>(acc[2], s1, s0, ab, bb, SQ);
    asm volatile("s_nop 7" ::: "memory");
#pragma unroll
    for (int c = 0; c < 3; ++c)
#pragma unroll
        for (int i = 0; i < 8; ++i) asm volatile("" : "+v"(acc[c][i]));
    STAMP_AT(4);
    hook();
#pragma unroll
    for (int c = 0; c < 3; ++c)
#pragma unroll
        for (int i = 0; i < 8; ++i) *(f32x4*)(smem + C_RED + c * 32768 + (wid * 8 + i) * 1024 + lane * 16) = acc[c][i];
    __syncthreads();
#pragma unroll
    for (int c = 0; c < 3; ++c) conv_finish(ch0 + c, smem + c * C_CH, smem + C_RED + c * 32768, bf1(in[c].dv), wsr);
}

constexpr int R_MOD = 147456;
constexpr int R_YT = 110592;
constexpr int R_HT = R_YT + 8 * 1168;
struct RowsPre { float x[18], gh[18]; u32 xw[9]; };
__device__ __forceinline__ void rows_load(RowsPre& r, int t0, const u16* __restrict__ q) {
    const int tid = threadIdx.x, lane = tid & 63, hl = lane & 31, row = 2 * (tid >> 6) + (lane >> 5);
    const u32* qrow = (const u32*)(q + (size_t)(t0 + row) * D_);
#pragma unroll
    for (int i = 0; i < 9; ++i) r.xw[i] = qrow[hl + 32 * i];
}
struct ModIn { u32x4 mw[4]; };
__device__ __forceinline__ void mod_load(ModIn& m, rsrc_t wsr) {
#pragma unroll
    for (int i = 0; i < 4; ++i) { const int j = threadIdx.x + 256 * i; m.mw[i] = ld16_dev(wsr, (u32)WS_MOD + (j < 864 ? j : 0) * 16); }
}
__device__ __forceinline__ void mod_put(const ModIn& m, char* sm) {
#pragma unroll
    for (int i = 0; i < 4; ++i) { const int j = threadIdx.x + 256 * i; if (j < 864) *(u32x4*)(sm + R_MOD + j * 16) = m.mw[i]; }
}
__device__ __forceinline__ void rows_pre(RowsPre& r, const char* sm) {
    const int hl = threadIdx.x & 31;
#pragma unroll
    for (int i = 0; i < 9; ++i) { r.x[2 * i] = bf_lo(r.xw[i]); r.x[2 * i + 1] = bf_hi(r.xw[i]); }
    float s = 0.f;
#pragma unroll
    for (int i = 0; i < 18; ++i) s += r.x[i];
    const float mean = half_sum(s) * (1.0f / 576.0f);
    float vs = 0.f;
#pragma unroll
    for (int i = 0; i < 18; ++i) { const float dl = r.x[i] - mean; vs += dl * dl; }
    const float rstd = __builtin_amdgcn_rsqf(half_sum(vs) * (1.0f / 576.0f) + 1e-5f);
    const float* mod = (const float*)(sm + R_MOD);
#pragma unroll
    for (int i = 0; i < 9; ++i) {
        const int c = 2 * (hl + 32 * i);
        const f32x2 sa = *(const f32x2*)(mod + c), c1 = *(const f32x2*)(mod + 576 + c), ga = *(const f32x2*)(mod + 2 * 576 + c);
        r.gh[2 * i] = ga[0] * ((r.x[2 * i] - mean) * rstd * c1[0] + sa[0]);
        r.gh[2 * i + 1] = ga[1] * ((r.x[2 * i + 1] - mean) * rstd * c1[1] + sa[1]);
    }
}
__device__ __forceinline__ void rows_fetch(int t0, char* sm, const char* ws) {
    const int lane = threadIdx.x & 63;
    const char* src = ws + WS_YYT + t0 * 2;
    const u32 dst = (u32)(uintptr_t)(sm + R_HT);
#pragma unroll
    for (int i = 0; i < 9; ++i)
        asm volatile("s_mov_b32 m0, %2\n\ts_nop 0\n\tglobal_load_lds_dwordx4 %0, %1 sc1" :: "v"((u32)((lane + 64 * i) * (S_ * 2))), "s"(src), "s"(dst + i * 1024) : "memory");
    asm volatile("s_waitcnt vmcnt(0)" ::: "memory");
}
__device__ __forceinline__ void rows_main(const RowsPre& r, int t0, char* sm, rsrc_t wsr) {
    const int tid = threadIdx.x, lane = tid & 63, hl = lane & 31, row = 2 * (tid >> 6) + (lane >> 5);
    const int cg = tid >> 1, th = tid & 1;
    if (tid < 144) {
        u32x2 yw[8];
#pragma unroll
        for (int c = 0; c < 8; ++c) yw[c] = *(const u32x2*)(sm + R_HT + (8 * cg + c) * 16 + th * 8);
#pragma unroll
        for (int t = 0; t < 4; ++t) {
            u32x4 o;
#pragma unroll
            for (int kk = 0; kk < 4; ++kk) {
                const u32 a = yw[2 * kk][t >> 1], bq = yw[2 * kk + 1][t >> 1];
                o[kk] = (t & 1) ? __builtin_amdgcn_perm(bq, a, 0x07060302u) : __builtin_amdgcn_perm(bq, a, 0x05040100u);
            }
            *(u32x4*)(sm + R_YT + (4 * th + t) * 1168 + cg * 16) = o;
        }
    }
    __syncthreads();
    const float* mod = (const float*)(sm + R_MOD);
    float x1[18];
#pragma unroll
    for (int i = 0; i < 9; ++i) {
        u32* yp = (u32*)(sm + R_YT + row * 1168 + (hl + 32 * i) * 4);
        const u32 yv = *yp;
        const u32 p = pk2(r.x[2 * i] + r.gh[2 * i] * bf_lo(yv), r.x[2 * i + 1] + r.gh[2 * i + 1] * bf_hi(yv));
        *yp = p;
        x1[2 * i] = bf_lo(p); x1[2 * i + 1] = bf_hi(p);
    }
    float s = 0.f;
#pragma unroll
    for (int i = 0; i < 18; ++i) s += x1[i];
    const float mean = half_sum(s) * (1.0f / 576.0f);
    float vs = 0.f;
#pragma unroll
    for (int i = 0; i < 18; ++i) { const float dl = x1[i] - mean; vs += dl * dl; }
    const float rstd = __builtin_amdgcn_rsqf(half_sum(vs) * (1.0f / 576.0f) + 1e-5f);
#pragma unroll
    for (int i = 0; i < 9; ++i) {
        const int c = 2 * (hl + 32 * i);
        const f32x2 sb = *(const f32x2*)(mod + 3 * 576 + c), c1 = *(const f32x2*)(mod + 4 * 576 + c);
        const float h0 = (x1[2 * i] - mean) * rstd * c1[0] + sb[0], h1 = (x1[2 * i + 1] - mean) * rstd * c1[1] + sb[1];
        const int ks = c >> 5, kk = c & 31, gq = (kk & 15) >> 2, j = ((kk >> 4) << 2) | (kk & 3);
        *(u32*)(sm + R_HT + ((ks * 4 + gq) * 8 + row) * 16 + j * 2) = pk2(h0, h1);
    }
    __syncthreads();
    const u32 h2t = (u32)WS_H2T + (u32)(t0 >> 4) * 18 * 1024 + (t0 & 15) * 16, x1g = (u32)WS_X1 + (u32)t0 * D_ * 2;
#pragma unroll
    for (int it = 0; it < 3; ++it) {
        const int i = tid + 256 * it;
        if (i < 576) {
            const int kg = i >> 3, rr8 = i & 7;
            st16_dev(wsr, h2t + (kg >> 2) * 1024 + (kg & 3) * 256 + rr8 * 16, *(const u32x4*)(sm + R_HT + i * 16));
            const int rr = i / 72, cc = i - rr * 72;
            st16_dev(wsr, x1g + rr * 1152 + cc * 16, *(const u32x4*)(sm + R_YT + rr * 1168 + cc * 16));
        }
    }
}

namespace mlp {
constexpr int STG = 36864, LDS_BYTES = 163840;
constexpr int G1S = 9 * 144, G2S = 18 * 36, NS = G1S + G2S;
constexpr int LA = 4;
constexpr int PRO = 9 + 4 * LA + 18;
constexpr int TUS = 147456;

constexpr int F01 = 288;
constexpr int stage_of(int gs) { return gs < F01 ? 0 : gs < G1S ? gs / 144 : 9 + (gs - G1S) / 72; }
constexpr int rel_of(int gs) { return gs < F01 ? gs : gs < G1S ? gs % 144 : (gs - G1S) % 72; }
constexpr int grp_of(int gs) { return rel_of(gs) >> 2; }
constexpr int t_of(int gs) { return rel_of(gs) & 3; }
constexpr bool barpt(int gs) { return gs >= F01 && gs < NS && t_of(gs) == 3 && grp_of(gs) == (gs < G1S ? 3 : 0) && stage_of(gs) < 17; }
constexpr int dma_id(int gs) {
    if (gs >= NS || t_of(gs) != 3) return -1;
    const int g = grp_of(gs), s = stage_of(gs);
    if (gs < F01) return (g >= 36 && g < 72 && (g & 3) == 3) ? 3 * 16 + (g - 36) / 4 : -1;
    if (s == 2) return (g >= 4 && g < 22) ? ((g & 1) ? 5 : 4) * 16 + (g - 4) / 2 : -1;
    if (s + 3 > 17) return -1;
    if (s == 8) return (g >= 4 && g < 13) ? 11 * 16 + g - 4 : -1;
    if (gs < G1S) { constexpr int G[9] = {5, 7, 9, 12, 14, 16, 20, 22, 24}; for (int i = 0; i < 9; ++i) if (G[i] == g) return (s + 3) * 16 + i; return -1; }
    return (g & 1) ? (s + 3) * 16 + (g >> 1) : -1;
}
constexpr int bload(int gs) {
    if (gs >= F01 || t_of(gs) != 1) return -1;
    const int ks = 2 * (gs / 32) + LA + (((gs % 32) >> 2) >> 2), i = (gs >> 2) & 3;
    return ks < 18 ? ks * 4 + i : -1;
}
constexpr bool unit_stores(int v) { return v == 1 || v == 3 || v == 4 || v == 6 || v == 8; }
constexpr int unit_end(int v) { return v < 2 ? F01 : 144 * (v + 1); }
constexpr int hload(int gs) {
    if (t_of(gs) != 3) return -1;
    const int s = stage_of(gs), g = grp_of(gs);
    if (s == 8) return (g >= 13 && g < 33) ? g - 13 : -1;
    if (s < 9 || g < 2 || g > 16 || (g & 1)) return -1;
    const int i = (g - 2) >> 1;
    if (s == 9) return 20 + i;
    if (s == 10) return 28 + i;
    if (s == 11) return i < 4 ? 36 + i : -1;
    if (s >= 12 && s <= 15) return 40 + 8 * (s - 12) + i;
    return -1;
}
constexpr int pub(int gs) { return (stage_of(gs) == 6 && grp_of(gs) == 27 && t_of(gs) == 3) ? 1 : (stage_of(gs) == 10 && grp_of(gs) == 9 && t_of(gs) == 1) ? 2 : 0; }
constexpr int poll_issue(int gs) { return (stage_of(gs) == 7 && grp_of(gs) == 27 && t_of(gs) == 3) ? 1 : (stage_of(gs) == 11 && grp_of(gs) == 9 && t_of(gs) == 1) ? 2 : 0; }
struct EpiSched { signed char ev[G1S], ep[G1S], sv[G1S], st[G1S]; };
constexpr EpiSched make_epi() {
    EpiSched e{};
    int v = 0, p = 0, pend_v = -1, pend_lo = 0, pend_hi = 0;
    for (int gs = 0; gs < G1S; ++gs) {
        e.ev[gs] = -1; e.ep[gs] = -1; e.sv[gs] = -1; e.st[gs] = -1;
        if (gs < F01) continue;
        const int t = t_of(gs);
        if (t == 0) continue;
        const bool vm = barpt(gs) || dma_id(gs) >= 0 || hload(gs) >= 0 || pub(gs) || poll_issue(gs);
        if (t == 3 && !vm && pend_lo < pend_hi) { e.sv[gs] = (signed char)pend_v; e.st[gs] = (signed char)pend_lo; ++pend_lo; continue; }
        if (t == 3 && vm) continue;
        if (v < 8 && gs >= unit_end(v) + 8 && (pend_lo == pend_hi || pend_v == v || !unit_stores(v) || true)) {
            e.ev[gs] = (signed char)v; e.ep[gs] = (signed char)p;
            ++p;
            if ((p & 15) == 0 && unit_stores(v)) { if (pend_v != v) { pend_v = v; pend_lo = p / 16 - 1; } pend_hi = p / 16; }
            if (p == 64) { p = 0; ++v; }
        }
    }
    return e;
}
constexpr EpiSched ES = make_epi();
constexpr int hstore(int gs) { return gs < G1S ? ES.st[gs] : -1; }
constexpr int epi_last(int v) { for (int gs = G1S - 1; gs >= 0; --gs) if (ES.ev[gs] == v) return gs; return -1; }
constexpr int store_last(int v) { for (int gs = G1S - 1; gs >= 0; --gs) if (ES.sv[gs] == v && ES.st[gs] == 3) return gs; return -1; }
constexpr bool epi_ok() {
    for (int v = 0; v < 8; ++v) {
        if (epi_last(v) < 0 || ES.ep[epi_last(v)] != 63) return false;
        if (v + 3 <= 8 && epi_last(v) >= 144 * (v + 3)) return false;
        if (unit_stores(v) && store_last(v) < 0) return false;
    }
    return store_last(4) + 40 < 6 * 144 + 27 * 4;
}
static_assert(epi_ok(), "epilogue schedule");
constexpr int vm_ops(int gs) { return (pub(gs) ? 1 : 0) + (poll_issue(gs) ? 1 : 0) + (hstore(gs) >= 0) + (bload(gs) >= 0) + (hload(gs) >= 0) + (dma_id(gs) >= 0); }
struct Ledger { int cum[NS + 1]; };
constexpr Ledger make_ledger() {
    Ledger l{}; int n = PRO;
    for (int gs = 0; gs < NS; ++gs) { if (gs == G1S) n += 4; l.cum[gs] = n; n += vm_ops(gs); }
    l.cum[NS] = n; return l;
}
constexpr Ledger LG = make_ledger();
constexpr int seq_in(int gs, int kind) {
    int n = LG.cum[gs];
    if (kind > 0 && pub(gs)) ++n;
    if (kind > 1 && poll_issue(gs)) ++n;
    if (kind > 2 && hstore(gs) >= 0) ++n;
    if (kind > 3 && bload(gs) >= 0) ++n;
    if (kind > 4 && hload(gs) >= 0) ++n;
    return n;
}
constexpr int seq_dma_last(int s) {
    if (s <= 2) return 9 * s + 8;
    for (int gs = 0; gs < NS; ++gs) if (dma_id(gs) == s * 16 + 8) return seq_in(gs, 5);
    return -1;
}
constexpr int seq_b(int ks) {
    if (ks < LA) return 27 + 4 * ks + 3;
    for (int gs = 0; gs < F01; ++gs) if (bload(gs) == ks * 4 + 3) return seq_in(gs, 3);
    return -1;
}
constexpr int seq_h(int kk) {
    for (int gs = 8 * 144; gs < NS; ++gs) if (hload(gs) == kk * 4 + 3) return seq_in(gs, 4);
    return -1;
}
constexpr int seq_poll(int part) {
    for (int gs = 0; gs < NS; ++gs) if (poll_issue(gs) == part) return seq_in(gs, 1);
    return -1;
}
constexpr int seq_store_last(int part) {
    if (part == 2) return LG.cum[G1S] - 1;
    return seq_in(store_last(4), 2);
}
struct Work { int unit, mat, k, t, n; };
constexpr Work work_of(int gs) {
    if (gs < F01) { const int p = gs / 32, um = (gs % 32) / 8, r = gs % 8; return {um >> 1, um & 1, 2 * p + (r & 1), r >> 1, 8 * p + 2 * um + (r & 1)}; }
    if (gs < G1S) { const int u = gs / 144, bi = (gs % 144) / 24, r = gs % 24; return {u, bi & 1, 6 * (bi >> 1) + r % 6, r / 6, 36 * u + 6 * bi + r % 6}; }
    const int s = (gs - G1S) / 72, r = (gs - G1S) % 72, du = r / 8, r8 = r % 8;
    return {du, 0, 2 * s + (r8 & 1), r8 >> 1, 324 + 18 * s + 2 * du + (r8 & 1)};
}
constexpr int NFRAG = 486, RQ = 12, FR0 = 8;
constexpr int frag_first(int n) {
    if (n < 72) return 32 * (n / 8) + 8 * ((n % 8) >> 1) + (n & 1);
    if (n < 324) return 144 * (n / 36) + 24 * ((n % 36) / 6) + n % 6;
    return G1S + 72 * ((n - 324) / 18) + 8 * (((n - 324) % 18) >> 1) + (n & 1);
}
constexpr int frag_last(int n) { return frag_first(n) + ((n >= 72 && n < 324) ? 18 : 6); }
constexpr int frag_stage(int n) { return n < 72 ? (n % 8) >> 2 : n < 324 ? n / 36 : 9 + (n - 324) / 18; }
constexpr int frag_off(int n) {
    if (n < 72) return (2 * (2 * (n / 8) + (n & 1)) + (((n % 8) >> 1) & 1)) * 1024;
    if (n < 324) { const int bi = (n % 36) / 6; return (2 * (6 * (bi >> 1) + n % 6) + (bi & 1)) * 1024; }
    return (n & 1) * 9216 + (((n - 324) % 18) >> 1) * 32;
}
constexpr int stage_ready(int s) {
    if (s <= 2) return 0;
    for (int gs = 0; gs < NS; ++gs) if (barpt(gs) && stage_of(gs) == s - 1) return gs + 1;
    return NS;
}
struct PfSched { short id[NS]; };
constexpr PfSched make_pf() {
    PfSched t{};
    int m = FR0, rdy = stage_ready(frag_stage(FR0));
    for (int gs = 0; gs < NS; ++gs) {
        t.id[gs] = -1;
        if ((gs & 3) != 0 || m >= NFRAG) continue;
        if (m >= RQ && frag_last(m - RQ) >= gs) continue;
        if (rdy > gs) continue;
        t.id[gs] = (short)m;
        ++m;
        if (m < NFRAG && frag_stage(m) != frag_stage(m - 1)) rdy = stage_ready(frag_stage(m));
    }
    return t;
}
constexpr PfSched PF = make_pf();
constexpr bool pf_ok() {
    int cnt = FR0;
    for (int gs = 0; gs < NS; ++gs) if (PF.id[gs] >= 0) { if (PF.id[gs] != cnt || frag_first(cnt) - gs < 8) return false; ++cnt; }
    return cnt == NFRAG;
}
static_assert(pf_ok(), "fragment prefetch schedule");
constexpr int clampv(int n) { return n > 63 ? 63 : (n < 0 ? 0 : n); }
constexpr int wait_for(int gs, int seq) { return clampv(LG.cum[gs] - seq - 1); }

template <int N> __device__ __forceinline__ void wait_vm() { asm volatile("s_waitcnt vmcnt(%0)" :: "n"(N) : "memory"); }

struct Ctx {
    bf16x8 Bv[2][4];
    bf16x8 Aq[12];
    f32x4 ac[3][2][4];
    f32x4 a2[9][4];
    float e0, e1;
    u32x4 hp[4];
    u32 vo1[4], vo2[4], vo3[4];
    u32 lane16, epoch;
    u32x4 pollv;
    const LAS char* rd[4]; const LAS char* rd2[4];
    const char* w1src; const char* w2src; const char* bsrc; const char* hsrc;
    u32 voff2[9];
    int sub;
    u32 dmadst;
    u32 hso;
    int jj;
    __amdgpu_buffer_rsrc_t hrs;
    u32* flag_pub; u32* flag_poll;
};

template <int KS, int T, bool ZERO> __device__ __forceinline__ void mfma(Ctx& cx, f32x4& c, const bf16x8& a) {
    constexpr int r = (KS * 4 + T) * 4;
    if constexpr (KS < 16) {
        if constexpr (ZERO) asm volatile("v_mfma_f32_16x16x32_bf16 %0, %1, a[%c2:%c3], 0" : "=v"(c) : "v"(a), "n"(r), "n"(r + 3));
        else asm volatile("v_mfma_f32_16x16x32_bf16 %0, %1, a[%c2:%c3], %0" :: "v"(c), "v"(a), "n"(r), "n"(r + 3));
    } else {
        asm volatile("v_mfma_f32_16x16x32_bf16 %0, %1, %2, %0" :: "v"(c), "v"(a), "v"(cx.Bv[KS - 16][T]));
    }
}
template <int KS, int T, bool SC1> __device__ __forceinline__ void gload(Ctx& cx, u32 voff, const char* sb) {
    constexpr int r = (KS * 4 + T) * 4, off = (KS & 3) * 1024;
    if constexpr (KS < 16) {
        if constexpr (SC1) asm volatile("global_load_dwordx4 a[%c2:%c3], %0, %1 offset:%4 sc1" :: "v"(voff), "s"(sb), "n"(r), "n"(r + 3), "n"(off) : "memory");
        else asm volatile("global_load_dwordx4 a[%c2:%c3], %0, %1 offset:%4" :: "v"(voff), "s"(sb), "n"(r), "n"(r + 3), "n"(off) : "memory");
    } else {
        if constexpr (SC1) asm volatile("global_load_dwordx4 %0, %1, %2 offset:%3 sc1" : "=v"(cx.Bv[KS - 16][T]) : "v"(voff), "s"(sb), "n"(off) : "memory");
        else asm volatile("global_load_dwordx4 %0, %1, %2 offset:%3" : "=v"(cx.Bv[KS - 16][T]) : "v"(voff), "s"(sb), "n"(off) : "memory");
    }
}
template <int S, int P> __device__ __forceinline__ void dma_m0(const Ctx& c) {
    asm volatile("s_mov_b32 m0, %0" :: "s"(c.dmadst + (u32)((S & 3) * STG + (P >> 2) * 4096)) : "memory");
}
constexpr int nat_kstep(int ks2) {
    if (ks2 < 10) { constexpr int T[5] = {0, 1, 5, 6, 4}; return 9 * (ks2 / 5) + T[ks2 % 5]; }
    const int jj = (ks2 - 10) >> 1, h = (ks2 - 10) & 1;
    return 9 * (jj >> 1) + ((jj & 1) ? 7 : 2) + h;
}
template <int S, int P> __device__ __forceinline__ void dma_go(const Ctx& c) {
    if constexpr (S < 9) {
        const char* sb = c.w1src + (size_t)S * STG + (P >> 2) * 4096;
        asm volatile("global_load_lds_dwordx4 %0, %1 offset:%2 sc1" :: "v"(c.lane16), "s"(sb), "n"((P & 3) * 1024) : "memory");
    } else {
        constexpr int n0 = nat_kstep(2 * (S - 9)), n1 = nat_kstep(2 * (S - 9) + 1);
        const char* sb = c.w2src + (size_t)(c.sub ? n1 : n0) * (32 * D_ * 2);
        asm volatile("global_load_lds_dwordx4 %0, %1 offset:%2" :: "v"(c.voff2[P]), "s"(sb), "n"((P & 3) * 1024) : "memory");
    }
}
template <int ID> __device__ __forceinline__ void load_b(Ctx& c) {
    constexpr int ks = ID >> 2, t = ID & 3;
    gload<ks, t, true>(c, c.vo1[t], c.bsrc + (ks >> 2) * 4096);
}
template <int ID> __device__ __forceinline__ void load_h(Ctx& c) {
    constexpr int kk = ID >> 2, t = ID & 3;
    gload<kk, t, true>(c, c.vo3[t], c.hsrc + (kk >> 2) * 4096);
}
template <int KS> __device__ __forceinline__ void pin_b(Ctx& c) {
#pragma unroll
    for (int t = 0; t < 4; ++t) { if constexpr (KS >= 16) asm volatile("" : "+v"(c.Bv[KS - 16][t])); }
}
template <int V, int P> __device__ __forceinline__ void epi_piece(Ctx& c) {
    constexpr int pr = P >> 3, st = P & 7, t = pr >> 1, r = 2 * (pr & 1), par = V % 3;
    if constexpr (st == 0) {
        asm volatile("v_mul_f32 %0, 0xbfb8aa3b, %1" : "=v"(c.e0) : "v"(c.ac[par][0][t][r]));
        asm volatile("v_mul_f32 %0, 0xbfb8aa3b, %1" : "=v"(c.e1) : "v"(c.ac[par][0][t][r + 1]));
    } else if constexpr (st == 1) asm volatile("v_exp_f32 %0, %0" :: "v"(c.e0));
    else if constexpr (st == 2) asm volatile("v_exp_f32 %0, %0" :: "v"(c.e1));
    else if constexpr (st == 3) {
        asm volatile("v_add_f32 %0, 1.0, %0" :: "v"(c.e0));
        asm volatile("v_add_f32 %0, 1.0, %0" :: "v"(c.e1));
    } else if constexpr (st == 4) asm volatile("v_rcp_f32 %0, %0" :: "v"(c.e0));
    else if constexpr (st == 5) asm volatile("v_rcp_f32 %0, %0" :: "v"(c.e1));
    else if constexpr (st == 6) {
        asm volatile("v_mul_f32 %0, %0, %1" :: "v"(c.e0), "v"(c.ac[par][1][t][r]));
        asm volatile("v_mul_f32 %0, %0, %1" :: "v"(c.e1), "v"(c.ac[par][1][t][r + 1]));
    } else {
        constexpr bool first = (V == 0 || V == 2 || V == 5 || V == 7);
        u32 o;
        asm volatile("v_cvt_pk_bf16_f32 %0, %1, %2" : "=v"(o) : "v"(c.e0), "v"(c.e1));
        c.hp[t][(first ? 0 : 2) + (pr & 1)] = o;
    }
}
template <int V, int T> __device__ __forceinline__ void store_h(Ctx& c) {
    const int m5 = 5 * (c.jj >> 1), ab = c.jj & 1;
    if constexpr (V == 4) {
        const u32x2 d = {c.hp[T][2], c.hp[T][3]};
        __builtin_amdgcn_raw_buffer_store_b64(d, c.hrs, c.vo2[T] + 8 * ab, c.hso + (m5 + 4) * 1024, 16);
    } else {
        const int kstep = (V == 1) ? m5 + 2 * ab : (V == 3) ? m5 + 2 * ab + 1 : (V == 6) ? 10 + 2 * c.jj : 11 + 2 * c.jj;
        __builtin_amdgcn_raw_buffer_store_b128(c.hp[T], c.hrs, c.vo2[T], c.hso + kstep * 1024, 16);
    }
}
template <int PART> __device__ __forceinline__ void poll_check(Ctx& c) {
    asm volatile("" : "+v"(c.pollv));
    const u32 e = c.epoch;
    if (!__all(c.pollv[0] == e && c.pollv[1] == e && c.pollv[2] == e && c.pollv[3] == e)) {
        const u32* p = c.flag_poll + (PART - 1) * 1024 + ((c.lane16 >> 4) & 3);
        poll_eq(p, e);
    }
}
template <int V, int P> __device__ __forceinline__ void epi_piece_padded(Ctx& c) {
    epi_piece<V, P>(c);
    if constexpr ((P & 7) == 2 || (P & 7) == 5) asm volatile("s_nop 0");
}
template <int V, int... P> __device__ __forceinline__ void epi_all(Ctx& c, std::integer_sequence<int, P...>) { (epi_piece_padded<V, P>(c), ...); }

template <int GS> __device__ __forceinline__ void slot(Ctx& c) {
    constexpr bool g1 = GS < G1S;
    constexpr int S = stage_of(GS), R = rel_of(GS), grp = R >> 2, t = R & 3;
    constexpr Work W = work_of(GS);
    constexpr bool f01 = GS < F01;
    if constexpr (GS == G1S) {
        asm volatile("s_nop 7" ::: "memory");
        epi_all<8>(c, std::make_integer_sequence<int, 64>{});
        store_h<8, 0>(c); store_h<8, 1>(c); store_h<8, 2>(c); store_h<8, 3>(c);
        SCHED();
    }
    if constexpr (f01 && GS % 32 == 0) { wait_vm<wait_for(GS, seq_b(2 * (GS / 32) + 1))>(); pin_b<2 * (GS / 32)>(c); pin_b<2 * (GS / 32) + 1>(c); }
    if constexpr (GS == 8 * 144) { wait_vm<wait_for(GS, seq_poll(1))>(); poll_check<1>(c); }
    if constexpr (!g1 && R == 0) { wait_vm<wait_for(GS, seq_h(2 * (S - 9) + 1))>(); pin_b<2 * (S - 9)>(c); pin_b<2 * (S - 9) + 1>(c); }
    if constexpr (dma_id(GS) >= 0) dma_m0<dma_id(GS) / 16, dma_id(GS) % 16>(c);
    if constexpr (g1) mfma<W.k, W.t, (W.k == 0)>(c, c.ac[W.unit % 3][W.mat][W.t], c.Aq[W.n % RQ]);
    else mfma<W.k, W.t, (W.k == 0)>(c, c.a2[W.unit][W.t], c.Aq[W.n % RQ]);
    if constexpr (barpt(GS)) {
        constexpr int n1 = wait_for(GS, seq_dma_last(S + 1));
        constexpr int n2 = (S == 12) ? wait_for(GS, seq_poll(2)) : 63;
        wait_vm<(n1 < n2 ? n1 : n2)>();
        if constexpr (S == 12) poll_check<2>(c);
        __builtin_amdgcn_s_barrier();
        asm volatile("" ::: "memory");
    }
    if constexpr (pub(GS)) {
        wait_vm<wait_for(GS, seq_store_last(pub(GS)))>();
        if (c.lane16 == 0) flag_set(c.flag_pub + (pub(GS) - 1) * 1024, c.epoch);
    }
    if constexpr (poll_issue(GS)) asm volatile("global_load_dwordx4 %0, %1, %2 sc1" : "=v"(c.pollv) : "v"(0u), "s"(c.flag_poll + (poll_issue(GS) - 1) * 1024) : "memory");
    {
        constexpr int f = PF.id[GS], fh = (GS & 3) == 1 ? PF.id[GS - ((GS & 3) == 1 ? 1 : 0)] : -1;
        if constexpr (f >= 0 && f < 324) c.Aq[f % RQ] = *(const LAS bf16x8*)(c.rd[frag_stage(f) & 3] + frag_off(f));
        constexpr int ft = f >= 324 ? f : fh >= 324 ? fh : -1;
        if constexpr (ft >= 0) {
            constexpr int half = f >= 324 ? 0 : 1;
            const s16x4 h = __builtin_amdgcn_ds_read_tr16_b64_v4i16((LAS s16x4*)(c.rd2[frag_stage(ft) & 3] + frag_off(ft) + half * (16 * 288)));
            const u32x2 hh = __builtin_bit_cast(u32x2, h);
            u32x4 a = __builtin_bit_cast(u32x4, c.Aq[ft % RQ]);
            a[2 * half] = hh[0]; a[2 * half + 1] = hh[1];
            c.Aq[ft % RQ] = __builtin_bit_cast(bf16x8, a);
        }
    }
    if constexpr (g1) {
        if constexpr (ES.ev[g1 ? GS : 0] >= 0) epi_piece<ES.ev[g1 ? GS : 0], ES.ep[g1 ? GS : 0]>(c);
        if constexpr (ES.sv[g1 ? GS : 0] >= 0) store_h<ES.sv[g1 ? GS : 0], ES.st[g1 ? GS : 0]>(c);
    }
    if constexpr (bload(GS) >= 0) load_b<bload(GS)>(c);
    if constexpr (hload(GS) >= 0) load_h<hload(GS)>(c);
    if constexpr (dma_id(GS) >= 0) dma_go<dma_id(GS) / 16, dma_id(GS) % 16>(c);
    SCHED();
}
template <int BASE, int... I> __device__ __forceinline__ void run_slots(Ctx& c, std::integer_sequence<int, I...>) { (slot<BASE + I>(c), ...); }
template <int S, int P> __device__ __forceinline__ void dma_one(const Ctx& c) { dma_m0<S, P>(c); asm volatile("s_nop 0"); dma_go<S, P>(c); }
template <int S, int... P> __device__ __forceinline__ void dma_stage(const Ctx& c, std::integer_sequence<int, P...>) { (dma_one<S, P>(c), ...); }
template <int... I> __device__ __forceinline__ void load_b_first(Ctx& c, std::integer_sequence<int, I...>) { (load_b<I>(c), ...); }
}

__device__ __forceinline__ void touch(u32& d, const char* p) { asm volatile("global_load_dword %0, %1, off" : "=v"(d) : "v"(p) : "memory"); }
__global__ __launch_bounds__(256, 1) void k_main(const u16* __restrict__ qin, const u16* __restrict__ kin, const u16* __restrict__ vin, const u16* __restrict__ shift_k,
                                                 const u16* __restrict__ ssm_k, const u16* __restrict__ ssm_D, const u16* __restrict__ wg, const u16* __restrict__ wu,
                                                 const u16* __restrict__ wd, const u16* __restrict__ cvec, const u16* __restrict__ w_mod,
                                                 char* __restrict__ ws, u16* __restrict__ out) {
    using namespace mlp;
    extern __shared__ __attribute__((aligned(16))) char smem[];
    const int tid = threadIdx.x, lane = tid & 63, w = __builtin_amdgcn_readfirstlane(tid >> 6);
    const int b = blockIdx.x, x = b & 7, idx = b >> 3, q = x >> 1;
    const int mt = 4 * (x & 1) + (idx & 3), fcl = idx >> 2, fc = 8 * q + fcl;
    const int mt2 = 8 * (x & 1) + (idx & 7), cc = idx >> 3, tg = w & 1, kh = w >> 1;
    const rsrc_t wsr = __builtin_amdgcn_make_buffer_rsrc(ws, 0, (int)WS_END, 0x00020000);
    STAMP_DECL; STAMP_AT(0);
    const u32 epoch_in = __hip_atomic_load(flag_ptr(ws, F_EPOCH), __ATOMIC_RELAXED, __HIP_MEMORY_SCOPE_AGENT);
    unsigned long long pc_start;
    asm volatile("s_getpc_b64 %0" : "=s"(pc_start));
    u32 epoch;
    {
        const int t0 = (x * 32 + idx) * 8;
        RowsPre rp;
        u32 bg[10] = {0, 0, 0, 0, 0, 0, 0, 0, 0, 0};
        if (b < 192) {
            const int c0 = 72 * x + 3 * idx;
            ConvIn in[3];
            KvIn kin_;
            if (w < 3) touch(bg[8], (const char*)(b < 96 ? kin : vin) + (size_t)((b % 96) * 192 + w * 64 + lane) * 128);
            kv_load(kin_, c0, kin, vin, shift_k);
            conv_load_k(in[0], c0, ssm_k, ssm_D); conv_load_k(in[1], c0 + 1, ssm_k, ssm_D); conv_load_k(in[2], c0 + 2, ssm_k, ssm_D);
            rows_load(rp, t0, qin);
            kv_put(kin_, smem + C_RED);
            conv_put_k(in[0], smem); conv_put_k(in[1], smem + C_CH); conv_put_k(in[2], smem + 2 * C_CH);
            __syncthreads();
            STAMP_AT(1);
            kv_compute(kin_, c0, smem + C_RED, smem + C_XR, C_CH);
            epoch = __builtin_amdgcn_readfirstlane(epoch_in) + 1u;
            if (w == 3) {
                DRAIN();
#pragma unroll
                for (int i = 0; i < 8; ++i) { const int l = lane + 64 * (i & 3); touch(bg[i], (const char*)(i < 4 ? wg : wu) + (size_t)b * 27648 + (l < 216 ? l : 0) * 128); }
            }
            STAMP_AT(2);
            ModIn mi;
            const u32* mf = flag_ptr(ws, F_MOD + (lane < 54 ? lane : 0));
            u32 pm;
            asm volatile("global_load_dword %0, %1, off sc1" : "=v"(pm) : "v"(mf) : "memory");
            conv_body(c0, in, smem, wsr, [&]() {
                DRAIN();
                asm volatile("" : "+v"(pm));
                if (!__all(pm == epoch)) poll_eq(mf, epoch);
                mod_load(mi, wsr); } STAMP_ARGS);
            STAMP_AT(5);
            mod_put(mi, smem);
            DRAIN();
#pragma unroll
            for (int i = 0; i < 10; ++i) asm volatile("" :: "v"(bg[i]));
            __syncthreads();
            if (tid < 8) flag_set(flag_ptr(ws, F_CONV + tid * 256 + b), epoch);
            if (w == 3) {
#pragma unroll
                for (int i = 0; i < 4; ++i) { const int l = lane + 64 * i; touch(bg[i], (const char*)wd + (size_t)b * 27648 + (l < 216 ? l : 0) * 128); }
            }
        } else {
            if (w == 3 && idx < 28) {
                const char* cb = (const char*)(pc_start & ~127ull) + lane * 128;
#pragma unroll
                for (int i = 0; i < 8; ++i) touch(bg[i], cb + (i < 7 || lane < 32 ? i * 8192 : 0));
            }
            if (w == 3 && idx >= 28) {
#pragma unroll
                for (int i = 0; i < 3; ++i) { const size_t o = (size_t)(((idx - 28) * 3 + i) * 64 + lane) << 16; touch(bg[i], ws + (o < WS_END ? o : 0)); }
                touch(bg[3], (const char*)out + (size_t)(lane < 36 ? lane : 0) * 65536);
                touch(bg[4], (const char*)wd + (size_t)lane * 65536 + (idx & 1 ? 0 : 64 * 65536 > (size_t)D_ * F_ * 2 - 65536 ? 0 : 0));
                touch(bg[5], (const char*)wg + (size_t)lane * 65536);
                touch(bg[6], (const char*)wu + (size_t)lane * 65536);
                touch(bg[7], (const char*)ssm_k + (size_t)(lane < 36 ? lane : 0) * 65536);
            }
            for (int i = 0; i < 40; ++i) __builtin_amdgcn_s_sleep(2);
            rows_load(rp, t0, qin);
            if (b - 192 < 54) mod_job(b - 192, cvec, w_mod, ws, (float*)(smem + R_MOD), (float*)(smem + R_MOD + 4096));
            epoch = __builtin_amdgcn_readfirstlane(epoch_in) + 1u;
            DRAIN();
            __syncthreads();
            if (tid == 0 && b - 192 < 54) flag_set(flag_ptr(ws, F_MOD + b - 192), epoch);
            STAMP_AT(1);
            tile_jobs(b - 192, wg, wu, smem, ws, wsr, epoch);
            STAMP_AT(2); STAMP_AT(3); STAMP_AT(4); STAMP_AT(5);
            poll_eq(flag_ptr(ws, F_MOD + (lane < 54 ? lane : 0)), epoch);
            ModIn mi;
            mod_load(mi, wsr);
            mod_put(mi, smem);
            __syncthreads();
        }
        rows_pre(rp, smem);
        STAMP_AT(6);
        if (w == 0) {
            poll_eq4(wsr, (u32)WS_CNT + (F_CONV + x * 256 + (lane < 48 ? lane : 0) * 4) * 4, epoch);
            rows_fetch(t0, smem, ws);
        }
        __syncthreads();
        STAMP_AT(7);
        rows_main(rp, t0, smem, wsr);
        DRAIN();
#pragma unroll
        for (int i = 0; i < 10; ++i) asm volatile("" :: "v"(bg[i]));
        __syncthreads();
        if (tid == 0) flag_set(flag_ptr(ws, F_ROWS + (t0 >> 6) * 8 + ((t0 >> 3) & 7)), epoch);
        STAMP_AT(8);
    }
    Ctx c;
#pragma unroll
    for (int t = 0; t < 4; ++t) c.hp[t] = (u32x4){0, 0, 0, 0};
    c.lane16 = lane * 16;
#pragma unroll
    for (int t = 0; t < 4; ++t) { c.vo1[t] = lane * 16 + t * 18432; c.vo2[t] = lane * 16 + t * TUS; c.vo3[t] = lane * 16 + ((t + 2 * kh) & 3) * TUS; }
    const LAS char* l0 = (const LAS char*)smem;
#pragma unroll
    for (int i = 0; i < 4; ++i) {
        c.rd[i] = l0 + i * STG + lane * 16; c.rd2[i] = l0 + i * STG + kh * 18432 + (4 * (lane >> 4) + ((lane & 15) >> 2)) * 288 + 8 * (lane & 3);
        asm volatile("" : "+v"(c.rd[i])); asm volatile("" : "+v"(c.rd2[i]));
    }
#pragma unroll
    for (int i = 0; i < 9; ++i) { const int ci = lane + 64 * i, row = (ci * 3641) >> 16, c16 = ci - row * 18; c.voff2[i] = (u32)(row * (D_ * 2) + c16 * 16 - (i & 3) * 1024); }
    c.sub = w & 1;
    c.w1src = ws + WS_WT1 + (size_t)fc * 331776 + w * 9216;
    c.w2src = (const char*)(wd + (size_t)(36 * q + 18 * kh) * 32 * D_ + cc * 144);
    c.bsrc = ws + WS_H2T + (size_t)(16 * mt + 4 * w) * 18432;
    c.hsrc = ws + WS_HT + (size_t)(8 * mt2 + 4 * tg) * TUS + (size_t)((q * 2 + kh) * 18) * 1024;
    c.dmadst = (u32)(uintptr_t)smem + w * 9216;
    c.jj = fcl & 3;
    c.hso = (u32)((16 * mt + 4 * w) * TUS + ((q * 2 + (fcl >> 2)) * 18) * 1024);
    c.hrs = __builtin_amdgcn_make_buffer_rsrc(ws + WS_HT, 0, (int)((size_t)S_ * F_ * 2), 0x00020000);
    c.epoch = epoch;
    c.flag_pub = flag_ptr(ws, F_H + ((((mt * 4 + w) * 4 + q) * 2 + (fcl >> 2))) * 4 + (fcl & 3));
    c.flag_poll = flag_ptr(ws, F_H + (((((mt2 >> 1) * 4 + 2 * (mt2 & 1) + tg) * 4 + q) * 2 + kh)) * 4);
    asm volatile("" ::: "a0", "a1", "a2", "a3", "a4", "a5", "a6", "a7", "a8", "a9", "a10", "a11", "a12", "a13", "a14", "a15", "a16", "a17", "a18", "a19", "a20", "a21", "a22", "a23", "a24", "a25", "a26", "a27", "a28", "a29", "a30", "a31", "a32", "a33", "a34", "a35", "a36", "a37", "a38", "a39", "a40", "a41", "a42", "a43", "a44", "a45", "a46", "a47", "a48", "a49", "a50", "a51", "a52", "a53", "a54", "a55", "a56", "a57", "a58", "a59", "a60", "a61", "a62", "a63", "a64", "a65", "a66", "a67", "a68", "a69", "a70", "a71", "a72", "a73", "a74", "a75", "a76", "a77", "a78", "a79", "a80", "a81", "a82", "a83", "a84", "a85", "a86", "a87", "a88", "a89", "a90", "a91", "a92", "a93", "a94", "a95", "a96", "a97", "a98", "a99", "a100", "a101", "a102", "a103", "a104", "a105", "a106", "a107", "a108", "a109", "a110", "a111", "a112", "a113", "a114", "a115", "a116", "a117", "a118", "a119", "a120", "a121", "a122", "a123", "a124", "a125", "a126", "a127", "a128", "a129", "a130", "a131", "a132", "a133", "a134", "a135", "a136", "a137", "a138", "a139", "a140", "a141", "a142", "a143", "a144", "a145", "a146", "a147", "a148", "a149", "a150", "a151", "a152", "a153", "a154", "a155", "a156", "a157", "a158", "a159", "a160", "a161", "a162", "a163", "a164", "a165", "a166", "a167", "a168", "a169", "a170", "a171", "a172", "a173", "a174", "a175", "a176", "a177", "a178", "a179", "a180", "a181", "a182", "a183", "a184", "a185", "a186", "a187", "a188", "a189", "a190", "a191", "a192", "a193", "a194", "a195", "a196", "a197", "a198", "a199", "a200", "a201", "a202", "a203", "a204", "a205", "a206", "a207", "a208", "a209", "a210", "a211", "a212", "a213", "a214", "a215", "a216", "a217", "a218", "a219", "a220", "a221", "a222", "a223", "a224", "a225", "a226", "a227", "a228", "a229", "a230", "a231", "a232", "a233", "a234", "a235", "a236", "a237", "a238", "a239", "a240", "a241", "a242", "a243", "a244", "a245", "a246", "a247", "a248", "a249", "a250", "a251", "a252", "a253", "a254", "a255");
    poll_eq(flag_ptr(ws, F_TILE + (fc >> 2) * 36 + (lane < 36 ? lane : 0)), epoch);
    dma_stage<0>(c, std::make_integer_sequence<int, 9>{});
    dma_stage<1>(c, std::make_integer_sequence<int, 9>{});
    dma_stage<2>(c, std::make_integer_sequence<int, 9>{});
    poll_eq(flag_ptr(ws, F_ROWS + (4 * mt + w) * 8 + (lane & 7)), epoch);
    STAMP_AT(9);
    load_b_first(c, std::make_integer_sequence<int, 4 * LA>{});
    wait_vm<4 * LA>();
    __builtin_amdgcn_s_barrier();
    asm volatile("" ::: "memory");
#pragma unroll
    for (int f = 0; f < FR0; ++f) c.Aq[f] = *(const LAS bf16x8*)(c.rd[frag_stage(f)] + frag_off(f));
    SCHED();
    STAMP_AT(10);
    run_slots<0>(c, std::make_integer_sequence<int, 288>{});
    STAMP_AT(11);
    run_slots<288>(c, std::make_integer_sequence<int, 144 * 3>{});
    run_slots<144 * 5>(c, std::make_integer_sequence<int, 144 * 4>{});
    STAMP_AT(12);
    run_slots<G1S>(c, std::make_integer_sequence<int, 324>{});
    run_slots<G1S + 324>(c, std::make_integer_sequence<int, 324>{});
    asm volatile("s_nop 7" ::: "memory");
#pragma unroll
    for (int u = 0; u < 9; ++u)
#pragma unroll
        for (int t = 0; t < 4; ++t) asm volatile("" : "+v"(c.a2[u][t]));
    SCHED();
    STAMP_AT(13);
    const int n = lane & 15, g = lane >> 4;
    u32x2 xr[3][2]; f32x4 gb[3];
#pragma unroll
    for (int i = 0; i < 3; ++i) {
        const int u = (w + 4 * i < 9) ? w + 4 * i : 8, d = 144 * cc + 16 * u + 4 * g;
#pragma unroll
        for (int t = 0; t < 2; ++t) xr[i][t] = __builtin_amdgcn_raw_buffer_load_b64(wsr, (u32)WS_X1 + (u32)((128 * mt2 + 32 * q + 16 * t + n) * D_ + d) * 2, 0, 16);
        gb[i] = *(const f32x4*)(smem + R_MOD + (5 * 576 + d) * 4);
    }
    {
        char* ex = smem + 73728 + w * 18432 + lane * 16;
#pragma unroll
        for (int t = 0; t < 2; ++t)
#pragma unroll
            for (int u = 0; u < 9; ++u) *(f32x4*)(ex + (t * 9 + u) * 1024) = c.a2[u][2 + t];
    }
    __syncthreads();
    const int j = 2 * tg + kh;
    u32* fred = flag_ptr(ws, F_RED + (mt2 * 4 + cc) * 16);
    const u32 slab = (u32)WS_SLAB + (u32)((mt2 * 4 + cc) * 16) * 9216;
    {
        const char* ey = smem + 73728 + (w ^ 2) * 18432 + lane * 16;
        char* own = smem + lane * 16;
#pragma unroll
        for (int u = 0; u < 9; ++u) {
            const f32x4 r0 = c.a2[u][0] + *(const f32x4*)(ey + u * 1024);
            const f32x4 r1 = c.a2[u][1] + *(const f32x4*)(ey + (9 + u) * 1024);
            if (j != q) st16_dev(wsr, slab + (j * 4 + q) * 9216 + u * 1024 + lane * 16, (u32x4){pk2(r0[0], r0[1]), pk2(r0[2], r0[3]), pk2(r1[0], r1[1]), pk2(r1[2], r1[3])});
            else { *(f32x4*)(own + u * 2048) = r0; *(f32x4*)(own + u * 2048 + 1024) = r1; }
        }
    }
    if (j != q) {
        DRAIN();
        if (lane == 0) flag_set(fred + j * 4 + q, epoch);
    }
    STAMP_AT(14);
    poll_eq(fred + q * 4 + (((lane & 3) == q) ? (q ^ 1) : (lane & 3)), epoch);
    __syncthreads();
    u32x4 rem[3][3];
#pragma unroll
    for (int i = 0; i < 3; ++i) {
        const int u = (w + 4 * i < 9) ? w + 4 * i : 8;
#pragma unroll
        for (int k = 0; k < 3; ++k) { const int qq = k + (k >= q ? 1 : 0); rem[i][k] = __builtin_amdgcn_raw_buffer_load_b128(wsr, slab + (q * 4 + qq) * 9216 + u * 1024 + lane * 16, 0, 16); }
    }
#pragma unroll
    for (int i = 0; i < 3; ++i) {
        const int u = w + 4 * i;
        if (u < 9) {
            const int d = 144 * cc + 16 * u + 4 * g;
#pragma unroll
            for (int t = 0; t < 2; ++t) {
                f32x4 s = *(const f32x4*)(smem + u * 2048 + t * 1024 + lane * 16);
#pragma unroll
                for (int k = 0; k < 3; ++k) {
                    const u32 a = rem[i][k][2 * t], bq = rem[i][k][2 * t + 1];
                    s += (f32x4){bf_lo(a), bf_hi(a), bf_lo(bq), bf_hi(bq)};
                }
                const float o0 = bf_lo(xr[i][t][0]) + rbf(gb[i][0] * rbf(s[0])), o1 = bf_hi(xr[i][t][0]) + rbf(gb[i][1] * rbf(s[1]));
                const float o2 = bf_lo(xr[i][t][1]) + rbf(gb[i][2] * rbf(s[2])), o3 = bf_hi(xr[i][t][1]) + rbf(gb[i][3] * rbf(s[3]));
                *(u32x2*)(out + (size_t)(128 * mt2 + 32 * q + 16 * t + n) * D_ + d) = (u32x2){pk2(o0, o1), pk2(o2, o3)};
            }
        }
    }
    if (b == 0 && tid == 0) flag_set(flag_ptr(ws, F_EPOCH), epoch);
    STAMP_AT(15);
    STAMP_OUT(4, 16);
}

extern "C" void kernel_launch(void* const* d_in, const int* in_sizes, int n_in, void* d_out, int out_size, void* d_ws, size_t ws_size,
                              hipStream_t stream) {
    const u16* q = (const u16*)d_in[0];
    const u16* k = (const u16*)d_in[1];
    const u16* v = (const u16*)d_in[2];
    const u16* shift_k = (const u16*)d_in[3];
    const u16* ssm_k = (const u16*)d_in[4];
    const u16* ssm_D = (const u16*)d_in[5];
    const u16* wg = (const u16*)d_in[7];
    const u16* wu = (const u16*)d_in[8];
    const u16* wd = (const u16*)d_in[9];
    const u16* c = (const u16*)d_in[10];
    const u16* w_mod = (const u16*)d_in[11];
    char* ws = (char*)d_ws;
    (void)hipFuncSetAttribute((const void*)k_main, hipFuncAttributeMaxDynamicSharedMemorySize, mlp::LDS_BYTES);
    k_main<<<256, 256, mlp::LDS_BYTES, stream>>>(q, k, v, shift_k, ssm_k, ssm_D, wg, wu, wd, c, w_mod, ws, (u16*)d_out);
}
```

```cpp
#include <hip/hip_runtime.h>
#include <stdint.h>
#include <utility>

typedef unsigned short u16;
typedef unsigned int u32;
typedef __bf16 bf16x8 __attribute__((ext_vector_type(8)));
typedef __bf16 bf16x2 __attribute__((ext_vector_type(2)));
typedef float f32x4 __attribute__((ext_vector_type(4)));
typedef float f32x2 __attribute__((ext_vector_type(2)));
typedef u32 u32x4 __attribute__((ext_vector_type(4)));
typedef u32 u32x2 __attribute__((ext_vector_type(2)));
typedef short s16x4 __attribute__((ext_vector_type(4)));
#define LAS __attribute__((address_space(3)))
typedef LAS char* lptr;

constexpr int S_ = 2048, D_ = 576, F_ = 4608;

constexpr size_t WS_MOD = 0;
constexpr size_t WS_CNT = 65536;
constexpr size_t WS_YYT = WS_CNT + 65536;
constexpr size_t WS_X1  = WS_YYT + (size_t)D_ * S_ * 2;
constexpr size_t WS_H2T = WS_X1 + (size_t)D_ * S_ * 2;
constexpr size_t WS_HT  = WS_H2T + (size_t)D_ * S_ * 2;
constexpr size_t WS_WT1 = WS_HT + (size_t)S_ * F_ * 2;
constexpr size_t WS_SLAB = WS_WT1 + (size_t)2 * D_ * F_ * 2;
constexpr size_t WS_END = WS_SLAB + (size_t)16 * 4 * 4 * 4 * 9216;

#define STAMP_DECL
#define STAMP_ARGS
#define STAMP_AT(i)
#define STAMP_OUT(kid, n)
__device__ __forceinline__ float bf_lo(u32 w) { return __uint_as_float(w << 16); }
__device__ __forceinline__ float bf_hi(u32 w) { return __uint_as_float(w & 0xffff0000u); }
__device__ __forceinline__ float bf1(u16 w) { return __uint_as_float((u32)w << 16); }
__device__ __forceinline__ u32 pk2(float lo, float hi) {
    f32x2 v = {lo, hi};
    return __builtin_bit_cast(u32, __builtin_convertvector(v, bf16x2));
}
__device__ __forceinline__ float rbf(float x) { return (float)(__bf16)x; }
__device__ __forceinline__ float sigmoidf_(float x) { return __builtin_amdgcn_rcpf(1.0f + __builtin_amdgcn_exp2f(-1.4426950408889634f * x)); }

constexpr int F_MOD = 256;
constexpr int F_TILE = 512;
constexpr int F_CONV = 1024;
constexpr int F_ROWS = 3072;
constexpr int F_H = 3328;
constexpr int F_RED = 5376;
constexpr int F_EPOCH = 6400;
typedef __amdgpu_buffer_rsrc_t rsrc_t;
__device__ __forceinline__ u32* flag_ptr(char* ws, int idx) { return (u32*)(ws + WS_CNT) + idx; }
__device__ __forceinline__ void flag_set(u32* p, u32 e) { __hip_atomic_store(p, e, __ATOMIC_RELAXED, __HIP_MEMORY_SCOPE_AGENT); }
__device__ __forceinline__ void st16_dev(rsrc_t rs, u32 off, u32x4 v) { __builtin_amdgcn_raw_buffer_store_b128(v, rs, off, 0, 16); }
__device__ __forceinline__ u32x4 ld16_dev(rsrc_t rs, u32 off) { return __builtin_amdgcn_raw_buffer_load_b128(rs, off, 0, 16); }
__device__ __forceinline__ void poll_eq(const u32* p, u32 e) {
    for (int it = 0; it < (1 << 21); ++it) {
        if (__all(__hip_atomic_load(p, __ATOMIC_RELAXED, __HIP_MEMORY_SCOPE_AGENT) == e)) break;
        __builtin_amdgcn_s_sleep(1);
    }
}
__device__ __forceinline__ void poll_eq4(rsrc_t rs, u32 off, u32 e) {
    for (int it = 0; it < (1 << 21); ++it) {
        asm volatile("" ::: "memory");
        const u32x4 v = ld16_dev(rs, off);
        if (__all(v[0] == e && v[1] == e && v[2] == e && v[3] == e)) break;
        __builtin_amdgcn_s_sleep(1);
    }
}
#define DRAIN() asm volatile("s_waitcnt vmcnt(0)" ::: "memory")

#define SCHED() __builtin_amdgcn_sched_barrier(0)
__device__ __forceinline__ float dpp_add(float v, const int ctrl_sel) {
    int r;
    const int iv = __builtin_bit_cast(int, v);
    switch (ctrl_sel) {
        case 0: r = __builtin_amdgcn_update_dpp(0, iv, 0xB1, 0xf, 0xf, true); break;
        case 1: r = __builtin_amdgcn_update_dpp(0, iv, 0x4E, 0xf, 0xf, true); break;
        case 2: r = __builtin_amdgcn_update_dpp(0, iv, 0x141, 0xf, 0xf, true); break;
        default: r = __builtin_amdgcn_update_dpp(0, iv, 0x140, 0xf, 0xf, true); break;
    }
    return v + __builtin_bit_cast(float, r);
}
__device__ __forceinline__ float half_sum(float v) {
    v = dpp_add(v, 0); v = dpp_add(v, 1); v = dpp_add(v, 2); v = dpp_add(v, 3);
    auto r = __builtin_amdgcn_permlane16_swap(__float_as_uint(v), __float_as_uint(v), false, false);
    return __uint_as_float(r[0]) + __uint_as_float(r[1]);
}

struct KvIn { u32x2 kw[8], vw[8]; u32x4 sw; u32x2 sw2; };
__device__ __forceinline__ void kv_load(KvIn& p, int c0, const u16* __restrict__ k, const u16* __restrict__ v, const u16* __restrict__ shift_k) {
    const int cw = c0 & ~1;
#pragma unroll
    for (int i = 0; i < 8; ++i) p.kw[i] = *(const u32x2*)(k + (size_t)(threadIdx.x + 256 * i) * D_ + cw);
#pragma unroll
    for (int i = 0; i < 8; ++i) p.vw[i] = *(const u32x2*)(v + (size_t)(threadIdx.x + 256 * i) * D_ + cw);
    p.sw = (u32x4){*(const u32*)(shift_k + c0 * 4), *(const u32*)(shift_k + c0 * 4 + 2), *(const u32*)(shift_k + c0 * 4 + 4), *(const u32*)(shift_k + c0 * 4 + 6)};
    p.sw2 = (u32x2){*(const u32*)(shift_k + c0 * 4 + 8), *(const u32*)(shift_k + c0 * 4 + 10)};
}
constexpr int KV_RAWV = 257 * 72 + 8;
__device__ __forceinline__ void kv_put(const KvIn& p, char* raw) {
    const int tid = threadIdx.x;
#pragma unroll
    for (int i = 0; i < 8; ++i) {
        const int t = tid + 256 * i, o = 72 * (t >> 3) + 8 * (t & 7);
        *(u32x2*)(raw + 72 + o) = p.kw[i];
        *(u32x2*)(raw + KV_RAWV + o) = p.vw[i];
    }
    if (tid < 8) *(u32x2*)(raw + 8 * tid) = (u32x2){0, 0};
}
__device__ __forceinline__ void kv_compute(const KvIn& p, int c0, const char* raw, char* xr, int stride) {
    const int tid = threadIdx.x;
    u32x2 kw[11], vw[8];
#pragma unroll
    for (int i = 0; i < 11; ++i) kw[i] = *(const u32x2*)(raw + 72 * tid + (i < 3 ? 40 + 8 * i : 72 + 8 * (i - 3)));
#pragma unroll
    for (int i = 0; i < 8; ++i) vw[i] = *(const u32x2*)(raw + KV_RAWV + 72 * tid + 8 * i);
    const bool odd = c0 & 1;
#pragma unroll
    for (int c = 0; c < 3; ++c) {
        const u32 wa = c == 0 ? p.sw[0] : c == 1 ? p.sw[2] : p.sw2[0], wb = c == 0 ? p.sw[1] : c == 1 ? p.sw[3] : p.sw2[1];
        const float w0 = bf_lo(wa), w1 = bf_hi(wa), w2 = bf_lo(wb), w3 = bf_hi(wb);
        float kf[11], r[8];
#define KV_PICK(d) (c == 0 ? (odd ? bf_hi((d)[0]) : bf_lo((d)[0])) : c == 1 ? (odd ? bf_lo((d)[1]) : bf_hi((d)[0])) : (odd ? bf_hi((d)[1]) : bf_lo((d)[1])))
#pragma unroll
        for (int i = 0; i < 11; ++i) kf[i] = KV_PICK(kw[i]);
#pragma unroll
        for (int i = 0; i < 8; ++i) r[i] = (kf[i] * w3 + kf[i + 1] * w2 + kf[i + 2] * w1 + kf[i + 3] * w0) * KV_PICK(vw[i]);
#undef KV_PICK
        *(u32x4*)(xr + c * stride + 2 * (2040 - 8 * tid)) = (u32x4){pk2(r[7], r[6]), pk2(r[5], r[4]), pk2(r[3], r[2]), pk2(r[1], r[0])};
    }
}

__device__ __forceinline__ void mod_job(int job, const u16* __restrict__ c, const u16* __restrict__ w_mod, char* __restrict__ ws, float* sc  , float* red  ) {
    const int tid = threadIdx.x, lane = tid & 63, wid = tid >> 6;
    const int col0 = job * 64, rg = tid >> 3, c8 = tid & 7;
    u32x4 w[18];
#pragma unroll
    for (int i = 0; i < 18; ++i) w[i] = *(const u32x4*)(w_mod + (size_t)(rg + 32 * i) * 3456 + col0 + c8 * 8);
#pragma unroll
    for (int j = 0; j < 3; ++j) { const int r = tid + 256 * j; if (r < 576) { const float x = bf1(c[r]); sc[r] = rbf(x * sigmoidf_(x)); } }
    __syncthreads();
    float acc[8] = {0, 0, 0, 0, 0, 0, 0, 0};
#pragma unroll
    for (int i = 0; i < 18; ++i) {
        const float s = sc[rg + 32 * i];
#pragma unroll
        for (int j = 0; j < 4; ++j) { acc[2 * j] += s * bf_lo(w[i][j]); acc[2 * j + 1] += s * bf_hi(w[i][j]); }
    }
#pragma unroll
    for (int j = 0; j < 8; ++j) {
        acc[j] += __shfl_xor(acc[j], 8); acc[j] += __shfl_xor(acc[j], 16); acc[j] += __shfl_xor(acc[j], 32);
    }
    if (lane < 8) {
#pragma unroll
        for (int j = 0; j < 8; ++j) red[wid * 64 + lane * 8 + j] = acc[j];
    }
    __syncthreads();
    if (tid < 64) {
        float m = rbf((red[tid] + red[64 + tid]) + (red[128 + tid] + red[192 + tid]));
        const int which = col0 / 576;
        if (which == 1 || which == 4) m = rbf(1.0f + m);
        __hip_atomic_store((u32*)(ws + WS_MOD) + col0 + tid, __float_as_uint(m), __ATOMIC_RELAXED, __HIP_MEMORY_SCOPE_AGENT);
    }
}

__device__ __forceinline__ const char* tile_base(int job, const u16* __restrict__ wg, const u16* __restrict__ wu) {
    const int ks = job % 18, mat = (job / 18) & 1, cg = job / 36;
    return (const char*)((mat ? wu : wg) + (size_t)(32 * ks) * F_ + cg * 576);
}
__device__ __forceinline__ void tile_dma(int job, const u16* __restrict__ wg, const u16* __restrict__ wu, char* tile, const u32 (&voff)[9]) {
    const int w = __builtin_amdgcn_readfirstlane(threadIdx.x >> 6);
    const char* base = tile_base(job, wg, wu) + (size_t)w * 8 * (F_ * 2);
    const u32 dst = (u32)(uintptr_t)tile + w * 9216;
#pragma unroll
    for (int i = 0; i < 9; ++i)
        asm volatile("s_mov_b32 m0, %2\n\ts_nop 0\n\tglobal_load_lds_dwordx4 %0, %1" :: "v"(voff[i]), "s"(base), "s"(dst + i * 1024) : "memory");
}
__device__ __forceinline__ void tile_out(int job, const char* tile, rsrc_t wsr) {
    const int lane = threadIdx.x & 63, w = threadIdx.x >> 6;
    const int ks = job % 18, mat = (job / 18) & 1, fc = 4 * (job / 36) + w;
    const int g = lane >> 4, q4 = (lane & 15) >> 2, p = lane & 3;
    const LAS char* rd = (const LAS char*)tile + (4 * g + q4) * 1152 + 8 * p + w * 288;
#pragma unroll
    for (int u = 0; u < 9; ++u) {
        const int pos = (fc & 1) ? (u == 0 ? 4 : u <= 4 ? u - 1 : u) : (u == 8 ? 4 : u < 4 ? u : u + 1);
        const s16x4 lo = __builtin_amdgcn_ds_read_tr16_b64_v4i16((LAS s16x4*)(rd + u * 32));
        const s16x4 hi = __builtin_amdgcn_ds_read_tr16_b64_v4i16((LAS s16x4*)(rd + u * 32 + 16 * 1152));
        typedef short s16x8 __attribute__((ext_vector_type(8)));
        const s16x8 r = __builtin_shufflevector(lo, hi, 0, 1, 2, 3, 4, 5, 6, 7);
        st16_dev(wsr, (u32)WS_WT1 + fc * 331776 + pos * 36864 + (ks * 2 + mat) * 1024 + lane * 16, __builtin_bit_cast(u32x4, r));
    }
}
__device__ __forceinline__ void tile_jobs(int i0, const u16* __restrict__ wg, const u16* __restrict__ wu, char* smem, char* ws, rsrc_t wsr, u32 epoch) {
    const int lane = threadIdx.x & 63;
    u32 voff[9];
#pragma unroll
    for (int i = 0; i < 9; ++i) { const int ci = lane + 64 * i, row = (ci * 58255) >> 22, c16 = ci - row * 72; voff[i] = (u32)(row * (F_ * 2) + c16 * 16); }
#pragma unroll
    for (int j = 0; j < 4; ++j) tile_dma(i0 + 64 * j, wg, wu, smem + j * 36864, voff);
#pragma unroll
    for (int j = 0; j < 4; ++j) {
        asm volatile("s_waitcnt vmcnt(27)" ::: "memory");
        __syncthreads();
        tile_out(i0 + 64 * j, smem + j * 36864, wsr);
    }
    if (i0 < 32) {
        __syncthreads();
        tile_dma(i0 + 256, wg, wu, smem, voff);
        asm volatile("s_waitcnt vmcnt(0)" ::: "memory");
        __syncthreads();
        tile_out(i0 + 256, smem, wsr);
    }
    DRAIN();
    __syncthreads();
    if (threadIdx.x < 5 && i0 + 64 * threadIdx.x < 288) flag_set(flag_ptr(ws, F_TILE + i0 + 64 * threadIdx.x), epoch);
}

constexpr int C_XR = 0;
constexpr int C_K0 = 4608;
constexpr int C_K1 = C_K0 + 4128 + 32;
constexpr int C_CH = C_K1 + 4160;
constexpr int C_RED = 3 * C_CH;
constexpr int C_LDS = C_RED + 3 * 32768;
static_assert(((C_K1 - C_K0) / 4) % 32 == 16, "bank offset");
static_assert(C_CH % 16 == 0 && C_K0 % 16 == 0 && C_K1 % 16 == 0, "align");
template <bool ZERO> __device__ __forceinline__ void mfma_v(f32x4& c, const bf16x8& a, const bf16x8& b) {
    if constexpr (ZERO) asm volatile("v_mfma_f32_16x16x32_bf16 %0, %1, %2, 0" : "=v"(c) : "v"(a), "v"(b));
    else asm volatile("v_mfma_f32_16x16x32_bf16 %0, %1, %2, %0" :: "v"(c), "v"(a), "v"(b));
}
struct ConvSet { bf16x8 A[8], B[8]; };
template <int J> __device__ __forceinline__ void conv_ld(ConvSet& s, const LAS char* ab, const LAS char* bb) {
    if constexpr (J < 8) {
        const LAS u32* p = (const LAS u32*)(ab + 512 * J);
        u32x4 t = {p[0], p[1], p[2], p[3]};
        s.A[J] = __builtin_bit_cast(bf16x8, t);
    } else s.B[J - 8] = *(const LAS bf16x8*)(bb + 512 * (15 - J));
}
template <int M> struct TriIdx {
    static constexpr int find(bool want_i) { int m = 0; for (int s = 0; s < 8; ++s) for (int j = 0; j <= s; ++j) { if (m == M) return want_i ? s : j; ++m; } return 0; }
    static constexpr int i = find(true), j = find(false);
};
template <int M, bool PF, bool FIRST> __device__ __forceinline__ void conv_slot(f32x4 (&acc)[8], const ConvSet& cur, ConvSet& nxt, const LAS char* ab, const LAS char* bb) {
    mfma_v<(FIRST && TriIdx<M>::j == 0)>(acc[TriIdx<M>::i], cur.A[TriIdx<M>::j], cur.B[TriIdx<M>::i - TriIdx<M>::j]);
    if constexpr (PF && (M & 1) == 0 && M / 2 < 16) conv_ld<((M / 2) & 1) * 8 + (M / 4)>(nxt, ab, bb);
    SCHED();
}
template <bool PF, bool FIRST, int... M> __device__ __forceinline__ void conv_step(f32x4 (&acc)[8], const ConvSet& cur, ConvSet& nxt, const LAS char* ab, const LAS char* bb, std::integer_sequence<int, M...>) {
    (conv_slot<M, PF, FIRST>(acc, cur, nxt, ab, bb), ...);
}
template <int... J> __device__ __forceinline__ void conv_ld_all(ConvSet& s, const LAS char* ab, const LAS char* bb, std::integer_sequence<int, J...>) { (conv_ld<J>(s, ab, bb), ...); }

struct ConvIn { u32x4 k; u32 kn; u16 dv; };
__device__ __forceinline__ void conv_load_k(ConvIn& in, int d, const u16* __restrict__ ssm_k, const u16* __restrict__ ssm_D) {
    const int tid = threadIdx.x;
    in.k = *(const u32x4*)(ssm_k + (size_t)d * S_ + tid * 8);
    in.kn = *(const u32*)(ssm_k + (size_t)d * S_ + (tid < 255 ? tid * 8 + 8 : 0));
    in.dv = ssm_D[d];
}
__device__ __forceinline__ void conv_put_k(const ConvIn& in, char* sm) {
    const int tid = threadIdx.x;
    const u32 kn = tid < 255 ? in.kn : 0u;
    *(u32x4*)(sm + C_K0 + 32 + tid * 16) = in.k;
    *(u32x4*)(sm + C_K1 + 32 + tid * 16) = (u32x4){__builtin_amdgcn_alignbit(in.k[1], in.k[0], 16), __builtin_amdgcn_alignbit(in.k[2], in.k[1], 16),
                                                 __builtin_amdgcn_alignbit(in.k[3], in.k[2], 16), __builtin_amdgcn_alignbit(kn, in.k[3], 16)};
    if (tid < 32) *(u32x4*)(sm + C_XR + 4096 + tid * 16) = (u32x4){0, 0, 0, 0};
    if (tid == 0) {
        *(u32x4*)(sm + C_K0) = (u32x4){0, 0, 0, 0}; *(u32x4*)(sm + C_K0 + 16) = (u32x4){0, 0, 0, 0};
        *(u32x4*)(sm + C_K1) = (u32x4){0, 0, 0, 0}; *(u32x4*)(sm + C_K1 + 16) = (u32x4){0, 0, 0, in.k[0] << 16};
    }
}
__device__ __forceinline__ void conv_finish(int d, const char* sm, const char* red, float Dv, rsrc_t wsr) {
    const int tid = threadIdx.x;
    const int i = tid >> 5, n = tid & 15, gp = (tid >> 4) & 1;
    f32x4 lo = {0.f, 0.f, 0.f, 0.f}, hi = {0.f, 0.f, 0.f, 0.f};
#pragma unroll
    for (int w = 0; w < 4; ++w) {
        lo += *(const f32x4*)(red + (w * 8 + i) * 1024 + (n + 32 * gp) * 16);
        hi += *(const f32x4*)(red + (w * 8 + i) * 1024 + (n + 32 * gp + 16) * 16);
    }
    const int s = 256 * i + 16 * n + 8 * gp;
    const u32x4 xw = *(const u32x4*)(sm + C_XR + 2 * (2040 - s));
    const float o0 = rbf(lo[0]) + rbf(bf_hi(xw[3]) * Dv), o1 = rbf(lo[1]) + rbf(bf_lo(xw[3]) * Dv), o2 = rbf(lo[2]) + rbf(bf_hi(xw[2]) * Dv), o3 = rbf(lo[3]) + rbf(bf_lo(xw[2]) * Dv);
    const float o4 = rbf(hi[0]) + rbf(bf_hi(xw[1]) * Dv), o5 = rbf(hi[1]) + rbf(bf_lo(xw[1]) * Dv), o6 = rbf(hi[2]) + rbf(bf_hi(xw[0]) * Dv), o7 = rbf(hi[3]) + rbf(bf_lo(xw[0]) * Dv);
    st16_dev(wsr, (u32)WS_YYT + (u32)(d * S_ + s) * 2, (u32x4){pk2(o0, o1), pk2(o2, o3), pk2(o4, o5), pk2(o6, o7)});
}
template <class Hook>
__device__ __forceinline__ void conv_body(int ch0, const ConvIn (&in)[3], char* smem, rsrc_t wsr, Hook hook
                                          ) {
    const int lane = threadIdx.x & 63, wid = __builtin_amdgcn_readfirstlane(threadIdx.x >> 6);
    __syncthreads();
    STAMP_AT(3);
    const int n = lane & 15, g = lane >> 4, dd = g >> 1, gh = g & 1;
    const LAS char* l0 = (const LAS char*)smem;
    const LAS char* bb = l0 + C_XR + 480 - 32 * n + 32 * dd + 16 * gh + 128 * wid;
    const LAS char* ab = l0 + ((n & 1) ? C_K0 + 2 * (16 * dd + 8 * gh + n + 1) : C_K1 + 2 * (16 * dd + 8 * gh + n)) + 128 * wid;
    f32x4 acc[3][8];
    ConvSet s0, s1;
    conv_ld_all(s0, ab, bb, std::make_integer_sequence<int, 16>{});
    SCHED();
    constexpr auto SQ = std::make_integer_sequence<int, 36>{};
    conv_step<true, true>(acc[0], s0, s1, ab + 64, bb + 64, SQ);
    conv_step<true, false>(acc[0], s1, s0, ab + C_CH, bb + C_CH, SQ);
    conv_step<true, true>(acc[1], s0, s1, ab + C_CH + 64, bb + C_CH + 64, SQ);
    conv_step<true, false>(acc[1], s1, s0, ab + 2 * C_CH, bb + 2 * C_CH, SQ);
    conv_step<true, true>(acc[2], s0, s1, ab + 2 * C_CH + 64, bb + 2 * C_CH + 64, SQ);
    conv_step<false, false>(acc[2], s1, s0, ab, bb, SQ);
    asm volatile("s_nop 7" ::: "memory");
#pragma unroll
    for (int c = 0; c < 3; ++c)
#pragma unroll
        for (int i = 0; i < 8; ++i) asm volatile("" : "+v"(acc[c][i]));
    STAMP_AT(4);
    hook();
#pragma unroll
    for (int c = 0; c < 3; ++c)
#pragma unroll
        for (int i = 0; i < 8; ++i) *(f32x4*)(smem + C_RED + c * 32768 + (wid * 8 + i) * 1024 + lane * 16) = acc[c][i];
    __syncthreads();
#pragma unroll
    for (int c = 0; c < 3; ++c) conv_finish(ch0 + c, smem + c * C_CH, smem + C_RED + c * 32768, bf1(in[c].dv), wsr);
}

constexpr int R_MOD = 147456;
constexpr int R_YT = 110592;
constexpr int R_HT = R_YT + 8 * 1168;
struct RowsPre { float x[18], gh[18]; u32 xw[9]; };
__device__ __forceinline__ void rows_load(RowsPre& r, int t0, const u16* __restrict__ q) {
    const int tid = threadIdx.x, lane = tid & 63, hl = lane & 31, row = 2 * (tid >> 6) + (lane >> 5);
    const u32* qrow = (const u32*)(q + (size_t)(t0 + row) * D_);
#pragma unroll
    for (int i = 0; i < 9; ++i) r.xw[i] = qrow[hl + 32 * i];
}
struct ModIn { u32x4 mw[4]; };
__device__ __forceinline__ void mod_load(ModIn& m, rsrc_t wsr) {
#pragma unroll
    for (int i = 0; i < 4; ++i) { const int j = threadIdx.x + 256 * i; m.mw[i] = ld16_dev(wsr, (u32)WS_MOD + (j < 864 ? j : 0) * 16); }
}
__device__ __forceinline__ void mod_put(const ModIn& m, char* sm) {
#pragma unroll
    for (int i = 0; i < 4; ++i) { const int j = threadIdx.x + 256 * i; if (j < 864) *(u32x4*)(sm + R_MOD + j * 16) = m.mw[i]; }
}
__device__ __forceinline__ void rows_pre(RowsPre& r, const char* sm) {
    const int hl = threadIdx.x & 31;
#pragma unroll
    for (int i = 0; i < 9; ++i) { r.x[2 * i] = bf_lo(r.xw[i]); r.x[2 * i + 1] = bf_hi(r.xw[i]); }
    float s = 0.f;
#pragma unroll
    for (int i = 0; i < 18; ++i) s += r.x[i];
    const float mean = half_sum(s) * (1.0f / 576.0f);
    float vs = 0.f;
#pragma unroll
    for (int i = 0; i < 18; ++i) { const float dl = r.x[i] - mean; vs += dl * dl; }
    const float rstd = __builtin_amdgcn_rsqf(half_sum(vs) * (1.0f / 576.0f) + 1e-5f);
    const float* mod = (const float*)(sm + R_MOD);
#pragma unroll
    for (int i = 0; i < 9; ++i) {
        const int c = 2 * (hl + 32 * i);
        const f32x2 sa = *(const f32x2*)(mod + c), c1 = *(const f32x2*)(mod + 576 + c), ga = *(const f32x2*)(mod + 2 * 576 + c);
        r.gh[2 * i] = ga[0] * ((r.x[2 * i] - mean) * rstd * c1[0] + sa[0]);
        r.gh[2 * i + 1] = ga[1] * ((r.x[2 * i + 1] - mean) * rstd * c1[1] + sa[1]);
    }
}
__device__ __forceinline__ void rows_fetch(int t0, char* sm, const char* ws) {
    const int lane = threadIdx.x & 63;
    const char* src = ws + WS_YYT + t0 * 2;
    const u32 dst = (u32)(uintptr_t)(sm + R_HT);
#pragma unroll
    for (int i = 0; i < 9; ++i)
        asm volatile("s_mov_b32 m0, %2\n\ts_nop 0\n\tglobal_load_lds_dwordx4 %0, %1 sc1" :: "v"((u32)((lane + 64 * i) * (S_ * 2))), "s"(src), "s"(dst + i * 1024) : "memory");
    asm volatile("s_waitcnt vmcnt(0)" ::: "memory");
}
__device__ __forceinline__ void rows_main(const RowsPre& r, int t0, char* sm, rsrc_t wsr) {
    const int tid = threadIdx.x, lane = tid & 63, hl = lane & 31, row = 2 * (tid >> 6) + (lane >> 5);
    const int cg = tid >> 1, th = tid & 1;
    if (tid < 144) {
        u32x2 yw[8];
#pragma unroll
        for (int c = 0; c < 8; ++c) yw[c] = *(const u32x2*)(sm + R_HT + (8 * cg + c) * 16 + th * 8);
#pragma unroll
        for (int t = 0; t < 4; ++t) {
            u32x4 o;
#pragma unroll
            for (int kk = 0; kk < 4; ++kk) {
                const u32 a = yw[2 * kk][t >> 1], bq = yw[2 * kk + 1][t >> 1];
                o[kk] = (t & 1) ? __builtin_amdgcn_perm(bq, a, 0x07060302u) : __builtin_amdgcn_perm(bq, a, 0x05040100u);
            }
            *(u32x4*)(sm + R_YT + (4 * th + t) * 1168 + cg * 16) = o;
        }
    }
    __syncthreads();
    const float* mod = (const float*)(sm + R_MOD);
    float x1[18];
#pragma unroll
    for (int i = 0; i < 9; ++i) {
        u32* yp = (u32*)(sm + R_YT + row * 1168 + (hl + 32 * i) * 4);
        const u32 yv = *yp;
        const u32 p = pk2(r.x[2 * i] + r.gh[2 * i] * bf_lo(yv), r.x[2 * i + 1] + r.gh[2 * i + 1] * bf_hi(yv));
        *yp = p;
        x1[2 * i] = bf_lo(p); x1[2 * i + 1] = bf_hi(p);
    }
    float s = 0.f;
#pragma unroll
    for (int i = 0; i < 18; ++i) s += x1[i];
    const float mean = half_sum(s) * (1.0f / 576.0f);
    float vs = 0.f;
#pragma unroll
    for (int i = 0; i < 18; ++i) { const float dl = x1[i] - mean; vs += dl * dl; }
    const float rstd = __builtin_amdgcn_rsqf(half_sum(vs) * (1.0f / 576.0f) + 1e-5f);
#pragma unroll
    for (int i = 0; i < 9; ++i) {
        const int c = 2 * (hl + 32 * i);
        const f32x2 sb = *(const f32x2*)(mod + 3 * 576 + c), c1 = *(const f32x2*)(mod + 4 * 576 + c);
        const float h0 = (x1[2 * i] - mean) * rstd * c1[0] + sb[0], h1 = (x1[2 * i + 1] - mean) * rstd * c1[1] + sb[1];
        const int ks = c >> 5, kk = c & 31, gq = (kk & 15) >> 2, j = ((kk >> 4) << 2) | (kk & 3);
        *(u32*)(sm + R_HT + ((ks * 4 + gq) * 8 + row) * 16 + j * 2) = pk2(h0, h1);
    }
    __syncthreads();
    const u32 h2t = (u32)WS_H2T + (u32)(t0 >> 4) * 18 * 1024 + (t0 & 15) * 16, x1g = (u32)WS_X1 + (u32)t0 * D_ * 2;
#pragma unroll
    for (int it = 0; it < 3; ++it) {
        const int i = tid + 256 * it;
        if (i < 576) {
            const int kg = i >> 3, rr8 = i & 7;
            st16_dev(wsr, h2t + (kg >> 2) * 1024 + (kg & 3) * 256 + rr8 * 16, *(const u32x4*)(sm + R_HT + i * 16));
            const int rr = i / 72, cc = i - rr * 72;
            st16_dev(wsr, x1g + rr * 1152 + cc * 16, *(const u32x4*)(sm + R_YT + rr * 1168 + cc * 16));
        }
    }
}

namespace mlp {
constexpr int STG = 36864, LDS_BYTES = 163840;
constexpr int G1S = 9 * 144, G2S = 18 * 36, NS = G1S + G2S;
constexpr int LA = 4;
constexpr int PRO = 9 + 4 * LA + 18;
constexpr int TUS = 147456;

constexpr int F01 = 288;
constexpr int stage_of(int gs) { return gs < F01 ? 0 : gs < G1S ? gs / 144 : 9 + (gs - G1S) / 72; }
constexpr int rel_of(int gs) { return gs < F01 ? gs : gs < G1S ? gs % 144 : (gs - G1S) % 72; }
constexpr int grp_of(int gs) { return rel_of(gs) >> 2; }
constexpr int t_of(int gs) { return rel_of(gs) & 3; }
constexpr bool barpt(int gs) { return gs >= F01 && gs < NS && t_of(gs) == 3 && grp_of(gs) == (gs < G1S ? 3 : 0) && stage_of(gs) < 17; }
constexpr int dma_id(int gs) {
    if (gs >= NS || t_of(gs) != 3) return -1;
    const int g = grp_of(gs), s = stage_of(gs);
    if (gs < F01) return (g >= 36 && g < 72 && (g & 3) == 3) ? 3 * 16 + (g - 36) / 4 : -1;
    if (s == 2) return (g >= 4 && g < 22) ? ((g & 1) ? 5 : 4) * 16 + (g - 4) / 2 : -1;
    if (s + 3 > 17) return -1;
    if (s == 8) return (g >= 4 && g < 13) ? 11 * 16 + g - 4 : -1;
    if (gs < G1S) { constexpr int G[9] = {5, 7, 9, 12, 14, 16, 20, 22, 24}; for (int i = 0; i < 9; ++i) if (G[i] == g) return (s + 3) * 16 + i; return -1; }
    return (g & 1) ? (s + 3) * 16 + (g >> 1) : -1;
}
constexpr int bload(int gs) {
    if (gs >= F01 || t_of(gs) != 1) return -1;
    const int ks = 2 * (gs / 32) + LA + (((gs % 32) >> 2) >> 2), i = (gs >> 2) & 3;
    return ks < 18 ? ks * 4 + i : -1;
}
constexpr bool unit_stores(int v) { return v == 1 || v == 3 || v == 4 || v == 6 || v == 8; }
constexpr int unit_end(int v) { return v < 2 ? F01 : 144 * (v + 1); }
constexpr int hload(int gs) {
    if (t_of(gs) != 3) return -1;
    const int s = stage_of(gs), g = grp_of(gs);
    if (s == 8) return (g >= 13 && g < 33) ? g - 13 : -1;
    if (s < 9 || g < 2 || g > 16 || (g & 1)) return -1;
    const int i = (g - 2) >> 1;
    if (s == 9) return 20 + i;
    if (s == 10) return 28 + i;
    if (s == 11) return i < 4 ? 36 + i : -1;
    if (s >= 12 && s <= 15) return 40 + 8 * (s - 12) + i;
    return -1;
}
constexpr int pub(int gs) { return (stage_of(gs) == 6 && grp_of(gs) == 27 && t_of(gs) == 3) ? 1 : (stage_of(gs) == 10 && grp_of(gs) == 9 && t_of(gs) == 1) ? 2 : 0; }
constexpr int poll_issue(int gs) { return (stage_of(gs) == 7 && grp_of(gs) == 27 && t_of(gs) == 3) ? 1 : (stage_of(gs) == 11 && grp_of(gs) == 9 && t_of(gs) == 1) ? 2 : 0; }
struct EpiSched { signed char ev[G1S], ep[G1S], sv[G1S], st[G1S]; };
constexpr EpiSched make_epi() {
    EpiSched e{};
    int v = 0, p = 0, pend_v = -1, pend_lo = 0, pend_hi = 0;
    for (int gs = 0; gs < G1S; ++gs) {
        e.ev[gs] = -1; e.ep[gs] = -1; e.sv[gs] = -1; e.st[gs] = -1;
        if (gs < F01) continue;
        const int t = t_of(gs);
        if (t == 0) continue;
        const bool vm = barpt(gs) || dma_id(gs) >= 0 || hload(gs) >= 0 || pub(gs) || poll_issue(gs);
        if (t == 3 && !vm && pend_lo < pend_hi) { e.sv[gs] = (signed char)pend_v; e.st[gs] = (signed char)pend_lo; ++pend_lo; continue; }
        if (t == 3 && vm) continue;
        if (v < 8 && gs >= unit_end(v) + 8 && (pend_lo == pend_hi || pend_v == v || !unit_stores(v) || true)) {
            e.ev[gs] = (signed char)v; e.ep[gs] = (signed char)p;
            ++p;
            if ((p & 15) == 0 && unit_stores(v)) { if (pend_v != v) { pend_v = v; pend_lo = p / 16 - 1; } pend_hi = p / 16; }
            if (p == 64) { p = 0; ++v; }
        }
    }
    return e;
}
constexpr EpiSched ES = make_epi();
constexpr int hstore(int gs) { return gs < G1S ? ES.st[gs] : -1; }
constexpr int epi_last(int v) { for (int gs = G1S - 1; gs >= 0; --gs) if (ES.ev[gs] == v) return gs; return -1; }
constexpr int store_last(int v) { for (int gs = G1S - 1; gs >= 0; --gs) if (ES.sv[gs] == v && ES.st[gs] == 3) return gs; return -1; }
constexpr bool epi_ok() {
    for (int v = 0; v < 8; ++v) {
        if (epi_last(v) < 0 || ES.ep[epi_last(v)] != 63) return false;
        if (v + 3 <= 8 && epi_last(v) >= 144 * (v + 3)) return false;
        if (unit_stores(v) && store_last(v) < 0) return false;
    }
    return store_last(4) + 40 < 6 * 144 + 27 * 4;
}
static_assert(epi_ok(), "epilogue schedule");
constexpr int vm_ops(int gs) { return (pub(gs) ? 1 : 0) + (poll_issue(gs) ? 1 : 0) + (hstore(gs) >= 0) + (bload(gs) >= 0) + (hload(gs) >= 0) + (dma_id(gs) >= 0); }
struct Ledger { int cum[NS + 1]; };
constexpr Ledger make_ledger() {
    Ledger l{}; int n = PRO;
    for (int gs = 0; gs < NS; ++gs) { if (gs == G1S) n += 4; l.cum[gs] = n; n += vm_ops(gs); }
    l.cum[NS] = n; return l;
}
constexpr Ledger LG = make_ledger();
constexpr int seq_in(int gs, int kind) {
    int n = LG.cum[gs];
    if (kind > 0 && pub(gs)) ++n;
    if (kind > 1 && poll_issue(gs)) ++n;
    if (kind > 2 && hstore(gs) >= 0) ++n;
    if (kind > 3 && bload(gs) >= 0) ++n;
    if (kind > 4 && hload(gs) >= 0) ++n;
    return n;
}
constexpr int seq_dma_last(int s) {
    if (s <= 2) return 9 * s + 8;
    for (int gs = 0; gs < NS; ++gs) if (dma_id(gs) == s * 16 + 8) return seq_in(gs, 5);
    return -1;
}
constexpr int seq_b(int ks) {
    if (ks < LA) return 27 + 4 * ks + 3;
    for (int gs = 0; gs < F01; ++gs) if (bload(gs) == ks * 4 + 3) return seq_in(gs, 3);
    return -1;
}
constexpr int seq_h(int kk) {
    for (int gs = 8 * 144; gs < NS; ++gs) if (hload(gs) == kk * 4 + 3) return seq_in(gs, 4);
    return -1;
}
constexpr int seq_poll(int part) {
    for (int gs = 0; gs < NS; ++gs) if (poll_issue(gs) == part) return seq_in(gs, 1);
    return -1;
}
constexpr int seq_store_last(int part) {
    if (part == 2) return LG.cum[G1S] - 1;
    return seq_in(store_last(4), 2);
}
struct Work { int unit, mat, k, t, n; };
constexpr Work work_of(int gs) {
    if (gs < F01) { const int p = gs / 32, um = (gs % 32) / 8, r = gs % 8; return {um >> 1, um & 1, 2 * p + (r & 1), r >> 1, 8 * p + 2 * um + (r & 1)}; }
    if (gs < G1S) { const int u = gs / 144, bi = (gs % 144) / 24, r = gs % 24; return {u, bi & 1, 6 * (bi >> 1) + r % 6, r / 6, 36 * u + 6 * bi + r % 6}; }
    const int s = (gs - G1S) / 72, r = (gs - G1S) % 72, du = r / 8, r8 = r % 8;
    return {du, 0, 2 * s + (r8 & 1), r8 >> 1, 324 + 18 * s + 2 * du + (r8 & 1)};
}
constexpr int NFRAG = 486, RQ = 12, FR0 = 8;
constexpr int frag_first(int n) {
    if (n < 72) return 32 * (n / 8) + 8 * ((n % 8) >> 1) + (n & 1);
    if (n < 324) return 144 * (n / 36) + 24 * ((n % 36) / 6) + n % 6;
    return G1S + 72 * ((n - 324) / 18) + 8 * (((n - 324) % 18) >> 1) + (n & 1);
}
constexpr int frag_last(int n) { return frag_first(n) + ((n >= 72 && n < 324) ? 18 : 6); }
constexpr int frag_stage(int n) { return n < 72 ? (n % 8) >> 2 : n < 324 ? n / 36 : 9 + (n - 324) / 18; }
constexpr int frag_off(int n) {
    if (n < 72) return (2 * (2 * (n / 8) + (n & 1)) + (((n % 8) >> 1) & 1)) * 1024;
    if (n < 324) { const int bi = (n % 36) / 6; return (2 * (6 * (bi >> 1) + n % 6) + (bi & 1)) * 1024; }
    return (n & 1) * 9216 + (((n - 324) % 18) >> 1) * 32;
}
constexpr int stage_ready(int s) {
    if (s <= 2) return 0;
    for (int gs = 0; gs < NS; ++gs) if (barpt(gs) && stage_of(gs) == s - 1) return gs + 1;
    return NS;
}
struct PfSched { short id[NS]; };
constexpr PfSched make_pf() {
    PfSched t{};
    int m = FR0, rdy = stage_ready(frag_stage(FR0));
    for (int gs = 0; gs < NS; ++gs) {
        t.id[gs] = -1;
        if ((gs & 3) != 0 || m >= NFRAG) continue;
        if (m >= RQ && frag_last(m - RQ) >= gs) continue;
        if (rdy > gs) continue;
        t.id[gs] = (short)m;
        ++m;
        if (m < NFRAG && frag_stage(m) != frag_stage(m - 1)) rdy = stage_ready(frag_stage(m));
    }
    return t;
}
constexpr PfSched PF = make_pf();
constexpr bool pf_ok() {
    int cnt = FR0;
    for (int gs = 0; gs < NS; ++gs) if (PF.id[gs] >= 0) { if (PF.id[gs] != cnt || frag_first(cnt) - gs < 8) return false; ++cnt; }
    return cnt == NFRAG;
}
static_assert(pf_ok(), "fragment prefetch schedule");
constexpr int clampv(int n) { return n > 63 ? 63 : (n < 0 ? 0 : n); }
constexpr int wait_for(int gs, int seq) { return clampv(LG.cum[gs] - seq - 1); }

template <int N> __device__ __forceinline__ void wait_vm() { asm volatile("s_waitcnt vmcnt(%0)" :: "n"(N) : "memory"); }

struct Ctx {
    bf16x8 Bv[2][4];
    bf16x8 Aq[12];
    f32x4 ac[3][2][4];
    f32x4 a2[9][4];
    float e0, e1;
    u32x4 hp[4];
    u32 vo1[4], vo2[4], vo3[4];
    u32 lane16, epoch;
    u32x4 pollv;
    const LAS char* rd[4]; const LAS char* rd2[4];
    const char* w1src; const char* w2src; const char* bsrc; const char* hsrc;
    u32 voff2[9];
    int sub;
    u32 dmadst;
    u32 hso;
    int jj;
    __amdgpu_buffer_rsrc_t hrs;
    u32* flag_pub; u32* flag_poll;
};

template <int KS, int T, bool ZERO> __device__ __forceinline__ void mfma(Ctx& cx, f32x4& c, const bf16x8& a) {
    constexpr int r = (KS * 4 + T) * 4;
    if constexpr (KS < 16) {
        if constexpr (ZERO) asm volatile("v_mfma_f32_16x16x32_bf16 %0, %1, a[%c2:%c3], 0" : "=v"(c) : "v"(a), "n"(r), "n"(r + 3));
        else asm volatile("v_mfma_f32_16x16x32_bf16 %0, %1, a[%c2:%c3], %0" :: "v"(c), "v"(a), "n"(r), "n"(r + 3));
    } else {
        asm volatile("v_mfma_f32_16x16x32_bf16 %0, %1, %2, %0" :: "v"(c), "v"(a), "v"(cx.Bv[KS - 16][T]));
    }
}
template <int KS, int T, bool SC1> __device__ __forceinline__ void gload(Ctx& cx, u32 voff, const char* sb) {
    constexpr int r = (KS * 4 + T) * 4, off = (KS & 3) * 1024;
    if constexpr (KS < 16) {
        if constexpr (SC1) asm volatile("global_load_dwordx4 a[%c2:%c3], %0, %1 offset:%4 sc1" :: "v"(voff), "s"(sb), "n"(r), "n"(r + 3), "n"(off) : "memory");
        else asm volatile("global_load_dwordx4 a[%c2:%c3], %0, %1 offset:%4" :: "v"(voff), "s"(sb), "n"(r), "n"(r + 3), "n"(off) : "memory");
    } else {
        if constexpr (SC1) asm volatile("global_load_dwordx4 %0, %1, %2 offset:%3 sc1" : "=v"(cx.Bv[KS - 16][T]) : "v"(voff), "s"(sb), "n"(off) : "memory");
        else asm volatile("global_load_dwordx4 %0, %1, %2 offset:%3" : "=v"(cx.Bv[KS - 16][T]) : "v"(voff), "s"(sb), "n"(off) : "memory");
    }
}
template <int S, int P> __device__ __forceinline__ void dma_m0(const Ctx& c) {
    asm volatile("s_mov_b32 m0, %0" :: "s"(c.dmadst + (u32)((S & 3) * STG + (P >> 2) * 4096)) : "memory");
}
constexpr int nat_kstep(int ks2) {
    if (ks2 < 10) { constexpr int T[5] = {0, 1, 5, 6, 4}; return 9 * (ks2 / 5) + T[ks2 % 5]; }
    const int jj = (ks2 - 10) >> 1, h = (ks2 - 10) & 1;
    return 9 * (jj >> 1) + ((jj & 1) ? 7 : 2) + h;
}
template <int S, int P> __device__ __forceinline__ void dma_go(const Ctx& c) {
    if constexpr (S < 9) {
        const char* sb = c.w1src + (size_t)S * STG + (P >> 2) * 4096;
        asm volatile("global_load_lds_dwordx4 %0, %1 offset:%2 sc1" :: "v"(c.lane16), "s"(sb), "n"((P & 3) * 1024) : "memory");
    } else {
        constexpr int n0 = nat_kstep(2 * (S - 9)), n1 = nat_kstep(2 * (S - 9) + 1);
        const char* sb = c.w2src + (size_t)(c.sub ? n1 : n0) * (32 * D_ * 2);
        asm volatile("global_load_lds_dwordx4 %0, %1 offset:%2" :: "v"(c.voff2[P]), "s"(sb), "n"((P & 3) * 1024) : "memory");
    }
}
template <int ID> __device__ __forceinline__ void load_b(Ctx& c) {
    constexpr int ks = ID >> 2, t = ID & 3;
    gload<ks, t, true>(c, c.vo1[t], c.bsrc + (ks >> 2) * 4096);
}
template <int ID> __device__ __forceinline__ void load_h(Ctx& c) {
    constexpr int kk = ID >> 2, t = ID & 3;
    gload<kk, t, true>(c, c.vo3[t], c.hsrc + (kk >> 2) * 4096);
}
template <int KS> __device__ __forceinline__ void pin_b(Ctx& c) {
#pragma unroll
    for (int t = 0; t < 4; ++t) { if constexpr (KS >= 16) asm volatile("" : "+v"(c.Bv[KS - 16][t])); }
}
template <int V, int P> __device__ __forceinline__ void epi_piece(Ctx& c) {
    constexpr int pr = P >> 3, st = P & 7, t = pr >> 1, r = 2 * (pr & 1), par = V % 3;
    if constexpr (st == 0) {
        asm volatile("v_mul_f32 %0, 0xbfb8aa3b, %1" : "=v"(c.e0) : "v"(c.ac[par][0][t][r]));
        asm volatile("v_mul_f32 %0, 0xbfb8aa3b, %1" : "=v"(c.e1) : "v"(c.ac[par][0][t][r + 1]));
    } else if constexpr (st == 1) asm volatile("v_exp_f32 %0, %0" :: "v"(c.e0));
    else if constexpr (st == 2) asm volatile("v_exp_f32 %0, %0" :: "v"(c.e1));
    else if constexpr (st == 3) {
        asm volatile("v_add_f32 %0, 1.0, %0" :: "v"(c.e0));
        asm volatile("v_add_f32 %0, 1.0, %0" :: "v"(c.e1));
    } else if constexpr (st == 4) asm volatile("v_rcp_f32 %0, %0" :: "v"(c.e0));
    else if constexpr (st == 5) asm volatile("v_rcp_f32 %0, %0" :: "v"(c.e1));
    else if constexpr (st == 6) {
        asm volatile("v_mul_f32 %0, %0, %1" :: "v"(c.e0), "v"(c.ac[par][1][t][r]));
        asm volatile("v_mul_f32 %0, %0, %1" :: "v"(c.e1), "v"(c.ac[par][1][t][r + 1]));
    } else {
        constexpr bool first = (V == 0 || V == 2 || V == 5 || V == 7);
        u32 o;
        asm volatile("v_cvt_pk_bf16_f32 %0, %1, %2" : "=v"(o) : "v"(c.e0), "v"(c.e1));
        c.hp[t][(first ? 0 : 2) + (pr & 1)] = o;
    }
}
template <int V, int T> __device__ __forceinline__ void store_h(Ctx& c) {
    const int m5 = 5 * (c.jj >> 1), ab = c.jj & 1;
    if constexpr (V == 4) {
        const u32x2 d = {c.hp[T][2], c.hp[T][3]};
        __builtin_amdgcn_raw_buffer_store_b64(d, c.hrs, c.vo2[T] + 8 * ab, c.hso + (m5 + 4) * 1024, 16);
    } else {
        const int kstep = (V == 1) ? m5 + 2 * ab : (V == 3) ? m5 + 2 * ab + 1 : (V == 6) ? 10 + 2 * c.jj : 11 + 2 * c.jj;
        __builtin_amdgcn_raw_buffer_store_b128(c.hp[T], c.hrs, c.vo2[T], c.hso + kstep * 1024, 16);
    }
}
template <int PART> __device__ __forceinline__ void poll_check(Ctx& c) {
    asm volatile("" : "+v"(c.pollv));
    const u32 e = c.epoch;
    if (!__all(c.pollv[0] == e && c.pollv[1] == e && c.pollv[2] == e && c.pollv[3] == e)) {
        const u32* p = c.flag_poll + (PART - 1) * 1024 + ((c.lane16 >> 4) & 3);
        poll_eq(p, e);
    }
}
template <int V, int P> __device__ __forceinline__ void epi_piece_padded(Ctx& c) {
    epi_piece<V, P>(c);
    if constexpr ((P & 7) == 2 || (P & 7) == 5) asm volatile("s_nop 0");
}
template <int V, int... P> __device__ __forceinline__ void epi_all(Ctx& c, std::integer_sequence<int, P...>) { (epi_piece_padded<V, P>(c), ...); }

template <int GS> __device__ __forceinline__ void slot(Ctx& c) {
    constexpr bool g1 = GS < G1S;
    constexpr int S = stage_of(GS), R = rel_of(GS), grp = R >> 2, t = R & 3;
    constexpr Work W = work_of(GS);
    constexpr bool f01 = GS < F01;
    if constexpr (GS == G1S) {
        asm volatile("s_nop 7" ::: "memory");
        epi_all<8>(c, std::make_integer_sequence<int, 64>{});
        store_h<8, 0>(c); store_h<8, 1>(c); store_h<8, 2>(c); store_h<8, 3>(c);
        SCHED();
    }
    if constexpr (f01 && GS % 32 == 0) { wait_vm<wait_for(GS, seq_b(2 * (GS / 32) + 1))>(); pin_b<2 * (GS / 32)>(c); pin_b<2 * (GS / 32) + 1>(c); }
    if constexpr (GS == 8 * 144) { wait_vm<wait_for(GS, seq_poll(1))>(); poll_check<1>(c); }
    if constexpr (!g1 && R == 0) { wait_vm<wait_for(GS, seq_h(2 * (S - 9) + 1))>(); pin_b<2 * (S - 9)>(c); pin_b<2 * (S - 9) + 1>(c); }
    if constexpr (dma_id(GS) >= 0) dma_m0<dma_id(GS) / 16, dma_id(GS) % 16>(c);
    if constexpr (g1) mfma<W.k, W.t, (W.k == 0)>(c, c.ac[W.unit % 3][W.mat][W.t], c.Aq[W.n % RQ]);
    else mfma<W.k, W.t, (W.k == 0)>(c, c.a2[W.unit][W.t], c.Aq[W.n % RQ]);
    if constexpr (barpt(GS)) {
        constexpr int n1 = wait_for(GS, seq_dma_last(S + 1));
        constexpr int n2 = (S == 12) ? wait_for(GS, seq_poll(2)) : 63;
        wait_vm<(n1 < n2 ? n1 : n2)>();
        if constexpr (S == 12) poll_check<2>(c);
        __builtin_amdgcn_s_barrier();
        asm volatile("" ::: "memory");
    }
    if constexpr (pub(GS)) {
        wait_vm<wait_for(GS, seq_store_last(pub(GS)))>();
        if (c.lane16 == 0) flag_set(c.flag_pub + (pub(GS) - 1) * 1024, c.epoch);
    }
    if constexpr (poll_issue(GS)) asm volatile("global_load_dwordx4 %0, %1, %2 sc1" : "=v"(c.pollv) : "v"(0u), "s"(c.flag_poll + (poll_issue(GS) - 1) * 1024) : "memory");
    {
        constexpr int f = PF.id[GS], fh = (GS & 3) == 1 ? PF.id[GS - ((GS & 3) == 1 ? 1 : 0)] : -1;
        if constexpr (f >= 0 && f < 324) c.Aq[f % RQ] = *(const LAS bf16x8*)(c.rd[frag_stage(f) & 3] + frag_off(f));
        constexpr int ft = f >= 324 ? f : fh >= 324 ? fh : -1;
        if constexpr (ft >= 0) {
            constexpr int half = f >= 324 ? 0 : 1;
            const s16x4 h = __builtin_amdgcn_ds_read_tr16_b64_v4i16((LAS s16x4*)(c.rd2[frag_stage(ft) & 3] + frag_off(ft) + half * (16 * 288)));
            const u32x2 hh = __builtin_bit_cast(u32x2, h);
            u32x4 a = __builtin_bit_cast(u32x4, c.Aq[ft % RQ]);
            a[2 * half] = hh[0]; a[2 * half + 1] = hh[1];
            c.Aq[ft % RQ] = __builtin_bit_cast(bf16x8, a);
        }
    }
    if constexpr (g1) {
        if constexpr (ES.ev[g1 ? GS : 0] >= 0) epi_piece<ES.ev[g1 ? GS : 0], ES.ep[g1 ? GS : 0]>(c);
        if constexpr (ES.sv[g1 ? GS : 0] >= 0) store_h<ES.sv[g1 ? GS : 0], ES.st[g1 ? GS : 0]>(c);
    }
    if constexpr (bload(GS) >= 0) load_b<bload(GS)>(c);
    if constexpr (hload(GS) >= 0) load_h<hload(GS)>(c);
    if constexpr (dma_id(GS) >= 0) dma_go<dma_id(GS) / 16, dma_id(GS) % 16>(c);
    SCHED();
}
template <int BASE, int... I> __device__ __forceinline__ void run_slots(Ctx& c, std::integer_sequence<int, I...>) { (slot<BASE + I>(c), ...); }
template <int S, int P> __device__ __forceinline__ void dma_one(const Ctx& c) { dma_m0<S, P>(c); asm volatile("s_nop 0"); dma_go<S, P>(c); }
template <int S, int... P> __device__ __forceinline__ void dma_stage(const Ctx& c, std::integer_sequence<int, P...>) { (dma_one<S, P>(c), ...); }
template <int... I> __device__ __forceinline__ void load_b_first(Ctx& c, std::integer_sequence<int, I...>) { (load_b<I>(c), ...); }
}

__device__ __forceinline__ void touch(u32& d, const char* p) { asm volatile("global_load_dword %0, %1, off" : "=v"(d) : "v"(p) : "memory"); }
__global__ __launch_bounds__(256, 1) void k_main(const u16* __restrict__ qin, const u16* __restrict__ kin, const u16* __restrict__ vin, const u16* __restrict__ shift_k,
                                                 const u16* __restrict__ ssm_k, const u16* __restrict__ ssm_D, const u16* __restrict__ wg, const u16* __restrict__ wu,
                                                 const u16* __restrict__ wd, const u16* __restrict__ cvec, const u16* __restrict__ w_mod,
                                                 char* __restrict__ ws, u16* __restrict__ out) {
    using namespace mlp;
    extern __shared__ __attribute__((aligned(16))) char smem[];
    const int tid = threadIdx.x, lane = tid & 63, w = __builtin_amdgcn_readfirstlane(tid >> 6);
    const int b = blockIdx.x, x = b & 7, idx = b >> 3, q = x >> 1;
    const int mt = 4 * (x & 1) + (idx & 3), fcl = idx >> 2, fc = 8 * q + fcl;
    const int mt2 = 8 * (x & 1) + (idx & 7), cc = idx >> 3, tg = w & 1, kh = w >> 1;
    const rsrc_t wsr = __builtin_amdgcn_make_buffer_rsrc(ws, 0, (int)WS_END, 0x00020000);
    STAMP_DECL; STAMP_AT(0);
    u32 epoch_in;
    unsigned long long pc_start;
    asm volatile("s_getpc_b64 %0" : "=s"(pc_start));
    u32 epoch;
    {
        const int t0 = (x * 32 + idx) * 8;
        RowsPre rp;
        u32 bg[10] = {0, 0, 0, 0, 0, 0, 0, 0, 0, 0};
        if (w == 3 && idx >= 24 && idx < 28) {
            const char* cb = (const char*)(pc_start & ~127ull) + lane * 128;
#pragma unroll
            for (int i = 0; i < 8; ++i) touch(bg[i], cb + (i < 7 || lane < 44 ? i * 8192 : 0));
        }
        if (__builtin_expect(b < 192, 1)) {
            const int c0 = 72 * x + 3 * idx;
            ConvIn in[3];
            KvIn kin_;
            touch(bg[8], (const char*)(b < 96 ? kin : vin) + (size_t)((b % 96) * 192 + (tid < 192 ? tid : 0)) * 128);
            kv_load(kin_, c0, kin, vin, shift_k);
            epoch_in = __hip_atomic_load(flag_ptr(ws, F_EPOCH), __ATOMIC_RELAXED, __HIP_MEMORY_SCOPE_AGENT);
            conv_load_k(in[0], c0, ssm_k, ssm_D); conv_load_k(in[1], c0 + 1, ssm_k, ssm_D); conv_load_k(in[2], c0 + 2, ssm_k, ssm_D);
            rows_load(rp, t0, qin);
            kv_put(kin_, smem + C_RED);
            conv_put_k(in[0], smem); conv_put_k(in[1], smem + C_CH); conv_put_k(in[2], smem + 2 * C_CH);
            __syncthreads();
            STAMP_AT(1);
            kv_compute(kin_, c0, smem + C_RED, smem + C_XR, C_CH);
            epoch = __builtin_amdgcn_readfirstlane(epoch_in) + 1u;
            if (w == 3) {
                DRAIN();
#pragma unroll
                for (int i = 0; i < 8; ++i) { const int l = lane + 64 * (i & 3); touch(bg[i], (const char*)(i < 4 ? wg : wu) + (size_t)b * 27648 + (l < 216 ? l : 0) * 128); }
            }
            STAMP_AT(2);
            ModIn mi;
            const u32* mf = flag_ptr(ws, F_MOD + (lane < 54 ? lane : 0));
            u32 pm;
            asm volatile("global_load_dword %0, %1, off sc1" : "=v"(pm) : "v"(mf) : "memory");
            conv_body(c0, in, smem, wsr, [&]() {
                DRAIN();
                asm volatile("" : "+v"(pm));
                if (!__all(pm == epoch)) poll_eq(mf, epoch);
                mod_load(mi, wsr); } STAMP_ARGS);
            STAMP_AT(5);
            mod_put(mi, smem);
            DRAIN();
#pragma unroll
            for (int i = 0; i < 10; ++i) asm volatile("" :: "v"(bg[i]));
            __syncthreads();
            if (tid < 8) flag_set(flag_ptr(ws, F_CONV + tid * 256 + b), epoch);
            if (w == 3) {
#pragma unroll
                for (int i = 0; i < 4; ++i) { const int l = lane + 64 * i; touch(bg[i], (const char*)wd + (size_t)b * 27648 + (l < 216 ? l : 0) * 128); }
            }
        } else {
            if (w == 3 && idx >= 28) {
#pragma unroll
                for (int i = 0; i < 3; ++i) { const size_t o = (size_t)(((idx - 28) * 3 + i) * 64 + lane) << 16; touch(bg[i], ws + (o < WS_END ? o : 0)); }
                touch(bg[3], (const char*)out + (size_t)(lane < 36 ? lane : 0) * 65536);
                const size_t pg = (size_t)(lane + 64 * (idx & 1) < 81 ? lane + 64 * (idx & 1) : 0) * 65536;
                touch(bg[4], (const char*)wd + pg); touch(bg[5], (const char*)wg + pg); touch(bg[6], (const char*)wu + pg);
            }
            for (int i = 0; i < 40; ++i) __builtin_amdgcn_s_sleep(2);
            rows_load(rp, t0, qin);
            epoch_in = __hip_atomic_load(flag_ptr(ws, F_EPOCH), __ATOMIC_RELAXED, __HIP_MEMORY_SCOPE_AGENT);
            if (b - 192 < 54) mod_job(b - 192, cvec, w_mod, ws, (float*)(smem + R_MOD), (float*)(smem + R_MOD + 4096));
            epoch = __builtin_amdgcn_readfirstlane(epoch_in) + 1u;
            DRAIN();
            __syncthreads();
            if (tid == 0 && b - 192 < 54) flag_set(flag_ptr(ws, F_MOD + b - 192), epoch);
            STAMP_AT(1);
            tile_jobs(b - 192, wg, wu, smem, ws, wsr, epoch);
            STAMP_AT(2); STAMP_AT(3); STAMP_AT(4); STAMP_AT(5);
            poll_eq(flag_ptr(ws, F_MOD + (lane < 54 ? lane : 0)), epoch);
            ModIn mi;
            mod_load(mi, wsr);
            mod_put(mi, smem);
            __syncthreads();
        }
        rows_pre(rp, smem);
        STAMP_AT(6);
        if (w == 0) {
            poll_eq4(wsr, (u32)WS_CNT + (F_CONV + x * 256 + (lane < 48 ? lane : 0) * 4) * 4, epoch);
            rows_fetch(t0, smem, ws);
        }
        __syncthreads();
        STAMP_AT(7);
        rows_main(rp, t0, smem, wsr);
        DRAIN();
#pragma unroll
        for (int i = 0; i < 10; ++i) asm volatile("" :: "v"(bg[i]));
        __syncthreads();
        if (tid == 0) flag_set(flag_ptr(ws, F_ROWS + (t0 >> 6) * 8 + ((t0 >> 3) & 7)), epoch);
        STAMP_AT(8);
    }
    Ctx c;
#pragma unroll
    for (int t = 0; t < 4; ++t) c.hp[t] = (u32x4){0, 0, 0, 0};
    c.lane16 = lane * 16;
#pragma unroll
    for (int t = 0; t < 4; ++t) { c.vo1[t] = lane * 16 + t * 18432; c.vo2[t] = lane * 16 + t * TUS; c.vo3[t] = lane * 16 + ((t + 2 * kh) & 3) * TUS; }
    const LAS char* l0 = (const LAS char*)smem;
#pragma unroll
    for (int i = 0; i < 4; ++i) {
        c.rd[i] = l0 + i * STG + lane * 16; c.rd2[i] = l0 + i * STG + kh * 18432 + (4 * (lane >> 4) + ((lane & 15) >> 2)) * 288 + 8 * (lane & 3);
        asm volatile("" : "+v"(c.rd[i])); asm volatile("" : "+v"(c.rd2[i]));
    }
#pragma unroll
    for (int i = 0; i < 9; ++i) { const int ci = lane + 64 * i, row = (ci * 3641) >> 16, c16 = ci - row * 18; c.voff2[i] = (u32)(row * (D_ * 2) + c16 * 16 - (i & 3) * 1024); }
    c.sub = w & 1;
    c.w1src = ws + WS_WT1 + (size_t)fc * 331776 + w * 9216;
    c.w2src = (const char*)(wd + (size_t)(36 * q + 18 * kh) * 32 * D_ + cc * 144);
    c.bsrc = ws + WS_H2T + (size_t)(16 * mt + 4 * w) * 18432;
    c.hsrc = ws + WS_HT + (size_t)(8 * mt2 + 4 * tg) * TUS + (size_t)((q * 2 + kh) * 18) * 1024;
    c.dmadst = (u32)(uintptr_t)smem + w * 9216;
    c.jj = fcl & 3;
    c.hso = (u32)((16 * mt + 4 * w) * TUS + ((q * 2 + (fcl >> 2)) * 18) * 1024);
    c.hrs = __builtin_amdgcn_make_buffer_rsrc(ws + WS_HT, 0, (int)((size_t)S_ * F_ * 2), 0x00020000);
    c.epoch = epoch;
    c.flag_pub = flag_ptr(ws, F_H + ((((mt * 4 + w) * 4 + q) * 2 + (fcl >> 2))) * 4 + (fcl & 3));
    c.flag_poll = flag_ptr(ws, F_H + (((((mt2 >> 1) * 4 + 2 * (mt2 & 1) + tg) * 4 + q) * 2 + kh)) * 4);
    asm volatile("" ::: "a0", "a1", "a2", "a3", "a4", "a5", "a6", "a7", "a8", "a9", "a10", "a11", "a12", "a13", "a14", "a15", "a16", "a17", "a18", "a19", "a20", "a21", "a22", "a23", "a24", "a25", "a26", "a27", "a28", "a29", "a30", "a31", "a32", "a33", "a34", "a35", "a36", "a37", "a38", "a39", "a40", "a41", "a42", "a43", "a44", "a45", "a46", "a47", "a48", "a49", "a50", "a51", "a52", "a53", "a54", "a55", "a56", "a57", "a58", "a59", "a60", "a61", "a62", "a63", "a64", "a65", "a66", "a67", "a68", "a69", "a70", "a71", "a72", "a73", "a74", "a75", "a76", "a77", "a78", "a79", "a80", "a81", "a82", "a83", "a84", "a85", "a86", "a87", "a88", "a89", "a90", "a91", "a92", "a93", "a94", "a95", "a96", "a97", "a98", "a99", "a100", "a101", "a102", "a103", "a104", "a105", "a106", "a107", "a108", "a109", "a110", "a111", "a112", "a113", "a114", "a115", "a116", "a117", "a118", "a119", "a120", "a121", "a122", "a123", "a124", "a125", "a126", "a127", "a128", "a129", "a130", "a131", "a132", "a133", "a134", "a135", "a136", "a137", "a138", "a139", "a140", "a141", "a142", "a143", "a144", "a145", "a146", "a147", "a148", "a149", "a150", "a151", "a152", "a153", "a154", "a155", "a156", "a157", "a158", "a159", "a160", "a161", "a162", "a163", "a164", "a165", "a166", "a167", "a168", "a169", "a170", "a171", "a172", "a173", "a174", "a175", "a176", "a177", "a178", "a179", "a180", "a181", "a182", "a183", "a184", "a185", "a186", "a187", "a188", "a189", "a190", "a191", "a192", "a193", "a194", "a195", "a196", "a197", "a198", "a199", "a200", "a201", "a202", "a203", "a204", "a205", "a206", "a207", "a208", "a209", "a210", "a211", "a212", "a213", "a214", "a215", "a216", "a217", "a218", "a219", "a220", "a221", "a222", "a223", "a224", "a225", "a226", "a227", "a228", "a229", "a230", "a231", "a232", "a233", "a234", "a235", "a236", "a237", "a238", "a239", "a240", "a241", "a242", "a243", "a244", "a245", "a246", "a247", "a248", "a249", "a250", "a251", "a252", "a253", "a254", "a255");
    poll_eq(flag_ptr(ws, F_TILE + (fc >> 2) * 36 + (lane < 36 ? lane : 0)), epoch);
    dma_stage<0>(c, std::make_integer_sequence<int, 9>{});
    dma_stage<1>(c, std::make_integer_sequence<int, 9>{});
    dma_stage<2>(c, std::make_integer_sequence<int, 9>{});
    poll_eq(flag_ptr(ws, F_ROWS + (4 * mt + w) * 8 + (lane & 7)), epoch);
    STAMP_AT(9);
    load_b_first(c, std::make_integer_sequence<int, 4 * LA>{});
    wait_vm<4 * LA>();
    __builtin_amdgcn_s_barrier();
    asm volatile("" ::: "memory");
#pragma unroll
    for (int f = 0; f < FR0; ++f) c.Aq[f] = *(const LAS bf16x8*)(c.rd[frag_stage(f)] + frag_off(f));
    SCHED();
    STAMP_AT(10);
    run_slots<0>(c, std::make_integer_sequence<int, 288>{});
    STAMP_AT(11);
    run_slots<288>(c, std::make_integer_sequence<int, 144 * 3>{});
    run_slots<144 * 5>(c, std::make_integer_sequence<int, 144 * 4>{});
    STAMP_AT(12);
    run_slots<G1S>(c, std::make_integer_sequence<int, 324>{});
    run_slots<G1S + 324>(c, std::make_integer_sequence<int, 324>{});
    asm volatile("s_nop 7" ::: "memory");
#pragma unroll
    for (int u = 0; u < 9; ++u)
#pragma unroll
        for (int t = 0; t < 4; ++t) asm volatile("" : "+v"(c.a2[u][t]));
    SCHED();
    STAMP_AT(13);
    const int n = lane & 15, g = lane >> 4;
    u32x2 xr[3][2]; f32x4 gb[3];
#pragma unroll
    for (int i = 0; i < 3; ++i) {
        const int u = (w + 4 * i < 9) ? w + 4 * i : 8, d = 144 * cc + 16 * u + 4 * g;
#pragma unroll
        for (int t = 0; t < 2; ++t) xr[i][t] = __builtin_amdgcn_raw_buffer_load_b64(wsr, (u32)WS_X1 + (u32)((128 * mt2 + 32 * q + 16 * t + n) * D_ + d) * 2, 0, 16);
        gb[i] = *(const f32x4*)(smem + R_MOD + (5 * 576 + d) * 4);
    }
    {
        char* ex = smem + 73728 + w * 18432 + lane * 16;
#pragma unroll
        for (int t = 0; t < 2; ++t)
#pragma unroll
            for (int u = 0; u < 9; ++u) *(f32x4*)(ex + (t * 9 + u) * 1024) = c.a2[u][2 + t];
    }
    __syncthreads();
    const int j = 2 * tg + kh;
    u32* fred = flag_ptr(ws, F_RED + (mt2 * 4 + cc) * 16);
    const u32 slab = (u32)WS_SLAB + (u32)((mt2 * 4 + cc) * 16) * 9216;
    {
        const char* ey = smem + 73728 + (w ^ 2) * 18432 + lane * 16;
        char* own = smem + lane * 16;
#pragma unroll
        for (int u = 0; u < 9; ++u) {
            const f32x4 r0 = c.a2[u][0] + *(const f32x4*)(ey + u * 1024);
            const f32x4 r1 = c.a2[u][1] + *(const f32x4*)(ey + (9 + u) * 1024);
            if (j != q) st16_dev(wsr, slab + (j * 4 + q) * 9216 + u * 1024 + lane * 16, (u32x4){pk2(r0[0], r0[1]), pk2(r0[2], r0[3]), pk2(r1[0], r1[1]), pk2(r1[2], r1[3])});
            else { *(f32x4*)(own + u * 2048) = r0; *(f32x4*)(own + u * 2048 + 1024) = r1; }
        }
    }
    if (j != q) {
        DRAIN();
        if (lane == 0) flag_set(fred + j * 4 + q, epoch);
    }
    STAMP_AT(14);
    poll_eq(fred + q * 4 + (((lane & 3) == q) ? (q ^ 1) : (lane & 3)), epoch);
    __syncthreads();
    u32x4 rem[3][3];
#pragma unroll
    for (int i = 0; i < 3; ++i) {
        const int u = (w + 4 * i < 9) ? w + 4 * i : 8;
#pragma unroll
        for (int k = 0; k < 3; ++k) { const int qq = k + (k >= q ? 1 : 0); rem[i][k] = __builtin_amdgcn_raw_buffer_load_b128(wsr, slab + (q * 4 + qq) * 9216 + u * 1024 + lane * 16, 0, 16); }
    }
#pragma unroll
    for (int i = 0; i < 3; ++i) {
        const int u = w + 4 * i;
        if (u < 9) {
            const int d = 144 * cc + 16 * u + 4 * g;
#pragma unroll
            for (int t = 0; t < 2; ++t) {
                f32x4 s = *(const f32x4*)(smem + u * 2048 + t * 1024 + lane * 16);
#pragma unroll
                for (int k = 0; k < 3; ++k) {
                    const u32 a = rem[i][k][2 * t], bq = rem[i][k][2 * t + 1];
                    s += (f32x4){bf_lo(a), bf_hi(a), bf_lo(bq), bf_hi(bq)};
                }
                const float o0 = bf_lo(xr[i][t][0]) + rbf(gb[i][0] * rbf(s[0])), o1 = bf_hi(xr[i][t][0]) + rbf(gb[i][1] * rbf(s[1]));
                const float o2 = bf_lo(xr[i][t][1]) + rbf(gb[i][2] * rbf(s[2])), o3 = bf_hi(xr[i][t][1]) + rbf(gb[i][3] * rbf(s[3]));
                *(u32x2*)(out + (size_t)(128 * mt2 + 32 * q + 16 * t + n) * D_ + d) = (u32x2){pk2(o0, o1), pk2(o2, o3)};
            }
        }
    }
    if (b == 0 && tid == 0) flag_set(flag_ptr(ws, F_EPOCH), epoch);
    STAMP_AT(15);
    STAMP_OUT(4, 16);
}

extern "C" void kernel_launch(void* const* d_in, const int* in_sizes, int n_in, void* d_out, int out_size, void* d_ws, size_t ws_size,
                              hipStream_t stream) {
    const u16* q = (const u16*)d_in[0];
    const u16* k = (const u16*)d_in[1];
    const u16* v = (const u16*)d_in[2];
    const u16* shift_k = (const u16*)d_in[3];
    const u16* ssm_k = (const u16*)d_in[4];
    const u16* ssm_D = (const u16*)d_in[5];
    const u16* wg = (const u16*)d_in[7];
    const u16* wu = (const u16*)d_in[8];
    const u16* wd = (const u16*)d_in[9];
    const u16* c = (const u16*)d_in[10];
    const u16* w_mod = (const u16*)d_in[11];
    char* ws = (char*)d_ws;
    (void)hipFuncSetAttribute((const void*)k_main, hipFuncAttributeMaxDynamicSharedMemorySize, mlp::LDS_BYTES);
    k_main<<<256, 256, mlp::LDS_BYTES, stream>>>(q, k, v, shift_k, ssm_k, ssm_D, wg, wu, wd, c, w_mod, ws, (u16*)d_out);
}
```

```cpp
#include <hip/hip_runtime.h>
#include <stdint.h>
#include <utility>

typedef unsigned short u16;
typedef unsigned int u32;
typedef __bf16 bf16x8 __attribute__((ext_vector_type(8)));
typedef __bf16 bf16x2 __attribute__((ext_vector_type(2)));
typedef float f32x4 __attribute__((ext_vector_type(4)));
typedef float f32x2 __attribute__((ext_vector_type(2)));
typedef u32 u32x4 __attribute__((ext_vector_type(4)));
typedef u32 u32x2 __attribute__((ext_vector_type(2)));
typedef short s16x4 __attribute__((ext_vector_type(4)));
#define LAS __attribute__((address_space(3)))
typedef LAS char* lptr;

constexpr int S_ = 2048, D_ = 576, F_ = 4608;

constexpr size_t WS_MOD = 0;
constexpr size_t WS_CNT = 65536;
constexpr size_t WS_YYT = WS_CNT + 65536;
constexpr size_t WS_X1  = WS_YYT + (size_t)D_ * S_ * 2;
constexpr size_t WS_H2T = WS_X1 + (size_t)D_ * S_ * 2;
constexpr size_t WS_HT  = WS_H2T + (size_t)D_ * S_ * 2;
constexpr size_t WS_WT1 = WS_HT + (size_t)S_ * F_ * 2;
constexpr size_t WS_SLAB = WS_WT1 + (size_t)2 * D_ * F_ * 2;
constexpr size_t WS_END = WS_SLAB + (size_t)16 * 4 * 4 * 4 * 9216;

#define STAMP_DECL
#define STAMP_ARGS
#define STAMP_AT(i)
#define STAMP_OUT(kid, n)
__device__ __forceinline__ float bf_lo(u32 w) { return __uint_as_float(w << 16); }
__device__ __forceinline__ float bf_hi(u32 w) { return __uint_as_float(w & 0xffff0000u); }
__device__ __forceinline__ float bf1(u16 w) { return __uint_as_float((u32)w << 16); }
__device__ __forceinline__ u32 pk2(float lo, float hi) {
    f32x2 v = {lo, hi};
    return __builtin_bit_cast(u32, __builtin_convertvector(v, bf16x2));
}
__device__ __forceinline__ float rbf(float x) { return (float)(__bf16)x; }
__device__ __forceinline__ float sigmoidf_(float x) { return __builtin_amdgcn_rcpf(1.0f + __builtin_amdgcn_exp2f(-1.4426950408889634f * x)); }

constexpr int F_MOD = 256;
constexpr int F_TILE = 512;
constexpr int F_CONV = 1024;
constexpr int F_ROWS = 3072;
constexpr int F_H = 3328;
constexpr int F_RED = 5376;
constexpr int F_EPOCH = 6400;
typedef __amdgpu_buffer_rsrc_t rsrc_t;
__device__ __forceinline__ u32* flag_ptr(char* ws, int idx) { return (u32*)(ws + WS_CNT) + idx; }
__device__ __forceinline__ void flag_set(u32* p, u32 e) { __hip_atomic_store(p, e, __ATOMIC_RELAXED, __HIP_MEMORY_SCOPE_AGENT); }
__device__ __forceinline__ void st16_dev(rsrc_t rs, u32 off, u32x4 v) { __builtin_amdgcn_raw_buffer_store_b128(v, rs, off, 0, 16); }
__device__ __forceinline__ u32x4 ld16_dev(rsrc_t rs, u32 off) { return __builtin_amdgcn_raw_buffer_load_b128(rs, off, 0, 16); }
__device__ __forceinline__ void poll_eq(const u32* p, u32 e) {
    for (int it = 0; it < (1 << 21); ++it) {
        if (__all(__hip_atomic_load(p, __ATOMIC_RELAXED, __HIP_MEMORY_SCOPE_AGENT) == e)) break;
        __builtin_amdgcn_s_sleep(1);
    }
}
__device__ __forceinline__ void poll_eq4(rsrc_t rs, u32 off, u32 e) {
    for (int it = 0; it < (1 << 21); ++it) {
        asm volatile("" ::: "memory");
        const u32x4 v = ld16_dev(rs, off);
        if (__all(v[0] == e && v[1] == e && v[2] == e && v[3] == e)) break;
        __builtin_amdgcn_s_sleep(1);
    }
}
#define DRAIN() asm volatile("s_waitcnt vmcnt(0)" ::: "memory")

#define SCHED() __builtin_amdgcn_sched_barrier(0)
__device__ __forceinline__ float dpp_add(float v, const int ctrl_sel) {
    int r;
    const int iv = __builtin_bit_cast(int, v);
    switch (ctrl_sel) {
        case 0: r = __builtin_amdgcn_update_dpp(0, iv, 0xB1, 0xf, 0xf, true); break;
        case 1: r = __builtin_amdgcn_update_dpp(0, iv, 0x4E, 0xf, 0xf, true); break;
        case 2: r = __builtin_amdgcn_update_dpp(0, iv, 0x141, 0xf, 0xf, true); break;
        default: r = __builtin_amdgcn_update_dpp(0, iv, 0x140, 0xf, 0xf, true); break;
    }
    return v + __builtin_bit_cast(float, r);
}
__device__ __forceinline__ float half_sum(float v) {
    v = dpp_add(v, 0); v = dpp_add(v, 1); v = dpp_add(v, 2); v = dpp_add(v, 3);
    auto r = __builtin_amdgcn_permlane16_swap(__float_as_uint(v), __float_as_uint(v), false, false);
    return __uint_as_float(r[0]) + __uint_as_float(r[1]);
}

struct KvIn { u32x2 kw[8], vw[8]; u32x4 sw; u32x2 sw2; };
__device__ __forceinline__ void kv_load(KvIn& p, int c0, const u16* __restrict__ k, const u16* __restrict__ v, const u16* __restrict__ shift_k) {
    const int cw = c0 & ~1;
#pragma unroll
    for (int i = 0; i < 8; ++i) p.kw[i] = *(const u32x2*)(k + (size_t)(threadIdx.x + 256 * i) * D_ + cw);
#pragma unroll
    for (int i = 0; i < 8; ++i) p.vw[i] = *(const u32x2*)(v + (size_t)(threadIdx.x + 256 * i) * D_ + cw);
    p.sw = (u32x4){*(const u32*)(shift_k + c0 * 4), *(const u32*)(shift_k + c0 * 4 + 2), *(const u32*)(shift_k + c0 * 4 + 4), *(const u32*)(shift_k + c0 * 4 + 6)};
    p.sw2 = (u32x2){*(const u32*)(shift_k + c0 * 4 + 8), *(const u32*)(shift_k + c0 * 4 + 10)};
}
constexpr int KV_RAWV = 257 * 72 + 8;
__device__ __forceinline__ void kv_put(const KvIn& p, char* raw) {
    const int tid = threadIdx.x;
#pragma unroll
    for (int i = 0; i < 8; ++i) {
        const int t = tid + 256 * i, o = 72 * (t >> 3) + 8 * (t & 7);
        *(u32x2*)(raw + 72 + o) = p.kw[i];
        *(u32x2*)(raw + KV_RAWV + o) = p.vw[i];
    }
    if (tid < 8) *(u32x2*)(raw + 8 * tid) = (u32x2){0, 0};
}
__device__ __forceinline__ void kv_compute(const KvIn& p, int c0, const char* raw, char* xr, int stride) {
    const int tid = threadIdx.x;
    u32x2 kw[11], vw[8];
#pragma unroll
    for (int i = 0; i < 11; ++i) kw[i] = *(const u32x2*)(raw + 72 * tid + (i < 3 ? 40 + 8 * i : 72 + 8 * (i - 3)));
#pragma unroll
    for (int i = 0; i < 8; ++i) vw[i] = *(const u32x2*)(raw + KV_RAWV + 72 * tid + 8 * i);
    const bool odd = c0 & 1;
#pragma unroll
    for (int c = 0; c < 3; ++c) {
        const u32 wa = c == 0 ? p.sw[0] : c == 1 ? p.sw[2] : p.sw2[0], wb = c == 0 ? p.sw[1] : c == 1 ? p.sw[3] : p.sw2[1];
        const float w0 = bf_lo(wa), w1 = bf_hi(wa), w2 = bf_lo(wb), w3 = bf_hi(wb);
        float kf[11], r[8];
#define KV_PICK(d) (c == 0 ? (odd ? bf_hi((d)[0]) : bf_lo((d)[0])) : c == 1 ? (odd ? bf_lo((d)[1]) : bf_hi((d)[0])) : (odd ? bf_hi((d)[1]) : bf_lo((d)[1])))
#pragma unroll
        for (int i = 0; i < 11; ++i) kf[i] = KV_PICK(kw[i]);
#pragma unroll
        for (int i = 0; i < 8; ++i) r[i] = (kf[i] * w3 + kf[i + 1] * w2 + kf[i + 2] * w1 + kf[i + 3] * w0) * KV_PICK(vw[i]);
#undef KV_PICK
        *(u32x4*)(xr + c * stride + 2 * (2040 - 8 * tid)) = (u32x4){pk2(r[7], r[6]), pk2(r[5], r[4]), pk2(r[3], r[2]), pk2(r[1], r[0])};
    }
}

__device__ __forceinline__ void mod_job(int job, const u16* __restrict__ c, const u16* __restrict__ w_mod, char* __restrict__ ws, float* sc  , float* red  ) {
    const int tid = threadIdx.x, lane = tid & 63, wid = tid >> 6;
    const int col0 = job * 64, rg = tid >> 3, c8 = tid & 7;
    u32x4 w[18];
#pragma unroll
    for (int i = 0; i < 18; ++i) w[i] = *(const u32x4*)(w_mod + (size_t)(rg + 32 * i) * 3456 + col0 + c8 * 8);
#pragma unroll
    for (int j = 0; j < 3; ++j) { const int r = tid + 256 * j; if (r < 576) { const float x = bf1(c[r]); sc[r] = rbf(x * sigmoidf_(x)); } }
    __syncthreads();
    float acc[8] = {0, 0, 0, 0, 0, 0, 0, 0};
#pragma unroll
    for (int i = 0; i < 18; ++i) {
        const float s = sc[rg + 32 * i];
#pragma unroll
        for (int j = 0; j < 4; ++j) { acc[2 * j] += s * bf_lo(w[i][j]); acc[2 * j + 1] += s * bf_hi(w[i][j]); }
    }
#pragma unroll
    for (int j = 0; j < 8; ++j) {
        acc[j] += __shfl_xor(acc[j], 8); acc[j] += __shfl_xor(acc[j], 16); acc[j] += __shfl_xor(acc[j], 32);
    }
    if (lane < 8) {
#pragma unroll
        for (int j = 0; j < 8; ++j) red[wid * 64 + lane * 8 + j] = acc[j];
    }
    __syncthreads();
    if (tid < 64) {
        float m = rbf((red[tid] + red[64 + tid]) + (red[128 + tid] + red[192 + tid]));
        const int which = col0 / 576;
        if (which == 1 || which == 4) m = rbf(1.0f + m);
        __hip_atomic_store((u32*)(ws + WS_MOD) + col0 + tid, __float_as_uint(m), __ATOMIC_RELAXED, __HIP_MEMORY_SCOPE_AGENT);
    }
}

__device__ __forceinline__ const char* tile_base(int job, const u16* __restrict__ wg, const u16* __restrict__ wu) {
    const int ks = job % 18, mat = (job / 18) & 1, cg = job / 36;
    return (const char*)((mat ? wu : wg) + (size_t)(32 * ks) * F_ + cg * 576);
}
__device__ __forceinline__ void tile_dma(int job, const u16* __restrict__ wg, const u16* __restrict__ wu, char* tile, const u32 (&voff)[9]) {
    const int w = __builtin_amdgcn_readfirstlane(threadIdx.x >> 6);
    const char* base = tile_base(job, wg, wu) + (size_t)w * 8 * (F_ * 2);
    const u32 dst = (u32)(uintptr_t)tile + w * 9216;
#pragma unroll
    for (int i = 0; i < 9; ++i)
        asm volatile("s_mov_b32 m0, %2\n\ts_nop 0\n\tglobal_load_lds_dwordx4 %0, %1" :: "v"(voff[i]), "s"(base), "s"(dst + i * 1024) : "memory");
}
__device__ __forceinline__ void tile_out(int job, const char* tile, rsrc_t wsr) {
    const int lane = threadIdx.x & 63, w = threadIdx.x >> 6;
    const int ks = job % 18, mat = (job / 18) & 1, fc = 4 * (job / 36) + w;
    const int g = lane >> 4, q4 = (lane & 15) >> 2, p = lane & 3;
    const LAS char* rd = (const LAS char*)tile + (4 * g + q4) * 1152 + 8 * p + w * 288;
#pragma unroll
    for (int u = 0; u < 9; ++u) {
        const int pos = (fc & 1) ? (u == 0 ? 4 : u <= 4 ? u - 1 : u) : (u == 8 ? 4 : u < 4 ? u : u + 1);
        const s16x4 lo = __builtin_amdgcn_ds_read_tr16_b64_v4i16((LAS s16x4*)(rd + u * 32));
        const s16x4 hi = __builtin_amdgcn_ds_read_tr16_b64_v4i16((LAS s16x4*)(rd + u * 32 + 16 * 1152));
        typedef short s16x8 __attribute__((ext_vector_type(8)));
        const s16x8 r = __builtin_shufflevector(lo, hi, 0, 1, 2, 3, 4, 5, 6, 7);
        st16_dev(wsr, (u32)WS_WT1 + fc * 331776 + pos * 36864 + (ks * 2 + mat) * 1024 + lane * 16, __builtin_bit_cast(u32x4, r));
    }
}
__device__ __forceinline__ void tile_jobs(int i0, const u16* __restrict__ wg, const u16* __restrict__ wu, char* smem, char* ws, rsrc_t wsr, u32 epoch) {
    const int lane = threadIdx.x & 63;
    u32 voff[9];
#pragma unroll
    for (int i = 0; i < 9; ++i) { const int ci = lane + 64 * i, row = (ci * 58255) >> 22, c16 = ci - row * 72; voff[i] = (u32)(row * (F_ * 2) + c16 * 16); }
#pragma unroll
    for (int j = 0; j < 4; ++j) tile_dma(i0 + 64 * j, wg, wu, smem + j * 36864, voff);
#pragma unroll
    for (int j = 0; j < 4; ++j) {
        asm volatile("s_waitcnt vmcnt(27)" ::: "memory");
        __syncthreads();
        tile_out(i0 + 64 * j, smem + j * 36864, wsr);
    }
    if (i0 < 32) {
        __syncthreads();
        tile_dma(i0 + 256, wg, wu, smem, voff);
        asm volatile("s_waitcnt vmcnt(0)" ::: "memory");
        __syncthreads();
        tile_out(i0 + 256, smem, wsr);
    }
    DRAIN();
    __syncthreads();
    if (threadIdx.x < 5 && i0 + 64 * threadIdx.x < 288) flag_set(flag_ptr(ws, F_TILE + i0 + 64 * threadIdx.x), epoch);
}

constexpr int C_XR = 0;
constexpr int C_K0 = 4608;
constexpr int C_K1 = C_K0 + 4128 + 32;
constexpr int C_CH = C_K1 + 4160;
constexpr int C_RED = 3 * C_CH;
constexpr int C_LDS = C_RED + 3 * 32768;
static_assert(((C_K1 - C_K0) / 4) % 32 == 16, "bank offset");
static_assert(C_CH % 16 == 0 && C_K0 % 16 == 0 && C_K1 % 16 == 0, "align");
template <bool ZERO> __device__ __forceinline__ void mfma_v(f32x4& c, const bf16x8& a, const bf16x8& b) {
    if constexpr (ZERO) asm volatile("v_mfma_f32_16x16x32_bf16 %0, %1, %2, 0" : "=v"(c) : "v"(a), "v"(b));
    else asm volatile("v_mfma_f32_16x16x32_bf16 %0, %1, %2, %0" :: "v"(c), "v"(a), "v"(b));
}
struct ConvSet { bf16x8 A[8], B[8]; };
template <int J> __device__ __forceinline__ void conv_ld(ConvSet& s, const LAS char* ab, const LAS char* bb) {
    if constexpr (J < 8) {
        const LAS u32* p = (const LAS u32*)(ab + 512 * J);
        u32x4 t = {p[0], p[1], p[2], p[3]};
        s.A[J] = __builtin_bit_cast(bf16x8, t);
    } else s.B[J - 8] = *(const LAS bf16x8*)(bb + 512 * (15 - J));
}
template <int M> struct TriIdx {
    static constexpr int find(bool want_i) { int m = 0; for (int s = 0; s < 8; ++s) for (int j = 0; j <= s; ++j) { if (m == M) return want_i ? s : j; ++m; } return 0; }
    static constexpr int i = find(true), j = find(false);
};
template <int M, bool PF, bool FIRST> __device__ __forceinline__ void conv_slot(f32x4 (&acc)[8], const ConvSet& cur, ConvSet& nxt, const LAS char* ab, const LAS char* bb) {
    mfma_v<(FIRST && TriIdx<M>::j == 0)>(acc[TriIdx<M>::i], cur.A[TriIdx<M>::j], cur.B[TriIdx<M>::i - TriIdx<M>::j]);
    if constexpr (PF && (M & 1) == 0 && M / 2 < 16) conv_ld<((M / 2) & 1) * 8 + (M / 4)>(nxt, ab, bb);
    SCHED();
}
template <bool PF, bool FIRST, int... M> __device__ __forceinline__ void conv_step(f32x4 (&acc)[8], const ConvSet& cur, ConvSet& nxt, const LAS char* ab, const LAS char* bb, std::integer_sequence<int, M...>) {
    (conv_slot<M, PF, FIRST>(acc, cur, nxt, ab, bb), ...);
}
template <int... J> __device__ __forceinline__ void conv_ld_all(ConvSet& s, const LAS char* ab, const LAS char* bb, std::integer_sequence<int, J...>) { (conv_ld<J>(s, ab, bb), ...); }

struct ConvIn { u32x4 k; u32 kn; u16 dv; };
__device__ __forceinline__ void conv_load_k(ConvIn& in, int d, const u16* __restrict__ ssm_k, const u16* __restrict__ ssm_D) {
    const int tid = threadIdx.x;
    in.k = *(const u32x4*)(ssm_k + (size_t)d * S_ + tid * 8);
    in.kn = *(const u32*)(ssm_k + (size_t)d * S_ + (tid < 255 ? tid * 8 + 8 : 0));
    in.dv = ssm_D[d];
}
__device__ __forceinline__ void conv_put_k(const ConvIn& in, char* sm) {
    const int tid = threadIdx.x;
    const u32 kn = tid < 255 ? in.kn : 0u;
    *(u32x4*)(sm + C_K0 + 32 + tid * 16) = in.k;
    *(u32x4*)(sm + C_K1 + 32 + tid * 16) = (u32x4){__builtin_amdgcn_alignbit(in.k[1], in.k[0], 16), __builtin_amdgcn_alignbit(in.k[2], in.k[1], 16),
                                                 __builtin_amdgcn_alignbit(in.k[3], in.k[2], 16), __builtin_amdgcn_alignbit(kn, in.k[3], 16)};
    if (tid < 32) *(u32x4*)(sm + C_XR + 4096 + tid * 16) = (u32x4){0, 0, 0, 0};
    if (tid == 0) {
        *(u32x4*)(sm + C_K0) = (u32x4){0, 0, 0, 0}; *(u32x4*)(sm + C_K0 + 16) = (u32x4){0, 0, 0, 0};
        *(u32x4*)(sm + C_K1) = (u32x4){0, 0, 0, 0}; *(u32x4*)(sm + C_K1 + 16) = (u32x4){0, 0, 0, in.k[0] << 16};
    }
}
__device__ __forceinline__ void conv_finish(int d, const char* sm, const char* red, float Dv, rsrc_t wsr) {
    const int tid = threadIdx.x;
    const int i = tid >> 5, n = tid & 15, gp = (tid >> 4) & 1;
    f32x4 lo = {0.f, 0.f, 0.f, 0.f}, hi = {0.f, 0.f, 0.f, 0.f};
#pragma unroll
    for (int w = 0; w < 4; ++w) {
        lo += *(const f32x4*)(red + (w * 8 + i) * 1024 + (n + 32 * gp) * 16);
        hi += *(const f32x4*)(red + (w * 8 + i) * 1024 + (n + 32 * gp + 16) * 16);
    }
    const int s = 256 * i + 16 * n + 8 * gp;
    const u32x4 xw = *(const u32x4*)(sm + C_XR + 2 * (2040 - s));
    const float o0 = rbf(lo[0]) + rbf(bf_hi(xw[3]) * Dv), o1 = rbf(lo[1]) + rbf(bf_lo(xw[3]) * Dv), o2 = rbf(lo[2]) + rbf(bf_hi(xw[2]) * Dv), o3 = rbf(lo[3]) + rbf(bf_lo(xw[2]) * Dv);
    const float o4 = rbf(hi[0]) + rbf(bf_hi(xw[1]) * Dv), o5 = rbf(hi[1]) + rbf(bf_lo(xw[1]) * Dv), o6 = rbf(hi[2]) + rbf(bf_hi(xw[0]) * Dv), o7 = rbf(hi[3]) + rbf(bf_lo(xw[0]) * Dv);
    st16_dev(wsr, (u32)WS_YYT + (u32)(d * S_ + s) * 2, (u32x4){pk2(o0, o1), pk2(o2, o3), pk2(o4, o5), pk2(o6, o7)});
}
template <class Hook>
__device__ __forceinline__ void conv_body(int ch0, const ConvIn (&in)[3], char* smem, rsrc_t wsr, Hook hook
                                          ) {
    const int lane = threadIdx.x & 63, wid = __builtin_amdgcn_readfirstlane(threadIdx.x >> 6);
    __syncthreads();
    STAMP_AT(3);
    const int n = lane & 15, g = lane >> 4, dd = g >> 1, gh = g & 1;
    const LAS char* l0 = (const LAS char*)smem;
    const LAS char* bb = l0 + C_XR + 480 - 32 * n + 32 * dd + 16 * gh + 128 * wid;
    const LAS char* ab = l0 + ((n & 1) ? C_K0 + 2 * (16 * dd + 8 * gh + n + 1) : C_K1 + 2 * (16 * dd + 8 * gh + n)) + 128 * wid;
    f32x4 acc[3][8];
    ConvSet s0, s1;
    conv_ld_all(s0, ab, bb, std::make_integer_sequence<int, 16>{});
    SCHED();
    constexpr auto SQ = std::make_integer_sequence<int, 36>{};
    conv_step<true, true>(acc[0], s0, s1, ab + 64, bb + 64, SQ);
    conv_step<true, false>(acc[0], s1, s0, ab + C_CH, bb + C_CH, SQ);
    conv_step<true, true>(acc[1], s0, s1, ab + C_CH + 64, bb + C_CH + 64, SQ);
    conv_step<true, false>(acc[1], s1, s0, ab + 2 * C_CH, bb + 2 * C_CH, SQ);
    conv_step<true, true>(acc[2], s0, s1, ab + 2 * C_CH + 64, bb + 2 * C_CH + 64, SQ);
    conv_step<false, false>(acc[2], s1, s0, ab, bb, SQ);
    asm volatile("s_nop 7" ::: "memory");
#pragma unroll
    for (int c = 0; c < 3; ++c)
#pragma unroll
        for (int i = 0; i < 8; ++i) asm volatile("" : "+v"(acc[c][i]));
    STAMP_AT(4);
    hook();
#pragma unroll
    for (int c = 0; c < 3; ++c)
#pragma unroll
        for (int i = 0; i < 8; ++i) *(f32x4*)(smem + C_RED + c * 32768 + (wid * 8 + i) * 1024 + lane * 16) = acc[c][i];
    __syncthreads();
#pragma unroll
    for (int c = 0; c < 3; ++c) conv_finish(ch0 + c, smem + c * C_CH, smem + C_RED + c * 32768, bf1(in[c].dv), wsr);
}

constexpr int R_MOD = 147456;
constexpr int R_YT = 110592;
constexpr int R_HT = R_YT + 8 * 1168;
struct RowsPre { float x[18], gh[18]; u32 xw[9]; };
__device__ __forceinline__ void rows_load(RowsPre& r, int t0, const u16* __restrict__ q) {
    const int tid = threadIdx.x, lane = tid & 63, hl = lane & 31, row = 2 * (tid >> 6) + (lane >> 5);
    const u32* qrow = (const u32*)(q + (size_t)(t0 + row) * D_);
#pragma unroll
    for (int i = 0; i < 9; ++i) r.xw[i] = qrow[hl + 32 * i];
}
struct ModIn { u32x4 mw[4]; };
__device__ __forceinline__ void mod_load(ModIn& m, rsrc_t wsr) {
#pragma unroll
    for (int i = 0; i < 4; ++i) { const int j = threadIdx.x + 256 * i; m.mw[i] = ld16_dev(wsr, (u32)WS_MOD + (j < 864 ? j : 0) * 16); }
}
__device__ __forceinline__ void mod_put(const ModIn& m, char* sm) {
#pragma unroll
    for (int i = 0; i < 4; ++i) { const int j = threadIdx.x + 256 * i; if (j < 864) *(u32x4*)(sm + R_MOD + j * 16) = m.mw[i]; }
}
__device__ __forceinline__ void rows_pre(RowsPre& r, const char* sm) {
    const int hl = threadIdx.x & 31;
#pragma unroll
    for (int i = 0; i < 9; ++i) { r.x[2 * i] = bf_lo(r.xw[i]); r.x[2 * i + 1] = bf_hi(r.xw[i]); }
    float s = 0.f;
#pragma unroll
    for (int i = 0; i < 18; ++i) s += r.x[i];
    const float mean = half_sum(s) * (1.0f / 576.0f);
    float vs = 0.f;
#pragma unroll
    for (int i = 0; i < 18; ++i) { const float dl = r.x[i] - mean; vs += dl * dl; }
    const float rstd = __builtin_amdgcn_rsqf(half_sum(vs) * (1.0f / 576.0f) + 1e-5f);
    const float* mod = (const float*)(sm + R_MOD);
#pragma unroll
    for (int i = 0; i < 9; ++i) {
        const int c = 2 * (hl + 32 * i);
        const f32x2 sa = *(const f32x2*)(mod + c), c1 = *(const f32x2*)(mod + 576 + c), ga = *(const f32x2*)(mod + 2 * 576 + c);
        r.gh[2 * i] = ga[0] * ((r.x[2 * i] - mean) * rstd * c1[0] + sa[0]);
        r.gh[2 * i + 1] = ga[1] * ((r.x[2 * i + 1] - mean) * rstd * c1[1] + sa[1]);
    }
}
__device__ __forceinline__ void rows_fetch(int t0, char* sm, const char* ws) {
    const int lane = threadIdx.x & 63;
    const char* src = ws + WS_YYT + t0 * 2;
    const u32 dst = (u32)(uintptr_t)(sm + R_HT);
#pragma unroll
    for (int i = 0; i < 9; ++i)
        asm volatile("s_mov_b32 m0, %2\n\ts_nop 0\n\tglobal_load_lds_dwordx4 %0, %1 sc1" :: "v"((u32)((lane + 64 * i) * (S_ * 2))), "s"(src), "s"(dst + i * 1024) : "memory");
    asm volatile("s_waitcnt vmcnt(0)" ::: "memory");
}
__device__ __forceinline__ void rows_main(const RowsPre& r, int t0, char* sm, rsrc_t wsr) {
    const int tid = threadIdx.x, lane = tid & 63, hl = lane & 31, row = 2 * (tid >> 6) + (lane >> 5);
    const int cg = tid >> 1, th = tid & 1;
    if (tid < 144) {
        u32x2 yw[8];
#pragma unroll
        for (int c = 0; c < 8; ++c) yw[c] = *(const u32x2*)(sm + R_HT + (8 * cg + c) * 16 + th * 8);
#pragma unroll
        for (int t = 0; t < 4; ++t) {
            u32x4 o;
#pragma unroll
            for (int kk = 0; kk < 4; ++kk) {
                const u32 a = yw[2 * kk][t >> 1], bq = yw[2 * kk + 1][t >> 1];
                o[kk] = (t & 1) ? __builtin_amdgcn_perm(bq, a, 0x07060302u) : __builtin_amdgcn_perm(bq, a, 0x05040100u);
            }
            *(u32x4*)(sm + R_YT + (4 * th + t) * 1168 + cg * 16) = o;
        }
    }
    __syncthreads();
    const float* mod = (const float*)(sm + R_MOD);
    float x1[18];
#pragma unroll
    for (int i = 0; i < 9; ++i) {
        u32* yp = (u32*)(sm + R_YT + row * 1168 + (hl + 32 * i) * 4);
        const u32 yv = *yp;
        const u32 p = pk2(r.x[2 * i] + r.gh[2 * i] * bf_lo(yv), r.x[2 * i + 1] + r.gh[2 * i + 1] * bf_hi(yv));
        *yp = p;
        x1[2 * i] = bf_lo(p); x1[2 * i + 1] = bf_hi(p);
    }
    float s = 0.f;
#pragma unroll
    for (int i = 0; i < 18; ++i) s += x1[i];
    const float mean = half_sum(s) * (1.0f / 576.0f);
    float vs = 0.f;
#pragma unroll
    for (int i = 0; i < 18; ++i) { const float dl = x1[i] - mean; vs += dl * dl; }
    const float rstd = __builtin_amdgcn_rsqf(half_sum(vs) * (1.0f / 576.0f) + 1e-5f);
#pragma unroll
    for (int i = 0; i < 9; ++i) {
        const int c = 2 * (hl + 32 * i);
        const f32x2 sb = *(const f32x2*)(mod + 3 * 576 + c), c1 = *(const f32x2*)(mod + 4 * 576 + c);
        const float h0 = (x1[2 * i] - mean) * rstd * c1[0] + sb[0], h1 = (x1[2 * i + 1] - mean) * rstd * c1[1] + sb[1];
        const int ks = c >> 5, kk = c & 31, gq = (kk & 15) >> 2, j = ((kk >> 4) << 2) | (kk & 3);
        *(u32*)(sm + R_HT + ((ks * 4 + gq) * 8 + row) * 16 + j * 2) = pk2(h0, h1);
    }
    __syncthreads();
    const u32 h2t = (u32)WS_H2T + (u32)(t0 >> 4) * 18 * 1024 + (t0 & 15) * 16, x1g = (u32)WS_X1 + (u32)t0 * D_ * 2;
#pragma unroll
    for (int it = 0; it < 3; ++it) {
        const int i = tid + 256 * it;
        if (i < 576) {
            const int kg = i >> 3, rr8 = i & 7;
            st16_dev(wsr, h2t + (kg >> 2) * 1024 + (kg & 3) * 256 + rr8 * 16, *(const u32x4*)(sm + R_HT + i * 16));
            const int rr = i / 72, cc = i - rr * 72;
            st16_dev(wsr, x1g + rr * 1152 + cc * 16, *(const u32x4*)(sm + R_YT + rr * 1168 + cc * 16));
        }
    }
}

namespace mlp {
constexpr int STG = 36864, LDS_BYTES = 163840;
constexpr int G1S = 9 * 144, G2S = 18 * 36, NS = G1S + G2S;
constexpr int LA = 4;
constexpr int PRO = 9 + 4 * LA + 18;
constexpr int TUS = 147456;

constexpr int F01 = 288;
constexpr int stage_of(int gs) { return gs < F01 ? 0 : gs < G1S ? gs / 144 : 9 + (gs - G1S) / 72; }
constexpr int rel_of(int gs) { return gs < F01 ? gs : gs < G1S ? gs % 144 : (gs - G1S) % 72; }
constexpr int grp_of(int gs) { return rel_of(gs) >> 2; }
constexpr int t_of(int gs) { return rel_of(gs) & 3; }
constexpr bool barpt(int gs) { return gs >= F01 && gs < NS && t_of(gs) == 3 && grp_of(gs) == (gs < G1S ? 3 : 0) && stage_of(gs) < 17; }
constexpr int dma_id(int gs) {
    if (gs >= NS || t_of(gs) != 3) return -1;
    const int g = grp_of(gs), s = stage_of(gs);
    if (gs < F01) return (g >= 36 && g < 72 && (g & 3) == 3) ? 3 * 16 + (g - 36) / 4 : -1;
    if (s == 2) return (g >= 4 && g < 22) ? ((g & 1) ? 5 : 4) * 16 + (g - 4) / 2 : -1;
    if (s + 3 > 17) return -1;
    if (s == 8) return (g >= 4 && g < 13) ? 11 * 16 + g - 4 : -1;
    if (gs < G1S) { constexpr int G[9] = {5, 7, 9, 12, 14, 16, 20, 22, 24}; for (int i = 0; i < 9; ++i) if (G[i] == g) return (s + 3) * 16 + i; return -1; }
    return (g & 1) ? (s + 3) * 16 + (g >> 1) : -1;
}
constexpr int bload(int gs) {
    if (gs >= F01 || t_of(gs) != 1) return -1;
    const int ks = 2 * (gs / 32) + LA + (((gs % 32) >> 2) >> 2), i = (gs >> 2) & 3;
    return ks < 18 ? ks * 4 + i : -1;
}
constexpr bool unit_stores(int v) { return v == 1 || v == 3 || v == 4 || v == 6 || v == 8; }
constexpr int unit_end(int v) { return v < 2 ? F01 : 144 * (v + 1); }
constexpr int hload(int gs) {
    if (t_of(gs) != 3) return -1;
    const int s = stage_of(gs), g = grp_of(gs);
    if (s == 8) return (g >= 13 && g < 33) ? g - 13 : -1;
    if (s < 9 || g < 2 || g > 16 || (g & 1)) return -1;
    const int i = (g - 2) >> 1;
    if (s == 9) return 20 + i;
    if (s == 10) return 28 + i;
    if (s == 11) return i < 4 ? 36 + i : -1;
    if (s >= 12 && s <= 15) return 40 + 8 * (s - 12) + i;
    return -1;
}
constexpr int pub(int gs) { return (stage_of(gs) == 6 && grp_of(gs) == 27 && t_of(gs) == 3) ? 1 : (stage_of(gs) == 10 && grp_of(gs) == 9 && t_of(gs) == 1) ? 2 : 0; }
constexpr int poll_issue(int gs) { return (stage_of(gs) == 7 && grp_of(gs) == 27 && t_of(gs) == 3) ? 1 : (stage_of(gs) == 11 && grp_of(gs) == 9 && t_of(gs) == 1) ? 2 : 0; }
struct EpiSched { signed char ev[G1S], ep[G1S], sv[G1S], st[G1S]; };
constexpr EpiSched make_epi() {
    EpiSched e{};
    int v = 0, p = 0, pend_v = -1, pend_lo = 0, pend_hi = 0;
    for (int gs = 0; gs < G1S; ++gs) {
        e.ev[gs] = -1; e.ep[gs] = -1; e.sv[gs] = -1; e.st[gs] = -1;
        if (gs < F01) continue;
        const int t = t_of(gs);
        if (t == 0) continue;
        const bool vm = barpt(gs) || dma_id(gs) >= 0 || hload(gs) >= 0 || pub(gs) || poll_issue(gs);
        if (t == 3 && !vm && pend_lo < pend_hi) { e.sv[gs] = (signed char)pend_v; e.st[gs] = (signed char)pend_lo; ++pend_lo; continue; }
        if (t == 3 && vm) continue;
        if (v < 8 && gs >= unit_end(v) + 8 && (pend_lo == pend_hi || pend_v == v || !unit_stores(v) || true)) {
            e.ev[gs] = (signed char)v; e.ep[gs] = (signed char)p;
            ++p;
            if ((p & 15) == 0 && unit_stores(v)) { if (pend_v != v) { pend_v = v; pend_lo = p / 16 - 1; } pend_hi = p / 16; }
            if (p == 64) { p = 0; ++v; }
        }
    }
    return e;
}
constexpr EpiSched ES = make_epi();
constexpr int hstore(int gs) { return gs < G1S ? ES.st[gs] : -1; }
constexpr int epi_last(int v) { for (int gs = G1S - 1; gs >= 0; --gs) if (ES.ev[gs] == v) return gs; return -1; }
constexpr int store_last(int v) { for (int gs = G1S - 1; gs >= 0; --gs) if (ES.sv[gs] == v && ES.st[gs] == 3) return gs; return -1; }
constexpr bool epi_ok() {
    for (int v = 0; v < 8; ++v) {
        if (epi_last(v) < 0 || ES.ep[epi_last(v)] != 63) return false;
        if (v + 3 <= 8 && epi_last(v) >= 144 * (v + 3)) return false;
        if (unit_stores(v) && store_last(v) < 0) return false;
    }
    return store_last(4) + 40 < 6 * 144 + 27 * 4;
}
static_assert(epi_ok(), "epilogue schedule");
constexpr int vm_ops(int gs) { return (pub(gs) ? 1 : 0) + (poll_issue(gs) ? 1 : 0) + (hstore(gs) >= 0) + (bload(gs) >= 0) + (hload(gs) >= 0) + (dma_id(gs) >= 0); }
struct Ledger { int cum[NS + 1]; };
constexpr Ledger make_ledger() {
    Ledger l{}; int n = PRO;
    for (int gs = 0; gs < NS; ++gs) { if (gs == G1S) n += 4; l.cum[gs] = n; n += vm_ops(gs); }
    l.cum[NS] = n; return l;
}
constexpr Ledger LG = make_ledger();
constexpr int seq_in(int gs, int kind) {
    int n = LG.cum[gs];
    if (kind > 0 && pub(gs)) ++n;
    if (kind > 1 && poll_issue(gs)) ++n;
    if (kind > 2 && hstore(gs) >= 0) ++n;
    if (kind > 3 && bload(gs) >= 0) ++n;
    if (kind > 4 && hload(gs) >= 0) ++n;
    return n;
}
constexpr int seq_dma_last(int s) {
    if (s <= 2) return 9 * s + 8;
    for (int gs = 0; gs < NS; ++gs) if (dma_id(gs) == s * 16 + 8) return seq_in(gs, 5);
    return -1;
}
constexpr int seq_b(int ks) {
    if (ks < LA) return 27 + 4 * ks + 3;
    for (int gs = 0; gs < F01; ++gs) if (bload(gs) == ks * 4 + 3) return seq_in(gs, 3);
    return -1;
}
constexpr int seq_h(int kk) {
    for (int gs = 8 * 144; gs < NS; ++gs) if (hload(gs) == kk * 4 + 3) return seq_in(gs, 4);
    return -1;
}
constexpr int seq_poll(int part) {
    for (int gs = 0; gs < NS; ++gs) if (poll_issue(gs) == part) return seq_in(gs, 1);
    return -1;
}
constexpr int seq_store_last(int part) {
    if (part == 2) return LG.cum[G1S] - 1;
    return seq_in(store_last(4), 2);
}
struct Work { int unit, mat, k, t, n; };
constexpr Work work_of(int gs) {
    if (gs < F01) { const int p = gs / 32, um = (gs % 32) / 8, r = gs % 8; return {um >> 1, um & 1, 2 * p + (r & 1), r >> 1, 8 * p + 2 * um + (r & 1)}; }
    if (gs < G1S) { const int u = gs / 144, bi = (gs % 144) / 24, r = gs % 24; return {u, bi & 1, 6 * (bi >> 1) + r % 6, r / 6, 36 * u + 6 * bi + r % 6}; }
    const int s = (gs - G1S) / 72, r = (gs - G1S) % 72, du = r / 8, r8 = r % 8;
    return {du, 0, 2 * s + (r8 & 1), r8 >> 1, 324 + 18 * s + 2 * du + (r8 & 1)};
}
constexpr int NFRAG = 486, RQ = 12, FR0 = 8;
constexpr int frag_first(int n) {
    if (n < 72) return 32 * (n / 8) + 8 * ((n % 8) >> 1) + (n & 1);
    if (n < 324) return 144 * (n / 36) + 24 * ((n % 36) / 6) + n % 6;
    return G1S + 72 * ((n - 324) / 18) + 8 * (((n - 324) % 18) >> 1) + (n & 1);
}
constexpr int frag_last(int n) { return frag_first(n) + ((n >= 72 && n < 324) ? 18 : 6); }
constexpr int frag_stage(int n) { return n < 72 ? (n % 8) >> 2 : n < 324 ? n / 36 : 9 + (n - 324) / 18; }
constexpr int frag_off(int n) {
    if (n < 72) return (2 * (2 * (n / 8) + (n & 1)) + (((n % 8) >> 1) & 1)) * 1024;
    if (n < 324) { const int bi = (n % 36) / 6; return (2 * (6 * (bi >> 1) + n % 6) + (bi & 1)) * 1024; }
    return (n & 1) * 9216 + (((n - 324) % 18) >> 1) * 32;
}
constexpr int stage_ready(int s) {
    if (s <= 2) return 0;
    for (int gs = 0; gs < NS; ++gs) if (barpt(gs) && stage_of(gs) == s - 1) return gs + 1;
    return NS;
}
struct PfSched { short id[NS]; };
constexpr PfSched make_pf() {
    PfSched t{};
    int m = FR0, rdy = stage_ready(frag_stage(FR0));
    for (int gs = 0; gs < NS; ++gs) {
        t.id[gs] = -1;
        if ((gs & 3) != 0 || m >= NFRAG) continue;
        if (m >= RQ && frag_last(m - RQ) >= gs) continue;
        if (rdy > gs) continue;
        t.id[gs] = (short)m;
        ++m;
        if (m < NFRAG && frag_stage(m) != frag_stage(m - 1)) rdy = stage_ready(frag_stage(m));
    }
    return t;
}
constexpr PfSched PF = make_pf();
constexpr bool pf_ok() {
    int cnt = FR0;
    for (int gs = 0; gs < NS; ++gs) if (PF.id[gs] >= 0) { if (PF.id[gs] != cnt || frag_first(cnt) - gs < 8) return false; ++cnt; }
    return cnt == NFRAG;
}
static_assert(pf_ok(), "fragment prefetch schedule");
constexpr int clampv(int n) { return n > 63 ? 63 : (n < 0 ? 0 : n); }
constexpr int wait_for(int gs, int seq) { return clampv(LG.cum[gs] - seq - 1); }

template <int N> __device__ __forceinline__ void wait_vm() { asm volatile("s_waitcnt vmcnt(%0)" :: "n"(N) : "memory"); }

struct Ctx {
    bf16x8 Bv[2][4];
    bf16x8 Aq[12];
    f32x4 ac[3][2][4];
    f32x4 a2[9][4];
    float e0, e1;
    u32x4 hp[4];
    u32 vo1[4], vo2[4], vo3[4];
    u32 lane16, epoch;
    u32x4 pollv;
    const LAS char* rd[4]; const LAS char* rd2[4];
    const char* w1src; const char* w2src; const char* bsrc; const char* hsrc;
    u32 voff2[9];
    int sub;
    u32 dmadst;
    u32 hso;
    int jj;
    __amdgpu_buffer_rsrc_t hrs;
    u32* flag_pub; u32* flag_poll;
};

template <int KS, int T, bool ZERO> __device__ __forceinline__ void mfma(Ctx& cx, f32x4& c, const bf16x8& a) {
    constexpr int r = (KS * 4 + T) * 4;
    if constexpr (KS < 16) {
        if constexpr (ZERO) asm volatile("v_mfma_f32_16x16x32_bf16 %0, %1, a[%c2:%c3], 0" : "=v"(c) : "v"(a), "n"(r), "n"(r + 3));
        else asm volatile("v_mfma_f32_16x16x32_bf16 %0, %1, a[%c2:%c3], %0" :: "v"(c), "v"(a), "n"(r), "n"(r + 3));
    } else {
        asm volatile("v_mfma_f32_16x16x32_bf16 %0, %1, %2, %0" :: "v"(c), "v"(a), "v"(cx.Bv[KS - 16][T]));
    }
}
template <int KS, int T, bool SC1> __device__ __forceinline__ void gload(Ctx& cx, u32 voff, const char* sb) {
    constexpr int r = (KS * 4 + T) * 4, off = (KS & 3) * 1024;
    if constexpr (KS < 16) {
        if constexpr (SC1) asm volatile("global_load_dwordx4 a[%c2:%c3], %0, %1 offset:%4 sc1" :: "v"(voff), "s"(sb), "n"(r), "n"(r + 3), "n"(off) : "memory");
        else asm volatile("global_load_dwordx4 a[%c2:%c3], %0, %1 offset:%4" :: "v"(voff), "s"(sb), "n"(r), "n"(r + 3), "n"(off) : "memory");
    } else {
        if constexpr (SC1) asm volatile("global_load_dwordx4 %0, %1, %2 offset:%3 sc1" : "=v"(cx.Bv[KS - 16][T]) : "v"(voff), "s"(sb), "n"(off) : "memory");
        else asm volatile("global_load_dwordx4 %0, %1, %2 offset:%3" : "=v"(cx.Bv[KS - 16][T]) : "v"(voff), "s"(sb), "n"(off) : "memory");
    }
}
template <int S, int P> __device__ __forceinline__ void dma_m0(const Ctx& c) {
    asm volatile("s_mov_b32 m0, %0" :: "s"(c.dmadst + (u32)((S & 3) * STG + (P >> 2) * 4096)) : "memory");
}
constexpr int nat_kstep(int ks2) {
    if (ks2 < 10) { constexpr int T[5] = {0, 1, 5, 6, 4}; return 9 * (ks2 / 5) + T[ks2 % 5]; }
    const int jj = (ks2 - 10) >> 1, h = (ks2 - 10) & 1;
    return 9 * (jj >> 1) + ((jj & 1) ? 7 : 2) + h;
}
template <int S, int P> __device__ __forceinline__ void dma_go(const Ctx& c) {
    if constexpr (S < 9) {
        const char* sb = c.w1src + (size_t)S * STG + (P >> 2) * 4096;
        asm volatile("global_load_lds_dwordx4 %0, %1 offset:%2 sc1" :: "v"(c.lane16), "s"(sb), "n"((P & 3) * 1024) : "memory");
    } else {
        constexpr int n0 = nat_kstep(2 * (S - 9)), n1 = nat_kstep(2 * (S - 9) + 1);
        const char* sb = c.w2src + (size_t)(c.sub ? n1 : n0) * (32 * D_ * 2);
        asm volatile("global_load_lds_dwordx4 %0, %1 offset:%2" :: "v"(c.voff2[P]), "s"(sb), "n"((P & 3) * 1024) : "memory");
    }
}
template <int ID> __device__ __forceinline__ void load_b(Ctx& c) {
    constexpr int ks = ID >> 2, t = ID & 3;
    gload<ks, t, true>(c, c.vo1[t], c.bsrc + (ks >> 2) * 4096);
}
template <int ID> __device__ __forceinline__ void load_h(Ctx& c) {
    constexpr int kk = ID >> 2, t = ID & 3;
    gload<kk, t, true>(c, c.vo3[t], c.hsrc + (kk >> 2) * 4096);
}
template <int KS> __device__ __forceinline__ void pin_b(Ctx& c) {
#pragma unroll
    for (int t = 0; t < 4; ++t) { if constexpr (KS >= 16) asm volatile("" : "+v"(c.Bv[KS - 16][t])); }
}
template <int V, int P> __device__ __forceinline__ void epi_piece(Ctx& c) {
    constexpr int pr = P >> 3, st = P & 7, t = pr >> 1, r = 2 * (pr & 1), par = V % 3;
    if constexpr (st == 0) {
        asm volatile("v_mul_f32 %0, 0xbfb8aa3b, %1" : "=v"(c.e0) : "v"(c.ac[par][0][t][r]));
        asm volatile("v_mul_f32 %0, 0xbfb8aa3b, %1" : "=v"(c.e1) : "v"(c.ac[par][0][t][r + 1]));
    } else if constexpr (st == 1) asm volatile("v_exp_f32 %0, %0" :: "v"(c.e0));
    else if constexpr (st == 2) asm volatile("v_exp_f32 %0, %0" :: "v"(c.e1));
    else if constexpr (st == 3) {
        asm volatile("v_add_f32 %0, 1.0, %0" :: "v"(c.e0));
        asm volatile("v_add_f32 %0, 1.0, %0" :: "v"(c.e1));
    } else if constexpr (st == 4) asm volatile("v_rcp_f32 %0, %0" :: "v"(c.e0));
    else if constexpr (st == 5) asm volatile("v_rcp_f32 %0, %0" :: "v"(c.e1));
    else if constexpr (st == 6) {
        asm volatile("v_mul_f32 %0, %0, %1" :: "v"(c.e0), "v"(c.ac[par][1][t][r]));
        asm volatile("v_mul_f32 %0, %0, %1" :: "v"(c.e1), "v"(c.ac[par][1][t][r + 1]));
    } else {
        constexpr bool first = (V == 0 || V == 2 || V == 5 || V == 7);
        u32 o;
        asm volatile("v_cvt_pk_bf16_f32 %0, %1, %2" : "=v"(o) : "v"(c.e0), "v"(c.e1));
        c.hp[t][(first ? 0 : 2) + (pr & 1)] = o;
    }
}
template <int V, int T> __device__ __forceinline__ void store_h(Ctx& c) {
    const int m5 = 5 * (c.jj >> 1), ab = c.jj & 1;
    if constexpr (V == 4) {
        const u32x2 d = {c.hp[T][2], c.hp[T][3]};
        __builtin_amdgcn_raw_buffer_store_b64(d, c.hrs, c.vo2[T] + 8 * ab, c.hso + (m5 + 4) * 1024, 16);
    } else {
        const int kstep = (V == 1) ? m5 + 2 * ab : (V == 3) ? m5 + 2 * ab + 1 : (V == 6) ? 10 + 2 * c.jj : 11 + 2 * c.jj;
        __builtin_amdgcn_raw_buffer_store_b128(c.hp[T], c.hrs, c.vo2[T], c.hso + kstep * 1024, 16);
    }
}
template <int PART> __device__ __forceinline__ void poll_check(Ctx& c) {
    asm volatile("" : "+v"(c.pollv));
    const u32 e = c.epoch;
    if (!__all(c.pollv[0] == e && c.pollv[1] == e && c.pollv[2] == e && c.pollv[3] == e)) {
        const u32* p = c.flag_poll + (PART - 1) * 1024 + ((c.lane16 >> 4) & 3);
        poll_eq(p, e);
    }
}
template <int V, int P> __device__ __forceinline__ void epi_piece_padded(Ctx& c) {
    epi_piece<V, P>(c);
    if constexpr ((P & 7) == 2 || (P & 7) == 5) asm volatile("s_nop 0");
}
template <int V, int... P> __device__ __forceinline__ void epi_all(Ctx& c, std::integer_sequence<int, P...>) { (epi_piece_padded<V, P>(c), ...); }

template <int GS> __device__ __forceinline__ void slot(Ctx& c) {
    constexpr bool g1 = GS < G1S;
    constexpr int S = stage_of(GS), R = rel_of(GS), grp = R >> 2, t = R & 3;
    constexpr Work W = work_of(GS);
    constexpr bool f01 = GS < F01;
    if constexpr (GS == G1S) {
        asm volatile("s_nop 7" ::: "memory");
        epi_all<8>(c, std::make_integer_sequence<int, 64>{});
        store_h<8, 0>(c); store_h<8, 1>(c); store_h<8, 2>(c); store_h<8, 3>(c);
        SCHED();
    }
    if constexpr (f01 && GS % 32 == 0) { wait_vm<wait_for(GS, seq_b(2 * (GS / 32) + 1))>(); pin_b<2 * (GS / 32)>(c); pin_b<2 * (GS / 32) + 1>(c); }
    if constexpr (GS == 8 * 144) { wait_vm<wait_for(GS, seq_poll(1))>(); poll_check<1>(c); }
    if constexpr (!g1 && R == 0) { wait_vm<wait_for(GS, seq_h(2 * (S - 9) + 1))>(); pin_b<2 * (S - 9)>(c); pin_b<2 * (S - 9) + 1>(c); }
    if constexpr (dma_id(GS) >= 0) dma_m0<dma_id(GS) / 16, dma_id(GS) % 16>(c);
    if constexpr (g1) mfma<W.k, W.t, (W.k == 0)>(c, c.ac[W.unit % 3][W.mat][W.t], c.Aq[W.n % RQ]);
    else mfma<W.k, W.t, (W.k == 0)>(c, c.a2[W.unit][W.t], c.Aq[W.n % RQ]);
    if constexpr (barpt(GS)) {
        constexpr int n1 = wait_for(GS, seq_dma_last(S + 1));
        constexpr int n2 = (S == 12) ? wait_for(GS, seq_poll(2)) : 63;
        wait_vm<(n1 < n2 ? n1 : n2)>();
        if constexpr (S == 12) poll_check<2>(c);
        __builtin_amdgcn_s_barrier();
        asm volatile("" ::: "memory");
    }
    if constexpr (pub(GS)) {
        wait_vm<wait_for(GS, seq_store_last(pub(GS)))>();
        if (c.lane16 == 0) flag_set(c.flag_pub + (pub(GS) - 1) * 1024, c.epoch);
    }
    if constexpr (poll_issue(GS)) asm volatile("global_load_dwordx4 %0, %1, %2 sc1" : "=v"(c.pollv) : "v"(0u), "s"(c.flag_poll + (poll_issue(GS) - 1) * 1024) : "memory");
    {
        constexpr int f = PF.id[GS], fh = (GS & 3) == 1 ? PF.id[GS - ((GS & 3) == 1 ? 1 : 0)] : -1;
        if constexpr (f >= 0 && f < 324) c.Aq[f % RQ] = *(const LAS bf16x8*)(c.rd[frag_stage(f) & 3] + frag_off(f));
        constexpr int ft = f >= 324 ? f : fh >= 324 ? fh : -1;
        if constexpr (ft >= 0) {
            constexpr int half = f >= 324 ? 0 : 1;
            const s16x4 h = __builtin_amdgcn_ds_read_tr16_b64_v4i16((LAS s16x4*)(c.rd2[frag_stage(ft) & 3] + frag_off(ft) + half * (16 * 288)));
            const u32x2 hh = __builtin_bit_cast(u32x2, h);
            u32x4 a = __builtin_bit_cast(u32x4, c.Aq[ft % RQ]);
            a[2 * half] = hh[0]; a[2 * half + 1] = hh[1];
            c.Aq[ft % RQ] = __builtin_bit_cast(bf16x8, a);
        }
    }
    if constexpr (g1) {
        if constexpr (ES.ev[g1 ? GS : 0] >= 0) epi_piece<ES.ev[g1 ? GS : 0], ES.ep[g1 ? GS : 0]>(c);
        if constexpr (ES.sv[g1 ? GS : 0] >= 0) store_h<ES.sv[g1 ? GS : 0], ES.st[g1 ? GS : 0]>(c);
    }
    if constexpr (bload(GS) >= 0) load_b<bload(GS)>(c);
    if constexpr (hload(GS) >= 0) load_h<hload(GS)>(c);
    if constexpr (dma_id(GS) >= 0) dma_go<dma_id(GS) / 16, dma_id(GS) % 16>(c);
    SCHED();
}
template <int BASE, int... I> __device__ __forceinline__ void run_slots(Ctx& c, std::integer_sequence<int, I...>) { (slot<BASE + I>(c), ...); }
template <int S, int P> __device__ __forceinline__ void dma_one(const Ctx& c) { dma_m0<S, P>(c); asm volatile("s_nop 0"); dma_go<S, P>(c); }
template <int S, int... P> __device__ __forceinline__ void dma_stage(const Ctx& c, std::integer_sequence<int, P...>) { (dma_one<S, P>(c), ...); }
template <int... I> __device__ __forceinline__ void load_b_first(Ctx& c, std::integer_sequence<int, I...>) { (load_b<I>(c), ...); }
}

__device__ __forceinline__ void touch(u32& d, const char* p) { asm volatile("global_load_dword %0, %1, off" : "=v"(d) : "v"(p) : "memory"); }
__global__ __launch_bounds__(256, 1) void k_main(const u16* __restrict__ qin, const u16* __restrict__ kin, const u16* __restrict__ vin, const u16* __restrict__ shift_k,
                                                 const u16* __restrict__ ssm_k, const u16* __restrict__ ssm_D, const u16* __restrict__ wg, const u16* __restrict__ wu,
                                                 const u16* __restrict__ wd, const u16* __restrict__ cvec, const u16* __restrict__ w_mod,
                                                 char* __restrict__ ws, u16* __restrict__ out) {
    using namespace mlp;
    extern __shared__ __attribute__((aligned(16))) char smem[];
    const int tid = threadIdx.x, lane = tid & 63, w = __builtin_amdgcn_readfirstlane(tid >> 6);
    const int b = blockIdx.x, x = b & 7, idx = b >> 3, q = x >> 1;
    const int mt = 4 * (x & 1) + (idx & 3), fcl = idx >> 2, fc = 8 * q + fcl;
    const int mt2 = 8 * (x & 1) + (idx & 7), cc = idx >> 3, tg = w & 1, kh = w >> 1;
    const rsrc_t wsr = __builtin_amdgcn_make_buffer_rsrc(ws, 0, (int)WS_END, 0x00020000);
    STAMP_DECL; STAMP_AT(0);
    u32 epoch_in;
    unsigned long long pc_start;
    asm volatile("s_getpc_b64 %0" : "=s"(pc_start));
    u32 epoch;
    {
        const int t0 = (x * 32 + idx) * 8;
        RowsPre rp;
        u32 bg[10] = {0, 0, 0, 0, 0, 0, 0, 0, 0, 0};
        if (b < 192) {
            const int c0 = 72 * x + 3 * idx;
            ConvIn in[3];
            KvIn kin_;
            touch(bg[8], (const char*)(b < 96 ? kin : vin) + (size_t)((b % 96) * 192 + (tid < 192 ? tid : 0)) * 128);
            kv_load(kin_, c0, kin, vin, shift_k);
            epoch_in = __hip_atomic_load(flag_ptr(ws, F_EPOCH), __ATOMIC_RELAXED, __HIP_MEMORY_SCOPE_AGENT);
            conv_load_k(in[0], c0, ssm_k, ssm_D); conv_load_k(in[1], c0 + 1, ssm_k, ssm_D); conv_load_k(in[2], c0 + 2, ssm_k, ssm_D);
            rows_load(rp, t0, qin);
            kv_put(kin_, smem + C_RED);
            conv_put_k(in[0], smem); conv_put_k(in[1], smem + C_CH); conv_put_k(in[2], smem + 2 * C_CH);
            __syncthreads();
            STAMP_AT(1);
            kv_compute(kin_, c0, smem + C_RED, smem + C_XR, C_CH);
            epoch = __builtin_amdgcn_readfirstlane(epoch_in) + 1u;
            if (w == 3) {
                DRAIN();
#pragma unroll
                for (int i = 0; i < 8; ++i) { const int l = lane + 64 * (i & 3); touch(bg[i], (const char*)(i < 4 ? wg : wu) + (size_t)b * 27648 + (l < 216 ? l : 0) * 128); }
            }
            STAMP_AT(2);
            ModIn mi;
            const u32* mf = flag_ptr(ws, F_MOD + (lane < 54 ? lane : 0));
            u32 pm;
            asm volatile("global_load_dword %0, %1, off sc1" : "=v"(pm) : "v"(mf) : "memory");
            conv_body(c0, in, smem, wsr, [&]() {
                DRAIN();
                asm volatile("" : "+v"(pm));
                if (!__all(pm == epoch)) poll_eq(mf, epoch);
                mod_load(mi, wsr); } STAMP_ARGS);
            STAMP_AT(5);
            mod_put(mi, smem);
            DRAIN();
#pragma unroll
            for (int i = 0; i < 10; ++i) asm volatile("" :: "v"(bg[i]));
            __syncthreads();
            if (tid < 8) flag_set(flag_ptr(ws, F_CONV + tid * 256 + b), epoch);
            if (w == 3) {
#pragma unroll
                for (int i = 0; i < 4; ++i) { const int l = lane + 64 * i; touch(bg[i], (const char*)wd + (size_t)b * 27648 + (l < 216 ? l : 0) * 128); }
            }
        } else {
            if (w == 3 && idx < 28) {
                const char* cb = (const char*)(pc_start & ~127ull) + lane * 128;
#pragma unroll
                for (int i = 0; i < 8; ++i) touch(bg[i], cb + (i < 7 || lane < 32 ? i * 8192 : 0));
            }
            if (w == 3 && idx >= 28) {
#pragma unroll
                for (int i = 0; i < 3; ++i) { const size_t o = (size_t)(((idx - 28) * 3 + i) * 64 + lane) << 16; touch(bg[i], ws + (o < WS_END ? o : 0)); }
                touch(bg[3], (const char*)out + (size_t)(lane < 36 ? lane : 0) * 65536);
                const size_t pg = (size_t)(lane + 64 * (idx & 1) < 81 ? lane + 64 * (idx & 1) : 0) * 65536;
                touch(bg[4], (const char*)wd + pg); touch(bg[5], (const char*)wg + pg); touch(bg[6], (const char*)wu + pg);
            }
            for (int i = 0; i < 40; ++i) __builtin_amdgcn_s_sleep(2);
            rows_load(rp, t0, qin);
            epoch_in = __hip_atomic_load(flag_ptr(ws, F_EPOCH), __ATOMIC_RELAXED, __HIP_MEMORY_SCOPE_AGENT);
            if (b - 192 < 54) mod_job(b - 192, cvec, w_mod, ws, (float*)(smem + R_MOD), (float*)(smem + R_MOD + 4096));
            epoch = __builtin_amdgcn_readfirstlane(epoch_in) + 1u;
            DRAIN();
            __syncthreads();
            if (tid == 0 && b - 192 < 54) flag_set(flag_ptr(ws, F_MOD + b - 192), epoch);
            STAMP_AT(1);
            tile_jobs(b - 192, wg, wu, smem, ws, wsr, epoch);
            STAMP_AT(2); STAMP_AT(3); STAMP_AT(4); STAMP_AT(5);
            poll_eq(flag_ptr(ws, F_MOD + (lane < 54 ? lane : 0)), epoch);
            ModIn mi;
            mod_load(mi, wsr);
            mod_put(mi, smem);
            __syncthreads();
        }
        rows_pre(rp, smem);
        STAMP_AT(6);
        if (w == 0) {
            poll_eq4(wsr, (u32)WS_CNT + (F_CONV + x * 256 + (lane < 48 ? lane : 0) * 4) * 4, epoch);
            rows_fetch(t0, smem, ws);
        }
        __syncthreads();
        STAMP_AT(7);
        rows_main(rp, t0, smem, wsr);
        DRAIN();
#pragma unroll
        for (int i = 0; i < 10; ++i) asm volatile("" :: "v"(bg[i]));
        __syncthreads();
        if (tid == 0) flag_set(flag_ptr(ws, F_ROWS + (t0 >> 6) * 8 + ((t0 >> 3) & 7)), epoch);
        STAMP_AT(8);
    }
    Ctx c;
#pragma unroll
    for (int t = 0; t < 4; ++t) c.hp[t] = (u32x4){0, 0, 0, 0};
    c.lane16 = lane * 16;
#pragma unroll
    for (int t = 0; t < 4; ++t) { c.vo1[t] = lane * 16 + t * 18432; c.vo2[t] = lane * 16 + t * TUS; c.vo3[t] = lane * 16 + ((t + 2 * kh) & 3) * TUS; }
    const LAS char* l0 = (const LAS char*)smem;
#pragma unroll
    for (int i = 0; i < 4; ++i) {
        c.rd[i] = l0 + i * STG + lane * 16; c.rd2[i] = l0 + i * STG + kh * 18432 + (4 * (lane >> 4) + ((lane & 15) >> 2)) * 288 + 8 * (lane & 3);
        asm volatile("" : "+v"(c.rd[i])); asm volatile("" : "+v"(c.rd2[i]));
    }
#pragma unroll
    for (int i = 0; i < 9; ++i) { const int ci = lane + 64 * i, row = (ci * 3641) >> 16, c16 = ci - row * 18; c.voff2[i] = (u32)(row * (D_ * 2) + c16 * 16 - (i & 3) * 1024); }
    c.sub = w & 1;
    c.w1src = ws + WS_WT1 + (size_t)fc * 331776 + w * 9216;
    c.w2src = (const char*)(wd + (size_t)(36 * q + 18 * kh) * 32 * D_ + cc * 144);
    c.bsrc = ws + WS_H2T + (size_t)(16 * mt + 4 * w) * 18432;
    c.hsrc = ws + WS_HT + (size_t)(8 * mt2 + 4 * tg) * TUS + (size_t)((q * 2 + kh) * 18) * 1024;
    c.dmadst = (u32)(uintptr_t)smem + w * 9216;
    c.jj = fcl & 3;
    c.hso = (u32)((16 * mt + 4 * w) * TUS + ((q * 2 + (fcl >> 2)) * 18) * 1024);
    c.hrs = __builtin_amdgcn_make_buffer_rsrc(ws + WS_HT, 0, (int)((size_t)S_ * F_ * 2), 0x00020000);
    c.epoch = epoch;
    c.flag_pub = flag_ptr(ws, F_H + ((((mt * 4 + w) * 4 + q) * 2 + (fcl >> 2))) * 4 + (fcl & 3));
    c.flag_poll = flag_ptr(ws, F_H + (((((mt2 >> 1) * 4 + 2 * (mt2 & 1) + tg) * 4 + q) * 2 + kh)) * 4);
    asm volatile("" ::: "a0", "a1", "a2", "a3", "a4", "a5", "a6", "a7", "a8", "a9", "a10", "a11", "a12", "a13", "a14", "a15", "a16", "a17", "a18", "a19", "a20", "a21", "a22", "a23", "a24", "a25", "a26", "a27", "a28", "a29", "a30", "a31", "a32", "a33", "a34", "a35", "a36", "a37", "a38", "a39", "a40", "a41", "a42", "a43", "a44", "a45", "a46", "a47", "a48", "a49", "a50", "a51", "a52", "a53", "a54", "a55", "a56", "a57", "a58", "a59", "a60", "a61", "a62", "a63", "a64", "a65", "a66", "a67", "a68", "a69", "a70", "a71", "a72", "a73", "a74", "a75", "a76", "a77", "a78", "a79", "a80", "a81", "a82", "a83", "a84", "a85", "a86", "a87", "a88", "a89", "a90", "a91", "a92", "a93", "a94", "a95", "a96", "a97", "a98", "a99", "a100", "a101", "a102", "a103", "a104", "a105", "a106", "a107", "a108", "a109", "a110", "a111", "a112", "a113", "a114", "a115", "a116", "a117", "a118", "a119", "a120", "a121", "a122", "a123", "a124", "a125", "a126", "a127", "a128", "a129", "a130", "a131", "a132", "a133", "a134", "a135", "a136", "a137", "a138", "a139", "a140", "a141", "a142", "a143", "a144", "a145", "a146", "a147", "a148", "a149", "a150", "a151", "a152", "a153", "a154", "a155", "a156", "a157", "a158", "a159", "a160", "a161", "a162", "a163", "a164", "a165", "a166", "a167", "a168", "a169", "a170", "a171", "a172", "a173", "a174", "a175", "a176", "a177", "a178", "a179", "a180", "a181", "a182", "a183", "a184", "a185", "a186", "a187", "a188", "a189", "a190", "a191", "a192", "a193", "a194", "a195", "a196", "a197", "a198", "a199", "a200", "a201", "a202", "a203", "a204", "a205", "a206", "a207", "a208", "a209", "a210", "a211", "a212", "a213", "a214", "a215", "a216", "a217", "a218", "a219", "a220", "a221", "a222", "a223", "a224", "a225", "a226", "a227", "a228", "a229", "a230", "a231", "a232", "a233", "a234", "a235", "a236", "a237", "a238", "a239", "a240", "a241", "a242", "a243", "a244", "a245", "a246", "a247", "a248", "a249", "a250", "a251", "a252", "a253", "a254", "a255");
    poll_eq(flag_ptr(ws, F_TILE + (fc >> 2) * 36 + (lane < 36 ? lane : 0)), epoch);
    dma_stage<0>(c, std::make_integer_sequence<int, 9>{});
    dma_stage<1>(c, std::make_integer_sequence<int, 9>{});
    dma_stage<2>(c, std::make_integer_sequence<int, 9>{});
    poll_eq(flag_ptr(ws, F_ROWS + (4 * mt + w) * 8 + (lane & 7)), epoch);
    STAMP_AT(9);
    load_b_first(c, std::make_integer_sequence<int, 4 * LA>{});
    wait_vm<4 * LA>();
    __builtin_amdgcn_s_barrier();
    asm volatile("" ::: "memory");
#pragma unroll
    for (int f = 0; f < FR0; ++f) c.Aq[f] = *(const LAS bf16x8*)(c.rd[frag_stage(f)] + frag_off(f));
    SCHED();
    STAMP_AT(10);
    run_slots<0>(c, std::make_integer_sequence<int, 288>{});
    STAMP_AT(11);
    run_slots<288>(c, std::make_integer_sequence<int, 144 * 3>{});
    run_slots<144 * 5>(c, std::make_integer_sequence<int, 144 * 4>{});
    STAMP_AT(12);
    run_slots<G1S>(c, std::make_integer_sequence<int, 324>{});
    run_slots<G1S + 324>(c, std::make_integer_sequence<int, 324>{});
    asm volatile("s_nop 7" ::: "memory");
#pragma unroll
    for (int u = 0; u < 9; ++u)
#pragma unroll
        for (int t = 0; t < 4; ++t) asm volatile("" : "+v"(c.a2[u][t]));
    SCHED();
    STAMP_AT(13);
    const int n = lane & 15, g = lane >> 4;
    u32x2 xr[3][2]; f32x4 gb[3];
#pragma unroll
    for (int i = 0; i < 3; ++i) {
        const int u = (w + 4 * i < 9) ? w + 4 * i : 8, d = 144 * cc + 16 * u + 4 * g;
#pragma unroll
        for (int t = 0; t < 2; ++t) xr[i][t] = __builtin_amdgcn_raw_buffer_load_b64(wsr, (u32)WS_X1 + (u32)((128 * mt2 + 32 * q + 16 * t + n) * D_ + d) * 2, 0, 16);
        gb[i] = *(const f32x4*)(smem + R_MOD + (5 * 576 + d) * 4);
    }
    {
        char* ex = smem + 73728 + w * 18432 + lane * 16;
#pragma unroll
        for (int t = 0; t < 2; ++t)
#pragma unroll
            for (int u = 0; u < 9; ++u) *(f32x4*)(ex + (t * 9 + u) * 1024) = c.a2[u][2 + t];
    }
    __syncthreads();
    const int j = 2 * tg + kh;
    u32* fred = flag_ptr(ws, F_RED + (mt2 * 4 + cc) * 16);
    const u32 slab = (u32)WS_SLAB + (u32)((mt2 * 4 + cc) * 16) * 9216;
    {
        const char* ey = smem + 73728 + (w ^ 2) * 18432 + lane * 16;
        char* own = smem + lane * 16;
#pragma unroll
        for (int u = 0; u < 9; ++u) {
            const f32x4 r0 = c.a2[u][0] + *(const f32x4*)(ey + u * 1024);
            const f32x4 r1 = c.a2[u][1] + *(const f32x4*)(ey + (9 + u) * 1024);
            if (j != q) st16_dev(wsr, slab + (j * 4 + q) * 9216 + u * 1024 + lane * 16, (u32x4){pk2(r0[0], r0[1]), pk2(r0[2], r0[3]), pk2(r1[0], r1[1]), pk2(r1[2], r1[3])});
            else { *(f32x4*)(own + u * 2048) = r0; *(f32x4*)(own + u * 2048 + 1024) = r1; }
        }
    }
    if (j != q) {
        DRAIN();
        if (lane == 0) flag_set(fred + j * 4 + q, epoch);
    }
    STAMP_AT(14);
    poll_eq(fred + q * 4 + (((lane & 3) == q) ? (q ^ 1) : (lane & 3)), epoch);
    __syncthreads();
    u32x4 rem[3][3];
#pragma unroll
    for (int i = 0; i < 3; ++i) {
        const int u = (w + 4 * i < 9) ? w + 4 * i : 8;
#pragma unroll
        for (int k = 0; k < 3; ++k) { const int qq = k + (k >= q ? 1 : 0); rem[i][k] = __builtin_amdgcn_raw_buffer_load_b128(wsr, slab + (q * 4 + qq) * 9216 + u * 1024 + lane * 16, 0, 16); }
    }
#pragma unroll
    for (int i = 0; i < 3; ++i) {
        const int u = w + 4 * i;
        if (u < 9) {
            const int d = 144 * cc + 16 * u + 4 * g;
#pragma unroll
            for (int t = 0; t < 2; ++t) {
                f32x4 s = *(const f32x4*)(smem + u * 2048 + t * 1024 + lane * 16);
#pragma unroll
                for (int k = 0; k < 3; ++k) {
                    const u32 a = rem[i][k][2 * t], bq = rem[i][k][2 * t + 1];
                    s += (f32x4){bf_lo(a), bf_hi(a), bf_lo(bq), bf_hi(bq)};
                }
                const float o0 = bf_lo(xr[i][t][0]) + rbf(gb[i][0] * rbf(s[0])), o1 = bf_hi(xr[i][t][0]) + rbf(gb[i][1] * rbf(s[1]));
                const float o2 = bf_lo(xr[i][t][1]) + rbf(gb[i][2] * rbf(s[2])), o3 = bf_hi(xr[i][t][1]) + rbf(gb[i][3] * rbf(s[3]));
                *(u32x2*)(out + (size_t)(128 * mt2 + 32 * q + 16 * t + n) * D_ + d) = (u32x2){pk2(o0, o1), pk2(o2, o3)};
            }
        }
    }
    if (b == 0 && tid == 0) flag_set(flag_ptr(ws, F_EPOCH), epoch);
    STAMP_AT(15);
    STAMP_OUT(4, 16);
}

extern "C" void kernel_launch(void* const* d_in, const int* in_sizes, int n_in, void* d_out, int out_size, void* d_ws, size_t ws_size,
                              hipStream_t stream) {
    const u16* q = (const u16*)d_in[0];
    const u16* k = (const u16*)d_in[1];
    const u16* v = (const u16*)d_in[2];
    const u16* shift_k = (const u16*)d_in[3];
    const u16* ssm_k = (const u16*)d_in[4];
    const u16* ssm_D = (const u16*)d_in[5];
    const u16* wg = (const u16*)d_in[7];
    const u16* wu = (const u16*)d_in[8];
    const u16* wd = (const u16*)d_in[9];
    const u16* c = (const u16*)d_in[10];
    const u16* w_mod = (const u16*)d_in[11];
    char* ws = (char*)d_ws;
    (void)hipFuncSetAttribute((const void*)k_main, hipFuncAttributeMaxDynamicSharedMemorySize, mlp::LDS_BYTES);
    k_main<<<256, 256, mlp::LDS_BYTES, stream>>>(q, k, v, shift_k, ssm_k, ssm_D, wg, wu, wd, c, w_mod, ws, (u16*)d_out);
}
```

```cpp
#include <hip/hip_runtime.h>
#include <stdint.h>
#include <utility>

typedef unsigned short u16;
typedef unsigned int u32;
typedef __bf16 bf16x8 __attribute__((ext_vector_type(8)));
typedef __bf16 bf16x2 __attribute__((ext_vector_type(2)));
typedef float f32x4 __attribute__((ext_vector_type(4)));
typedef float f32x2 __attribute__((ext_vector_type(2)));
typedef u32 u32x4 __attribute__((ext_vector_type(4)));
typedef u32 u32x2 __attribute__((ext_vector_type(2)));
typedef short s16x4 __attribute__((ext_vector_type(4)));
#define LAS __attribute__((address_space(3)))
typedef LAS char* lptr;

constexpr int S_ = 2048, D_ = 576, F_ = 4608;

constexpr size_t WS_MOD = 0;
constexpr size_t WS_CNT = 65536;
constexpr size_t WS_YYT = WS_CNT + 65536;
constexpr size_t WS_X1  = WS_YYT + (size_t)D_ * S_ * 2;
constexpr size_t WS_H2T = WS_X1 + (size_t)D_ * S_ * 2;
constexpr size_t WS_HT  = WS_H2T + (size_t)D_ * S_ * 2;
constexpr size_t WS_WT1 = WS_HT + (size_t)S_ * F_ * 2;
constexpr size_t WS_SLAB = WS_WT1 + (size_t)2 * D_ * F_ * 2;
constexpr size_t WS_END = WS_SLAB + (size_t)16 * 4 * 4 * 4 * 9216;

#define STAMP_DECL
#define STAMP_ARGS
#define STAMP_AT(i)
#define STAMP_OUT(kid, n)
__device__ __forceinline__ float bf_lo(u32 w) { return __uint_as_float(w << 16); }
__device__ __forceinline__ float bf_hi(u32 w) { return __uint_as_float(w & 0xffff0000u); }
__device__ __forceinline__ float bf1(u16 w) { return __uint_as_float((u32)w << 16); }
__device__ __forceinline__ u32 pk2(float lo, float hi) {
    f32x2 v = {lo, hi};
    return __builtin_bit_cast(u32, __builtin_convertvector(v, bf16x2));
}
__device__ __forceinline__ float rbf(float x) { return (float)(__bf16)x; }
__device__ __forceinline__ float sigmoidf_(float x) { return __builtin_amdgcn_rcpf(1.0f + __builtin_amdgcn_exp2f(-1.4426950408889634f * x)); }

constexpr int F_MOD = 256;
constexpr int F_TILE = 512;
constexpr int F_CONV = 1024;
constexpr int F_ROWS = 3072;
constexpr int F_H = 3328;
constexpr int F_RED = 5376;
constexpr int F_EPOCH = 6400;
typedef __amdgpu_buffer_rsrc_t rsrc_t;
__device__ __forceinline__ u32* flag_ptr(char* ws, int idx) { return (u32*)(ws + WS_CNT) + idx; }
__device__ __forceinline__ void flag_set(u32* p, u32 e) { __hip_atomic_store(p, e, __ATOMIC_RELAXED, __HIP_MEMORY_SCOPE_AGENT); }
__device__ __forceinline__ void st16_dev(rsrc_t rs, u32 off, u32x4 v) { __builtin_amdgcn_raw_buffer_store_b128(v, rs, off, 0, 16); }
__device__ __forceinline__ u32x4 ld16_dev(rsrc_t rs, u32 off) { return __builtin_amdgcn_raw_buffer_load_b128(rs, off, 0, 16); }
__device__ __forceinline__ void poll_eq(const u32* p, u32 e) {
    for (int it = 0; it < (1 << 21); ++it) {
        if (__all(__hip_atomic_load(p, __ATOMIC_RELAXED, __HIP_MEMORY_SCOPE_AGENT) == e)) break;
        __builtin_amdgcn_s_sleep(1);
    }
}
__device__ __forceinline__ void poll_eq4(rsrc_t rs, u32 off, u32 e) {
    for (int it = 0; it < (1 << 21); ++it) {
        asm volatile("" ::: "memory");
        const u32x4 v = ld16_dev(rs, off);
        if (__all(v[0] == e && v[1] == e && v[2] == e && v[3] == e)) break;
        __builtin_amdgcn_s_sleep(1);
    }
}
#define DRAIN() asm volatile("s_waitcnt vmcnt(0)" ::: "memory")

#define SCHED() __builtin_amdgcn_sched_barrier(0)
__device__ __forceinline__ float dpp_add(float v, const int ctrl_sel) {
    int r;
    const int iv = __builtin_bit_cast(int, v);
    switch (ctrl_sel) {
        case 0: r = __builtin_amdgcn_update_dpp(0, iv, 0xB1, 0xf, 0xf, true); break;
        case 1: r = __builtin_amdgcn_update_dpp(0, iv, 0x4E, 0xf, 0xf, true); break;
        case 2: r = __builtin_amdgcn_update_dpp(0, iv, 0x141, 0xf, 0xf, true); break;
        default: r = __builtin_amdgcn_update_dpp(0, iv, 0x140, 0xf, 0xf, true); break;
    }
    return v + __builtin_bit_cast(float, r);
}
__device__ __forceinline__ float half_sum(float v) {
    v = dpp_add(v, 0); v = dpp_add(v, 1); v = dpp_add(v, 2); v = dpp_add(v, 3);
    auto r = __builtin_amdgcn_permlane16_swap(__float_as_uint(v), __float_as_uint(v), false, false);
    return __uint_as_float(r[0]) + __uint_as_float(r[1]);
}

struct KvIn { u32x2 kw[8], vw[8]; u32x4 sw; u32x2 sw2; };
__device__ __forceinline__ void kv_load(KvIn& p, int c0, const u16* __restrict__ k, const u16* __restrict__ v, const u16* __restrict__ shift_k) {
    const int cw = c0 & ~1;
#pragma unroll
    for (int i = 0; i < 8; ++i) p.kw[i] = *(const u32x2*)(k + (size_t)(threadIdx.x + 256 * i) * D_ + cw);
#pragma unroll
    for (int i = 0; i < 8; ++i) p.vw[i] = *(const u32x2*)(v + (size_t)(threadIdx.x + 256 * i) * D_ + cw);
    p.sw = (u32x4){*(const u32*)(shift_k + c0 * 4), *(const u32*)(shift_k + c0 * 4 + 2), *(const u32*)(shift_k + c0 * 4 + 4), *(const u32*)(shift_k + c0 * 4 + 6)};
    p.sw2 = (u32x2){*(const u32*)(shift_k + c0 * 4 + 8), *(const u32*)(shift_k + c0 * 4 + 10)};
}
constexpr int KV_RAWV = 257 * 72 + 8;
__device__ __forceinline__ void kv_put(const KvIn& p, char* raw) {
    const int tid = threadIdx.x;
#pragma unroll
    for (int i = 0; i < 8; ++i) {
        const int t = tid + 256 * i, o = 72 * (t >> 3) + 8 * (t & 7);
        *(u32x2*)(raw + 72 + o) = p.kw[i];
        *(u32x2*)(raw + KV_RAWV + o) = p.vw[i];
    }
    if (tid < 8) *(u32x2*)(raw + 8 * tid) = (u32x2){0, 0};
}
__device__ __forceinline__ void kv_compute(const KvIn& p, int c0, const char* raw, char* xr, int stride) {
    const int tid = threadIdx.x;
    u32x2 kw[11], vw[8];
#pragma unroll
    for (int i = 0; i < 11; ++i) kw[i] = *(const u32x2*)(raw + 72 * tid + (i < 3 ? 40 + 8 * i : 72 + 8 * (i - 3)));
#pragma unroll
    for (int i = 0; i < 8; ++i) vw[i] = *(const u32x2*)(raw + KV_RAWV + 72 * tid + 8 * i);
    const bool odd = c0 & 1;
#pragma unroll
    for (int c = 0; c < 3; ++c) {
        const u32 wa = c == 0 ? p.sw[0] : c == 1 ? p.sw[2] : p.sw2[0], wb = c == 0 ? p.sw[1] : c == 1 ? p.sw[3] : p.sw2[1];
        const float w0 = bf_lo(wa), w1 = bf_hi(wa), w2 = bf_lo(wb), w3 = bf_hi(wb);
        float kf[11], r[8];
#define KV_PICK(d) (c == 0 ? (odd ? bf_hi((d)[0]) : bf_lo((d)[0])) : c == 1 ? (odd ? bf_lo((d)[1]) : bf_hi((d)[0])) : (odd ? bf_hi((d)[1]) : bf_lo((d)[1])))
#pragma unroll
        for (int i = 0; i < 11; ++i) kf[i] = KV_PICK(kw[i]);
#pragma unroll
        for (int i = 0; i < 8; ++i) r[i] = (kf[i] * w3 + kf[i + 1] * w2 + kf[i + 2] * w1 + kf[i + 3] * w0) * KV_PICK(vw[i]);
#undef KV_PICK
        *(u32x4*)(xr + c * stride + 2 * (2040 - 8 * tid)) = (u32x4){pk2(r[7], r[6]), pk2(r[5], r[4]), pk2(r[3], r[2]), pk2(r[1], r[0])};
    }
}

__device__ __forceinline__ void mod_job(int job, const u16* __restrict__ c, const u16* __restrict__ w_mod, char* __restrict__ ws, float* sc  , float* red  ) {
    const int tid = threadIdx.x, lane = tid & 63, wid = tid >> 6;
    const int col0 = job * 64, rg = tid >> 3, c8 = tid & 7;
    u32x4 w[18];
#pragma unroll
    for (int i = 0; i < 18; ++i) w[i] = *(const u32x4*)(w_mod + (size_t)(rg + 32 * i) * 3456 + col0 + c8 * 8);
#pragma unroll
    for (int j = 0; j < 3; ++j) { const int r = tid + 256 * j; if (r < 576) { const float x = bf1(c[r]); sc[r] = rbf(x * sigmoidf_(x)); } }
    __syncthreads();
    float acc[8] = {0, 0, 0, 0, 0, 0, 0, 0};
#pragma unroll
    for (int i = 0; i < 18; ++i) {
        const float s = sc[rg + 32 * i];
#pragma unroll
        for (int j = 0; j < 4; ++j) { acc[2 * j] += s * bf_lo(w[i][j]); acc[2 * j + 1] += s * bf_hi(w[i][j]); }
    }
#pragma unroll
    for (int j = 0; j < 8; ++j) {
        acc[j] += __shfl_xor(acc[j], 8); acc[j] += __shfl_xor(acc[j], 16); acc[j] += __shfl_xor(acc[j], 32);
    }
    if (lane < 8) {
#pragma unroll
        for (int j = 0; j < 8; ++j) red[wid * 64 + lane * 8 + j] = acc[j];
    }
    __syncthreads();
    if (tid < 64) {
        float m = rbf((red[tid] + red[64 + tid]) + (red[128 + tid] + red[192 + tid]));
        const int which = col0 / 576;
        if (which == 1 || which == 4) m = rbf(1.0f + m);
        __hip_atomic_store((u32*)(ws + WS_MOD) + col0 + tid, __float_as_uint(m), __ATOMIC_RELAXED, __HIP_MEMORY_SCOPE_AGENT);
    }
}

__device__ __forceinline__ const char* tile_base(int job, const u16* __restrict__ wg, const u16* __restrict__ wu) {
    const int ks = job % 18, mat = (job / 18) & 1, cg = job / 36;
    return (const char*)((mat ? wu : wg) + (size_t)(32 * ks) * F_ + cg * 576);
}
__device__ __forceinline__ void tile_dma(int job, const u16* __restrict__ wg, const u16* __restrict__ wu, char* tile, const u32 (&voff)[9]) {
    const int w = __builtin_amdgcn_readfirstlane(threadIdx.x >> 6);
    const char* base = tile_base(job, wg, wu) + (size_t)w * 8 * (F_ * 2);
    const u32 dst = (u32)(uintptr_t)tile + w * 9216;
#pragma unroll
    for (int i = 0; i < 9; ++i)
        asm volatile("s_mov_b32 m0, %2\n\ts_nop 0\n\tglobal_load_lds_dwordx4 %0, %1" :: "v"(voff[i]), "s"(base), "s"(dst + i * 1024) : "memory");
}
__device__ __forceinline__ void tile_out(int job, const char* tile, rsrc_t wsr) {
    const int lane = threadIdx.x & 63, w = threadIdx.x >> 6;
    const int ks = job % 18, mat = (job / 18) & 1, fc = 4 * (job / 36) + w;
    const int g = lane >> 4, q4 = (lane & 15) >> 2, p = lane & 3;
    const LAS char* rd = (const LAS char*)tile + (4 * g + q4) * 1152 + 8 * p + w * 288;
#pragma unroll
    for (int u = 0; u < 9; ++u) {
        const int pos = (fc & 1) ? (u == 0 ? 4 : u <= 4 ? u - 1 : u) : (u == 8 ? 4 : u < 4 ? u : u + 1);
        const s16x4 lo = __builtin_amdgcn_ds_read_tr16_b64_v4i16((LAS s16x4*)(rd + u * 32));
        const s16x4 hi = __builtin_amdgcn_ds_read_tr16_b64_v4i16((LAS s16x4*)(rd + u * 32 + 16 * 1152));
        typedef short s16x8 __attribute__((ext_vector_type(8)));
        const s16x8 r = __builtin_shufflevector(lo, hi, 0, 1, 2, 3, 4, 5, 6, 7);
        st16_dev(wsr, (u32)WS_WT1 + fc * 331776 + pos * 36864 + (ks * 2 + mat) * 1024 + lane * 16, __builtin_bit_cast(u32x4, r));
    }
}
__device__ __forceinline__ void tile_jobs(int i0, const u16* __restrict__ wg, const u16* __restrict__ wu, char* smem, char* ws, rsrc_t wsr, u32 epoch) {
    const int lane = threadIdx.x & 63;
    u32 voff[9];
#pragma unroll
    for (int i = 0; i < 9; ++i) { const int ci = lane + 64 * i, row = (ci * 58255) >> 22, c16 = ci - row * 72; voff[i] = (u32)(row * (F_ * 2) + c16 * 16); }
#pragma unroll
    for (int j = 0; j < 4; ++j) tile_dma(i0 + 64 * j, wg, wu, smem + j * 36864, voff);
#pragma unroll
    for (int j = 0; j < 4; ++j) {
        asm volatile("s_waitcnt vmcnt(27)" ::: "memory");
        __syncthreads();
        tile_out(i0 + 64 * j, smem + j * 36864, wsr);
    }
    if (i0 < 32) {
        __syncthreads();
        tile_dma(i0 + 256, wg, wu, smem, voff);
        asm volatile("s_waitcnt vmcnt(0)" ::: "memory");
        __syncthreads();
        tile_out(i0 + 256, smem, wsr);
    }
    DRAIN();
    __syncthreads();
    if (threadIdx.x < 5 && i0 + 64 * threadIdx.x < 288) flag_set(flag_ptr(ws, F_TILE + i0 + 64 * threadIdx.x), epoch);
}

constexpr int C_XR = 0;
constexpr int C_K0 = 4608;
constexpr int C_K1 = C_K0 + 4128 + 32;
constexpr int C_CH = C_K1 + 4160;
constexpr int C_RED = 3 * C_CH;
constexpr int C_LDS = C_RED + 3 * 32768;
static_assert(((C_K1 - C_K0) / 4) % 32 == 16, "bank offset");
static_assert(C_CH % 16 == 0 && C_K0 % 16 == 0 && C_K1 % 16 == 0, "align");
template <bool ZERO> __device__ __forceinline__ void mfma_v(f32x4& c, const bf16x8& a, const bf16x8& b) {
    if constexpr (ZERO) asm volatile("v_mfma_f32_16x16x32_bf16 %0, %1, %2, 0" : "=v"(c) : "v"(a), "v"(b));
    else asm volatile("v_mfma_f32_16x16x32_bf16 %0, %1, %2, %0" :: "v"(c), "v"(a), "v"(b));
}
struct ConvSet { bf16x8 A[8], B[8]; };
template <int J> __device__ __forceinline__ void conv_ld(ConvSet& s, const LAS char* ab, const LAS char* bb) {
    if constexpr (J < 8) {
        const LAS u32* p = (const LAS u32*)(ab + 512 * J);
        u32x4 t = {p[0], p[1], p[2], p[3]};
        s.A[J] = __builtin_bit_cast(bf16x8, t);
    } else s.B[J - 8] = *(const LAS bf16x8*)(bb + 512 * (15 - J));
}
template <int M> struct TriIdx {
    static constexpr int find(bool want_i) { int m = 0; for (int s = 0; s < 8; ++s) for (int j = 0; j <= s; ++j) { if (m == M) return want_i ? s : j; ++m; } return 0; }
    static constexpr int i = find(true), j = find(false);
};
template <int M, bool PF, bool FIRST> __device__ __forceinline__ void conv_slot(f32x4 (&acc)[8], const ConvSet& cur, ConvSet& nxt, const LAS char* ab, const LAS char* bb) {
    mfma_v<(FIRST && TriIdx<M>::j == 0)>(acc[TriIdx<M>::i], cur.A[TriIdx<M>::j], cur.B[TriIdx<M>::i - TriIdx<M>::j]);
    if constexpr (PF && (M & 1) == 0 && M / 2 < 16) conv_ld<((M / 2) & 1) * 8 + (M / 4)>(nxt, ab, bb);
    SCHED();
}
template <bool PF, bool FIRST, int... M> __device__ __forceinline__ void conv_step(f32x4 (&acc)[8], const ConvSet& cur, ConvSet& nxt, const LAS char* ab, const LAS char* bb, std::integer_sequence<int, M...>) {
    (conv_slot<M, PF, FIRST>(acc, cur, nxt, ab, bb), ...);
}
template <int... J> __device__ __forceinline__ void conv_ld_all(ConvSet& s, const LAS char* ab, const LAS char* bb, std::integer_sequence<int, J...>) { (conv_ld<J>(s, ab, bb), ...); }

struct ConvIn { u32x4 k; u32 kn; u16 dv; };
__device__ __forceinline__ void conv_load_k(ConvIn& in, int d, const u16* __restrict__ ssm_k, const u16* __restrict__ ssm_D) {
    const int tid = threadIdx.x;
    in.k = *(const u32x4*)(ssm_k + (size_t)d * S_ + tid * 8);
    in.kn = *(const u32*)(ssm_k + (size_t)d * S_ + (tid < 255 ? tid * 8 + 8 : 0));
    in.dv = ssm_D[d];
}
__device__ __forceinline__ void conv_put_k(const ConvIn& in, char* sm) {
    const int tid = threadIdx.x;
    const u32 kn = tid < 255 ? in.kn : 0u;
    *(u32x4*)(sm + C_K0 + 32 + tid * 16) = in.k;
    *(u32x4*)(sm + C_K1 + 32 + tid * 16) = (u32x4){__builtin_amdgcn_alignbit(in.k[1], in.k[0], 16), __builtin_amdgcn_alignbit(in.k[2], in.k[1], 16),
                                                 __builtin_amdgcn_alignbit(in.k[3], in.k[2], 16), __builtin_amdgcn_alignbit(kn, in.k[3], 16)};
    if (tid < 32) *(u32x4*)(sm + C_XR + 4096 + tid * 16) = (u32x4){0, 0, 0, 0};
    if (tid == 0) {
        *(u32x4*)(sm + C_K0) = (u32x4){0, 0, 0, 0}; *(u32x4*)(sm + C_K0 + 16) = (u32x4){0, 0, 0, 0};
        *(u32x4*)(sm + C_K1) = (u32x4){0, 0, 0, 0}; *(u32x4*)(sm + C_K1 + 16) = (u32x4){0, 0, 0, in.k[0] << 16};
    }
}
__device__ __forceinline__ void conv_finish(int d, const char* sm, const char* red, float Dv, rsrc_t wsr) {
    const int tid = threadIdx.x;
    const int i = tid >> 5, n = tid & 15, gp = (tid >> 4) & 1;
    f32x4 lo = {0.f, 0.f, 0.f, 0.f}, hi = {0.f, 0.f, 0.f, 0.f};
#pragma unroll
    for (int w = 0; w < 4; ++w) {
        lo += *(const f32x4*)(red + (w * 8 + i) * 1024 + (n + 32 * gp) * 16);
        hi += *(const f32x4*)(red + (w * 8 + i) * 1024 + (n + 32 * gp + 16) * 16);
    }
    const int s = 256 * i + 16 * n + 8 * gp;
    const u32x4 xw = *(const u32x4*)(sm + C_XR + 2 * (2040 - s));
    const float o0 = rbf(lo[0]) + rbf(bf_hi(xw[3]) * Dv), o1 = rbf(lo[1]) + rbf(bf_lo(xw[3]) * Dv), o2 = rbf(lo[2]) + rbf(bf_hi(xw[2]) * Dv), o3 = rbf(lo[3]) + rbf(bf_lo(xw[2]) * Dv);
    const float o4 = rbf(hi[0]) + rbf(bf_hi(xw[1]) * Dv), o5 = rbf(hi[1]) + rbf(bf_lo(xw[1]) * Dv), o6 = rbf(hi[2]) + rbf(bf_hi(xw[0]) * Dv), o7 = rbf(hi[3]) + rbf(bf_lo(xw[0]) * Dv);
    st16_dev(wsr, (u32)WS_YYT + (u32)(d * S_ + s) * 2, (u32x4){pk2(o0, o1), pk2(o2, o3), pk2(o4, o5), pk2(o6, o7)});
}
template <class Hook>
__device__ __forceinline__ void conv_body(int ch0, const ConvIn (&in)[3], char* smem, rsrc_t wsr, Hook hook
                                          ) {
    const int lane = threadIdx.x & 63, wid = __builtin_amdgcn_readfirstlane(threadIdx.x >> 6);
    __syncthreads();
    STAMP_AT(3);
    const int n = lane & 15, g = lane >> 4, dd = g >> 1, gh = g & 1;
    const LAS char* l0 = (const LAS char*)smem;
    const LAS char* bb = l0 + C_XR + 480 - 32 * n + 32 * dd + 16 * gh + 128 * wid;
    const LAS char* ab = l0 + ((n & 1) ? C_K0 + 2 * (16 * dd + 8 * gh + n + 1) : C_K1 + 2 * (16 * dd + 8 * gh + n)) + 128 * wid;
    f32x4 acc[3][8];
    ConvSet s0, s1;
    conv_ld_all(s0, ab, bb, std::make_integer_sequence<int, 16>{});
    SCHED();
    constexpr auto SQ = std::make_integer_sequence<int, 36>{};
    conv_step<true, true>(acc[0], s0, s1, ab + 64, bb + 64, SQ);
    conv_step<true, false>(acc[0], s1, s0, ab + C_CH, bb + C_CH, SQ);
    conv_step<true, true>(acc[1], s0, s1, ab + C_CH + 64, bb + C_CH + 64, SQ);
    conv_step<true, false>(acc[1], s1, s0, ab + 2 * C_CH, bb + 2 * C_CH, SQ);
    conv_step<true, true>(acc[2], s0, s1, ab + 2 * C_CH + 64, bb + 2 * C_CH + 64, SQ);
    conv_step<false, false>(acc[2], s1, s0, ab, bb, SQ);
    asm volatile("s_nop 7" ::: "memory");
#pragma unroll
    for (int c = 0; c < 3; ++c)
#pragma unroll
        for (int i = 0; i < 8; ++i) asm volatile("" : "+v"(acc[c][i]));
    STAMP_AT(4);
    hook();
#pragma unroll
    for (int c = 0; c < 3; ++c)
#pragma unroll
        for (int i = 0; i < 8; ++i) *(f32x4*)(smem + C_RED + c * 32768 + (wid * 8 + i) * 1024 + lane * 16) = acc[c][i];
    __syncthreads();
#pragma unroll
    for (int c = 0; c < 3; ++c) conv_finish(ch0 + c, smem + c * C_CH, smem + C_RED + c * 32768, bf1(in[c].dv), wsr);
}

constexpr int R_MOD = 147456;
constexpr int R_YT = 110592;
constexpr int R_HT = R_YT + 8 * 1168;
struct RowsPre { float x[18], gh[18]; u32 xw[9]; };
__device__ __forceinline__ void rows_load(RowsPre& r, int t0, const u16* __restrict__ q) {
    const int tid = threadIdx.x, lane = tid & 63, hl = lane & 31, row = 2 * (tid >> 6) + (lane >> 5);
    const u32* qrow = (const u32*)(q + (size_t)(t0 + row) * D_);
#pragma unroll
    for (int i = 0; i < 9; ++i) r.xw[i] = qrow[hl + 32 * i];
}
struct ModIn { u32x4 mw[4]; };
__device__ __forceinline__ void mod_load(ModIn& m, rsrc_t wsr) {
#pragma unroll
    for (int i = 0; i < 4; ++i) { const int j = threadIdx.x + 256 * i; m.mw[i] = ld16_dev(wsr, (u32)WS_MOD + (j < 864 ? j : 0) * 16); }
}
__device__ __forceinline__ void mod_put(const ModIn& m, char* sm) {
#pragma unroll
    for (int i = 0; i < 4; ++i) { const int j = threadIdx.x + 256 * i; if (j < 864) *(u32x4*)(sm + R_MOD + j * 16) = m.mw[i]; }
}
__device__ __forceinline__ void rows_pre(RowsPre& r, const char* sm) {
    const int hl = threadIdx.x & 31;
#pragma unroll
    for (int i = 0; i < 9; ++i) { r.x[2 * i] = bf_lo(r.xw[i]); r.x[2 * i + 1] = bf_hi(r.xw[i]); }
    float s = 0.f;
#pragma unroll
    for (int i = 0; i < 18; ++i) s += r.x[i];
    const float mean = half_sum(s) * (1.0f / 576.0f);
    float vs = 0.f;
#pragma unroll
    for (int i = 0; i < 18; ++i) { const float dl = r.x[i] - mean; vs += dl * dl; }
    const float rstd = __builtin_amdgcn_rsqf(half_sum(vs) * (1.0f / 576.0f) + 1e-5f);
    const float* mod = (const float*)(sm + R_MOD);
#pragma unroll
    for (int i = 0; i < 9; ++i) {
        const int c = 2 * (hl + 32 * i);
        const f32x2 sa = *(const f32x2*)(mod + c), c1 = *(const f32x2*)(mod + 576 + c), ga = *(const f32x2*)(mod + 2 * 576 + c);
        r.gh[2 * i] = ga[0] * ((r.x[2 * i] - mean) * rstd * c1[0] + sa[0]);
        r.gh[2 * i + 1] = ga[1] * ((r.x[2 * i + 1] - mean) * rstd * c1[1] + sa[1]);
    }
}
__device__ __forceinline__ void rows_fetch(int t0, char* sm, const char* ws) {
    const int lane = threadIdx.x & 63;
    const char* src = ws + WS_YYT + t0 * 2;
    const u32 dst = (u32)(uintptr_t)(sm + R_HT);
#pragma unroll
    for (int i = 0; i < 9; ++i)
        asm volatile("s_mov_b32 m0, %2\n\ts_nop 0\n\tglobal_load_lds_dwordx4 %0, %1 sc1" :: "v"((u32)((lane + 64 * i) * (S_ * 2))), "s"(src), "s"(dst + i * 1024) : "memory");
    asm volatile("s_waitcnt vmcnt(0)" ::: "memory");
}
__device__ __forceinline__ void rows_main(const RowsPre& r, int t0, char* sm, rsrc_t wsr) {
    const int tid = threadIdx.x, lane = tid & 63, hl = lane & 31, row = 2 * (tid >> 6) + (lane >> 5);
    const int cg = tid >> 1, th = tid & 1;
    if (tid < 144) {
        u32x2 yw[8];
#pragma unroll
        for (int c = 0; c < 8; ++c) yw[c] = *(const u32x2*)(sm + R_HT + (8 * cg + c) * 16 + th * 8);
#pragma unroll
        for (int t = 0; t < 4; ++t) {
            u32x4 o;
#pragma unroll
            for (int kk = 0; kk < 4; ++kk) {
                const u32 a = yw[2 * kk][t >> 1], bq = yw[2 * kk + 1][t >> 1];
                o[kk] = (t & 1) ? __builtin_amdgcn_perm(bq, a, 0x07060302u) : __builtin_amdgcn_perm(bq, a, 0x05040100u);
            }
            *(u32x4*)(sm + R_YT + (4 * th + t) * 1168 + cg * 16) = o;
        }
    }
    __syncthreads();
    const float* mod = (const float*)(sm + R_MOD);
    float x1[18];
#pragma unroll
    for (int i = 0; i < 9; ++i) {
        u32* yp = (u32*)(sm + R_YT + row * 1168 + (hl + 32 * i) * 4);
        const u32 yv = *yp;
        const u32 p = pk2(r.x[2 * i] + r.gh[2 * i] * bf_lo(yv), r.x[2 * i + 1] + r.gh[2 * i + 1] * bf_hi(yv));
        *yp = p;
        x1[2 * i] = bf_lo(p); x1[2 * i + 1] = bf_hi(p);
    }
    float s = 0.f;
#pragma unroll
    for (int i = 0; i < 18; ++i) s += x1[i];
    const float mean = half_sum(s) * (1.0f / 576.0f);
    float vs = 0.f;
#pragma unroll
    for (int i = 0; i < 18; ++i) { const float dl = x1[i] - mean; vs += dl * dl; }
    const float rstd = __builtin_amdgcn_rsqf(half_sum(vs) * (1.0f / 576.0f) + 1e-5f);
#pragma unroll
    for (int i = 0; i < 9; ++i) {
        const int c = 2 * (hl + 32 * i);
        const f32x2 sb = *(const f32x2*)(mod + 3 * 576 + c), c1 = *(const f32x2*)(mod + 4 * 576 + c);
        const float h0 = (x1[2 * i] - mean) * rstd * c1[0] + sb[0], h1 = (x1[2 * i + 1] - mean) * rstd * c1[1] + sb[1];
        const int ks = c >> 5, kk = c & 31, gq = (kk & 15) >> 2, j = ((kk >> 4) << 2) | (kk & 3);
        *(u32*)(sm + R_HT + ((ks * 4 + gq) * 8 + row) * 16 + j * 2) = pk2(h0, h1);
    }
    __syncthreads();
    const u32 h2t = (u32)WS_H2T + (u32)(t0 >> 4) * 18 * 1024 + (t0 & 15) * 16, x1g = (u32)WS_X1 + (u32)t0 * D_ * 2;
#pragma unroll
    for (int it = 0; it < 3; ++it) {
        const int i = tid + 256 * it;
        if (i < 576) {
            const int kg = i >> 3, rr8 = i & 7;
            st16_dev(wsr, h2t + (kg >> 2) * 1024 + (kg & 3) * 256 + rr8 * 16, *(const u32x4*)(sm + R_HT + i * 16));
            const int rr = i / 72, cc = i - rr * 72;
            st16_dev(wsr, x1g + rr * 1152 + cc * 16, *(const u32x4*)(sm + R_YT + rr * 1168 + cc * 16));
        }
    }
}

namespace mlp {
constexpr int STG = 36864, LDS_BYTES = 163840;
constexpr int G1S = 9 * 144, G2S = 18 * 36, NS = G1S + G2S;
constexpr int LA = 4;
constexpr int PRO = 9 + 4 * LA + 18;
constexpr int TUS = 147456;

constexpr int F01 = 288;
constexpr int stage_of(int gs) { return gs < F01 ? 0 : gs < G1S ? gs / 144 : 9 + (gs - G1S) / 72; }
constexpr int rel_of(int gs) { return gs < F01 ? gs : gs < G1S ? gs % 144 : (gs - G1S) % 72; }
constexpr int grp_of(int gs) { return rel_of(gs) >> 2; }
constexpr int t_of(int gs) { return rel_of(gs) & 3; }
constexpr bool barpt(int gs) { return gs >= F01 && gs < NS && t_of(gs) == 3 && grp_of(gs) == (gs < G1S ? 3 : 0) && stage_of(gs) < 17; }
constexpr int dma_id(int gs) {
    if (gs >= NS || t_of(gs) != 3) return -1;
    const int g = grp_of(gs), s = stage_of(gs);
    if (gs < F01) return (g >= 36 && g < 72 && (g & 3) == 3) ? 3 * 16 + (g - 36) / 4 : -1;
    if (s == 2) return (g >= 4 && g < 22) ? ((g & 1) ? 5 : 4) * 16 + (g - 4) / 2 : -1;
    if (s + 3 > 17) return -1;
    if (s == 8) return (g >= 4 && g < 13) ? 11 * 16 + g - 4 : -1;
    if (gs < G1S) { constexpr int G[9] = {5, 7, 9, 12, 14, 16, 20, 22, 24}; for (int i = 0; i < 9; ++i) if (G[i] == g) return (s + 3) * 16 + i; return -1; }
    return (g & 1) ? (s + 3) * 16 + (g >> 1) : -1;
}
constexpr int bload(int gs) {
    if (gs >= F01 || t_of(gs) != 1) return -1;
    const int ks = 2 * (gs / 32) + LA + (((gs % 32) >> 2) >> 2), i = (gs >> 2) & 3;
    return ks < 18 ? ks * 4 + i : -1;
}
constexpr bool unit_stores(int v) { return v == 1 || v == 3 || v == 4 || v == 6 || v == 8; }
constexpr int unit_end(int v) { return v < 2 ? F01 : 144 * (v + 1); }
constexpr int hload(int gs) {
    if (t_of(gs) != 3) return -1;
    const int s = stage_of(gs), g = grp_of(gs);
    if (s == 8) return (g >= 13 && g < 33) ? g - 13 : -1;
    if (s < 9 || g < 2 || g > 16 || (g & 1)) return -1;
    const int i = (g - 2) >> 1;
    if (s == 9) return 20 + i;
    if (s == 10) return 28 + i;
    if (s == 11) return i < 4 ? 36 + i : -1;
    if (s >= 12 && s <= 15) return 40 + 8 * (s - 12) + i;
    return -1;
}
constexpr int pub(int gs) { return (stage_of(gs) == 6 && grp_of(gs) == 27 && t_of(gs) == 3) ? 1 : (stage_of(gs) == 10 && grp_of(gs) == 9 && t_of(gs) == 1) ? 2 : 0; }
constexpr int poll_issue(int gs) { return (stage_of(gs) == 7 && grp_of(gs) == 27 && t_of(gs) == 3) ? 1 : (stage_of(gs) == 11 && grp_of(gs) == 9 && t_of(gs) == 1) ? 2 : 0; }
struct EpiSched { signed char ev[G1S], ep[G1S], sv[G1S], st[G1S]; };
constexpr EpiSched make_epi() {
    EpiSched e{};
    int v = 0, p = 0, pend_v = -1, pend_lo = 0, pend_hi = 0;
    for (int gs = 0; gs < G1S; ++gs) {
        e.ev[gs] = -1; e.ep[gs] = -1; e.sv[gs] = -1; e.st[gs] = -1;
        if (gs < F01) continue;
        const int t = t_of(gs);
        if (t == 0) continue;
        const bool vm = barpt(gs) || dma_id(gs) >= 0 || hload(gs) >= 0 || pub(gs) || poll_issue(gs);
        if (t == 3 && !vm && pend_lo < pend_hi) { e.sv[gs] = (signed char)pend_v; e.st[gs] = (signed char)pend_lo; ++pend_lo; continue; }
        if (t == 3 && vm) continue;
        if (v < 8 && gs >= unit_end(v) + 8 && (pend_lo == pend_hi || pend_v == v || !unit_stores(v) || true)) {
            e.ev[gs] = (signed char)v; e.ep[gs] = (signed char)p;
            ++p;
            if ((p & 15) == 0 && unit_stores(v)) { if (pend_v != v) { pend_v = v; pend_lo = p / 16 - 1; } pend_hi = p / 16; }
            if (p == 64) { p = 0; ++v; }
        }
    }
    return e;
}
constexpr EpiSched ES = make_epi();
constexpr int hstore(int gs) { return gs < G1S ? ES.st[gs] : -1; }
constexpr int epi_last(int v) { for (int gs = G1S - 1; gs >= 0; --gs) if (ES.ev[gs] == v) return gs; return -1; }
constexpr int store_last(int v) { for (int gs = G1S - 1; gs >= 0; --gs) if (ES.sv[gs] == v && ES.st[gs] == 3) return gs; return -1; }
constexpr bool epi_ok() {
    for (int v = 0; v < 8; ++v) {
        if (epi_last(v) < 0 || ES.ep[epi_last(v)] != 63) return false;
        if (v + 3 <= 8 && epi_last(v) >= 144 * (v + 3)) return false;
        if (unit_stores(v) && store_last(v) < 0) return false;
    }
    return store_last(4) + 40 < 6 * 144 + 27 * 4;
}
static_assert(epi_ok(), "epilogue schedule");
constexpr int vm_ops(int gs) { return (pub(gs) ? 1 : 0) + (poll_issue(gs) ? 1 : 0) + (hstore(gs) >= 0) + (bload(gs) >= 0) + (hload(gs) >= 0) + (dma_id(gs) >= 0); }
struct Ledger { int cum[NS + 1]; };
constexpr Ledger make_ledger() {
    Ledger l{}; int n = PRO;
    for (int gs = 0; gs < NS; ++gs) { if (gs == G1S) n += 4; l.cum[gs] = n; n += vm_ops(gs); }
    l.cum[NS] = n; return l;
}
constexpr Ledger LG = make_ledger();
constexpr int seq_in(int gs, int kind) {
    int n = LG.cum[gs];
    if (kind > 0 && pub(gs)) ++n;
    if (kind > 1 && poll_issue(gs)) ++n;
    if (kind > 2 && hstore(gs) >= 0) ++n;
    if (kind > 3 && bload(gs) >= 0) ++n;
    if (kind > 4 && hload(gs) >= 0) ++n;
    return n;
}
constexpr int seq_dma_last(int s) {
    if (s <= 2) return 9 * s + 8;
    for (int gs = 0; gs < NS; ++gs) if (dma_id(gs) == s * 16 + 8) return seq_in(gs, 5);
    return -1;
}
constexpr int seq_b(int ks) {
    if (ks < LA) return 27 + 4 * ks + 3;
    for (int gs = 0; gs < F01; ++gs) if (bload(gs) == ks * 4 + 3) return seq_in(gs, 3);
    return -1;
}
constexpr int seq_h(int kk) {
    for (int gs = 8 * 144; gs < NS; ++gs) if (hload(gs) == kk * 4 + 3) return seq_in(gs, 4);
    return -1;
}
constexpr int seq_poll(int part) {
    for (int gs = 0; gs < NS; ++gs) if (poll_issue(gs) == part) return seq_in(gs, 1);
    return -1;
}
constexpr int seq_store_last(int part) {
    if (part == 2) return LG.cum[G1S] - 1;
    return seq_in(store_last(4), 2);
}
struct Work { int unit, mat, k, t, n; };
constexpr Work work_of(int gs) {
    if (gs < F01) { const int p = gs / 32, um = (gs % 32) / 8, r = gs % 8; return {um >> 1, um & 1, 2 * p + (r & 1), r >> 1, 8 * p + 2 * um + (r & 1)}; }
    if (gs < G1S) { const int u = gs / 144, bi = (gs % 144) / 24, r = gs % 24; return {u, bi & 1, 6 * (bi >> 1) + r % 6, r / 6, 36 * u + 6 * bi + r % 6}; }
    const int s = (gs - G1S) / 72, r = (gs - G1S) % 72, du = r / 8, r8 = r % 8;
    return {du, 0, 2 * s + (r8 & 1), r8 >> 1, 324 + 18 * s + 2 * du + (r8 & 1)};
}
constexpr int NFRAG = 486, RQ = 12, FR0 = 8;
constexpr int frag_first(int n) {
    if (n < 72) return 32 * (n / 8) + 8 * ((n % 8) >> 1) + (n & 1);
    if (n < 324) return 144 * (n / 36) + 24 * ((n % 36) / 6) + n % 6;
    return G1S + 72 * ((n - 324) / 18) + 8 * (((n - 324) % 18) >> 1) + (n & 1);
}
constexpr int frag_last(int n) { return frag_first(n) + ((n >= 72 && n < 324) ? 18 : 6); }
constexpr int frag_stage(int n) { return n < 72 ? (n % 8) >> 2 : n < 324 ? n / 36 : 9 + (n - 324) / 18; }
constexpr int frag_off(int n) {
    if (n < 72) return (2 * (2 * (n / 8) + (n & 1)) + (((n % 8) >> 1) & 1)) * 1024;
    if (n < 324) { const int bi = (n % 36) / 6; return (2 * (6 * (bi >> 1) + n % 6) + (bi & 1)) * 1024; }
    return (n & 1) * 9216 + (((n - 324) % 18) >> 1) * 32;
}
constexpr int stage_ready(int s) {
    if (s <= 2) return 0;
    for (int gs = 0; gs < NS; ++gs) if (barpt(gs) && stage_of(gs) == s - 1) return gs + 1;
    return NS;
}
struct PfSched { short id[NS]; };
constexpr PfSched make_pf() {
    PfSched t{};
    int m = FR0, rdy = stage_ready(frag_stage(FR0));
    for (int gs = 0; gs < NS; ++gs) {
        t.id[gs] = -1;
        if ((gs & 3) != 0 || m >= NFRAG) continue;
        if (m >= RQ && frag_last(m - RQ) >= gs) continue;
        if (rdy > gs) continue;
        t.id[gs] = (short)m;
        ++m;
        if (m < NFRAG && frag_stage(m) != frag_stage(m - 1)) rdy = stage_ready(frag_stage(m));
    }
    return t;
}
constexpr PfSched PF = make_pf();
constexpr bool pf_ok() {
    int cnt = FR0;
    for (int gs = 0; gs < NS; ++gs) if (PF.id[gs] >= 0) { if (PF.id[gs] != cnt || frag_first(cnt) - gs < 8) return false; ++cnt; }
    return cnt == NFRAG;
}
static_assert(pf_ok(), "fragment prefetch schedule");
constexpr int clampv(int n) { return n > 63 ? 63 : (n < 0 ? 0 : n); }
constexpr int wait_for(int gs, int seq) { return clampv(LG.cum[gs] - seq - 1); }

template <int N> __device__ __forceinline__ void wait_vm() { asm volatile("s_waitcnt vmcnt(%0)" :: "n"(N) : "memory"); }

struct Ctx {
    bf16x8 Bv[2][4];
    bf16x8 Aq[12];
    f32x4 ac[3][2][4];
    f32x4 a2[9][4];
    float e0, e1;
    u32x4 hp[4];
    u32 vo1[4], vo2[4], vo3[4];
    u32 lane16, epoch;
    u32x4 pollv;
    const LAS char* rd[4]; const LAS char* rd2[4];
    const char* w1src; const char* w2src; const char* bsrc; const char* hsrc;
    u32 voff2[9];
    int sub;
    u32 dmadst;
    u32 hso;
    int jj;
    __amdgpu_buffer_rsrc_t hrs;
    u32* flag_pub; u32* flag_poll;
};

template <int KS, int T, bool ZERO> __device__ __forceinline__ void mfma(Ctx& cx, f32x4& c, const bf16x8& a) {
    constexpr int r = (KS * 4 + T) * 4;
    if constexpr (KS < 16) {
        if constexpr (ZERO) asm volatile("v_mfma_f32_16x16x32_bf16 %0, %1, a[%c2:%c3], 0" : "=v"(c) : "v"(a), "n"(r), "n"(r + 3));
        else asm volatile("v_mfma_f32_16x16x32_bf16 %0, %1, a[%c2:%c3], %0" :: "v"(c), "v"(a), "n"(r), "n"(r + 3));
    } else {
        asm volatile("v_mfma_f32_16x16x32_bf16 %0, %1, %2, %0" :: "v"(c), "v"(a), "v"(cx.Bv[KS - 16][T]));
    }
}
template <int KS, int T, bool SC1> __device__ __forceinline__ void gload(Ctx& cx, u32 voff, const char* sb) {
    constexpr int r = (KS * 4 + T) * 4, off = (KS & 3) * 1024;
    if constexpr (KS < 16) {
        if constexpr (SC1) asm volatile("global_load_dwordx4 a[%c2:%c3], %0, %1 offset:%4 sc1" :: "v"(voff), "s"(sb), "n"(r), "n"(r + 3), "n"(off) : "memory");
        else asm volatile("global_load_dwordx4 a[%c2:%c3], %0, %1 offset:%4" :: "v"(voff), "s"(sb), "n"(r), "n"(r + 3), "n"(off) : "memory");
    } else {
        if constexpr (SC1) asm volatile("global_load_dwordx4 %0, %1, %2 offset:%3 sc1" : "=v"(cx.Bv[KS - 16][T]) : "v"(voff), "s"(sb), "n"(off) : "memory");
        else asm volatile("global_load_dwordx4 %0, %1, %2 offset:%3" : "=v"(cx.Bv[KS - 16][T]) : "v"(voff), "s"(sb), "n"(off) : "memory");
    }
}
template <int S, int P> __device__ __forceinline__ void dma_m0(const Ctx& c) {
    asm volatile("s_mov_b32 m0, %0" :: "s"(c.dmadst + (u32)((S & 3) * STG + (P >> 2) * 4096)) : "memory");
}
constexpr int nat_kstep(int ks2) {
    if (ks2 < 10) { constexpr int T[5] = {0, 1, 5, 6, 4}; return 9 * (ks2 / 5) + T[ks2 % 5]; }
    const int jj = (ks2 - 10) >> 1, h = (ks2 - 10) & 1;
    return 9 * (jj >> 1) + ((jj & 1) ? 7 : 2) + h;
}
template <int S, int P> __device__ __forceinline__ void dma_go(const Ctx& c) {
    if constexpr (S < 9) {
        const char* sb = c.w1src + (size_t)S * STG + (P >> 2) * 4096;
        asm volatile("global_load_lds_dwordx4 %0, %1 offset:%2 sc1" :: "v"(c.lane16), "s"(sb), "n"((P & 3) * 1024) : "memory");
    } else {
        constexpr int n0 = nat_kstep(2 * (S - 9)), n1 = nat_kstep(2 * (S - 9) + 1);
        const char* sb = c.w2src + (size_t)(c.sub ? n1 : n0) * (32 * D_ * 2);
        asm volatile("global_load_lds_dwordx4 %0, %1 offset:%2" :: "v"(c.voff2[P]), "s"(sb), "n"((P & 3) * 1024) : "memory");
    }
}
template <int ID> __device__ __forceinline__ void load_b(Ctx& c) {
    constexpr int ks = ID >> 2, t = ID & 3;
    gload<ks, t, true>(c, c.vo1[t], c.bsrc + (ks >> 2) * 4096);
}
template <int ID> __device__ __forceinline__ void load_h(Ctx& c) {
    constexpr int kk = ID >> 2, t = ID & 3;
    gload<kk, t, true>(c, c.vo3[t], c.hsrc + (kk >> 2) * 4096);
}
template <int KS> __device__ __forceinline__ void pin_b(Ctx& c) {
#pragma unroll
    for (int t = 0; t < 4; ++t) { if constexpr (KS >= 16) asm volatile("" : "+v"(c.Bv[KS - 16][t])); }
}
template <int V, int P> __device__ __forceinline__ void epi_piece(Ctx& c) {
    constexpr int pr = P >> 3, st = P & 7, t = pr >> 1, r = 2 * (pr & 1), par = V % 3;
    if constexpr (st == 0) {
        asm volatile("v_mul_f32 %0, 0xbfb8aa3b, %1" : "=v"(c.e0) : "v"(c.ac[par][0][t][r]));
        asm volatile("v_mul_f32 %0, 0xbfb8aa3b, %1" : "=v"(c.e1) : "v"(c.ac[par][0][t][r + 1]));
    } else if constexpr (st == 1) asm volatile("v_exp_f32 %0, %0" :: "v"(c.e0));
    else if constexpr (st == 2) asm volatile("v_exp_f32 %0, %0" :: "v"(c.e1));
    else if constexpr (st == 3) {
        asm volatile("v_add_f32 %0, 1.0, %0" :: "v"(c.e0));
        asm volatile("v_add_f32 %0, 1.0, %0" :: "v"(c.e1));
    } else if constexpr (st == 4) asm volatile("v_rcp_f32 %0, %0" :: "v"(c.e0));
    else if constexpr (st == 5) asm volatile("v_rcp_f32 %0, %0" :: "v"(c.e1));
    else if constexpr (st == 6) {
        asm volatile("v_mul_f32 %0, %0, %1" :: "v"(c.e0), "v"(c.ac[par][1][t][r]));
        asm volatile("v_mul_f32 %0, %0, %1" :: "v"(c.e1), "v"(c.ac[par][1][t][r + 1]));
    } else {
        constexpr bool first = (V == 0 || V == 2 || V == 5 || V == 7);
        u32 o;
        asm volatile("v_cvt_pk_bf16_f32 %0, %1, %2" : "=v"(o) : "v"(c.e0), "v"(c.e1));
        c.hp[t][(first ? 0 : 2) + (pr & 1)] = o;
    }
}
template <int V, int T> __device__ __forceinline__ void store_h(Ctx& c) {
    const int m5 = 5 * (c.jj >> 1), ab = c.jj & 1;
    if constexpr (V == 4) {
        const u32x2 d = {c.hp[T][2], c.hp[T][3]};
        __builtin_amdgcn_raw_buffer_store_b64(d, c.hrs, c.vo2[T] + 8 * ab, c.hso + (m5 + 4) * 1024, 16);
    } else {
        const int kstep = (V == 1) ? m5 + 2 * ab : (V == 3) ? m5 + 2 * ab + 1 : (V == 6) ? 10 + 2 * c.jj : 11 + 2 * c.jj;
        __builtin_amdgcn_raw_buffer_store_b128(c.hp[T], c.hrs, c.vo2[T], c.hso + kstep * 1024, 16);
    }
}
template <int PART> __device__ __forceinline__ void poll_check(Ctx& c) {
    asm volatile("" : "+v"(c.pollv));
    const u32 e = c.epoch;
    if (!__all(c.pollv[0] == e && c.pollv[1] == e && c.pollv[2] == e && c.pollv[3] == e)) {
        const u32* p = c.flag_poll + (PART - 1) * 1024 + ((c.lane16 >> 4) & 3);
        poll_eq(p, e);
    }
}
template <int V, int P> __device__ __forceinline__ void epi_piece_padded(Ctx& c) {
    epi_piece<V, P>(c);
    if constexpr ((P & 7) == 2 || (P & 7) == 5) asm volatile("s_nop 0");
}
template <int V, int... P> __device__ __forceinline__ void epi_all(Ctx& c, std::integer_sequence<int, P...>) { (epi_piece_padded<V, P>(c), ...); }

template <int GS> __device__ __forceinline__ void slot(Ctx& c) {
    constexpr bool g1 = GS < G1S;
    constexpr int S = stage_of(GS), R = rel_of(GS), grp = R >> 2, t = R & 3;
    constexpr Work W = work_of(GS);
    constexpr bool f01 = GS < F01;
    if constexpr (GS == G1S) {
        asm volatile("s_nop 7" ::: "memory");
        epi_all<8>(c, std::make_integer_sequence<int, 64>{});
        store_h<8, 0>(c); store_h<8, 1>(c); store_h<8, 2>(c); store_h<8, 3>(c);
        SCHED();
    }
    if constexpr (f01 && GS % 32 == 0) { wait_vm<wait_for(GS, seq_b(2 * (GS / 32) + 1))>(); pin_b<2 * (GS / 32)>(c); pin_b<2 * (GS / 32) + 1>(c); }
    if constexpr (GS == 8 * 144) { wait_vm<wait_for(GS, seq_poll(1))>(); poll_check<1>(c); }
    if constexpr (!g1 && R == 0) { wait_vm<wait_for(GS, seq_h(2 * (S - 9) + 1))>(); pin_b<2 * (S - 9)>(c); pin_b<2 * (S - 9) + 1>(c); }
    if constexpr (dma_id(GS) >= 0) dma_m0<dma_id(GS) / 16, dma_id(GS) % 16>(c);
    if constexpr (g1) mfma<W.k, W.t, (W.k == 0)>(c, c.ac[W.unit % 3][W.mat][W.t], c.Aq[W.n % RQ]);
    else mfma<W.k, W.t, (W.k == 0)>(c, c.a2[W.unit][W.t], c.Aq[W.n % RQ]);
    if constexpr (barpt(GS)) {
        constexpr int n1 = wait_for(GS, seq_dma_last(S + 1));
        constexpr int n2 = (S == 12) ? wait_for(GS, seq_poll(2)) : 63;
        wait_vm<(n1 < n2 ? n1 : n2)>();
        if constexpr (S == 12) poll_check<2>(c);
        __builtin_amdgcn_s_barrier();
        asm volatile("" ::: "memory");
    }
    if constexpr (pub(GS)) {
        wait_vm<wait_for(GS, seq_store_last(pub(GS)))>();
        if (c.lane16 == 0) flag_set(c.flag_pub + (pub(GS) - 1) * 1024, c.epoch);
    }
    if constexpr (poll_issue(GS)) asm volatile("global_load_dwordx4 %0, %1, %2 sc1" : "=v"(c.pollv) : "v"(0u), "s"(c.flag_poll + (poll_issue(GS) - 1) * 1024) : "memory");
    {
        constexpr int f = PF.id[GS], fh = (GS & 3) == 1 ? PF.id[GS - ((GS & 3) == 1 ? 1 : 0)] : -1;
        if constexpr (f >= 0 && f < 324) c.Aq[f % RQ] = *(const LAS bf16x8*)(c.rd[frag_stage(f) & 3] + frag_off(f));
        constexpr int ft = f >= 324 ? f : fh >= 324 ? fh : -1;
        if constexpr (ft >= 0) {
            constexpr int half = f >= 324 ? 0 : 1;
            const s16x4 h = __builtin_amdgcn_ds_read_tr16_b64_v4i16((LAS s16x4*)(c.rd2[frag_stage(ft) & 3] + frag_off(ft) + half * (16 * 288)));
            const u32x2 hh = __builtin_bit_cast(u32x2, h);
            u32x4 a = __builtin_bit_cast(u32x4, c.Aq[ft % RQ]);
            a[2 * half] = hh[0]; a[2 * half + 1] = hh[1];
            c.Aq[ft % RQ] = __builtin_bit_cast(bf16x8, a);
        }
    }
    if constexpr (g1) {
        if constexpr (ES.ev[g1 ? GS : 0] >= 0) epi_piece<ES.ev[g1 ? GS : 0], ES.ep[g1 ? GS : 0]>(c);
        if constexpr (ES.sv[g1 ? GS : 0] >= 0) store_h<ES.sv[g1 ? GS : 0], ES.st[g1 ? GS : 0]>(c);
    }
    if constexpr (bload(GS) >= 0) load_b<bload(GS)>(c);
    if constexpr (hload(GS) >= 0) load_h<hload(GS)>(c);
    if constexpr (dma_id(GS) >= 0) dma_go<dma_id(GS) / 16, dma_id(GS) % 16>(c);
    SCHED();
}
template <int BASE, int... I> __device__ __forceinline__ void run_slots(Ctx& c, std::integer_sequence<int, I...>) { (slot<BASE + I>(c), ...); }
template <int S, int P> __device__ __forceinline__ void dma_one(const Ctx& c) { dma_m0<S, P>(c); asm volatile("s_nop 0"); dma_go<S, P>(c); }
template <int S, int... P> __device__ __forceinline__ void dma_stage(const Ctx& c, std::integer_sequence<int, P...>) { (dma_one<S, P>(c), ...); }
template <int... I> __device__ __forceinline__ void load_b_first(Ctx& c, std::integer_sequence<int, I...>) { (load_b<I>(c), ...); }
}

__device__ __forceinline__ void touch(u32& d, const char* p) { asm volatile("global_load_dword %0, %1, off" : "=v"(d) : "v"(p) : "memory"); }
__global__ __launch_bounds__(256, 1) void k_main(const u16* __restrict__ qin, const u16* __restrict__ kin, const u16* __restrict__ vin, const u16* __restrict__ shift_k,
                                                 const u16* __restrict__ ssm_k, const u16* __restrict__ ssm_D, const u16* __restrict__ wg, const u16* __restrict__ wu,
                                                 const u16* __restrict__ wd, const u16* __restrict__ cvec, const u16* __restrict__ w_mod,
                                                 char* __restrict__ ws, u16* __restrict__ out) {
    using namespace mlp;
    extern __shared__ __attribute__((aligned(16))) char smem[];
    const int tid = threadIdx.x, lane = tid & 63, w = __builtin_amdgcn_readfirstlane(tid >> 6);
    const int b = blockIdx.x, x = b & 7, idx = b >> 3, q = x >> 1;
    const int mt = 4 * (x & 1) + (idx & 3), fcl = idx >> 2, fc = 8 * q + fcl;
    const int mt2 = 8 * (x & 1) + (idx & 7), cc = idx >> 3, tg = w & 1, kh = w >> 1;
    const rsrc_t wsr = __builtin_amdgcn_make_buffer_rsrc(ws, 0, (int)WS_END, 0x00020000);
    STAMP_DECL; STAMP_AT(0);
    u32 epoch_in;
    unsigned long long pc_start;
    asm volatile("s_getpc_b64 %0" : "=s"(pc_start));
    u32 epoch;
    {
        const int t0 = (x * 32 + idx) * 8;
        RowsPre rp;
        u32 bg[10] = {0, 0, 0, 0, 0, 0, 0, 0, 0, 0};
        const int c0 = 72 * x + 3 * idx;
        KvIn kin_;
        if (b < 192) {
            touch(bg[8], (const char*)(b < 96 ? kin : vin) + (size_t)((b % 96) * 192 + (tid < 192 ? tid : 0)) * 128);
            kv_load(kin_, c0, kin, vin, shift_k);
        }
        int b2 = b;
        asm volatile("" : "+s"(b2));
        if (b2 < 192) {
            ConvIn in[3];
            epoch_in = __hip_atomic_load(flag_ptr(ws, F_EPOCH), __ATOMIC_RELAXED, __HIP_MEMORY_SCOPE_AGENT);
            conv_load_k(in[0], c0, ssm_k, ssm_D); conv_load_k(in[1], c0 + 1, ssm_k, ssm_D); conv_load_k(in[2], c0 + 2, ssm_k, ssm_D);
            rows_load(rp, t0, qin);
            kv_put(kin_, smem + C_RED);
            conv_put_k(in[0], smem); conv_put_k(in[1], smem + C_CH); conv_put_k(in[2], smem + 2 * C_CH);
            __syncthreads();
            STAMP_AT(1);
            kv_compute(kin_, c0, smem + C_RED, smem + C_XR, C_CH);
            epoch = __builtin_amdgcn_readfirstlane(epoch_in) + 1u;
            if (w == 3) {
                DRAIN();
#pragma unroll
                for (int i = 0; i < 8; ++i) { const int l = lane + 64 * (i & 3); touch(bg[i], (const char*)(i < 4 ? wg : wu) + (size_t)b * 27648 + (l < 216 ? l : 0) * 128); }
            }
            STAMP_AT(2);
            ModIn mi;
            const u32* mf = flag_ptr(ws, F_MOD + (lane < 54 ? lane : 0));
            u32 pm;
            asm volatile("global_load_dword %0, %1, off sc1" : "=v"(pm) : "v"(mf) : "memory");
            conv_body(c0, in, smem, wsr, [&]() {
                DRAIN();
                asm volatile("" : "+v"(pm));
                if (!__all(pm == epoch)) poll_eq(mf, epoch);
                mod_load(mi, wsr); } STAMP_ARGS);
            STAMP_AT(5);
            mod_put(mi, smem);
            DRAIN();
#pragma unroll
            for (int i = 0; i < 10; ++i) asm volatile("" :: "v"(bg[i]));
            __syncthreads();
            if (tid < 8) flag_set(flag_ptr(ws, F_CONV + tid * 256 + b), epoch);
            if (w == 3) {
#pragma unroll
                for (int i = 0; i < 4; ++i) { const int l = lane + 64 * i; touch(bg[i], (const char*)wd + (size_t)b * 27648 + (l < 216 ? l : 0) * 128); }
            }
        } else {
            if (w == 3 && idx < 28) {
                const char* cb = (const char*)(pc_start & ~127ull) + lane * 128;
#pragma unroll
                for (int i = 0; i < 8; ++i) touch(bg[i], cb + (i < 7 || lane < 32 ? i * 8192 : 0));
            }
            if (w == 3 && idx >= 28) {
#pragma unroll
                for (int i = 0; i < 3; ++i) { const size_t o = (size_t)(((idx - 28) * 3 + i) * 64 + lane) << 16; touch(bg[i], ws + (o < WS_END ? o : 0)); }
                touch(bg[3], (const char*)out + (size_t)(lane < 36 ? lane : 0) * 65536);
                const size_t pg = (size_t)(lane + 64 * (idx & 1) < 81 ? lane + 64 * (idx & 1) : 0) * 65536;
                touch(bg[4], (const char*)wd + pg); touch(bg[5], (const char*)wg + pg); touch(bg[6], (const char*)wu + pg);
            }
            for (int i = 0; i < 40; ++i) __builtin_amdgcn_s_sleep(2);
            rows_load(rp, t0, qin);
            epoch_in = __hip_atomic_load(flag_ptr(ws, F_EPOCH), __ATOMIC_RELAXED, __HIP_MEMORY_SCOPE_AGENT);
            if (b - 192 < 54) mod_job(b - 192, cvec, w_mod, ws, (float*)(smem + R_MOD), (float*)(smem + R_MOD + 4096));
            epoch = __builtin_amdgcn_readfirstlane(epoch_in) + 1u;
            DRAIN();
            __syncthreads();
            if (tid == 0 && b - 192 < 54) flag_set(flag_ptr(ws, F_MOD + b - 192), epoch);
            STAMP_AT(1);
            tile_jobs(b - 192, wg, wu, smem, ws, wsr, epoch);
            STAMP_AT(2); STAMP_AT(3); STAMP_AT(4); STAMP_AT(5);
            poll_eq(flag_ptr(ws, F_MOD + (lane < 54 ? lane : 0)), epoch);
            ModIn mi;
            mod_load(mi, wsr);
            mod_put(mi, smem);
            __syncthreads();
        }
        rows_pre(rp, smem);
        STAMP_AT(6);
        if (w == 0) {
            poll_eq4(wsr, (u32)WS_CNT + (F_CONV + x * 256 + (lane < 48 ? lane : 0) * 4) * 4, epoch);
            rows_fetch(t0, smem, ws);
        }
        __syncthreads();
        STAMP_AT(7);
        rows_main(rp, t0, smem, wsr);
        DRAIN();
#pragma unroll
        for (int i = 0; i < 10; ++i) asm volatile("" :: "v"(bg[i]));
        __syncthreads();
        if (tid == 0) flag_set(flag_ptr(ws, F_ROWS + (t0 >> 6) * 8 + ((t0 >> 3) & 7)), epoch);
        STAMP_AT(8);
    }
    Ctx c;
#pragma unroll
    for (int t = 0; t < 4; ++t) c.hp[t] = (u32x4){0, 0, 0, 0};
    c.lane16 = lane * 16;
#pragma unroll
    for (int t = 0; t < 4; ++t) { c.vo1[t] = lane * 16 + t * 18432; c.vo2[t] = lane * 16 + t * TUS; c.vo3[t] = lane * 16 + ((t + 2 * kh) & 3) * TUS; }
    const LAS char* l0 = (const LAS char*)smem;
#pragma unroll
    for (int i = 0; i < 4; ++i) {
        c.rd[i] = l0 + i * STG + lane * 16; c.rd2[i] = l0 + i * STG + kh * 18432 + (4 * (lane >> 4) + ((lane & 15) >> 2)) * 288 + 8 * (lane & 3);
        asm volatile("" : "+v"(c.rd[i])); asm volatile("" : "+v"(c.rd2[i]));
    }
#pragma unroll
    for (int i = 0; i < 9; ++i) { const int ci = lane + 64 * i, row = (ci * 3641) >> 16, c16 = ci - row * 18; c.voff2[i] = (u32)(row * (D_ * 2) + c16 * 16 - (i & 3) * 1024); }
    c.sub = w & 1;
    c.w1src = ws + WS_WT1 + (size_t)fc * 331776 + w * 9216;
    c.w2src = (const char*)(wd + (size_t)(36 * q + 18 * kh) * 32 * D_ + cc * 144);
    c.bsrc = ws + WS_H2T + (size_t)(16 * mt + 4 * w) * 18432;
    c.hsrc = ws + WS_HT + (size_t)(8 * mt2 + 4 * tg) * TUS + (size_t)((q * 2 + kh) * 18) * 1024;
    c.dmadst = (u32)(uintptr_t)smem + w * 9216;
    c.jj = fcl & 3;
    c.hso = (u32)((16 * mt + 4 * w) * TUS + ((q * 2 + (fcl >> 2)) * 18) * 1024);
    c.hrs = __builtin_amdgcn_make_buffer_rsrc(ws + WS_HT, 0, (int)((size_t)S_ * F_ * 2), 0x00020000);
    c.epoch = epoch;
    c.flag_pub = flag_ptr(ws, F_H + ((((mt * 4 + w) * 4 + q) * 2 + (fcl >> 2))) * 4 + (fcl & 3));
    c.flag_poll = flag_ptr(ws, F_H + (((((mt2 >> 1) * 4 + 2 * (mt2 & 1) + tg) * 4 + q) * 2 + kh)) * 4);
    asm volatile("" ::: "a0", "a1", "a2", "a3", "a4", "a5", "a6", "a7", "a8", "a9", "a10", "a11", "a12", "a13", "a14", "a15", "a16", "a17", "a18", "a19", "a20", "a21", "a22", "a23", "a24", "a25", "a26", "a27", "a28", "a29", "a30", "a31", "a32", "a33", "a34", "a35", "a36", "a37", "a38", "a39", "a40", "a41", "a42", "a43", "a44", "a45", "a46", "a47", "a48", "a49", "a50", "a51", "a52", "a53", "a54", "a55", "a56", "a57", "a58", "a59", "a60", "a61", "a62", "a63", "a64", "a65", "a66", "a67", "a68", "a69", "a70", "a71", "a72", "a73", "a74", "a75", "a76", "a77", "a78", "a79", "a80", "a81", "a82", "a83", "a84", "a85", "a86", "a87", "a88", "a89", "a90", "a91", "a92", "a93", "a94", "a95", "a96", "a97", "a98", "a99", "a100", "a101", "a102", "a103", "a104", "a105", "a106", "a107", "a108", "a109", "a110", "a111", "a112", "a113", "a114", "a115", "a116", "a117", "a118", "a119", "a120", "a121", "a122", "a123", "a124", "a125", "a126", "a127", "a128", "a129", "a130", "a131", "a132", "a133", "a134", "a135", "a136", "a137", "a138", "a139", "a140", "a141", "a142", "a143", "a144", "a145", "a146", "a147", "a148", "a149", "a150", "a151", "a152", "a153", "a154", "a155", "a156", "a157", "a158", "a159", "a160", "a161", "a162", "a163", "a164", "a165", "a166", "a167", "a168", "a169", "a170", "a171", "a172", "a173", "a174", "a175", "a176", "a177", "a178", "a179", "a180", "a181", "a182", "a183", "a184", "a185", "a186", "a187", "a188", "a189", "a190", "a191", "a192", "a193", "a194", "a195", "a196", "a197", "a198", "a199", "a200", "a201", "a202", "a203", "a204", "a205", "a206", "a207", "a208", "a209", "a210", "a211", "a212", "a213", "a214", "a215", "a216", "a217", "a218", "a219", "a220", "a221", "a222", "a223", "a224", "a225", "a226", "a227", "a228", "a229", "a230", "a231", "a232", "a233", "a234", "a235", "a236", "a237", "a238", "a239", "a240", "a241", "a242", "a243", "a244", "a245", "a246", "a247", "a248", "a249", "a250", "a251", "a252", "a253", "a254", "a255");
    poll_eq(flag_ptr(ws, F_TILE + (fc >> 2) * 36 + (lane < 36 ? lane : 0)), epoch);
    dma_stage<0>(c, std::make_integer_sequence<int, 9>{});
    dma_stage<1>(c, std::make_integer_sequence<int, 9>{});
    dma_stage<2>(c, std::make_integer_sequence<int, 9>{});
    poll_eq(flag_ptr(ws, F_ROWS + (4 * mt + w) * 8 + (lane & 7)), epoch);
    STAMP_AT(9);
    load_b_first(c, std::make_integer_sequence<int, 4 * LA>{});
    wait_vm<4 * LA>();
    __builtin_amdgcn_s_barrier();
    asm volatile("" ::: "memory");
#pragma unroll
    for (int f = 0; f < FR0; ++f) c.Aq[f] = *(const LAS bf16x8*)(c.rd[frag_stage(f)] + frag_off(f));
    SCHED();
    STAMP_AT(10);
    run_slots<0>(c, std::make_integer_sequence<int, 288>{});
    STAMP_AT(11);
    run_slots<288>(c, std::make_integer_sequence<int, 144 * 3>{});
    run_slots<144 * 5>(c, std::make_integer_sequence<int, 144 * 4>{});
    STAMP_AT(12);
    run_slots<G1S>(c, std::make_integer_sequence<int, 324>{});
    run_slots<G1S + 324>(c, std::make_integer_sequence<int, 324>{});
    asm volatile("s_nop 7" ::: "memory");
#pragma unroll
    for (int u = 0; u < 9; ++u)
#pragma unroll
        for (int t = 0; t < 4; ++t) asm volatile("" : "+v"(c.a2[u][t]));
    SCHED();
    STAMP_AT(13);
    const int n = lane & 15, g = lane >> 4;
    u32x2 xr[3][2]; f32x4 gb[3];
#pragma unroll
    for (int i = 0; i < 3; ++i) {
        const int u = (w + 4 * i < 9) ? w + 4 * i : 8, d = 144 * cc + 16 * u + 4 * g;
#pragma unroll
        for (int t = 0; t < 2; ++t) xr[i][t] = __builtin_amdgcn_raw_buffer_load_b64(wsr, (u32)WS_X1 + (u32)((128 * mt2 + 32 * q + 16 * t + n) * D_ + d) * 2, 0, 16);
        gb[i] = *(const f32x4*)(smem + R_MOD + (5 * 576 + d) * 4);
    }
    {
        char* ex = smem + 73728 + w * 18432 + lane * 16;
#pragma unroll
        for (int t = 0; t < 2; ++t)
#pragma unroll
            for (int u = 0; u < 9; ++u) *(f32x4*)(ex + (t * 9 + u) * 1024) = c.a2[u][2 + t];
    }
    __syncthreads();
    const int j = 2 * tg + kh;
    u32* fred = flag_ptr(ws, F_RED + (mt2 * 4 + cc) * 16);
    const u32 slab = (u32)WS_SLAB + (u32)((mt2 * 4 + cc) * 16) * 9216;
    {
        const char* ey = smem + 73728 + (w ^ 2) * 18432 + lane * 16;
        char* own = smem + lane * 16;
#pragma unroll
        for (int u = 0; u < 9; ++u) {
            const f32x4 r0 = c.a2[u][0] + *(const f32x4*)(ey + u * 1024);
            const f32x4 r1 = c.a2[u][1] + *(const f32x4*)(ey + (9 + u) * 1024);
            if (j != q) st16_dev(wsr, slab + (j * 4 + q) * 9216 + u * 1024 + lane * 16, (u32x4){pk2(r0[0], r0[1]), pk2(r0[2], r0[3]), pk2(r1[0], r1[1]), pk2(r1[2], r1[3])});
            else { *(f32x4*)(own + u * 2048) = r0; *(f32x4*)(own + u * 2048 + 1024) = r1; }
        }
    }
    if (j != q) {
        DRAIN();
        if (lane == 0) flag_set(fred + j * 4 + q, epoch);
    }
    STAMP_AT(14);
    poll_eq(fred + q * 4 + (((lane & 3) == q) ? (q ^ 1) : (lane & 3)), epoch);
    __syncthreads();
    u32x4 rem[3][3];
#pragma unroll
    for (int i = 0; i < 3; ++i) {
        const int u = (w + 4 * i < 9) ? w + 4 * i : 8;
#pragma unroll
        for (int k = 0; k < 3; ++k) { const int qq = k + (k >= q ? 1 : 0); rem[i][k] = __builtin_amdgcn_raw_buffer_load_b128(wsr, slab + (q * 4 + qq) * 9216 + u * 1024 + lane * 16, 0, 16); }
    }
#pragma unroll
    for (int i = 0; i < 3; ++i) {
        const int u = w + 4 * i;
        if (u < 9) {
            const int d = 144 * cc + 16 * u + 4 * g;
#pragma unroll
            for (int t = 0; t < 2; ++t) {
                f32x4 s = *(const f32x4*)(smem + u * 2048 + t * 1024 + lane * 16);
#pragma unroll
                for (int k = 0; k < 3; ++k) {
                    const u32 a = rem[i][k][2 * t], bq = rem[i][k][2 * t + 1];
                    s += (f32x4){bf_lo(a), bf_hi(a), bf_lo(bq), bf_hi(bq)};
                }
                const float o0 = bf_lo(xr[i][t][0]) + rbf(gb[i][0] * rbf(s[0])), o1 = bf_hi(xr[i][t][0]) + rbf(gb[i][1] * rbf(s[1]));
                const float o2 = bf_lo(xr[i][t][1]) + rbf(gb[i][2] * rbf(s[2])), o3 = bf_hi(xr[i][t][1]) + rbf(gb[i][3] * rbf(s[3]));
                *(u32x2*)(out + (size_t)(128 * mt2 + 32 * q + 16 * t + n) * D_ + d) = (u32x2){pk2(o0, o1), pk2(o2, o3)};
            }
        }
    }
    if (b == 0 && tid == 0) flag_set(flag_ptr(ws, F_EPOCH), epoch);
    STAMP_AT(15);
    STAMP_OUT(4, 16);
}

extern "C" void kernel_launch(void* const* d_in, const int* in_sizes, int n_in, void* d_out, int out_size, void* d_ws, size_t ws_size,
                              hipStream_t stream) {
    const u16* q = (const u16*)d_in[0];
    const u16* k = (const u16*)d_in[1];
    const u16* v = (const u16*)d_in[2];
    const u16* shift_k = (const u16*)d_in[3];
    const u16* ssm_k = (const u16*)d_in[4];
    const u16* ssm_D = (const u16*)d_in[5];
    const u16* wg = (const u16*)d_in[7];
    const u16* wu = (const u16*)d_in[8];
    const u16* wd = (const u16*)d_in[9];
    const u16* c = (const u16*)d_in[10];
    const u16* w_mod = (const u16*)d_in[11];
    char* ws = (char*)d_ws;
    (void)hipFuncSetAttribute((const void*)k_main, hipFuncAttributeMaxDynamicSharedMemorySize, mlp::LDS_BYTES);
    k_main<<<256, 256, mlp::LDS_BYTES, stream>>>(q, k, v, shift_k, ssm_k, ssm_D, wg, wu, wd, c, w_mod, ws, (u16*)d_out);
}
```

```cpp
#include <hip/hip_runtime.h>
#include <stdint.h>
#include <utility>

typedef unsigned short u16;
typedef unsigned int u32;
typedef __bf16 bf16x8 __attribute__((ext_vector_type(8)));
typedef __bf16 bf16x2 __attribute__((ext_vector_type(2)));
typedef float f32x4 __attribute__((ext_vector_type(4)));
typedef float f32x2 __attribute__((ext_vector_type(2)));
typedef u32 u32x4 __attribute__((ext_vector_type(4)));
typedef u32 u32x2 __attribute__((ext_vector_type(2)));
typedef short s16x4 __attribute__((ext_vector_type(4)));
#define LAS __attribute__((address_space(3)))

constexpr int S_ = 2048, D_ = 576, F_ = 4608;

constexpr size_t WS_MOD = 0;
constexpr size_t WS_CNT = 65536;
constexpr size_t WS_YYT = WS_CNT + 65536;
constexpr size_t WS_X1  = WS_YYT + (size_t)D_ * S_ * 2;
constexpr size_t WS_H2T = WS_X1 + (size_t)D_ * S_ * 2;
constexpr size_t WS_HT  = WS_H2T + (size_t)D_ * S_ * 2;
constexpr size_t WS_WT1 = WS_HT + (size_t)S_ * F_ * 2;
constexpr size_t WS_SLAB = WS_WT1 + (size_t)2 * D_ * F_ * 2;
constexpr size_t WS_END = WS_SLAB + (size_t)16 * 4 * 4 * 4 * 9216;

__device__ __forceinline__ float bf_lo(u32 w) { return __uint_as_float(w << 16); }
__device__ __forceinline__ float bf_hi(u32 w) { return __uint_as_float(w & 0xffff0000u); }
__device__ __forceinline__ float bf1(u16 w) { return __uint_as_float((u32)w << 16); }
__device__ __forceinline__ u32 pk2(float lo, float hi) {
    f32x2 v = {lo, hi};
    return __builtin_bit_cast(u32, __builtin_convertvector(v, bf16x2));
}
__device__ __forceinline__ float rbf(float x) { return (float)(__bf16)x; }
__device__ __forceinline__ float sigmoidf_(float x) { return __builtin_amdgcn_rcpf(1.0f + __builtin_amdgcn_exp2f(-1.4426950408889634f * x)); }

constexpr int F_MOD = 256;
constexpr int F_TILE = 512;
constexpr int F_CONV = 1024;
constexpr int F_ROWS = 3072;
constexpr int F_H = 3328;
constexpr int F_RED = 5376;
constexpr int F_EPOCH = 6400;
typedef __amdgpu_buffer_rsrc_t rsrc_t;
__device__ __forceinline__ u32* flag_ptr(char* ws, int idx) { return (u32*)(ws + WS_CNT) + idx; }
__device__ __forceinline__ void flag_set(u32* p, u32 e) { __hip_atomic_store(p, e, __ATOMIC_RELAXED, __HIP_MEMORY_SCOPE_AGENT); }
__device__ __forceinline__ void st16_dev(rsrc_t rs, u32 off, u32x4 v) { __builtin_amdgcn_raw_buffer_store_b128(v, rs, off, 0, 16); }
__device__ __forceinline__ u32x4 ld16_dev(rsrc_t rs, u32 off) { return __builtin_amdgcn_raw_buffer_load_b128(rs, off, 0, 16); }
__device__ __forceinline__ void poll_eq(const u32* p, u32 e) {
    for (int it = 0; it < (1 << 21); ++it) {
        if (__all(__hip_atomic_load(p, __ATOMIC_RELAXED, __HIP_MEMORY_SCOPE_AGENT) == e)) break;
        __builtin_amdgcn_s_sleep(1);
    }
}
__device__ __forceinline__ void poll_eq4(rsrc_t rs, u32 off, u32 e) {
    for (int it = 0; it < (1 << 21); ++it) {
        asm volatile("" ::: "memory");
        const u32x4 v = ld16_dev(rs, off);
        if (__all(v[0] == e && v[1] == e && v[2] == e && v[3] == e)) break;
        __builtin_amdgcn_s_sleep(1);
    }
}
#define DRAIN() asm volatile("s_waitcnt vmcnt(0)" ::: "memory")

#define SCHED() __builtin_amdgcn_sched_barrier(0)
__device__ __forceinline__ float dpp_add(float v, const int ctrl_sel) {
    int r;
    const int iv = __builtin_bit_cast(int, v);
    switch (ctrl_sel) {
        case 0: r = __builtin_amdgcn_update_dpp(0, iv, 0xB1, 0xf, 0xf, true); break;
        case 1: r = __builtin_amdgcn_update_dpp(0, iv, 0x4E, 0xf, 0xf, true); break;
        case 2: r = __builtin_amdgcn_update_dpp(0, iv, 0x141, 0xf, 0xf, true); break;
        default: r = __builtin_amdgcn_update_dpp(0, iv, 0x140, 0xf, 0xf, true); break;
    }
    return v + __builtin_bit_cast(float, r);
}
__device__ __forceinline__ float half_sum(float v) {
    v = dpp_add(v, 0); v = dpp_add(v, 1); v = dpp_add(v, 2); v = dpp_add(v, 3);
    auto r = __builtin_amdgcn_permlane16_swap(__float_as_uint(v), __float_as_uint(v), false, false);
    return __uint_as_float(r[0]) + __uint_as_float(r[1]);
}

struct KvIn { u32x2 kw[8], vw[8]; u32x4 sw; u32x2 sw2; };
__device__ __forceinline__ void kv_load(KvIn& p, int c0, const u16* __restrict__ k, const u16* __restrict__ v, const u16* __restrict__ shift_k) {
    const int cw = c0 & ~1;
#pragma unroll
    for (int i = 0; i < 8; ++i) p.kw[i] = *(const u32x2*)(k + (size_t)(threadIdx.x + 256 * i) * D_ + cw);
#pragma unroll
    for (int i = 0; i < 8; ++i) p.vw[i] = *(const u32x2*)(v + (size_t)(threadIdx.x + 256 * i) * D_ + cw);
    p.sw = (u32x4){*(const u32*)(shift_k + c0 * 4), *(const u32*)(shift_k + c0 * 4 + 2), *(const u32*)(shift_k + c0 * 4 + 4), *(const u32*)(shift_k + c0 * 4 + 6)};
    p.sw2 = (u32x2){*(const u32*)(shift_k + c0 * 4 + 8), *(const u32*)(shift_k + c0 * 4 + 10)};
}
constexpr int KV_RAWV = 257 * 72 + 8;
__device__ __forceinline__ void kv_put(const KvIn& p, char* raw) {
    const int tid = threadIdx.x;
#pragma unroll
    for (int i = 0; i < 8; ++i) {
        const int t = tid + 256 * i, o = 72 * (t >> 3) + 8 * (t & 7);
        *(u32x2*)(raw + 72 + o) = p.kw[i];
        *(u32x2*)(raw + KV_RAWV + o) = p.vw[i];
    }
    if (tid < 8) *(u32x2*)(raw + 8 * tid) = (u32x2){0, 0};
}
__device__ __forceinline__ void kv_compute(const KvIn& p, int c0, const char* raw, char* xr, int stride) {
    const int tid = threadIdx.x;
    u32x2 kw[11], vw[8];
#pragma unroll
    for (int i = 0; i < 11; ++i) kw[i] = *(const u32x2*)(raw + 72 * tid + (i < 3 ? 40 + 8 * i : 72 + 8 * (i - 3)));
#pragma unroll
    for (int i = 0; i < 8; ++i) vw[i] = *(const u32x2*)(raw + KV_RAWV + 72 * tid + 8 * i);
    const bool odd = c0 & 1;
#pragma unroll
    for (int c = 0; c < 3; ++c) {
        const u32 wa = c == 0 ? p.sw[0] : c == 1 ? p.sw[2] : p.sw2[0], wb = c == 0 ? p.sw[1] : c == 1 ? p.sw[3] : p.sw2[1];
        const float w0 = bf_lo(wa), w1 = bf_hi(wa), w2 = bf_lo(wb), w3 = bf_hi(wb);
        float kf[11], r[8];
#define KV_PICK(d) (c == 0 ? (odd ? bf_hi((d)[0]) : bf_lo((d)[0])) : c == 1 ? (odd ? bf_lo((d)[1]) : bf_hi((d)[0])) : (odd ? bf_hi((d)[1]) : bf_lo((d)[1])))
#pragma unroll
        for (int i = 0; i < 11; ++i) kf[i] = KV_PICK(kw[i]);
#pragma unroll
        for (int i = 0; i < 8; ++i) r[i] = (kf[i] * w3 + kf[i + 1] * w2 + kf[i + 2] * w1 + kf[i + 3] * w0) * KV_PICK(vw[i]);
#undef KV_PICK
        *(u32x4*)(xr + c * stride + 2 * (2040 - 8 * tid)) = (u32x4){pk2(r[7], r[6]), pk2(r[5], r[4]), pk2(r[3], r[2]), pk2(r[1], r[0])};
    }
}

__device__ __forceinline__ void mod_job(int job, const u16* __restrict__ c, const u16* __restrict__ w_mod, char* __restrict__ ws, float* sc  , float* red  ) {
    const int tid = threadIdx.x, lane = tid & 63, wid = tid >> 6;
    const int col0 = job * 64, rg = tid >> 3, c8 = tid & 7;
    u32x4 w[18];
#pragma unroll
    for (int i = 0; i < 18; ++i) w[i] = *(const u32x4*)(w_mod + (size_t)(rg + 32 * i) * 3456 + col0 + c8 * 8);
#pragma unroll
    for (int j = 0; j < 3; ++j) { const int r = tid + 256 * j; if (r < 576) { const float x = bf1(c[r]); sc[r] = rbf(x * sigmoidf_(x)); } }
    __syncthreads();
    float acc[8] = {0, 0, 0, 0, 0, 0, 0, 0};
#pragma unroll
    for (int i = 0; i < 18; ++i) {
        const float s = sc[rg + 32 * i];
#pragma unroll
        for (int j = 0; j < 4; ++j) { acc[2 * j] += s * bf_lo(w[i][j]); acc[2 * j + 1] += s * bf_hi(w[i][j]); }
    }
#pragma unroll
    for (int j = 0; j < 8; ++j) {
        acc[j] += __shfl_xor(acc[j], 8); acc[j] += __shfl_xor(acc[j], 16); acc[j] += __shfl_xor(acc[j], 32);
    }
    if (lane < 8) {
#pragma unroll
        for (int j = 0; j < 8; ++j) red[wid * 64 + lane * 8 + j] = acc[j];
    }
    __syncthreads();
    if (tid < 64) {
        float m = rbf((red[tid] + red[64 + tid]) + (red[128 + tid] + red[192 + tid]));
        const int which = col0 / 576;
        if (which == 1 || which == 4) m = rbf(1.0f + m);
        __hip_atomic_store((u32*)(ws + WS_MOD) + col0 + tid, __float_as_uint(m), __ATOMIC_RELAXED, __HIP_MEMORY_SCOPE_AGENT);
    }
}

__device__ __forceinline__ const char* tile_base(int job, const u16* __restrict__ wg, const u16* __restrict__ wu) {
    const int ks = job % 18, mat = (job / 18) & 1, cg = job / 36;
    return (const char*)((mat ? wu : wg) + (size_t)(32 * ks) * F_ + cg * 576);
}
__device__ __forceinline__ void tile_dma(int job, const u16* __restrict__ wg, const u16* __restrict__ wu, char* tile, const u32 (&voff)[9]) {
    const int w = __builtin_amdgcn_readfirstlane(threadIdx.x >> 6);
    const char* base = tile_base(job, wg, wu) + (size_t)w * 8 * (F_ * 2);
    const u32 dst = (u32)(uintptr_t)tile + w * 9216;
#pragma unroll
    for (int i = 0; i < 9; ++i)
        asm volatile("s_mov_b32 m0, %2\n\ts_nop 0\n\tglobal_load_lds_dwordx4 %0, %1" :: "v"(voff[i]), "s"(base), "s"(dst + i * 1024) : "memory");
}
__device__ __forceinline__ void tile_out(int job, const char* tile, rsrc_t wsr) {
    const int lane = threadIdx.x & 63, w = threadIdx.x >> 6;
    const int ks = job % 18, mat = (job / 18) & 1, fc = 4 * (job / 36) + w;
    const int g = lane >> 4, q4 = (lane & 15) >> 2, p = lane & 3;
    const LAS char* rd = (const LAS char*)tile + (4 * g + q4) * 1152 + 8 * p + w * 288;
#pragma unroll
    for (int u = 0; u < 9; ++u) {
        const int pos = (fc & 1) ? (u == 0 ? 4 : u <= 4 ? u - 1 : u) : (u == 8 ? 4 : u < 4 ? u : u + 1);
        const s16x4 lo = __builtin_amdgcn_ds_read_tr16_b64_v4i16((LAS s16x4*)(rd + u * 32));
        const s16x4 hi = __builtin_amdgcn_ds_read_tr16_b64_v4i16((LAS s16x4*)(rd + u * 32 + 16 * 1152));
        typedef short s16x8 __attribute__((ext_vector_type(8)));
        const s16x8 r = __builtin_shufflevector(lo, hi, 0, 1, 2, 3, 4, 5, 6, 7);
        st16_dev(wsr, (u32)WS_WT1 + fc * 331776 + pos * 36864 + (ks * 2 + mat) * 1024 + lane * 16, __builtin_bit_cast(u32x4, r));
    }
}
__device__ __forceinline__ void tile_jobs(int i0, const u16* __restrict__ wg, const u16* __restrict__ wu, char* smem, char* ws, rsrc_t wsr, u32 epoch) {
    const int lane = threadIdx.x & 63;
    u32 voff[9];
#pragma unroll
    for (int i = 0; i < 9; ++i) { const int ci = lane + 64 * i, row = (ci * 58255) >> 22, c16 = ci - row * 72; voff[i] = (u32)(row * (F_ * 2) + c16 * 16); }
#pragma unroll
    for (int j = 0; j < 4; ++j) tile_dma(i0 + 64 * j, wg, wu, smem + j * 36864, voff);
#pragma unroll
    for (int j = 0; j < 4; ++j) {
        asm volatile("s_waitcnt vmcnt(27)" ::: "memory");
        __syncthreads();
        tile_out(i0 + 64 * j, smem + j * 36864, wsr);
    }
    if (i0 < 32) {
        __syncthreads();
        tile_dma(i0 + 256, wg, wu, smem, voff);
        asm volatile("s_waitcnt vmcnt(0)" ::: "memory");
        __syncthreads();
        tile_out(i0 + 256, smem, wsr);
    }
    DRAIN();
    __syncthreads();
    if (threadIdx.x < 5 && i0 + 64 * threadIdx.x < 288) flag_set(flag_ptr(ws, F_TILE + i0 + 64 * threadIdx.x), epoch);
}

constexpr int C_XR = 0;
constexpr int C_K0 = 4608;
constexpr int C_K1 = C_K0 + 4128 + 32;
constexpr int C_CH = C_K1 + 4160;
constexpr int C_RED = 3 * C_CH;
static_assert(C_RED + 3 * 32768 <= 147456, "the convolution buffers stay below the mod table");
static_assert(((C_K1 - C_K0) / 4) % 32 == 16, "bank offset");
static_assert(C_CH % 16 == 0 && C_K0 % 16 == 0 && C_K1 % 16 == 0, "align");
template <bool ZERO> __device__ __forceinline__ void mfma_v(f32x4& c, const bf16x8& a, const bf16x8& b) {
    if constexpr (ZERO) asm volatile("v_mfma_f32_16x16x32_bf16 %0, %1, %2, 0" : "=v"(c) : "v"(a), "v"(b));
    else asm volatile("v_mfma_f32_16x16x32_bf16 %0, %1, %2, %0" :: "v"(c), "v"(a), "v"(b));
}
struct ConvSet { bf16x8 A[8], B[8]; };
template <int J> __device__ __forceinline__ void conv_ld(ConvSet& s, const LAS char* ab, const LAS char* bb) {
    if constexpr (J < 8) {
        const LAS u32* p = (const LAS u32*)(ab + 512 * J);
        u32x4 t = {p[0], p[1], p[2], p[3]};
        s.A[J] = __builtin_bit_cast(bf16x8, t);
    } else s.B[J - 8] = *(const LAS bf16x8*)(bb + 512 * (15 - J));
}
template <int M> struct TriIdx {
    static constexpr int find(bool want_i) { int m = 0; for (int s = 0; s < 8; ++s) for (int j = 0; j <= s; ++j) { if (m == M) return want_i ? s : j; ++m; } return 0; }
    static constexpr int i = find(true), j = find(false);
};
template <int M, bool PF, bool FIRST> __device__ __forceinline__ void conv_slot(f32x4 (&acc)[8], const ConvSet& cur, ConvSet& nxt, const LAS char* ab, const LAS char* bb) {
    mfma_v<(FIRST && TriIdx<M>::j == 0)>(acc[TriIdx<M>::i], cur.A[TriIdx<M>::j], cur.B[TriIdx<M>::i - TriIdx<M>::j]);
    if constexpr (PF && (M & 1) == 0 && M / 2 < 16) conv_ld<((M / 2) & 1) * 8 + (M / 4)>(nxt, ab, bb);
    SCHED();
}
template <bool PF, bool FIRST, int... M> __device__ __forceinline__ void conv_step(f32x4 (&acc)[8], const ConvSet& cur, ConvSet& nxt, const LAS char* ab, const LAS char* bb, std::integer_sequence<int, M...>) {
    (conv_slot<M, PF, FIRST>(acc, cur, nxt, ab, bb), ...);
}
template <int... J> __device__ __forceinline__ void conv_ld_all(ConvSet& s, const LAS char* ab, const LAS char* bb, std::integer_sequence<int, J...>) { (conv_ld<J>(s, ab, bb), ...); }

struct ConvIn { u32x4 k; u32 kn; u16 dv; };
__device__ __forceinline__ void conv_load_k(ConvIn& in, int d, const u16* __restrict__ ssm_k, const u16* __restrict__ ssm_D) {
    const int tid = threadIdx.x;
    in.k = *(const u32x4*)(ssm_k + (size_t)d * S_ + tid * 8);
    in.kn = *(const u32*)(ssm_k + (size_t)d * S_ + (tid < 255 ? tid * 8 + 8 : 0));
    in.dv = ssm_D[d];
}
__device__ __forceinline__ void conv_put_k(const ConvIn& in, char* sm) {
    const int tid = threadIdx.x;
    const u32 kn = tid < 255 ? in.kn : 0u;
    *(u32x4*)(sm + C_K0 + 32 + tid * 16) = in.k;
    *(u32x4*)(sm + C_K1 + 32 + tid * 16) = (u32x4){__builtin_amdgcn_alignbit(in.k[1], in.k[0], 16), __builtin_amdgcn_alignbit(in.k[2], in.k[1], 16),
                                                 __builtin_amdgcn_alignbit(in.k[3], in.k[2], 16), __builtin_amdgcn_alignbit(kn, in.k[3], 16)};
    if (tid < 32) *(u32x4*)(sm + C_XR + 4096 + tid * 16) = (u32x4){0, 0, 0, 0};
    if (tid == 0) {
        *(u32x4*)(sm + C_K0) = (u32x4){0, 0, 0, 0}; *(u32x4*)(sm + C_K0 + 16) = (u32x4){0, 0, 0, 0};
        *(u32x4*)(sm + C_K1) = (u32x4){0, 0, 0, 0}; *(u32x4*)(sm + C_K1 + 16) = (u32x4){0, 0, 0, in.k[0] << 16};
    }
}
__device__ __forceinline__ void conv_finish(int d, const char* sm, const char* red, float Dv, rsrc_t wsr) {
    const int tid = threadIdx.x;
    const int i = tid >> 5, n = tid & 15, gp = (tid >> 4) & 1;
    f32x4 lo = {0.f, 0.f, 0.f, 0.f}, hi = {0.f, 0.f, 0.f, 0.f};
#pragma unroll
    for (int w = 0; w < 4; ++w) {
        lo += *(const f32x4*)(red + (w * 8 + i) * 1024 + (n + 32 * gp) * 16);
        hi += *(const f32x4*)(red + (w * 8 + i) * 1024 + (n + 32 * gp + 16) * 16);
    }
    const int s = 256 * i + 16 * n + 8 * gp;
    const u32x4 xw = *(const u32x4*)(sm + C_XR + 2 * (2040 - s));
    const float o0 = rbf(lo[0]) + rbf(bf_hi(xw[3]) * Dv), o1 = rbf(lo[1]) + rbf(bf_lo(xw[3]) * Dv), o2 = rbf(lo[2]) + rbf(bf_hi(xw[2]) * Dv), o3 = rbf(lo[3]) + rbf(bf_lo(xw[2]) * Dv);
    const float o4 = rbf(hi[0]) + rbf(bf_hi(xw[1]) * Dv), o5 = rbf(hi[1]) + rbf(bf_lo(xw[1]) * Dv), o6 = rbf(hi[2]) + rbf(bf_hi(xw[0]) * Dv), o7 = rbf(hi[3]) + rbf(bf_lo(xw[0]) * Dv);
    st16_dev(wsr, (u32)WS_YYT + (u32)(d * S_ + s) * 2, (u32x4){pk2(o0, o1), pk2(o2, o3), pk2(o4, o5), pk2(o6, o7)});
}
template <class Hook>
__device__ __forceinline__ void conv_body(int ch0, const ConvIn (&in)[3], char* smem, rsrc_t wsr, Hook hook) {
    const int lane = threadIdx.x & 63, wid = __builtin_amdgcn_readfirstlane(threadIdx.x >> 6);
    __syncthreads();
    const int n = lane & 15, g = lane >> 4, dd = g >> 1, gh = g & 1;
    const LAS char* l0 = (const LAS char*)smem;
    const LAS char* bb = l0 + C_XR + 480 - 32 * n + 32 * dd + 16 * gh + 128 * wid;
    const LAS char* ab = l0 + ((n & 1) ? C_K0 + 2 * (16 * dd + 8 * gh + n + 1) : C_K1 + 2 * (16 * dd + 8 * gh + n)) + 128 * wid;
    f32x4 acc[3][8];
    ConvSet s0, s1;
    conv_ld_all(s0, ab, bb, std::make_integer_sequence<int, 16>{});
    SCHED();
    constexpr auto SQ = std::make_integer_sequence<int, 36>{};
    conv_step<true, true>(acc[0], s0, s1, ab + 64, bb + 64, SQ);
    conv_step<true, false>(acc[0], s1, s0, ab + C_CH, bb + C_CH, SQ);
    conv_step<true, true>(acc[1], s0, s1, ab + C_CH + 64, bb + C_CH + 64, SQ);
    conv_step<true, false>(acc[1], s1, s0, ab + 2 * C_CH, bb + 2 * C_CH, SQ);
    conv_step<true, true>(acc[2], s0, s1, ab + 2 * C_CH + 64, bb + 2 * C_CH + 64, SQ);
    conv_step<false, false>(acc[2], s1, s0, ab, bb, SQ);
    asm volatile("s_nop 7" ::: "memory");
#pragma unroll
    for (int c = 0; c < 3; ++c)
#pragma unroll
        for (int i = 0; i < 8; ++i) asm volatile("" : "+v"(acc[c][i]));
    hook();
#pragma unroll
    for (int c = 0; c < 3; ++c)
#pragma unroll
        for (int i = 0; i < 8; ++i) *(f32x4*)(smem + C_RED + c * 32768 + (wid * 8 + i) * 1024 + lane * 16) = acc[c][i];
    __syncthreads();
#pragma unroll
    for (int c = 0; c < 3; ++c) conv_finish(ch0 + c, smem + c * C_CH, smem + C_RED + c * 32768, bf1(in[c].dv), wsr);
}

constexpr int R_MOD = 147456;
constexpr int R_YT = 110592;
constexpr int R_HT = R_YT + 8 * 1168;
struct RowsPre { float x[18], gh[18]; u32 xw[9]; };
__device__ __forceinline__ void rows_load(RowsPre& r, int t0, const u16* __restrict__ q) {
    const int tid = threadIdx.x, lane = tid & 63, hl = lane & 31, row = 2 * (tid >> 6) + (lane >> 5);
    const u32* qrow = (const u32*)(q + (size_t)(t0 + row) * D_);
#pragma unroll
    for (int i = 0; i < 9; ++i) r.xw[i] = qrow[hl + 32 * i];
}
struct ModIn { u32x4 mw[4]; };
__device__ __forceinline__ void mod_load(ModIn& m, rsrc_t wsr) {
#pragma unroll
    for (int i = 0; i < 4; ++i) { const int j = threadIdx.x + 256 * i; m.mw[i] = ld16_dev(wsr, (u32)WS_MOD + (j < 864 ? j : 0) * 16); }
}
__device__ __forceinline__ void mod_put(const ModIn& m, char* sm) {
#pragma unroll
    for (int i = 0; i < 4; ++i) { const int j = threadIdx.x + 256 * i; if (j < 864) *(u32x4*)(sm + R_MOD + j * 16) = m.mw[i]; }
}
__device__ __forceinline__ void rows_pre(RowsPre& r, const char* sm) {
    const int hl = threadIdx.x & 31;
#pragma unroll
    for (int i = 0; i < 9; ++i) { r.x[2 * i] = bf_lo(r.xw[i]); r.x[2 * i + 1] = bf_hi(r.xw[i]); }
    float s = 0.f;
#pragma unroll
    for (int i = 0; i < 18; ++i) s += r.x[i];
    const float mean = half_sum(s) * (1.0f / 576.0f);
    float vs = 0.f;
#pragma unroll
    for (int i = 0; i < 18; ++i) { const float dl = r.x[i] - mean; vs += dl * dl; }
    const float rstd = __builtin_amdgcn_rsqf(half_sum(vs) * (1.0f / 576.0f) + 1e-5f);
    const float* mod = (const float*)(sm + R_MOD);
#pragma unroll
    for (int i = 0; i < 9; ++i) {
        const int c = 2 * (hl + 32 * i);
        const f32x2 sa = *(const f32x2*)(mod + c), c1 = *(const f32x2*)(mod + 576 + c), ga = *(const f32x2*)(mod + 2 * 576 + c);
        r.gh[2 * i] = ga[0] * ((r.x[2 * i] - mean) * rstd * c1[0] + sa[0]);
        r.gh[2 * i + 1] = ga[1] * ((r.x[2 * i + 1] - mean) * rstd * c1[1] + sa[1]);
    }
}
__device__ __forceinline__ void rows_fetch(int t0, char* sm, const char* ws) {
    const int lane = threadIdx.x & 63;
    const char* src = ws + WS_YYT + t0 * 2;
    const u32 dst = (u32)(uintptr_t)(sm + R_HT);
#pragma unroll
    for (int i = 0; i < 9; ++i)
        asm volatile("s_mov_b32 m0, %2\n\ts_nop 0\n\tglobal_load_lds_dwordx4 %0, %1 sc1" :: "v"((u32)((lane + 64 * i) * (S_ * 2))), "s"(src), "s"(dst + i * 1024) : "memory");
    asm volatile("s_waitcnt vmcnt(0)" ::: "memory");
}
__device__ __forceinline__ void rows_main(const RowsPre& r, int t0, char* sm, rsrc_t wsr) {
    const int tid = threadIdx.x, lane = tid & 63, hl = lane & 31, row = 2 * (tid >> 6) + (lane >> 5);
    const int cg = tid >> 1, th = tid & 1;
    if (tid < 144) {
        u32x2 yw[8];
#pragma unroll
        for (int c = 0; c < 8; ++c) yw[c] = *(const u32x2*)(sm + R_HT + (8 * cg + c) * 16 + th * 8);
#pragma unroll
        for (int t = 0; t < 4; ++t) {
            u32x4 o;
#pragma unroll
            for (int kk = 0; kk < 4; ++kk) {
                const u32 a = yw[2 * kk][t >> 1], bq = yw[2 * kk + 1][t >> 1];
                o[kk] = (t & 1) ? __builtin_amdgcn_perm(bq, a, 0x07060302u) : __builtin_amdgcn_perm(bq, a, 0x05040100u);
            }
            *(u32x4*)(sm + R_YT + (4 * th + t) * 1168 + cg * 16) = o;
        }
    }
    __syncthreads();
    const float* mod = (const float*)(sm + R_MOD);
    float x1[18];
#pragma unroll
    for (int i = 0; i < 9; ++i) {
        u32* yp = (u32*)(sm + R_YT + row * 1168 + (hl + 32 * i) * 4);
        const u32 yv = *yp;
        const u32 p = pk2(r.x[2 * i] + r.gh[2 * i] * bf_lo(yv), r.x[2 * i + 1] + r.gh[2 * i + 1] * bf_hi(yv));
        *yp = p;
        x1[2 * i] = bf_lo(p); x1[2 * i + 1] = bf_hi(p);
    }
    float s = 0.f;
#pragma unroll
    for (int i = 0; i < 18; ++i) s += x1[i];
    const float mean = half_sum(s) * (1.0f / 576.0f);
    float vs = 0.f;
#pragma unroll
    for (int i = 0; i < 18; ++i) { const float dl = x1[i] - mean; vs += dl * dl; }
    const float rstd = __builtin_amdgcn_rsqf(half_sum(vs) * (1.0f / 576.0f) + 1e-5f);
#pragma unroll
    for (int i = 0; i < 9; ++i) {
        const int c = 2 * (hl + 32 * i);
        const f32x2 sb = *(const f32x2*)(mod + 3 * 576 + c), c1 = *(const f32x2*)(mod + 4 * 576 + c);
        const float h0 = (x1[2 * i] - mean) * rstd * c1[0] + sb[0], h1 = (x1[2 * i + 1] - mean) * rstd * c1[1] + sb[1];
        const int ks = c >> 5, kk = c & 31, gq = (kk & 15) >> 2, j = ((kk >> 4) << 2) | (kk & 3);
        *(u32*)(sm + R_HT + ((ks * 4 + gq) * 8 + row) * 16 + j * 2) = pk2(h0, h1);
    }
    __syncthreads();
    const u32 h2t = (u32)WS_H2T + (u32)(t0 >> 4) * 18 * 1024 + (t0 & 15) * 16, x1g = (u32)WS_X1 + (u32)t0 * D_ * 2;
#pragma unroll
    for (int it = 0; it < 3; ++it) {
        const int i = tid + 256 * it;
        if (i < 576) {
            const int kg = i >> 3, rr8 = i & 7;
            st16_dev(wsr, h2t + (kg >> 2) * 1024 + (kg & 3) * 256 + rr8 * 16, *(const u32x4*)(sm + R_HT + i * 16));
            const int rr = i / 72, cc = i - rr * 72;
            st16_dev(wsr, x1g + rr * 1152 + cc * 16, *(const u32x4*)(sm + R_YT + rr * 1168 + cc * 16));
        }
    }
}

namespace mlp {
constexpr int STG = 36864, LDS_BYTES = 163840;
constexpr int G1S = 9 * 144, G2S = 18 * 36, NS = G1S + G2S;
constexpr int LA = 4;
constexpr int PRO = 9 + 4 * LA + 18;
constexpr int TUS = 147456;

constexpr int F01 = 288;
constexpr int stage_of(int gs) { return gs < F01 ? 0 : gs < G1S ? gs / 144 : 9 + (gs - G1S) / 72; }
constexpr int rel_of(int gs) { return gs < F01 ? gs : gs < G1S ? gs % 144 : (gs - G1S) % 72; }
constexpr int grp_of(int gs) { return rel_of(gs) >> 2; }
constexpr int t_of(int gs) { return rel_of(gs) & 3; }
constexpr bool barpt(int gs) { return gs >= F01 && gs < NS && t_of(gs) == 3 && grp_of(gs) == (gs < G1S ? 3 : 0) && stage_of(gs) < 17; }
constexpr int dma_id(int gs) {
    if (gs >= NS || t_of(gs) != 3) return -1;
    const int g = grp_of(gs), s = stage_of(gs);
    if (gs < F01) return (g >= 36 && g < 72 && (g & 3) == 3) ? 3 * 16 + (g - 36) / 4 : -1;
    if (s == 2) return (g >= 4 && g < 22) ? ((g & 1) ? 5 : 4) * 16 + (g - 4) / 2 : -1;
    if (s + 3 > 17) return -1;
    if (s == 8) return (g >= 4 && g < 13) ? 11 * 16 + g - 4 : -1;
    if (gs < G1S) { constexpr int G[9] = {5, 7, 9, 12, 14, 16, 20, 22, 24}; for (int i = 0; i < 9; ++i) if (G[i] == g) return (s + 3) * 16 + i; return -1; }
    return (g & 1) ? (s + 3) * 16 + (g >> 1) : -1;
}
constexpr int bload(int gs) {
    if (gs >= F01 || t_of(gs) != 1) return -1;
    const int ks = 2 * (gs / 32) + LA + (((gs % 32) >> 2) >> 2), i = (gs >> 2) & 3;
    return ks < 18 ? ks * 4 + i : -1;
}
constexpr bool unit_stores(int v) { return v == 1 || v == 3 || v == 4 || v == 6 || v == 8; }
constexpr int unit_end(int v) { return v < 2 ? F01 : 144 * (v + 1); }
constexpr int hload(int gs) {
    if (t_of(gs) != 3) return -1;
    const int s = stage_of(gs), g = grp_of(gs);
    if (s == 8) return (g >= 13 && g < 33) ? g - 13 : -1;
    if (s < 9 || g < 2 || g > 16 || (g & 1)) return -1;
    const int i = (g - 2) >> 1;
    if (s == 9) return 20 + i;
    if (s == 10) return 28 + i;
    if (s == 11) return i < 4 ? 36 + i : -1;
    if (s >= 12 && s <= 15) return 40 + 8 * (s - 12) + i;
    return -1;
}
constexpr int pub(int gs) { return (stage_of(gs) == 6 && grp_of(gs) == 27 && t_of(gs) == 3) ? 1 : (stage_of(gs) == 10 && grp_of(gs) == 9 && t_of(gs) == 1) ? 2 : 0; }
constexpr int poll_issue(int gs) { return (stage_of(gs) == 7 && grp_of(gs) == 27 && t_of(gs) == 3) ? 1 : (stage_of(gs) == 11 && grp_of(gs) == 9 && t_of(gs) == 1) ? 2 : 0; }
struct EpiSched { signed char ev[G1S], ep[G1S], sv[G1S], st[G1S]; };
constexpr EpiSched make_epi() {
    EpiSched e{};
    int v = 0, p = 0, pend_v = -1, pend_lo = 0, pend_hi = 0;
    for (int gs = 0; gs < G1S; ++gs) {
        e.ev[gs] = -1; e.ep[gs] = -1; e.sv[gs] = -1; e.st[gs] = -1;
        if (gs < F01) continue;
        const int t = t_of(gs);
        if (t == 0) continue;
        const bool vm = barpt(gs) || dma_id(gs) >= 0 || hload(gs) >= 0 || pub(gs) || poll_issue(gs);
        if (t == 3 && !vm && pend_lo < pend_hi) { e.sv[gs] = (signed char)pend_v; e.st[gs] = (signed char)pend_lo; ++pend_lo; continue; }
        if (t == 3 && vm) continue;
        if (v < 8 && gs >= unit_end(v) + 8 && (pend_lo == pend_hi || pend_v == v || !unit_stores(v) || true)) {
            e.ev[gs] = (signed char)v; e.ep[gs] = (signed char)p;
            ++p;
            if ((p & 15) == 0 && unit_stores(v)) { if (pend_v != v) { pend_v = v; pend_lo = p / 16 - 1; } pend_hi = p / 16; }
            if (p == 64) { p = 0; ++v; }
        }
    }
    return e;
}
constexpr EpiSched ES = make_epi();
constexpr int hstore(int gs) { return gs < G1S ? ES.st[gs] : -1; }
constexpr int epi_last(int v) { for (int gs = G1S - 1; gs >= 0; --gs) if (ES.ev[gs] == v) return gs; return -1; }
constexpr int store_last(int v) { for (int gs = G1S - 1; gs >= 0; --gs) if (ES.sv[gs] == v && ES.st[gs] == 3) return gs; return -1; }
constexpr bool epi_ok() {
    for (int v = 0; v < 8; ++v) {
        if (epi_last(v) < 0 || ES.ep[epi_last(v)] != 63) return false;
        if (v + 3 <= 8 && epi_last(v) >= 144 * (v + 3)) return false;
        if (unit_stores(v) && store_last(v) < 0) return false;
    }
    return store_last(4) + 40 < 6 * 144 + 27 * 4;
}
static_assert(epi_ok(), "epilogue schedule");
constexpr int vm_ops(int gs) { return (pub(gs) ? 1 : 0) + (poll_issue(gs) ? 1 : 0) + (hstore(gs) >= 0) + (bload(gs) >= 0) + (hload(gs) >= 0) + (dma_id(gs) >= 0); }
struct Ledger { int cum[NS + 1]; };
constexpr Ledger make_ledger() {
    Ledger l{}; int n = PRO;
    for (int gs = 0; gs < NS; ++gs) { if (gs == G1S) n += 4; l.cum[gs] = n; n += vm_ops(gs); }
    l.cum[NS] = n; return l;
}
constexpr Ledger LG = make_ledger();
constexpr int seq_in(int gs, int kind) {
    int n = LG.cum[gs];
    if (kind > 0 && pub(gs)) ++n;
    if (kind > 1 && poll_issue(gs)) ++n;
    if (kind > 2 && hstore(gs) >= 0) ++n;
    if (kind > 3 && bload(gs) >= 0) ++n;
    if (kind > 4 && hload(gs) >= 0) ++n;
    return n;
}
constexpr int seq_dma_last(int s) {
    if (s <= 2) return 9 * s + 8;
    for (int gs = 0; gs < NS; ++gs) if (dma_id(gs) == s * 16 + 8) return seq_in(gs, 5);
    return -1;
}
constexpr int seq_b(int ks) {
    if (ks < LA) return 27 + 4 * ks + 3;
    for (int gs = 0; gs < F01; ++gs) if (bload(gs) == ks * 4 + 3) return seq_in(gs, 3);
    return -1;
}
constexpr int seq_h(int kk) {
    for (int gs = 8 * 144; gs < NS; ++gs) if (hload(gs) == kk * 4 + 3) return seq_in(gs, 4);
    return -1;
}
constexpr int seq_poll(int part) {
    for (int gs = 0; gs < NS; ++gs) if (poll_issue(gs) == part) return seq_in(gs, 1);
    return -1;
}
constexpr int seq_store_last(int part) {
    if (part == 2) return LG.cum[G1S] - 1;
    return seq_in(store_last(4), 2);
}
struct Work { int unit, mat, k, t, n; };
constexpr Work work_of(int gs) {
    if (gs < F01) { const int p = gs / 32, um = (gs % 32) / 8, r = gs % 8; return {um >> 1, um & 1, 2 * p + (r & 1), r >> 1, 8 * p + 2 * um + (r & 1)}; }
    if (gs < G1S) { const int u = gs / 144, bi = (gs % 144) / 24, r = gs % 24; return {u, bi & 1, 6 * (bi >> 1) + r % 6, r / 6, 36 * u + 6 * bi + r % 6}; }
    const int s = (gs - G1S) / 72, r = (gs - G1S) % 72, du = r / 8, r8 = r % 8;
    return {du, 0, 2 * s + (r8 & 1), r8 >> 1, 324 + 18 * s + 2 * du + (r8 & 1)};
}
constexpr int NFRAG = 486, RQ = 12, FR0 = 8;
constexpr int frag_first(int n) {
    if (n < 72) return 32 * (n / 8) + 8 * ((n % 8) >> 1) + (n & 1);
    if (n < 324) return 144 * (n / 36) + 24 * ((n % 36) / 6) + n % 6;
    return G1S + 72 * ((n - 324) / 18) + 8 * (((n - 324) % 18) >> 1) + (n & 1);
}
constexpr int frag_last(int n) { return frag_first(n) + ((n >= 72 && n < 324) ? 18 : 6); }
constexpr int frag_stage(int n) { return n < 72 ? (n % 8) >> 2 : n < 324 ? n / 36 : 9 + (n - 324) / 18; }
constexpr int frag_off(int n) {
    if (n < 72) return (2 * (2 * (n / 8) + (n & 1)) + (((n % 8) >> 1) & 1)) * 1024;
    if (n < 324) { const int bi = (n % 36) / 6; return (2 * (6 * (bi >> 1) + n % 6) + (bi & 1)) * 1024; }
    return (n & 1) * 9216 + (((n - 324) % 18) >> 1) * 32;
}
constexpr int stage_ready(int s) {
    if (s <= 2) return 0;
    for (int gs = 0; gs < NS; ++gs) if (barpt(gs) && stage_of(gs) == s - 1) return gs + 1;
    return NS;
}
struct PfSched { short id[NS]; };
constexpr PfSched make_pf() {
    PfSched t{};
    int m = FR0, rdy = stage_ready(frag_stage(FR0));
    for (int gs = 0; gs < NS; ++gs) {
        t.id[gs] = -1;
        if ((gs & 3) != 0 || m >= NFRAG) continue;
        if (m >= RQ && frag_last(m - RQ) >= gs) continue;
        if (rdy > gs) continue;
        t.id[gs] = (short)m;
        ++m;
        if (m < NFRAG && frag_stage(m) != frag_stage(m - 1)) rdy = stage_ready(frag_stage(m));
    }
    return t;
}
constexpr PfSched PF = make_pf();
constexpr bool pf_ok() {
    int cnt = FR0;
    for (int gs = 0; gs < NS; ++gs) if (PF.id[gs] >= 0) { if (PF.id[gs] != cnt || frag_first(cnt) - gs < 8) return false; ++cnt; }
    return cnt == NFRAG;
}
static_assert(pf_ok(), "fragment prefetch schedule");
constexpr int clampv(int n) { return n > 63 ? 63 : (n < 0 ? 0 : n); }
constexpr int wait_for(int gs, int seq) { return clampv(LG.cum[gs] - seq - 1); }

template <int N> __device__ __forceinline__ void wait_vm() { asm volatile("s_waitcnt vmcnt(%0)" :: "n"(N) : "memory"); }

struct Ctx {
    bf16x8 Bv[2][4];
    bf16x8 Aq[12];
    f32x4 ac[3][2][4];
    f32x4 a2[9][4];
    float e0, e1;
    u32x4 hp[4];
    u32 vo1[4], vo2[4], vo3[4];
    u32 lane16, epoch;
    u32x4 pollv;
    const LAS char* rd[4]; const LAS char* rd2[4];
    const char* w1src; const char* w2src; const char* bsrc; const char* hsrc;
    u32 voff2[9];
    int sub;
    u32 dmadst;
    u32 hso;
    int jj;
    __amdgpu_buffer_rsrc_t hrs;
    u32* flag_pub; u32* flag_poll;
};

template <int KS, int T, bool ZERO> __device__ __forceinline__ void mfma(Ctx& cx, f32x4& c, const bf16x8& a) {
    constexpr int r = (KS * 4 + T) * 4;
    if constexpr (KS < 16) {
        if constexpr (ZERO) asm volatile("v_mfma_f32_16x16x32_bf16 %0, %1, a[%c2:%c3], 0" : "=v"(c) : "v"(a), "n"(r), "n"(r + 3));
        else asm volatile("v_mfma_f32_16x16x32_bf16 %0, %1, a[%c2:%c3], %0" :: "v"(c), "v"(a), "n"(r), "n"(r + 3));
    } else {
        asm volatile("v_mfma_f32_16x16x32_bf16 %0, %1, %2, %0" :: "v"(c), "v"(a), "v"(cx.Bv[KS - 16][T]));
    }
}
template <int KS, int T, bool SC1> __device__ __forceinline__ void gload(Ctx& cx, u32 voff, const char* sb) {
    constexpr int r = (KS * 4 + T) * 4, off = (KS & 3) * 1024;
    if constexpr (KS < 16) {
        if constexpr (SC1) asm volatile("global_load_dwordx4 a[%c2:%c3], %0, %1 offset:%4 sc1" :: "v"(voff), "s"(sb), "n"(r), "n"(r + 3), "n"(off) : "memory");
        else asm volatile("global_load_dwordx4 a[%c2:%c3], %0, %1 offset:%4" :: "v"(voff), "s"(sb), "n"(r), "n"(r + 3), "n"(off) : "memory");
    } else {
        if constexpr (SC1) asm volatile("global_load_dwordx4 %0, %1, %2 offset:%3 sc1" : "=v"(cx.Bv[KS - 16][T]) : "v"(voff), "s"(sb), "n"(off) : "memory");
        else asm volatile("global_load_dwordx4 %0, %1, %2 offset:%3" : "=v"(cx.Bv[KS - 16][T]) : "v"(voff), "s"(sb), "n"(off) : "memory");
    }
}
template <int S, int P> __device__ __forceinline__ void dma_m0(const Ctx& c) {
    asm volatile("s_mov_b32 m0, %0" :: "s"(c.dmadst + (u32)((S & 3) * STG + (P >> 2) * 4096)) : "memory");
}
constexpr int nat_kstep(int ks2) {
    if (ks2 < 10) { constexpr int T[5] = {0, 1, 5, 6, 4}; return 9 * (ks2 / 5) + T[ks2 % 5]; }
    const int jj = (ks2 - 10) >> 1, h = (ks2 - 10) & 1;
    return 9 * (jj >> 1) + ((jj & 1) ? 7 : 2) + h;
}
template <int S, int P> __device__ __forceinline__ void dma_go(const Ctx& c) {
    if constexpr (S < 9) {
        const char* sb = c.w1src + (size_t)S * STG + (P >> 2) * 4096;
        asm volatile("global_load_lds_dwordx4 %0, %1 offset:%2 sc1" :: "v"(c.lane16), "s"(sb), "n"((P & 3) * 1024) : "memory");
    } else {
        constexpr int n0 = nat_kstep(2 * (S - 9)), n1 = nat_kstep(2 * (S - 9) + 1);
        const char* sb = c.w2src + (size_t)(c.sub ? n1 : n0) * (32 * D_ * 2);
        asm volatile("global_load_lds_dwordx4 %0, %1 offset:%2" :: "v"(c.voff2[P]), "s"(sb), "n"((P & 3) * 1024) : "memory");
    }
}
template <int ID> __device__ __forceinline__ void load_b(Ctx& c) {
    constexpr int ks = ID >> 2, t = ID & 3;
    gload<ks, t, true>(c, c.vo1[t], c.bsrc + (ks >> 2) * 4096);
}
template <int ID> __device__ __forceinline__ void load_h(Ctx& c) {
    constexpr int kk = ID >> 2, t = ID & 3;
    gload<kk, t, true>(c, c.vo3[t], c.hsrc + (kk >> 2) * 4096);
}
template <int KS> __device__ __forceinline__ void pin_b(Ctx& c) {
#pragma unroll
    for (int t = 0; t < 4; ++t) { if constexpr (KS >= 16) asm volatile("" : "+v"(c.Bv[KS - 16][t])); }
}
template <int V, int P> __device__ __forceinline__ void epi_piece(Ctx& c) {
    constexpr int pr = P >> 3, st = P & 7, t = pr >> 1, r = 2 * (pr & 1), par = V % 3;
    if constexpr (st == 0) {
        asm volatile("v_mul_f32 %0, 0xbfb8aa3b, %1" : "=v"(c.e0) : "v"(c.ac[par][0][t][r]));
        asm volatile("v_mul_f32 %0, 0xbfb8aa3b, %1" : "=v"(c.e1) : "v"(c.ac[par][0][t][r + 1]));
    } else if constexpr (st == 1) asm volatile("v_exp_f32 %0, %0" :: "v"(c.e0));
    else if constexpr (st == 2) asm volatile("v_exp_f32 %0, %0" :: "v"(c.e1));
    else if constexpr (st == 3) {
        asm volatile("v_add_f32 %0, 1.0, %0" :: "v"(c.e0));
        asm volatile("v_add_f32 %0, 1.0, %0" :: "v"(c.e1));
    } else if constexpr (st == 4) asm volatile("v_rcp_f32 %0, %0" :: "v"(c.e0));
    else if constexpr (st == 5) asm volatile("v_rcp_f32 %0, %0" :: "v"(c.e1));
    else if constexpr (st == 6) {
        asm volatile("v_mul_f32 %0, %0, %1" :: "v"(c.e0), "v"(c.ac[par][1][t][r]));
        asm volatile("v_mul_f32 %0, %0, %1" :: "v"(c.e1), "v"(c.ac[par][1][t][r + 1]));
    } else {
        constexpr bool first = (V == 0 || V == 2 || V == 5 || V == 7);
        u32 o;
        asm volatile("v_cvt_pk_bf16_f32 %0, %1, %2" : "=v"(o) : "v"(c.e0), "v"(c.e1));
        c.hp[t][(first ? 0 : 2) + (pr & 1)] = o;
    }
}
template <int V, int T> __device__ __forceinline__ void store_h(Ctx& c) {
    const int m5 = 5 * (c.jj >> 1), ab = c.jj & 1;
    if constexpr (V == 4) {
        const u32x2 d = {c.hp[T][2], c.hp[T][3]};
        __builtin_amdgcn_raw_buffer_store_b64(d, c.hrs, c.vo2[T] + 8 * ab, c.hso + (m5 + 4) * 1024, 16);
    } else {
        const int kstep = (V == 1) ? m5 + 2 * ab : (V == 3) ? m5 + 2 * ab + 1 : (V == 6) ? 10 + 2 * c.jj : 11 + 2 * c.jj;
        __builtin_amdgcn_raw_buffer_store_b128(c.hp[T], c.hrs, c.vo2[T], c.hso + kstep * 1024, 16);
    }
}
template <int PART> __device__ __forceinline__ void poll_check(Ctx& c) {
    asm volatile("" : "+v"(c.pollv));
    const u32 e = c.epoch;
    if (!__all(c.pollv[0] == e && c.pollv[1] == e && c.pollv[2] == e && c.pollv[3] == e)) {
        const u32* p = c.flag_poll + (PART - 1) * 1024 + ((c.lane16 >> 4) & 3);
        poll_eq(p, e);
    }
}
template <int V, int P> __device__ __forceinline__ void epi_piece_padded(Ctx& c) {
    epi_piece<V, P>(c);
    if constexpr ((P & 7) == 2 || (P & 7) == 5) asm volatile("s_nop 0");
}
template <int V, int... P> __device__ __forceinline__ void epi_all(Ctx& c, std::integer_sequence<int, P...>) { (epi_piece_padded<V, P>(c), ...); }

template <int GS> __device__ __forceinline__ void slot(Ctx& c) {
    constexpr bool g1 = GS < G1S;
    constexpr int S = stage_of(GS), R = rel_of(GS);
    constexpr Work W = work_of(GS);
    constexpr bool f01 = GS < F01;
    if constexpr (GS == G1S) {
        asm volatile("s_nop 7" ::: "memory");
        epi_all<8>(c, std::make_integer_sequence<int, 64>{});
        store_h<8, 0>(c); store_h<8, 1>(c); store_h<8, 2>(c); store_h<8, 3>(c);
        SCHED();
    }
    if constexpr (f01 && GS % 32 == 0) { wait_vm<wait_for(GS, seq_b(2 * (GS / 32) + 1))>(); pin_b<2 * (GS / 32)>(c); pin_b<2 * (GS / 32) + 1>(c); }
    if constexpr (GS == 8 * 144) { wait_vm<wait_for(GS, seq_poll(1))>(); poll_check<1>(c); }
    if constexpr (!g1 && R == 0) { wait_vm<wait_for(GS, seq_h(2 * (S - 9) + 1))>(); pin_b<2 * (S - 9)>(c); pin_b<2 * (S - 9) + 1>(c); }
    if constexpr (dma_id(GS) >= 0) dma_m0<dma_id(GS) / 16, dma_id(GS) % 16>(c);
    if constexpr (g1) mfma<W.k, W.t, (W.k == 0)>(c, c.ac[W.unit % 3][W.mat][W.t], c.Aq[W.n % RQ]);
    else mfma<W.k, W.t, (W.k == 0)>(c, c.a2[W.unit][W.t], c.Aq[W.n % RQ]);
    if constexpr (barpt(GS)) {
        constexpr int n1 = wait_for(GS, seq_dma_last(S + 1));
        constexpr int n2 = (S == 12) ? wait_for(GS, seq_poll(2)) : 63;
        wait_vm<(n1 < n2 ? n1 : n2)>();
        if constexpr (S == 12) poll_check<2>(c);
        __builtin_amdgcn_s_barrier();
        asm volatile("" ::: "memory");
    }
    if constexpr (pub(GS)) {
        wait_vm<wait_for(GS, seq_store_last(pub(GS)))>();
        if (c.lane16 == 0) flag_set(c.flag_pub + (pub(GS) - 1) * 1024, c.epoch);
    }
    if constexpr (poll_issue(GS)) asm volatile("global_load_dwordx4 %0, %1, %2 sc1" : "=v"(c.pollv) : "v"(0u), "s"(c.flag_poll + (poll_issue(GS) - 1) * 1024) : "memory");
    {
        constexpr int f = PF.id[GS], fh = (GS & 3) == 1 ? PF.id[GS - ((GS & 3) == 1 ? 1 : 0)] : -1;
        if constexpr (f >= 0 && f < 324) c.Aq[f % RQ] = *(const LAS bf16x8*)(c.rd[frag_stage(f) & 3] + frag_off(f));
        constexpr int ft = f >= 324 ? f : fh >= 324 ? fh : -1;
        if constexpr (ft >= 0) {
            constexpr int half = f >= 324 ? 0 : 1;
            const s16x4 h = __builtin_amdgcn_ds_read_tr16_b64_v4i16((LAS s16x4*)(c.rd2[frag_stage(ft) & 3] + frag_off(ft) + half * (16 * 288)));
            const u32x2 hh = __builtin_bit_cast(u32x2, h);
            u32x4 a = __builtin_bit_cast(u32x4, c.Aq[ft % RQ]);
            a[2 * half] = hh[0]; a[2 * half + 1] = hh[1];
            c.Aq[ft % RQ] = __builtin_bit_cast(bf16x8, a);
        }
    }
    if constexpr (g1) {
        if constexpr (ES.ev[g1 ? GS : 0] >= 0) epi_piece<ES.ev[g1 ? GS : 0], ES.ep[g1 ? GS : 0]>(c);
        if constexpr (ES.sv[g1 ? GS : 0] >= 0) store_h<ES.sv[g1 ? GS : 0], ES.st[g1 ? GS : 0]>(c);
    }
    if constexpr (bload(GS) >= 0) load_b<bload(GS)>(c);
    if constexpr (hload(GS) >= 0) load_h<hload(GS)>(c);
    if constexpr (dma_id(GS) >= 0) dma_go<dma_id(GS) / 16, dma_id(GS) % 16>(c);
    SCHED();
}
template <int BASE, int... I> __device__ __forceinline__ void run_slots(Ctx& c, std::integer_sequence<int, I...>) { (slot<BASE + I>(c), ...); }
template <int S, int P> __device__ __forceinline__ void dma_one(const Ctx& c) { dma_m0<S, P>(c); asm volatile("s_nop 0"); dma_go<S, P>(c); }
template <int S, int... P> __device__ __forceinline__ void dma_stage(const Ctx& c, std::integer_sequence<int, P...>) { (dma_one<S, P>(c), ...); }
template <int... I> __device__ __forceinline__ void load_b_first(Ctx& c, std::integer_sequence<int, I...>) { (load_b<I>(c), ...); }
}

__device__ __forceinline__ void touch(u32& d, const char* p) { asm volatile("global_load_dword %0, %1, off" : "=v"(d) : "v"(p) : "memory"); }
__global__ __launch_bounds__(256, 1) void k_main(const u16* __restrict__ qin, const u16* __restrict__ kin, const u16* __restrict__ vin, const u16* __restrict__ shift_k,
                                                 const u16* __restrict__ ssm_k, const u16* __restrict__ ssm_D, const u16* __restrict__ wg, const u16* __restrict__ wu,
                                                 const u16* __restrict__ wd, const u16* __restrict__ cvec, const u16* __restrict__ w_mod,
                                                 char* __restrict__ ws, u16* __restrict__ out) {
    using namespace mlp;
    extern __shared__ __attribute__((aligned(16))) char smem[];
    const int tid = threadIdx.x, lane = tid & 63, w = __builtin_amdgcn_readfirstlane(tid >> 6);
    const int b = blockIdx.x, x = b & 7, idx = b >> 3, q = x >> 1;
    const int mt = 4 * (x & 1) + (idx & 3), fcl = idx >> 2, fc = 8 * q + fcl;
    const int mt2 = 8 * (x & 1) + (idx & 7), cc = idx >> 3, tg = w & 1, kh = w >> 1;
    const rsrc_t wsr = __builtin_amdgcn_make_buffer_rsrc(ws, 0, (int)WS_END, 0x00020000);
    u32 epoch_in;
    unsigned long long pc_start;
    asm volatile("s_getpc_b64 %0" : "=s"(pc_start));
    u32 epoch;
    {
        const int t0 = (x * 32 + idx) * 8;
        RowsPre rp;
        u32 bg[10] = {0, 0, 0, 0, 0, 0, 0, 0, 0, 0};
        const int c0 = 72 * x + 3 * idx;
        KvIn kin_;
        if (b < 192) {
            touch(bg[8], (const char*)(b < 96 ? kin : vin) + (size_t)((b % 96) * 192 + (tid < 192 ? tid : 0)) * 128);
            kv_load(kin_, c0, kin, vin, shift_k);
        }
        int b2 = b;
        asm volatile("" : "+s"(b2));
        if (b2 < 192) {
            ConvIn in[3];
            epoch_in = __hip_atomic_load(flag_ptr(ws, F_EPOCH), __ATOMIC_RELAXED, __HIP_MEMORY_SCOPE_AGENT);
            conv_load_k(in[0], c0, ssm_k, ssm_D); conv_load_k(in[1], c0 + 1, ssm_k, ssm_D); conv_load_k(in[2], c0 + 2, ssm_k, ssm_D);
            rows_load(rp, t0, qin);
            kv_put(kin_, smem + C_RED);
            conv_put_k(in[0], smem); conv_put_k(in[1], smem + C_CH); conv_put_k(in[2], smem + 2 * C_CH);
            __syncthreads();
            kv_compute(kin_, c0, smem + C_RED, smem + C_XR, C_CH);
            epoch = __builtin_amdgcn_readfirstlane(epoch_in) + 1u;
            if (w == 3) {
                DRAIN();
#pragma unroll
                for (int i = 0; i < 8; ++i) { const int l = lane + 64 * (i & 3); touch(bg[i], (const char*)(i < 4 ? wg : wu) + (size_t)b * 27648 + (l < 216 ? l : 0) * 128); }
            }
            ModIn mi;
            const u32* mf = flag_ptr(ws, F_MOD + (lane < 54 ? lane : 0));
            u32 pm;
            asm volatile("global_load_dword %0, %1, off sc1" : "=v"(pm) : "v"(mf) : "memory");
            conv_body(c0, in, smem, wsr, [&]() {
                DRAIN();
                asm volatile("" : "+v"(pm));
                if (!__all(pm == epoch)) poll_eq(mf, epoch);
                mod_load(mi, wsr); });
            mod_put(mi, smem);
            DRAIN();
#pragma unroll
            for (int i = 0; i < 10; ++i) asm volatile("" :: "v"(bg[i]));
            __syncthreads();
            if (tid < 8) flag_set(flag_ptr(ws, F_CONV + tid * 256 + b), epoch);
            if (w == 3) {
#pragma unroll
                for (int i = 0; i < 4; ++i) { const int l = lane + 64 * i; touch(bg[i], (const char*)wd + (size_t)b * 27648 + (l < 216 ? l : 0) * 128); }
            }
        } else {
            if (w == 3 && idx < 28) {
                const char* cb = (const char*)(pc_start & ~127ull) + lane * 128;
#pragma unroll
                for (int i = 0; i < 8; ++i) touch(bg[i], cb + (i < 7 || lane < 32 ? i * 8192 : 0));
            }
            if (w == 3 && idx >= 28) {
#pragma unroll
                for (int i = 0; i < 3; ++i) { const size_t o = (size_t)(((idx - 28) * 3 + i) * 64 + lane) << 16; touch(bg[i], ws + (o < WS_END ? o : 0)); }
                touch(bg[3], (const char*)out + (size_t)(lane < 36 ? lane : 0) * 65536);
                const size_t pg = (size_t)(lane + 64 * (idx & 1) < 81 ? lane + 64 * (idx & 1) : 0) * 65536;
                touch(bg[4], (const char*)wd + pg); touch(bg[5], (const char*)wg + pg); touch(bg[6], (const char*)wu + pg);
            }
            for (int i = 0; i < 40; ++i) __builtin_amdgcn_s_sleep(2);
            rows_load(rp, t0, qin);
            epoch_in = __hip_atomic_load(flag_ptr(ws, F_EPOCH), __ATOMIC_RELAXED, __HIP_MEMORY_SCOPE_AGENT);
            if (b - 192 < 54) mod_job(b - 192, cvec, w_mod, ws, (float*)(smem + R_MOD), (float*)(smem + R_MOD + 4096));
            epoch = __builtin_amdgcn_readfirstlane(epoch_in) + 1u;
            DRAIN();
            __syncthreads();
            if (tid == 0 && b - 192 < 54) flag_set(flag_ptr(ws, F_MOD + b - 192), epoch);
            tile_jobs(b - 192, wg, wu, smem, ws, wsr, epoch);
            poll_eq(flag_ptr(ws, F_MOD + (lane < 54 ? lane : 0)), epoch);
            ModIn mi;
            mod_load(mi, wsr);
            mod_put(mi, smem);
            __syncthreads();
        }
        rows_pre(rp, smem);
        if (w == 0) {
            poll_eq4(wsr, (u32)WS_CNT + (F_CONV + x * 256 + (lane < 48 ? lane : 0) * 4) * 4, epoch);
            rows_fetch(t0, smem, ws);
        }
        __syncthreads();
        rows_main(rp, t0, smem, wsr);
        DRAIN();
#pragma unroll
        for (int i = 0; i < 10; ++i) asm volatile("" :: "v"(bg[i]));
        __syncthreads();
        if (tid == 0) flag_set(flag_ptr(ws, F_ROWS + (t0 >> 6) * 8 + ((t0 >> 3) & 7)), epoch);
    }
    Ctx c;
#pragma unroll
    for (int t = 0; t < 4; ++t) c.hp[t] = (u32x4){0, 0, 0, 0};
    c.lane16 = lane * 16;
#pragma unroll
    for (int t = 0; t < 4; ++t) { c.vo1[t] = lane * 16 + t * 18432; c.vo2[t] = lane * 16 + t * TUS; c.vo3[t] = lane * 16 + ((t + 2 * kh) & 3) * TUS; }
    const LAS char* l0 = (const LAS char*)smem;
#pragma unroll
    for (int i = 0; i < 4; ++i) {
        c.rd[i] = l0 + i * STG + lane * 16; c.rd2[i] = l0 + i * STG + kh * 18432 + (4 * (lane >> 4) + ((lane & 15) >> 2)) * 288 + 8 * (lane & 3);
        asm volatile("" : "+v"(c.rd[i])); asm volatile("" : "+v"(c.rd2[i]));
    }
#pragma unroll
    for (int i = 0; i < 9; ++i) { const int ci = lane + 64 * i, row = (ci * 3641) >> 16, c16 = ci - row * 18; c.voff2[i] = (u32)(row * (D_ * 2) + c16 * 16 - (i & 3) * 1024); }
    c.sub = w & 1;
    c.w1src = ws + WS_WT1 + (size_t)fc * 331776 + w * 9216;
    c.w2src = (const char*)(wd + (size_t)(36 * q + 18 * kh) * 32 * D_ + cc * 144);
    c.bsrc = ws + WS_H2T + (size_t)(16 * mt + 4 * w) * 18432;
    c.hsrc = ws + WS_HT + (size_t)(8 * mt2 + 4 * tg) * TUS + (size_t)((q * 2 + kh) * 18) * 1024;
    c.dmadst = (u32)(uintptr_t)smem + w * 9216;
    c.jj = fcl & 3;
    c.hso = (u32)((16 * mt + 4 * w) * TUS + ((q * 2 + (fcl >> 2)) * 18) * 1024);
    c.hrs = __builtin_amdgcn_make_buffer_rsrc(ws + WS_HT, 0, (int)((size_t)S_ * F_ * 2), 0x00020000);
    c.epoch = epoch;
    c.flag_pub = flag_ptr(ws, F_H + ((((mt * 4 + w) * 4 + q) * 2 + (fcl >> 2))) * 4 + (fcl & 3));
    c.flag_poll = flag_ptr(ws, F_H + (((((mt2 >> 1) * 4 + 2 * (mt2 & 1) + tg) * 4 + q) * 2 + kh)) * 4);
    asm volatile("" ::: "a0", "a1", "a2", "a3", "a4", "a5", "a6", "a7", "a8", "a9", "a10", "a11", "a12", "a13", "a14", "a15", "a16", "a17", "a18", "a19", "a20", "a21", "a22", "a23", "a24", "a25", "a26", "a27", "a28", "a29", "a30", "a31", "a32", "a33", "a34", "a35", "a36", "a37", "a38", "a39", "a40", "a41", "a42", "a43", "a44", "a45", "a46", "a47", "a48", "a49", "a50", "a51", "a52", "a53", "a54", "a55", "a56", "a57", "a58", "a59", "a60", "a61", "a62", "a63", "a64", "a65", "a66", "a67", "a68", "a69", "a70", "a71", "a72", "a73", "a74", "a75", "a76", "a77", "a78", "a79", "a80", "a81", "a82", "a83", "a84", "a85", "a86", "a87", "a88", "a89", "a90", "a91", "a92", "a93", "a94", "a95", "a96", "a97", "a98", "a99", "a100", "a101", "a102", "a103", "a104", "a105", "a106", "a107", "a108", "a109", "a110", "a111", "a112", "a113", "a114", "a115", "a116", "a117", "a118", "a119", "a120", "a121", "a122", "a123", "a124", "a125", "a126", "a127", "a128", "a129", "a130", "a131", "a132", "a133", "a134", "a135", "a136", "a137", "a138", "a139", "a140", "a141", "a142", "a143", "a144", "a145", "a146", "a147", "a148", "a149", "a150", "a151", "a152", "a153", "a154", "a155", "a156", "a157", "a158", "a159", "a160", "a161", "a162", "a163", "a164", "a165", "a166", "a167", "a168", "a169", "a170", "a171", "a172", "a173", "a174", "a175", "a176", "a177", "a178", "a179", "a180", "a181", "a182", "a183", "a184", "a185", "a186", "a187", "a188", "a189", "a190", "a191", "a192", "a193", "a194", "a195", "a196", "a197", "a198", "a199", "a200", "a201", "a202", "a203", "a204", "a205", "a206", "a207", "a208", "a209", "a210", "a211", "a212", "a213", "a214", "a215", "a216", "a217", "a218", "a219", "a220", "a221", "a222", "a223", "a224", "a225", "a226", "a227", "a228", "a229", "a230", "a231", "a232", "a233", "a234", "a235", "a236", "a237", "a238", "a239", "a240", "a241", "a242", "a243", "a244", "a245", "a246", "a247", "a248", "a249", "a250", "a251", "a252", "a253", "a254", "a255");
    poll_eq(flag_ptr(ws, F_TILE + (fc >> 2) * 36 + (lane < 36 ? lane : 0)), epoch);
    dma_stage<0>(c, std::make_integer_sequence<int, 9>{});
    dma_stage<1>(c, std::make_integer_sequence<int, 9>{});
    dma_stage<2>(c, std::make_integer_sequence<int, 9>{});
    poll_eq(flag_ptr(ws, F_ROWS + (4 * mt + w) * 8 + (lane & 7)), epoch);
    load_b_first(c, std::make_integer_sequence<int, 4 * LA>{});
    wait_vm<4 * LA>();
    __builtin_amdgcn_s_barrier();
    asm volatile("" ::: "memory");
#pragma unroll
    for (int f = 0; f < FR0; ++f) c.Aq[f] = *(const LAS bf16x8*)(c.rd[frag_stage(f)] + frag_off(f));
    SCHED();
    run_slots<0>(c, std::make_integer_sequence<int, 288>{});
    run_slots<288>(c, std::make_integer_sequence<int, 144 * 3>{});
    run_slots<144 * 5>(c, std::make_integer_sequence<int, 144 * 4>{});
    run_slots<G1S>(c, std::make_integer_sequence<int, 324>{});
    run_slots<G1S + 324>(c, std::make_integer_sequence<int, 324>{});
    asm volatile("s_nop 7" ::: "memory");
#pragma unroll
    for (int u = 0; u < 9; ++u)
#pragma unroll
        for (int t = 0; t < 4; ++t) asm volatile("" : "+v"(c.a2[u][t]));
    SCHED();
    const int n = lane & 15, g = lane >> 4;
    u32x2 xr[3][2]; f32x4 gb[3];
#pragma unroll
    for (int i = 0; i < 3; ++i) {
        const int u = (w + 4 * i < 9) ? w + 4 * i : 8, d = 144 * cc + 16 * u + 4 * g;
#pragma unroll
        for (int t = 0; t < 2; ++t) xr[i][t] = __builtin_amdgcn_raw_buffer_load_b64(wsr, (u32)WS_X1 + (u32)((128 * mt2 + 32 * q + 16 * t + n) * D_ + d) * 2, 0, 16);
        gb[i] = *(const f32x4*)(smem + R_MOD + (5 * 576 + d) * 4);
    }
    {
        char* ex = smem + 73728 + w * 18432 + lane * 16;
#pragma unroll
        for (int t = 0; t < 2; ++t)
#pragma unroll
            for (int u = 0; u < 9; ++u) *(f32x4*)(ex + (t * 9 + u) * 1024) = c.a2[u][2 + t];
    }
    __syncthreads();
    const int j = 2 * tg + kh;
    u32* fred = flag_ptr(ws, F_RED + (mt2 * 4 + cc) * 16);
    const u32 slab = (u32)WS_SLAB + (u32)((mt2 * 4 + cc) * 16) * 9216;
    {
        const char* ey = smem + 73728 + (w ^ 2) * 18432 + lane * 16;
        char* own = smem + lane * 16;
#pragma unroll
        for (int u = 0; u < 9; ++u) {
            const f32x4 r0 = c.a2[u][0] + *(const f32x4*)(ey + u * 1024);
            const f32x4 r1 = c.a2[u][1] + *(const f32x4*)(ey + (9 + u) * 1024);
            if (j != q) st16_dev(wsr, slab + (j * 4 + q) * 9216 + u * 1024 + lane * 16, (u32x4){pk2(r0[0], r0[1]), pk2(r0[2], r0[3]), pk2(r1[0], r1[1]), pk2(r1[2], r1[3])});
            else { *(f32x4*)(own + u * 2048) = r0; *(f32x4*)(own + u * 2048 + 1024) = r1; }
        }
    }
    if (j != q) {
        DRAIN();
        if (lane == 0) flag_set(fred + j * 4 + q, epoch);
    }
    poll_eq(fred + q * 4 + (((lane & 3) == q) ? (q ^ 1) : (lane & 3)), epoch);
    __syncthreads();
    u32x4 rem[3][3];
#pragma unroll
    for (int i = 0; i < 3; ++i) {
        const int u = (w + 4 * i < 9) ? w + 4 * i : 8;
#pragma unroll
        for (int k = 0; k < 3; ++k) { const int qq = k + (k >= q ? 1 : 0); rem[i][k] = __builtin_amdgcn_raw_buffer_load_b128(wsr, slab + (q * 4 + qq) * 9216 + u * 1024 + lane * 16, 0, 16); }
    }
#pragma unroll
    for (int i = 0; i < 3; ++i) {
        const int u = w + 4 * i;
        if (u < 9) {
            const int d = 144 * cc + 16 * u + 4 * g;
#pragma unroll
            for (int t = 0; t < 2; ++t) {
                f32x4 s = *(const f32x4*)(smem + u * 2048 + t * 1024 + lane * 16);
#pragma unroll
                for (int k = 0; k < 3; ++k) {
                    const u32 a = rem[i][k][2 * t], bq = rem[i][k][2 * t + 1];
                    s += (f32x4){bf_lo(a), bf_hi(a), bf_lo(bq), bf_hi(bq)};
                }
                const float o0 = bf_lo(xr[i][t][0]) + rbf(gb[i][0] * rbf(s[0])), o1 = bf_hi(xr[i][t][0]) + rbf(gb[i][1] * rbf(s[1]));
                const float o2 = bf_lo(xr[i][t][1]) + rbf(gb[i][2] * rbf(s[2])), o3 = bf_hi(xr[i][t][1]) + rbf(gb[i][3] * rbf(s[3]));
                *(u32x2*)(out + (size_t)(128 * mt2 + 32 * q + 16 * t + n) * D_ + d) = (u32x2){pk2(o0, o1), pk2(o2, o3)};
            }
        }
    }
    if (b == 0 && tid == 0) flag_set(flag_ptr(ws, F_EPOCH), epoch);
}

extern "C" void kernel_launch(void* const* d_in, const int* in_sizes, int n_in, void* d_out, int out_size, void* d_ws, size_t ws_size,
                              hipStream_t stream) {
    const u16* q = (const u16*)d_in[0];
    const u16* k = (const u16*)d_in[1];
    const u16* v = (const u16*)d_in[2];
    const u16* shift_k = (const u16*)d_in[3];
    const u16* ssm_k = (const u16*)d_in[4];
    const u16* ssm_D = (const u16*)d_in[5];
    const u16* wg = (const u16*)d_in[7];
    const u16* wu = (const u16*)d_in[8];
    const u16* wd = (const u16*)d_in[9];
    const u16* c = (const u16*)d_in[10];
    const u16* w_mod = (const u16*)d_in[11];
    char* ws = (char*)d_ws;
    (void)hipFuncSetAttribute((const void*)k_main, hipFuncAttributeMaxDynamicSharedMemorySize, mlp::LDS_BYTES);
    k_main<<<256, 256, mlp::LDS_BYTES, stream>>>(q, k, v, shift_k, ssm_k, ssm_D, wg, wu, wd, c, w_mod, ws, (u16*)d_out);
}
```
